```python
import torch

def _softmax(x, axis=-1):
    return torch.softmax(x, dim=axis)
def _gelu(x):
    return torch.nn.functional.gelu(x, approximate="tanh")
def _silu(x):
    return torch.nn.functional.silu(x)
def _sigmoid(x):
    return torch.sigmoid(x)
def _layernorm(x, axis=-1, eps=1e-5):
    m = x.mean(dim=axis, keepdim=True)
    v = x.var(dim=axis, keepdim=True, unbiased=False)
    return (x - m) / torch.sqrt(v + eps)
def _scatter_add(tgt, idx, src, axis=0):
    return tgt.clone().index_add_(axis, idx, src)
def _segment_sum(src, seg, n):
    out = torch.zeros((n,) + src.shape[1:], dtype=src.dtype)
    return out.index_add_(0, seg, src)
def _pad(x, pw):
    flat = tuple(v for pair in reversed(pw) for v in pair)
    return torch.nn.functional.pad(x, flat)

kernel_name = 'interleave_layers__linear_kernel_hedgehog__ssm_conv_h3__prompt'

def setup_inputs():

    _w = (lambda a: (lambda t: t.to(torch.bfloat16) if t.is_floating_point() else t)(torch.as_tensor(a)))
    def _A_setup():
        B, S, D, H = 1, 2048, 576, 32
        Dh = D // H
        return {
            "x": _w((lambda *s: torch.randn(*s, dtype=torch.bfloat16) * 0.05)(B, S, D)),
            "wqkv": _w((lambda *s: torch.randn(*s, dtype=torch.bfloat16) * 0.4)(D, 3 * D)),
            "w_phi": _w((lambda *s: torch.randn(*s, dtype=torch.bfloat16) * 0.4)(H, Dh, Dh) * (Dh ** -0.5)),
            "wo": _w((lambda *s: torch.randn(*s, dtype=torch.bfloat16) * 0.05)(D, D)),
        }
    _A_in = _A_setup()

    def _B_setup():
        B, S, D = 1, 2048, 576
        ks = 4
        return {
            "q": _w((lambda *s: torch.randn(*s, dtype=torch.bfloat16) * 0.05)(B, S, D)),
            "k": _w((lambda *s: torch.randn(*s, dtype=torch.bfloat16) * 0.8)(B, S, D)),
            "v": _w((lambda *s: torch.randn(*s, dtype=torch.bfloat16) * 0.8)(B, S, D)),
            "shift_k": _w((lambda *s: torch.randn(*s, dtype=torch.bfloat16) * 0.8)(D, ks) * 0.5),
            "ssm_k": _w((lambda *s: torch.randn(*s, dtype=torch.bfloat16) * 1.6)(D, S) * (1.0 / S ** 0.5)),
            "ssm_D": _w((lambda *s: torch.randn(*s, dtype=torch.bfloat16) * 0.8)(D)),
        }
    _B_in = _B_setup()

    return {
        "A_x": _A_in["x"],
        "A_wqkv": _A_in["wqkv"],
        "A_w_phi": _A_in["w_phi"],
        "A_wo": _A_in["wo"],
        "B_q": _B_in["q"],
        "B_k": _B_in["k"],
        "B_v": _B_in["v"],
        "B_shift_k": _B_in["shift_k"],
        "B_ssm_k": _B_in["ssm_k"],
        "B_ssm_D": _B_in["ssm_D"],
    }

def reference(A_x, A_wqkv, A_w_phi, A_wo, B_q, B_k, B_v, B_shift_k, B_ssm_k, B_ssm_D):
    def _A_ref(x, wqkv, w_phi, wo):
        B, S, D = x.shape
        H = 32; Dh = D // H
        qkv = (lambda x, *d: x.permute(*d))((x @ wqkv).reshape(B, S, 3, H, Dh), 2, 0, 3, 1, 4)
        q, k, v = qkv[0], qkv[1], qkv[2]
        pq = torch.einsum("bhsd,hdf->bhsf", q, w_phi)
        pk = torch.einsum("bhsd,hdf->bhsf", k, w_phi)
        phi_q = torch.concatenate([_softmax(pq, axis=-1), _softmax(-pq, axis=-1)], axis=-1)
        phi_k = torch.concatenate([_softmax(pk, axis=-1), _softmax(-pk, axis=-1)], axis=-1)
        kv = torch.einsum("bhsf,bhsd->bhfd", phi_k, v)
        z  = torch.einsum("bhsf->bhf", phi_k)
        num = torch.einsum("bhqf,bhfd->bhqd", phi_q, kv)
        den = torch.einsum("bhqf,bhf->bhq", phi_q, z)[..., None] + 1e-6
        o = num / den
        return (lambda x, *d: x.permute(*d))(o, 0, 2, 1, 3).reshape(B, S, D) @ wo

    def _B_ref(q, k, v, shift_k, ssm_k, ssm_D):
        B, S, D = q.shape
        ks = shift_k.shape[-1]
        k_pad = _pad(k, ((0, 0), (ks - 1, 0), (0, 0)))
        k_shift = torch.zeros_like(k)
        for t in range(ks):
            k_shift = k_shift + k_pad[:, t:t + S, :] * shift_k[:, ks - 1 - t][None, None, :]
        kv = k_shift * v
        L = 2 * S
        kv_pad = _pad(kv, ((0, 0), (0, S), (0, 0)))
        kf_pad = _pad(ssm_k, ((0, 0), (0, S)))
        kv_f = (lambda x, axes=(-2,-1): torch.fft.rfft2(x.float(), dim=axes))(kv_pad[:, None, :, :], axes=(1, 2))
        kf_f = (lambda x, axes=(-2,-1): torch.fft.rfft2(x.float(), dim=axes))(kf_pad[:, None, :], axes=(1, 2))
        kf_f = (lambda x, *d: x.permute(*d))(kf_f, 1, 2, 0)
        y_f = kv_f * kf_f[None, :, :, :]
        y = (lambda x, s, axes=(-2,-1): torch.fft.irfft2(x, s=s, dim=axes).to(torch.bfloat16))(y_f, (1, L), axes=(1, 2))[:, 0, :S, :]
        y = y + kv * ssm_D[None, None, :]
        return q * y

    x = A_x
    x = x + _A_ref(_layernorm(x, axis=-1), A_wqkv, A_w_phi, A_wo)
    x = x + _B_ref(_layernorm(x, axis=-1), B_k, B_v, B_shift_k, B_ssm_k, B_ssm_D)
    x = x + _A_ref(_layernorm(x, axis=-1), A_wqkv, A_w_phi, A_wo)
    x = x + _B_ref(_layernorm(x, axis=-1), B_k, B_v, B_shift_k, B_ssm_k, B_ssm_D)
    return x
```

```cpp
#include <hip/hip_runtime.h>
#include <hip/hip_bf16.h>
#include <stdint.h>
typedef unsigned short u16;
#define S_ 2048
#define D_ 576
#define H_ 32
#define DH 18
__device__ __forceinline__ float b2f(u16 v) { return __uint_as_float(((unsigned)v) << 16); }
__device__ __forceinline__ u16 f2b(float f) { __hip_bfloat16 h = __float2bfloat16(f); return *(u16*)&h; }
__device__ __forceinline__ float rb(float f) { return b2f(f2b(f)); }

__global__ void k_u(const u16* k, const u16* v, const u16* sk, u16* uT) {
    int i = blockIdx.x * 256 + threadIdx.x; int t = i / D_, d = i % D_;
    float acc = 0.f;
    for (int j = 0; j < 4; ++j) {
        int s = t + j - 3; float kv = s >= 0 ? b2f(k[s * D_ + d]) : 0.f;
        acc = rb(acc + rb(kv * b2f(sk[d * 4 + 3 - j])));
    }
    uT[d * S_ + t] = f2b(acc * b2f(v[i]));
}
__global__ void k_conv(const u16* uT, const u16* h, const u16* Dv, u16* y) {
    __shared__ float su[S_], sh[S_];
    int d = blockIdx.x;
    for (int i = threadIdx.x; i < S_; i += 256) { su[i] = b2f(uT[d * S_ + i]); sh[i] = b2f(h[d * S_ + i]); }
    __syncthreads();
    for (int t = threadIdx.x; t < S_; t += 256) {
        float a = 0.f;
        for (int s = 0; s <= t; ++s) a += sh[t - s] * su[s];
        y[t * D_ + d] = f2b(rb(a) + rb(su[t] * b2f(Dv[d])));
    }
}
__global__ void k_ln(const u16* x, u16* xn) {
    int t = blockIdx.x; int l = threadIdx.x;
    float v[9]; float s = 0.f;
    for (int j = 0; j < 9; ++j) { v[j] = b2f(x[t * D_ + l + 64 * j]); s += v[j]; }
    for (int o = 32; o; o >>= 1) s += __shfl_xor(s, o);
    float m = s / D_; float q = 0.f;
    for (int j = 0; j < 9; ++j) { float dd = v[j] - m; q += dd * dd; }
    for (int o = 32; o; o >>= 1) q += __shfl_xor(q, o);
    float mb = rb(m), var = rb(q / D_); float sd = rb(sqrtf(rb(var + 1e-5f)));
    for (int j = 0; j < 9; ++j) xn[t * D_ + l + 64 * j] = f2b(rb(v[j] - mb) / sd);
}
__global__ void k_gemm(const u16* A, const u16* B, u16* C, const u16* res, int N, int K) {
    __shared__ float sa[16][33], sb[32][17];
    int tx = threadIdx.x & 15, ty = threadIdx.x >> 4;
    int m0 = blockIdx.y * 16, n0 = blockIdx.x * 16;
    float acc = 0.f;
    for (int k0 = 0; k0 < K; k0 += 32) {
        for (int i = threadIdx.x; i < 512; i += 256) { sa[i >> 5][i & 31] = b2f(A[(m0 + (i >> 5)) * K + k0 + (i & 31)]); sb[i >> 4][i & 15] = b2f(B[(k0 + (i >> 4)) * N + n0 + (i & 15)]); }
        __syncthreads();
        for (int kk = 0; kk < 32; ++kk) acc += sa[ty][kk] * sb[kk][tx];
        __syncthreads();
    }
    float r = rb(acc);
    if (res) r = r + b2f(res[(m0 + ty) * N + n0 + tx]);
    C[(m0 + ty) * N + n0 + tx] = f2b(r);
}
__global__ void __launch_bounds__(256) k_phi(const u16* qkv, const u16* wphi, u16* phi) {
    int i = blockIdx.x * 256 + threadIdx.x;
    int t = i % S_, h = (i / S_) % H_, w = i / (S_ * H_);
    float q[DH], p[DH];
    for (int d = 0; d < DH; ++d) q[d] = b2f(qkv[t * 1728 + w * D_ + h * DH + d]);
    float mx = -1e30f, mn = 1e30f;
    #pragma unroll
    for (int f = 0; f < DH; ++f) { float a = 0.f; for (int d = 0; d < DH; ++d) a += q[d] * b2f(wphi[(h * DH + d) * DH + f]); p[f] = rb(a); mx = fmaxf(mx, p[f]); mn = fminf(mn, p[f]); }
    float s1 = 0.f, s2 = 0.f;
    #pragma unroll
    for (int f = 0; f < DH; ++f) { s1 += expf(p[f] - mx); s2 += expf(mn - p[f]); }
    #pragma unroll
    for (int f = 0; f < DH; ++f) { phi[(size_t)i * 36 + f] = f2b(expf(p[f] - mx) / s1); phi[(size_t)i * 36 + 18 + f] = f2b(expf(mn - p[f]) / s2); }
}
__global__ void k_kv(const u16* phi, const u16* qkv, float* kv) {
    int h = blockIdx.x; int e = blockIdx.y * 64 + threadIdx.x; if (e >= 36 * 19) return;
    int f = e / 19, d = e % 19; float a = 0.f;
    const u16* pk = phi + (size_t)(H_ + h) * S_ * 36;
    for (int t = 0; t < S_; ++t) a += b2f(pk[t * 36 + f]) * (d < 18 ? b2f(qkv[t * 1728 + 2 * D_ + h * DH + d]) : 1.f);
    kv[h * 684 + e] = rb(a);
}
__global__ void k_o(const u16* phi, const float* kv, u16* o) {
    int i = blockIdx.x * 256 + threadIdx.x; int t = i / D_, c = i % D_, h = c / DH, d = c % DH;
    const u16* pq = phi + ((size_t)h * S_ + t) * 36; float n = 0.f, dn = 0.f;
    for (int f = 0; f < 36; ++f) { float p = b2f(pq[f]); n += p * kv[h * 684 + f * 19 + d]; dn += p * kv[h * 684 + f * 19 + 18]; }
    o[i] = f2b(rb(n) / rb(rb(dn) + 1e-6f));
}
__global__ void k_gate(const u16* x, const u16* xn, const u16* y, u16* out) {
    int i = blockIdx.x * 256 + threadIdx.x;
    out[i] = f2b(b2f(x[i]) + rb(b2f(xn[i]) * b2f(y[i])));
}
__global__ void k_empty(int* p) { if (p == nullptr) p[0] = 1; }
#define EXTRA 0
extern "C" void kernel_launch(void* const* d_in, const int* in_sizes, int n_in,
                              void* d_out, int out_size, void* d_ws, size_t ws_size,
                              hipStream_t stream) {
    const u16* x = (const u16*)d_in[0]; const u16* wqkv = (const u16*)d_in[1]; const u16* wphi = (const u16*)d_in[2]; const u16* wo = (const u16*)d_in[3];
    const u16* bk = (const u16*)d_in[5]; const u16* bv = (const u16*)d_in[6]; const u16* sk = (const u16*)d_in[7]; const u16* sh = (const u16*)d_in[8]; const u16* sD = (const u16*)d_in[9];
    char* ws = (char*)d_ws; const size_t MB = 1 << 20;
    u16* uT = (u16*)(ws); u16* y = (u16*)(ws + 4 * MB); u16* xn = (u16*)(ws + 8 * MB); u16* qkv = (u16*)(ws + 12 * MB);
    u16* phi = (u16*)(ws + 24 * MB); float* kv = (float*)(ws + 40 * MB); u16* o = (u16*)(ws + 44 * MB); u16* x1 = (u16*)(ws + 48 * MB); u16* x2 = (u16*)(ws + 52 * MB);
    int NE = S_ * D_ / 256;
    k_u<<<NE, 256, 0, stream>>>(bk, bv, sk, uT);
    k_conv<<<D_, 256, 0, stream>>>(uT, sh, sD, y);
    const u16* xin = x;
    for (int L = 0; L < 2; ++L) {
        k_ln<<<S_, 64, 0, stream>>>(xin, xn);
        k_gemm<<<dim3(1728 / 16, S_ / 16), 256, 0, stream>>>(xn, wqkv, qkv, nullptr, 1728, D_);
        k_phi<<<2 * H_ * S_ / 256, 256, 0, stream>>>(qkv, wphi, phi);
        k_kv<<<dim3(H_, 11), 64, 0, stream>>>(phi, qkv, kv);
        k_o<<<NE, 256, 0, stream>>>(phi, kv, o);
        k_gemm<<<dim3(D_ / 16, S_ / 16), 256, 0, stream>>>(o, wo, x1, xin, D_, D_);
        k_ln<<<S_, 64, 0, stream>>>(x1, xn);
        u16* dst = L == 0 ? x2 : (u16*)d_out;
        k_gate<<<NE, 256, 0, stream>>>(x1, xn, y, dst);
        xin = x2;
    }
    for (int i = 0; i < EXTRA; ++i) k_empty<<<256, 256, 0, stream>>>((int*)d_ws);
}
```

```cpp
#define VARIANT 0
#include <hip/hip_runtime.h>
#include <hip/hip_bf16.h>
#include <stdint.h>
typedef unsigned short u16;
#define S_ 2048
#define D_ 576
#define H_ 32
#define DH 18
__device__ __forceinline__ float b2f(u16 v) { return __uint_as_float(((unsigned)v) << 16); }
__device__ __forceinline__ u16 f2b(float f) { __hip_bfloat16 h = __float2bfloat16(f); return *(u16*)&h; }
__device__ __forceinline__ float rb(float f) { return b2f(f2b(f)); }

__global__ void k_u(const u16* k, const u16* v, const u16* sk, u16* uT) {
    int i = blockIdx.x * 256 + threadIdx.x; int t = i / D_, d = i % D_;
    float acc = 0.f;
    for (int j = 0; j < 4; ++j) {
        int s = t + j - 3; float kv = s >= 0 ? b2f(k[s * D_ + d]) : 0.f;
        acc = rb(acc + rb(kv * b2f(sk[d * 4 + 3 - j])));
    }
    uT[d * S_ + t] = f2b(acc * b2f(v[i]));
}
__global__ void k_conv(const u16* uT, const u16* h, const u16* Dv, u16* y) {
    __shared__ float su[S_], sh[S_];
    int d = blockIdx.x;
    for (int i = threadIdx.x; i < S_; i += 256) { su[i] = b2f(uT[d * S_ + i]); sh[i] = b2f(h[d * S_ + i]); }
    __syncthreads();
    for (int t = threadIdx.x; t < S_; t += 256) {
        float a = 0.f;
        for (int s = 0; s <= t; ++s) a += sh[t - s] * su[s];
        y[t * D_ + d] = f2b(rb(a) + rb(su[t] * b2f(Dv[d])));
    }
}
__global__ void k_ln(const u16* x, u16* xn) {
    int t = blockIdx.x; int l = threadIdx.x;
    float v[9]; float s = 0.f;
    for (int j = 0; j < 9; ++j) { v[j] = b2f(x[t * D_ + l + 64 * j]); s += v[j]; }
    for (int o = 32; o; o >>= 1) s += __shfl_xor(s, o);
    float m = s / D_; float q = 0.f;
    for (int j = 0; j < 9; ++j) { float dd = v[j] - m; q += dd * dd; }
    for (int o = 32; o; o >>= 1) q += __shfl_xor(q, o);
    float mb = rb(m), var = rb(q / D_); float sd = rb(sqrtf(rb(var + 1e-5f)));
    for (int j = 0; j < 9; ++j) xn[t * D_ + l + 64 * j] = f2b(rb(v[j] - mb) / sd);
}
__global__ void k_gemm(const u16* A, const u16* B, u16* C, const u16* res, int N, int K) {
    __shared__ float sa[16][33], sb[32][17];
    int tx = threadIdx.x & 15, ty = threadIdx.x >> 4;
    int m0 = blockIdx.y * 16, n0 = blockIdx.x * 16;
    float acc = 0.f;
    for (int k0 = 0; k0 < K; k0 += 32) {
        for (int i = threadIdx.x; i < 512; i += 256) { sa[i >> 5][i & 31] = b2f(A[(m0 + (i >> 5)) * K + k0 + (i & 31)]); sb[i >> 4][i & 15] = b2f(B[(k0 + (i >> 4)) * N + n0 + (i & 15)]); }
        __syncthreads();
        for (int kk = 0; kk < 32; ++kk) acc += sa[ty][kk] * sb[kk][tx];
        __syncthreads();
    }
    float r = rb(acc);
    if (res) r = r + b2f(res[(m0 + ty) * N + n0 + tx]);
    C[(m0 + ty) * N + n0 + tx] = f2b(r);
}
__global__ void __launch_bounds__(256) k_phi(const u16* qkv, const u16* wphi, u16* phi) {
    int i = blockIdx.x * 256 + threadIdx.x;
    int t = i % S_, h = (i / S_) % H_, w = i / (S_ * H_);
    float q[DH], p[DH];
    for (int d = 0; d < DH; ++d) q[d] = b2f(qkv[t * 1728 + w * D_ + h * DH + d]);
    float mx = -1e30f, mn = 1e30f;
    #pragma unroll
    for (int f = 0; f < DH; ++f) { float a = 0.f; for (int d = 0; d < DH; ++d) a += q[d] * b2f(wphi[(h * DH + d) * DH + f]); p[f] = rb(a); mx = fmaxf(mx, p[f]); mn = fminf(mn, p[f]); }
    float s1 = 0.f, s2 = 0.f;
    #pragma unroll
    for (int f = 0; f < DH; ++f) { s1 += expf(p[f] - mx); s2 += expf(mn - p[f]); }
    #pragma unroll
    for (int f = 0; f < DH; ++f) { phi[(size_t)i * 36 + f] = f2b(expf(p[f] - mx) / s1); phi[(size_t)i * 36 + 18 + f] = f2b(expf(mn - p[f]) / s2); }
}
__global__ void k_kv(const u16* phi, const u16* qkv, float* kv) {
    int h = blockIdx.x; int e = blockIdx.y * 64 + threadIdx.x; if (e >= 36 * 19) return;
    int f = e / 19, d = e % 19; float a = 0.f;
    const u16* pk = phi + (size_t)(H_ + h) * S_ * 36;
    for (int t = 0; t < S_; ++t) a += b2f(pk[t * 36 + f]) * (d < 18 ? b2f(qkv[t * 1728 + 2 * D_ + h * DH + d]) : 1.f);
    kv[h * 684 + e] = rb(a);
}
__global__ void k_o(const u16* phi, const float* kv, u16* o) {
    int i = blockIdx.x * 256 + threadIdx.x; int t = i / D_, c = i % D_, h = c / DH, d = c % DH;
    const u16* pq = phi + ((size_t)h * S_ + t) * 36; float n = 0.f, dn = 0.f;
    for (int f = 0; f < 36; ++f) { float p = b2f(pq[f]); n += p * kv[h * 684 + f * 19 + d]; dn += p * kv[h * 684 + f * 19 + 18]; }
    o[i] = f2b(rb(n) / rb(rb(dn) + 1e-6f));
}
__global__ void k_gate(const u16* x, const u16* xn, const u16* y, u16* out) {
    int i = blockIdx.x * 256 + threadIdx.x;
    out[i] = f2b(b2f(x[i]) + rb(b2f(xn[i]) * b2f(y[i])));
}
__global__ void k_empty(int* p) { if (p == nullptr) p[0] = 1; }
#define EXTRA 0

typedef unsigned v4u __attribute__((ext_vector_type(4)));
#define LAS __attribute__((address_space(3)))
#define GAS __attribute__((address_space(1)))
__device__ __forceinline__ unsigned long long rt() { return __builtin_amdgcn_s_memrealtime(); }
__global__ void __launch_bounds__(256, 1) k_probe(const u16* x, const u16* w, const u16* wo, const u16* bk, const u16* bv, const u16* sh, unsigned long long* stamps, unsigned* sink) {
    extern __shared__ __attribute__((aligned(16))) char smem[];
    unsigned long long t0 = rt();
    const int b = blockIdx.x, wave = __builtin_amdgcn_readfirstlane(threadIdx.x >> 6), lane = threadIdx.x & 63, tid = threadIdx.x;
    const int xc = b & 7, i = b >> 3, rg = 2 * xc + (i >> 4), hg = i & 15;
    unsigned acc = 0;
    unsigned long long t2 = rt();
    acc ^= *(unsigned*)(smem + lane * 4);
    if (lane == 0) { unsigned long long* s = stamps + (b * 4 + wave) * 2; s[0] = t0; s[1] = t2; }
    if (acc == 0x12345) sink[0] = acc;
}
__global__ void k_spin(const unsigned long long* stamps) {
    unsigned long long mn = ~0ull, mx = 0;
    for (int i = threadIdx.x; i < 1024; i += 64) { mn = min(mn, stamps[2 * i]); mx = max(mx, stamps[2 * i + 1]); }
    for (int o = 32; o; o >>= 1) { mn = min(mn, (unsigned long long)__shfl_xor((long long)mn, o)); mx = max(mx, (unsigned long long)__shfl_xor((long long)mx, o)); }
    unsigned long long d = mx - mn; if (d > 3000) d = 3000;
    unsigned long long t = rt();
    while (rt() - t < d * 100) __builtin_amdgcn_s_sleep(4);
}
extern "C" void kernel_launch(void* const* d_in, const int* in_sizes, int n_in,
                              void* d_out, int out_size, void* d_ws, size_t ws_size,
                              hipStream_t stream) {
    const u16* x = (const u16*)d_in[0]; const u16* wqkv = (const u16*)d_in[1]; const u16* wphi = (const u16*)d_in[2]; const u16* wo = (const u16*)d_in[3];
    const u16* bk = (const u16*)d_in[5]; const u16* bv = (const u16*)d_in[6]; const u16* sk = (const u16*)d_in[7]; const u16* sh = (const u16*)d_in[8]; const u16* sD = (const u16*)d_in[9];
    char* ws = (char*)d_ws; const size_t MB = 1 << 20;
    u16* uT = (u16*)(ws); u16* y = (u16*)(ws + 4 * MB); u16* xn = (u16*)(ws + 8 * MB); u16* qkv = (u16*)(ws + 12 * MB);
    u16* phi = (u16*)(ws + 24 * MB); float* kv = (float*)(ws + 40 * MB); u16* o = (u16*)(ws + 44 * MB); u16* x1 = (u16*)(ws + 48 * MB); u16* x2 = (u16*)(ws + 52 * MB);
    int NE = S_ * D_ / 256;
    hipFuncSetAttribute((void*)k_probe, hipFuncAttributeMaxDynamicSharedMemorySize, 130 * 1024);
    k_probe<<<256, 256, 130 * 1024, stream>>>(x, wqkv, wo, bk, bv, sh, (unsigned long long*)(ws + 100 * MB), (unsigned*)(ws + 101 * MB));
    k_u<<<NE, 256, 0, stream>>>(bk, bv, sk, uT);
    k_conv<<<D_, 256, 0, stream>>>(uT, sh, sD, y);
    const u16* xin = x;
    for (int L = 0; L < 2; ++L) {
        k_ln<<<S_, 64, 0, stream>>>(xin, xn);
        k_gemm<<<dim3(1728 / 16, S_ / 16), 256, 0, stream>>>(xn, wqkv, qkv, nullptr, 1728, D_);
        k_phi<<<2 * H_ * S_ / 256, 256, 0, stream>>>(qkv, wphi, phi);
        k_kv<<<dim3(H_, 11), 64, 0, stream>>>(phi, qkv, kv);
        k_o<<<NE, 256, 0, stream>>>(phi, kv, o);
        k_gemm<<<dim3(D_ / 16, S_ / 16), 256, 0, stream>>>(o, wo, x1, xin, D_, D_);
        k_ln<<<S_, 64, 0, stream>>>(x1, xn);
        u16* dst = L == 0 ? x2 : (u16*)d_out;
        k_gate<<<NE, 256, 0, stream>>>(x1, xn, y, dst);
        xin = x2;
    }
    k_spin<<<1, 64, 0, stream>>>((const unsigned long long*)(ws + 100 * MB));
    for (int i = 0; i < EXTRA; ++i) k_empty<<<256, 256, 0, stream>>>((int*)d_ws);
}
```

```cpp
#include <hip/hip_runtime.h>
#include <stdint.h>
#pragma clang diagnostic ignored "-Wint-to-pointer-cast"
#pragma clang diagnostic ignored "-Wunused-value"
typedef unsigned short u16;
typedef unsigned u32;
typedef u32 v4u __attribute__((ext_vector_type(4)));
typedef u32 v2u __attribute__((ext_vector_type(2)));
typedef float f4 __attribute__((ext_vector_type(4)));
typedef float f2 __attribute__((ext_vector_type(2)));
typedef float f16v __attribute__((ext_vector_type(16)));
typedef __bf16 b8 __attribute__((ext_vector_type(8)));
typedef __bf16 b2 __attribute__((ext_vector_type(2)));
typedef short s4 __attribute__((ext_vector_type(4)));
#define LAS __attribute__((address_space(3)))
#define DEV __device__ __forceinline__
#define SC1 16

constexpr u32 IMG = 130048;
constexpr u32 L_K = 0, L_V = 36864, L_QT = 73728, L_Q = 93184, L_STG = 130048, L_KVA = 159744, L_CS = 162864, L_END = 163312;
constexpr u32 QTS = 528, STGS = 232, CVS = 21216, HCS = 4160;
constexpr u32 W_IMG = 0, W_WOT = W_IMG + 16 * IMG, W_CSP = W_WOT + 16 * 36864, W_UT = W_CSP + 16 * 18 * 448,
              W_XF0 = W_UT + 576 * 4096, W_CLR = W_XF0 + 1024 * 2560, W_XF1 = W_CLR, W_O = W_XF1 + 1024 * 2560,
              W_Y = W_O + 2 * 1024 * 2304, W_KV = W_Y + 576 * 4096, W_ST = W_KV + 2 * 256 * 2816, W_END = W_ST + 2 * 1024 * 256,
              W_DBG = W_END;
constexpr u32 CLRB = (W_END - W_CLR) / 256;
static_assert(CLRB * 256 == W_END - W_CLR && CLRB % 16 == 0, "");

DEV float bf_lo(u32 v) { return __uint_as_float(v << 16); }
DEV float bf_hi(u32 v) { return __uint_as_float(v & 0xffff0000u); }
DEV u32 pk(float lo, float hi) { b2 r = __builtin_convertvector((f2){lo, hi}, b2); return __builtin_bit_cast(u32, r); }
DEV float rb(float f) { return bf_lo(pk(f, f)); }
DEV f16v mm32(v4u a, v4u b, f16v c) { return __builtin_amdgcn_mfma_f32_32x32x16_bf16(__builtin_bit_cast(b8, a), __builtin_bit_cast(b8, b), c, 0, 0, 0); }
DEV f4 mm16(v4u a, v4u b, f4 c) { return __builtin_amdgcn_mfma_f32_16x16x32_bf16(__builtin_bit_cast(b8, a), __builtin_bit_cast(b8, b), c, 0, 0, 0); }
DEV __amdgpu_buffer_rsrc_t mkrs(const void* p, u32 n) { return __builtin_amdgcn_make_buffer_rsrc((void*)p, 0, (int)n, 0x00020000); }
DEV v4u lds4(u32 a) { return *(const LAS v4u*)a; }
DEV v2u lds2(u32 a) { return *(const LAS v2u*)a; }
DEV void barrier() { asm volatile("s_waitcnt lgkmcnt(0)\n\ts_barrier" ::: "memory"); }
DEV void dma16(const void* g, u32 voff, u32 lds) {
    asm volatile("s_mov_b32 m0, %2\n\ts_nop 0\n\tglobal_load_lds_dwordx4 %0, %1" ::"v"(voff), "s"(g), "s"(lds) : "m0", "memory");
}
template <int I> struct IC { static constexpr int v = I; };
template <int B, int E, class F> DEV void sfor(F&& f) { if constexpr (B < E) { f(IC<B>{}); sfor<B + 1, E>(f); } }
#define LAM(I) [&](auto I) __attribute__((always_inline))
#define FENCE() __builtin_amdgcn_sched_barrier(0)
#define OPQV(x) asm volatile("" : "+v"(x))
#define OPQS(x) asm volatile("" : "+s"(x))
DEV float halves_sum(float a, float b) {
    auto r = __builtin_amdgcn_permlane32_swap(__float_as_uint(a), __float_as_uint(b), false, false);
    return __uint_as_float(r[0]) + __uint_as_float(r[1]);
}
#define STAMP_INIT()
#define STAMP(i)
#define STAMP_END()

__global__ void __launch_bounds__(256) k_pre(const u16* __restrict__ x, const u16* __restrict__ wqkv, const u16* __restrict__ wphi, const u16* __restrict__ wo,
                                             const u16* __restrict__ bk, const u16* __restrict__ bv, const u16* __restrict__ sk, const u16* __restrict__ sh, char* __restrict__ ws) {
    __shared__ __attribute__((aligned(16))) char sm[72 * 144];
    const int tid = threadIdx.x, lane = tid & 63, wave = __builtin_amdgcn_readfirstlane(tid >> 6);
    if (blockIdx.x < 288) {
        const int task = blockIdx.x * 4 + wave, P = task & 15, sec = (task >> 4) & 3, rt = task >> 6;
        const int rho = lane & 31, kh = lane >> 5, hD = (rho >> 2) & 1, r = (rho & 3) + 4 * (rho >> 3);
        const int k = rt < 16 ? 36 * rt + 18 * hD + r : 36 * (4 * (2 * (rt - 16) + (r >> 3)) + ((r & 7) >> 1)) + 18 * hD + 16 + (r & 1);
        const u16* src = sec < 3 ? wqkv + k * 1728 + (sec == 0 ? 576 : sec == 1 ? 1152 : 0) + P * 36 : wo + k * 576 + P * 36;
        v4u a0[2]; u32 a1[2]; u32 bw[2][5];
        const bool fold = (sec == 0 || sec == 2);
        const int f = rho;
#pragma unroll
        for (int hh = 0; hh < 2; ++hh) {
            const u32* s32 = (const u32*)(src + 18 * hh);
            a0[hh] = (v4u){s32[4 * kh], s32[4 * kh + 1], s32[4 * kh + 2], s32[4 * kh + 3]};
            a1[hh] = kh == 0 ? s32[8] : 0u;
            if (fold) {
                const u16* ph = wphi + (2 * P + hh) * 324 + (f < 18 ? f : 0);
#pragma unroll
                for (int j = 0; j < 4; ++j) bw[hh][j] = (u32)ph[(8 * kh + 2 * j) * 18] | ((u32)ph[(8 * kh + 2 * j + 1) * 18] << 16);
                bw[hh][4] = (u32)ph[16 * 18] | ((u32)ph[17 * 18] << 16);
            } else {
#pragma unroll
                for (int j = 0; j < 4; ++j) bw[hh][j] = (8 * kh + 2 * j == f ? 0x3f80u : 0u) | (8 * kh + 2 * j + 1 == f ? 0x3f800000u : 0u);
                bw[hh][4] = (f == 16 ? 0x3f80u : 0u) | (f == 17 ? 0x3f800000u : 0u);
            }
        }
#pragma unroll
        for (int hh = 0; hh < 2; ++hh) {
            f16v d = {};
            d = mm32(a0[hh], (v4u){bw[hh][0], bw[hh][1], bw[hh][2], bw[hh][3]}, d);
            d = mm32((v4u){a1[hh], 0, 0, 0}, (v4u){kh == 0 ? bw[hh][4] : 0u, 0, 0, 0}, d);
            u32 w[8]; float cs = 0.f;
#pragma unroll
            for (int j = 0; j < 8; ++j) { w[j] = pk(d[2 * j], d[2 * j + 1]); cs += bf_lo(w[j]) + bf_hi(w[j]); }
            cs = halves_sum(cs, cs);
            const int m = rho;
            char* dst; u32 st;
            if (m < 16) {
                const u32 lo = (u32)(((m & 3) + 8 * (m >> 2) + 4 * hh + 32 * kh) * 16 + 2 * rt * 1024);
                dst = sec < 3 ? ws + W_IMG + P * IMG + (sec == 0 ? L_K : sec == 1 ? L_V : L_Q) + lo : ws + W_WOT + P * 36864 + lo;
                st = 1024;
            } else {
                const u32 e = sec < 2 ? (u32)(((hh * 4 + sec * 2 + (m - 16)) * 2 + kh) * 16) : (u32)((sec == 2 ? 256 : 384) + ((hh * 2 + (m - 16)) * 2 + kh) * 16);
                dst = ws + W_IMG + P * IMG + L_QT + 2 * rt * QTS + e;
                st = QTS;
            }
            if (m < 18) {
                *(v4u*)dst = (v4u){w[0], w[1], w[2], w[3]};
                *(v4u*)(dst + st) = (v4u){w[4], w[5], w[6], w[7]};
                if (sec < 3 && kh == 0) {
                    const int e = m < 16 ? sec * 16 + m : (sec < 2 ? 48 + sec * 2 + (m - 16) : 52 + (m - 16));
                    ((float*)(ws + W_CSP))[((P * 18 + rt) * 2 + hh) * 56 + e] = cs;
                }
            }
            if (sec == 3 && hh == 0 && m == 18) *(v4u*)(ws + W_IMG + P * IMG + L_QT + (2 * rt + kh) * QTS + 512) = (v4u){0, 0, 0, 0};
            if (sec == 2 && kh == 0 && (m == 18 || m == 19)) ((float*)(ws + W_CSP))[((P * 18 + rt) * 2 + hh) * 56 + 36 + m] = 0.f;
        }
        return;
    }
    const int bb = blockIdx.x - 288;
    const int tok = bb * 8 + (tid & 7), c = tid >> 3;
    const u32* xs = (const u32*)(x + tok * 576 + c * 18);
    u32 xv[9];
#pragma unroll
    for (int j = 0; j < 9; ++j) xv[j] = xs[j];
    const int tg = (bb & 7) * 4 + (bb >> 6), cg = (bb >> 3) & 7;
    const int pc = tid % 9, tq = tid / 9;
    v4u kr[7], vr[4], wr[4];
    if (tid < 144) {
        const int t0 = tg * 64 + tq * 4;
#pragma unroll
        for (int j = 0; j < 7; ++j) { const int t = t0 + j - 3; kr[j] = t >= 0 ? *(const v4u*)(bk + t * 576 + cg * 72 + pc * 8) : (v4u){0, 0, 0, 0}; }
#pragma unroll
        for (int j = 0; j < 4; ++j) { vr[j] = *(const v4u*)(bv + (t0 + j) * 576 + cg * 72 + pc * 8); wr[j] = *(const v4u*)(sk + (cg * 72 + pc * 8) * 4 + j * 8); }
    }
    {
        const v4u* ph = (const v4u*)((const char*)sh + bb * 9216);
        v4u p0 = ph[tid], p1 = ph[256 + tid], p2 = ph[512 + (tid & 63)];
        asm volatile("" ::"v"(p0), "v"(p1), "v"(p2));
    }
    {
        const __amdgpu_buffer_rsrc_t rs = mkrs(ws + W_CLR + bb * CLRB, CLRB);
#pragma unroll
        for (int i = 0; i < 12; ++i) if (i * 4096 + tid * 16 < CLRB) __builtin_amdgcn_raw_buffer_store_b128((v4u){~0u, ~0u, ~0u, ~0u}, rs, tid * 16, i * 4096, SC1);
    }
    {
        float s1 = 0.f, s2 = 0.f;
#pragma unroll
        for (int j = 0; j < 9; ++j) { const float a = bf_lo(xv[j]), b = bf_hi(xv[j]); s1 += a + b; s2 += a * a + b * b; }
        s1 += __shfl_xor(s1, 8); s2 += __shfl_xor(s2, 8);
        const int h = c & 1, p = c >> 1;
        char* d = ws + W_XF0 + (((tok >> 7) * 16 + p) * 4 + ((tok >> 5) & 3)) * 2560;
        const int l = h * 32 + (tok & 31);
        *(v4u*)(d + l * 16) = (v4u){xv[0], xv[1], xv[2], xv[3]};
        *(v4u*)(d + 1024 + l * 16) = (v4u){xv[4], xv[5], xv[6], xv[7]};
        *(u32*)(d + 2048 + l * 4) = xv[8];
        *(float*)(d + 2304 + l * 4) = h ? s2 : s1;
    }
    if (tid < 144) {
#pragma unroll
        for (int e = 0; e < 8; ++e) {
            const u32 w01 = wr[e >> 1][(e & 1) * 2], w23 = wr[e >> 1][(e & 1) * 2 + 1];
            const float w0 = bf_lo(w01), w1 = bf_hi(w01), w2 = bf_lo(w23), w3 = bf_hi(w23);
            float kk[7], u[4];
#pragma unroll
            for (int j = 0; j < 7; ++j) kk[j] = (e & 1) ? bf_hi(kr[j][e >> 1]) : bf_lo(kr[j][e >> 1]);
#pragma unroll
            for (int j = 0; j < 4; ++j) {
                const float ks = kk[j] * w3 + kk[j + 1] * w2 + kk[j + 2] * w1 + kk[j + 3] * w0;
                u[j] = rb(ks) * ((e & 1) ? bf_hi(vr[j][e >> 1]) : bf_lo(vr[j][e >> 1]));
            }
            *(v2u*)(sm + (pc * 8 + e) * 144 + (tq >> 1) * 16 + ((tq & 1) ? 0 : 8)) = (v2u){pk(u[3], u[2]), pk(u[1], u[0])};
        }
    }
    __syncthreads();
    for (int it = tid; it < 576; it += 256) {
        const int ch = it >> 3, q = it & 7;
        *(v4u*)(ws + W_UT + (cg * 72 + ch) * 4096 + tg * 128 + q * 16) = *(const v4u*)(sm + ch * 144 + q * 16);
    }
}

constexpr int SPIN = 1 << 14;
DEV bool bad(u32 m) { return __builtin_amdgcn_ballot_w64(m == ~0u) != 0; }
DEV u32 mx3(u32 a, u32 b, u32 c) { return max(max(a, b), c); }

template <int ST, int lo, int hi>
DEV void gather_issue(__amdgpu_buffer_rsrc_t rs, u32 l16, u32 l4, v4u (&xf)[36], u32 (&st)[16]) {
    u32 so = (lo < 32 ? (lo >> 1) : ((lo - 32) & 15)) * 4 * ST;
    OPQS(so);
    sfor<lo, hi>(LAM(I) {
        constexpr int i = I.v;
        if constexpr ((i == 32 || i == 48) && i != lo) { so = 0; OPQS(so); }
        if constexpr (i < 32) xf[i] = __builtin_amdgcn_raw_buffer_load_b128(rs, l16 + (i & 1) * 1024, so, SC1);
        else if constexpr (i < 48) xf[32 + ((i - 32) >> 2)][i & 3] = __builtin_amdgcn_raw_buffer_load_b32(rs, l4 + 2048, so, SC1);
        else st[i - 48] = __builtin_amdgcn_raw_buffer_load_b32(rs, l4 + 2304, so, SC1);
        if constexpr ((i >= 32 || (i & 1)) && i + 1 < hi) { so += 4 * ST; OPQS(so); }
    });
}
template <int ST, int G>
DEV u32 group_max(v4u (&xf)[36], u32 (&st)[16]) {
    u32 m = 0;
    if constexpr (G < 4) {
#pragma unroll
        for (int i = 0; i < 8; i += 2) m = mx3(m, xf[8 * G + i].x, xf[8 * G + i + 1].x);
    } else {
#pragma unroll
        for (int i = 0; i < 4; ++i) m = mx3(mx3(m, xf[32 + i].x, xf[32 + i].y), xf[32 + i].z, xf[32 + i].w);
        if (ST == 2560) {
#pragma unroll
            for (int i = 0; i < 16; i += 2) m = mx3(m, st[i], st[i + 1]);
        }
    }
    return m;
}
template <int ST, int G>
DEV void check8(__amdgpu_buffer_rsrc_t rs, u32 l16, u32 l4, v4u (&xf)[36], u32 (&st)[16]) {
    if (__builtin_expect(bad(group_max<ST, G>(xf, st)), 0)) {
#pragma unroll 1
        for (int sp = 0; sp < SPIN; ++sp) {
            __builtin_amdgcn_s_sleep(2);
            asm volatile("" ::: "memory");
            gather_issue<ST, 8 * G, (G < 4 ? 8 * G + 8 : ST == 2560 ? 64 : 48)>(rs, l16, l4, xf, st);
            if (!bad(group_max<ST, G>(xf, st))) break;
        }
    }
}
DEV void hedgehog(const float (&t)[18], float a, u32 (&ph)[18]) {
    float mx = t[0], mn = t[0];
#pragma unroll
    for (int i = 1; i < 18; ++i) { mx = fmaxf(mx, t[i]); mn = fminf(mn, t[i]); }
    const float c1 = -a * mx, c2 = a * mn;
    float e1[18], e2[18], s1 = 0.f, s2 = 0.f;
#pragma unroll
    for (int i = 0; i < 18; ++i) {
        e1[i] = __builtin_amdgcn_exp2f(__builtin_fmaf(t[i], a, c1)); s1 += e1[i];
        e2[i] = __builtin_amdgcn_exp2f(__builtin_fmaf(t[i], -a, c2)); s2 += e2[i];
    }
    const float r1 = __builtin_amdgcn_rcpf(s1), r2 = __builtin_amdgcn_rcpf(s2);
#pragma unroll
    for (int i = 0; i < 9; ++i) { ph[i] = pk(e1[2 * i] * r1, e1[2 * i + 1] * r1); ph[9 + i] = pk(e2[2 * i] * r2, e2[2 * i + 1] * r2); }
}
DEV v4u tr2(u32 a0, u32 a1) {
    s4 lo = __builtin_amdgcn_ds_read_tr16_b64_v4i16((LAS s4*)a0), hi = __builtin_amdgcn_ds_read_tr16_b64_v4i16((LAS s4*)a1);
    v2u l = __builtin_bit_cast(v2u, lo), h = __builtin_bit_cast(v2u, hi);
    return (v4u){l.x, l.y, h.x, h.y};
}
DEV void y_store(f4 acc, u32 ua, int j, float Dv, __amdgpu_buffer_rsrc_t rs, u32 yo) {
    const v2u uu = lds2(ua + 512 * j);
    const float y0 = rb(acc[0]) + rb(bf_hi(uu.y) * Dv), y1 = rb(acc[1]) + rb(bf_lo(uu.y) * Dv), y2 = rb(acc[2]) + rb(bf_hi(uu.x) * Dv), y3 = rb(acc[3]) + rb(bf_lo(uu.x) * Dv);
    __builtin_amdgcn_raw_buffer_store_b64((v2u){pk(y0, y1), pk(y2, y3)}, rs, yo + 512 * j, 0, SC1);
}
template <int N, int n, int r>
DEV void conv_read(u32 aa, u32 ba, v4u (&A)[N], v4u (&B)[N]) {
    constexpr int i = n / 3, k = n % 3;
    if constexpr (k == 0) B[i] = lds4(ba + 64 * (8 * i - r + 7));
    else { const v2u v = lds2(aa + 2 + 64 * (r + 8 * i) + 8 * (k - 1)); if constexpr (k == 1) { A[i].x = v.x; A[i].y = v.y; } else { A[i].z = v.x; A[i].w = v.y; } }
}
constexpr int conv_ij(int N, int MASK, int n) {
    for (int d = 0; d < N; ++d) for (int i = 0; i < N; ++i) for (int j = i; j < N; ++j)
        if (((MASK >> j) & 1) && (i > j - i ? i : j - i) == d && n-- == 0) return i * 8 + j;
    return -1;
}

__global__ void __launch_bounds__(256, 1) k_main(const u16* __restrict__ sh, const u16* __restrict__ sD, u16* __restrict__ out, char* __restrict__ ws) {
    extern __shared__ __attribute__((aligned(16))) char smem[];
    const int tid = threadIdx.x, lane = tid & 63, wave = __builtin_amdgcn_readfirstlane(tid >> 6);
    const int b = blockIdx.x, rg = 2 * (b & 7) + (b >> 7), p = (b >> 3) & 15;
    u32 lb = (u32)(uintptr_t)(LAS char*)smem;
    OPQS(lb);
    STAMP_INIT();
    v4u xf[36]; u32 st[16];
    u32 l16 = lane * 16, l4 = lane * 4;
    OPQV(l16); OPQV(l4);
    const char* img = ws + W_IMG + p * IMG;
    const int ch0 = (rg * 12 + p) * 3;
    const __amdgpu_buffer_rsrc_t rx0 = mkrs(ws + W_XF0 + (rg * 64 + wave) * 2560, 64 * 2560);
    if (p < 12) {
        v4u hd[3]; v2u he[3];
#pragma unroll
        for (int c = 0; c < 3; ++c) {
            const char* g = (const char*)sh + (ch0 + c) * 4096;
            asm volatile("global_load_dwordx4 %0, %1, %2" : "=v"(hd[c]) : "v"(tid * 16), "s"(g) : "memory");
            asm volatile("global_load_dwordx2 %0, %1, %2" : "=v"(he[c]) : "v"(tid == 255 ? 0 : tid * 16 + 16), "s"(g) : "memory");
        }
#pragma unroll
        for (int c = 0; c < 3; ++c) dma16(ws + W_UT + (ch0 + c) * 4096 + wave * 1024, l16, lb + L_Q + c * CVS + 4 * HCS + 480 + wave * 1024);
#pragma unroll 1
        for (int i = wave; i < 32; i += 4) dma16(img + i * 1024, l16, lb + i * 1024);
        asm volatile("s_waitcnt vmcnt(8)" : "+v"(hd[0]), "+v"(hd[1]), "+v"(hd[2]), "+v"(he[0]), "+v"(he[1]), "+v"(he[2])::"memory");
        STAMP(26);
#pragma unroll
        for (int c = 0; c < 3; ++c) {
            const u32 d0 = hd[c].x, d1 = hd[c].y, d2 = hd[c].z, d3 = hd[c].w, d4 = he[c].x, d5 = he[c].y;
            const u32 a0 = __builtin_amdgcn_alignbit(d1, d0, 16), a1 = __builtin_amdgcn_alignbit(d2, d1, 16), a2 = __builtin_amdgcn_alignbit(d3, d2, 16),
                      a3 = __builtin_amdgcn_alignbit(d4, d3, 16), a4 = __builtin_amdgcn_alignbit(d5, d4, 16);
            u32 o = lb + L_Q + c * CVS + 32 + tid * 16;
            OPQV(o);
            *(LAS v4u*)(o) = (v4u){d0, d1, d2, d3};
            *(LAS v4u*)(o + HCS) = (v4u){a0, a1, a2, a3};
            *(LAS v4u*)(o + 2 * HCS) = (v4u){d1, d2, d3, d4};
            *(LAS v4u*)(o + 3 * HCS) = (v4u){a1, a2, a3, a4};
            if (tid == 0) {
#pragma unroll
                for (int k = 0; k < 4; ++k) *(LAS v4u*)(o - 32 + k * HCS) = (v4u){0, 0, 0, 0};
                *(LAS v4u*)(o - 16) = (v4u){0, 0, 0, 0};
                *(LAS v4u*)(o - 16 + HCS) = (v4u){0, 0, 0, d0 << 16};
                *(LAS v4u*)(o - 16 + 2 * HCS) = (v4u){0, 0, 0, d0};
                *(LAS v4u*)(o - 16 + 3 * HCS) = (v4u){0, 0, d0 << 16, a0};
            }
            if (tid >= 64 && tid < 94) *(LAS v4u*)(o + 4 * HCS - 32 - 1024) = (v4u){0, 0, 0, 0};
        }
#pragma unroll 1
        for (int i = 32 + wave; i < 91; i += 4) dma16(img + i * 1024, l16, lb + i * 1024);
        barrier();
        STAMP(1);
        const int bq = lane & 15, g = lane >> 4, cc = (bq + 1) & 3;
        const u32 aoff = lb + L_Q + cc * HCS + 2 * (bq - 8 * g + 24 - cc), boff = lb + L_Q + 4 * HCS + 480 + 2 * (16 * bq + 8 * g - 16) - 7 * 64;
        const u32 uoff = lb + L_Q + 4 * HCS + 480 + 32 * bq + 16 * (g >> 1) + ((g & 1) ? 0 : 8), yo = 32 * bq + 8 * g;
        if (wave < 3) {
            u32 aa = aoff + wave * CVS, ba = boff + wave * CVS;
            OPQV(aa); OPQV(ba);
            f4 acc[8] = {};
            v4u A[2][8], B[2][8];
            sfor<0, 24>(LAM(N_) { conv_read<8, N_.v, 0>(aa, ba, A[0], B[0]); });
            FENCE();
            sfor<0, 8 * 27>(LAM(Q_) {
                constexpr int r = Q_.v / 27, n = Q_.v % 27, i = conv_ij(8, 0xf1, n) >> 3, j = conv_ij(8, 0xf1, n) & 7;
                acc[j] = mm16(A[r & 1][i], B[r & 1][j - i], acc[j]);
                if constexpr (r < 7 && n < 24) conv_read<8, n, r + 1>(aa, ba, A[(r + 1) & 1], B[(r + 1) & 1]);
                if constexpr (n >= 19) gather_issue<2560, 8 * r + n - 19, 8 * r + n - 18>(rx0, l16, l4, xf, st);
                FENCE();
            });
            const float Dv = bf_lo(sD[ch0 + wave]);
            const __amdgpu_buffer_rsrc_t ry = mkrs(ws + W_Y + (ch0 + wave) * 4096, 4096);
            y_store(acc[0], uoff + wave * CVS, 0, Dv, ry, yo);
#pragma unroll
            for (int j = 4; j < 8; ++j) y_store(acc[j], uoff + wave * CVS, j, Dv, ry, yo);
        } else {
            gather_issue<2560, 0, 32>(rx0, l16, l4, xf, st);
            FENCE();
#pragma unroll 1
            for (int c = 0; c < 3; ++c) {
                u32 aa = aoff + c * CVS, ba = boff + c * CVS;
                OPQV(aa); OPQV(ba);
                f4 acc[4] = {};
                v4u A[2][4], B[2][4];
                sfor<0, 12>(LAM(N_) { conv_read<4, N_.v, 0>(aa, ba, A[0], B[0]); });
                FENCE();
                sfor<0, 8 * 9>(LAM(Q_) {
                    constexpr int r = Q_.v / 9, n = Q_.v % 9, i = conv_ij(4, 0xe, n) >> 3, j = conv_ij(4, 0xe, n) & 7;
                    acc[j] = mm16(A[r & 1][i], B[r & 1][j - i], acc[j]);
                    if constexpr (r < 7) { conv_read<4, n, r + 1>(aa, ba, A[(r + 1) & 1], B[(r + 1) & 1]); if constexpr (n < 3) conv_read<4, 9 + n, r + 1>(aa, ba, A[(r + 1) & 1], B[(r + 1) & 1]); }
                    FENCE();
                });
                const float Dv = bf_lo(sD[ch0 + c]);
                const __amdgpu_buffer_rsrc_t ry = mkrs(ws + W_Y + (ch0 + c) * 4096, 4096);
#pragma unroll
                for (int j = 1; j < 4; ++j) y_store(acc[j], uoff + c * CVS, j, Dv, ry, yo);
            }
            gather_issue<2560, 32, 64>(rx0, l16, l4, xf, st);
        }
    } else {
#pragma unroll 1
        for (int i = wave; i < 91; i += 4) dma16(img + i * 1024, l16, lb + i * 1024);
        gather_issue<2560, 0, 64>(rx0, l16, l4, xf, st);
    }
    STAMP(2);
    {
        float cs = 0.f;
        if (tid < 112) {
            const float* cp = (const float*)(ws + W_CSP) + p * 18 * 112 + tid;
            float cv[18];
#pragma unroll
            for (int i = 0; i < 18; ++i) cv[i] = cp[i * 112];
            FENCE();
#pragma unroll
            for (int i = 0; i < 18; ++i) cs += cv[i];
            *(LAS float*)(lb + L_CS + tid * 4) = cs;
        }
        if (tid >= 128 && tid < 166) *(LAS v2u*)(lb + L_KVA + (tid - 128) * 80 + 72) = (v2u){0, 0};
        if (tid >= 192 && tid < 197) *(LAS v4u*)(lb + L_KVA + 38 * 80 + (tid - 192) * 16) = (v4u){0, 0, 0, 0};
    }
    asm volatile("s_waitcnt vmcnt(0) lgkmcnt(0)\n\ts_barrier" ::: "memory");
#pragma unroll 1
    for (int i = wave; i < 36; i += 4) dma16(img + L_Q + i * 1024, l16, lb + L_Q + i * 1024);
    v4u wf[36];
    {
        const __amdgpu_buffer_rsrc_t rw = mkrs(ws + W_WOT + p * 36864, 36864);
        u32 so = 0;
#pragma unroll
        for (int s = 0; s < 36; ++s) { if ((s & 3) == 0) { OPQS(so); } wf[s] = __builtin_amdgcn_raw_buffer_load_b128(rw, l16 + (s & 3) * 1024, so, 0); if ((s & 3) == 3) so += 4096; }
    }
    u32 xr[9], yv[9];
    {
        const v4u t0 = __builtin_amdgcn_raw_buffer_load_b128(rx0, l16, p * 4 * 2560, 0), t1 = __builtin_amdgcn_raw_buffer_load_b128(rx0, l16 + 1024, p * 4 * 2560, 0);
        xr[8] = __builtin_amdgcn_raw_buffer_load_b32(rx0, l4 + 2048, p * 4 * 2560, 0);
        xr[0] = t0.x; xr[1] = t0.y; xr[2] = t0.z; xr[3] = t0.w; xr[4] = t1.x; xr[5] = t1.y; xr[6] = t1.z; xr[7] = t1.w;
#pragma unroll
        for (int i = 0; i < 9; ++i) yv[i] = 0;
    }
    FENCE();
    u32 kvw;
    {
        const int pi = wave * 44 + lane, hh = pi >= 88, ip = pi - 88 * hh;
        int o0, o1;
        if (ip < 64) { o0 = (ip & 15) * 80 + (ip >> 4) * 8; o1 = o0 + 32; }
        else if (ip < 72) { o0 = 2 * (ip - 64) * 80 + 64; o1 = o0 + 80; }
        else {
            const int s0 = 2 * (ip - 72), s1 = s0 + 1;
            o0 = s0 < 24 ? (16 + (s0 % 6) % 3) * 80 + ((s0 % 6) / 3) * 32 + (s0 / 6) * 8 : s0 < 27 ? (16 + s0 - 24) * 80 + 64 : -1;
            o1 = s1 < 24 ? (16 + (s1 % 6) % 3) * 80 + ((s1 % 6) / 3) * 32 + (s1 / 6) * 8 : s1 < 27 ? (16 + s1 - 24) * 80 + 64 : -1;
        }
        kvw = (u32)(o0 < 0 || lane >= 44 ? 0xffff : o0 + hh * 1520) | ((u32)(o1 < 0 || lane >= 44 ? 0xffff : o1 + hh * 1520) << 16);
        OPQV(kvw);
    }
    STAMP(3);

#pragma unroll 1
    for (int L = 0; L < 2; ++L) {
        const __amdgpu_buffer_rsrc_t rx = mkrs(ws + (L ? W_XF1 : W_XF0) + (rg * 64 + wave) * 2560, 64 * 2560);
        int ln = lane;
        OPQV(ln);
        const int tl = ln & 31, h = ln >> 5, row = ln & 15, kg = ln >> 4, gq = row >> 2, rq = row & 3;
        const bool qa = (gq & 1) == (kg & 1);
        u32 aK_ = lb + L_K + l16, aV_ = lb + L_V + l16;
        u32 aKV = lb + L_QT + (qa ? (((gq >> 1) * 4 + rq) * 2 + (kg >> 1)) * 16 : 512);
        u32 aQq = lb + L_QT + (qa && rq < 2 ? 256 + (((gq >> 1) * 2 + rq) * 2 + (kg >> 1)) * 16 : 512);
        u32 aCS = lb + L_CS + h * 224;
        OPQV(aK_); OPQV(aV_); OPQV(aKV); OPQV(aQq); OPQV(aCS);
        f16v aK = {}; f4 qKV = {}, qQ = {};
        float mean, al, rstd;
        v4u rV[4], cK[5];
        {
            v4u r0[3], r1[3], r2[3];
#pragma unroll
            for (int s = 0; s < 3; ++s) { r0[s] = lds4(aK_ + s * 1024); r1[s] = lds4(aKV + s * QTS); r2[s] = lds4(aQq + s * QTS); }
            sfor<0, 36>(LAM(SI) {
                constexpr int s = SI.v;
                if constexpr ((s & 7) == 0) { FENCE(); check8<2560, (s >> 3)>(rx, l16, l4, xf, st); FENCE(); }
                aK = mm32(r0[s % 3], xf[s], aK);
                if constexpr (s + 3 < 36) r0[s % 3] = lds4(aK_ + (s + 3) * 1024); else rV[s - 33] = lds4(aV_ + (s - 33) * 1024);
                if constexpr (s == 32) {
                    float t = 0.f;
#pragma unroll
                    for (int i = 0; i < 16; ++i) t += __uint_as_float(st[i]);
                    auto r = __builtin_amdgcn_permlane32_swap(__float_as_uint(t), __float_as_uint(t), false, false);
                    mean = __uint_as_float(r[0]) * (1.f / 576.f);
                    const float var = fmaxf(__uint_as_float(r[1]) * (1.f / 576.f) - mean * mean, 0.f);
                    rstd = __builtin_amdgcn_rsqf(var + 1e-5f);
                    al = rstd * 1.4426950408889634f;
                }
                FENCE();
                qKV = mm16(r1[s % 3], xf[s], qKV);
                if constexpr (s + 3 < 36) r1[s % 3] = lds4(aKV + (s + 3) * QTS); else cK[s - 33] = lds4(aCS + (s - 33) * 16);
                FENCE();
                qQ = mm16(r2[s % 3], xf[s], qQ);
                if constexpr (s + 3 < 36) r2[s % 3] = lds4(aQq + (s + 3) * QTS); else if constexpr (s == 33) cK[3] = lds4(aCS + 48); else if constexpr (s == 34) cK[4] = lds4(aCS + 192); else rV[3] = lds4(aV_ + 3 * 1024);
                FENCE();
            });
        }
        asm volatile("" : "+a"(aK), "+a"(qKV), "+a"(qQ));
        STAMP(4 + 12 * L);
        v4u rQ[4];
        {
            f16v aV = {};
            float t[18], e1[18], e2[18], mx, mn, c1, c2, s1 = 0.f, s2 = 0.f;
            v4u cV[4];
            u32 aS = lb + L_STG + (wave * 32 + tl) * STGS + 72 * h;
            OPQV(aS);
#pragma unroll
            for (int s = 0; s < 36; ++s) {
                aV = mm32(rV[s & 3], xf[s], aV);
                if (s + 4 < 36) rV[s & 3] = lds4(aV_ + (s + 4) * 1024); else cV[s - 32] = lds4(aCS + 64 + (s - 32) * 16);
                if (s >= 2 && s < 5) {
#pragma unroll
                    for (int i = 6 * (s - 2); i < 6 * (s - 2) + 6; ++i) t[i] = __builtin_fmaf(-mean, __uint_as_float(cK[i >> 2][i & 3]), i < 16 ? aK[i] : qKV[i - 16]);
                }
                if (s == 5) { mx = fmaxf(fmaxf(t[0], t[1]), t[2]); mn = fminf(fminf(t[0], t[1]), t[2]);
#pragma unroll
                    for (int i = 3; i < 7; i += 2) { mx = fmaxf(fmaxf(mx, t[i]), t[i + 1]); mn = fminf(fminf(mn, t[i]), t[i + 1]); } }
                if (s == 6) {
#pragma unroll
                    for (int i = 7; i < 13; i += 2) { mx = fmaxf(fmaxf(mx, t[i]), t[i + 1]); mn = fminf(fminf(mn, t[i]), t[i + 1]); } }
                if (s == 7) {
#pragma unroll
                    for (int i = 13; i < 17; i += 2) { mx = fmaxf(fmaxf(mx, t[i]), t[i + 1]); mn = fminf(fminf(mn, t[i]), t[i + 1]); }
                    mx = fmaxf(mx, t[17]); mn = fminf(mn, t[17]); c1 = -al * mx; c2 = al * mn; }
                if (s >= 8 && s < 32) {
#pragma unroll
                    for (int q = (3 * (s - 8)) / 2; q < (3 * (s - 7)) / 2; ++q) {
                        const int i = q >> 1;
                        if (q & 1) { e2[i] = __builtin_amdgcn_exp2f(__builtin_fmaf(t[i], -al, c2)); s2 += e2[i]; }
                        else { e1[i] = __builtin_amdgcn_exp2f(__builtin_fmaf(t[i], al, c1)); s1 += e1[i]; }
                    }
                }
                if (s == 32) { s1 = __builtin_amdgcn_rcpf(s1); s2 = __builtin_amdgcn_rcpf(s2); }
                if (s >= 33) {
                    const int i = s - 33;
                    *(LAS v2u*)(aS + 8 * i) = (v2u){pk(e1[4 * i] * s1, e1[4 * i + 1] * s1), pk(e1[4 * i + 2] * s1, e1[4 * i + 3] * s1)};
                }
                FENCE();
            }
            *(LAS v2u*)(aS + 24) = (v2u){pk(e1[12] * s1, e1[13] * s1), pk(e1[14] * s1, e1[15] * s1)};
            *(LAS v2u*)(aS + 32) = (v2u){pk(e1[16] * s1, e1[17] * s1), pk(e2[0] * s2, e2[1] * s2)};
#pragma unroll
            for (int i = 0; i < 4; ++i) *(LAS v2u*)(aS + 40 + 8 * i) = (v2u){pk(e2[4 * i + 2] * s2, e2[4 * i + 3] * s2), pk(e2[4 * i + 4] * s2, e2[4 * i + 5] * s2)};
            const u32 aSv = aS + 144 - 32 * h;
#pragma unroll
            for (int i = 0; i < 4; ++i)
                *(LAS v2u*)(aSv + 8 * i) = (v2u){pk(__builtin_fmaf(-mean, __uint_as_float(cV[i].x), aV[4 * i]) * rstd, __builtin_fmaf(-mean, __uint_as_float(cV[i].y), aV[4 * i + 1]) * rstd),
                                                 pk(__builtin_fmaf(-mean, __uint_as_float(cV[i].z), aV[4 * i + 2]) * rstd, __builtin_fmaf(-mean, __uint_as_float(cV[i].w), aV[4 * i + 3]) * rstd)};
            *(LAS v2u*)(aSv + 32) = (v2u){pk(__builtin_fmaf(-mean, __uint_as_float(cK[4].z), qKV[2]) * rstd, __builtin_fmaf(-mean, __uint_as_float(cK[4].w), qKV[3]) * rstd), 0x3f80u};
        }
        asm volatile("s_waitcnt vmcnt(0) lgkmcnt(0)\n\ts_barrier" ::: "memory");
        STAMP(5 + 12 * L);
        u32 aQ_ = lb + L_Q + l16;
        OPQV(aQ_);
        {
            const int hh = wave >> 1, dt = wave & 1;
            const u32 r0 = lb + L_STG + (4 * (4 * (kg & 1) + gq) + (kg >> 1)) * STGS + 8 * rq;
            u32 pa = r0 + 72 * hh, pb = r0 + 144 + 40 * hh + 32 * dt;
            OPQV(pa); OPQV(pb);
            f4 c[3] = {};
            v4u B[4], A[4][3];
#pragma unroll
            for (int ks = 0; ks < 4; ++ks) {
                B[ks] = tr2(pb + ks * 32 * STGS, pb + (ks * 32 + 2) * STGS);
#pragma unroll
                for (int ft = 0; ft < 3; ++ft) A[ks][ft] = tr2(pa + ks * 32 * STGS + 32 * ft, pa + (ks * 32 + 2) * STGS + 32 * ft);
            }
#pragma unroll
            for (int s = 0; s < 4; ++s) rQ[s] = lds4(aQ_ + s * 1024);
            FENCE();
#pragma unroll
            for (int ks = 0; ks < 4; ++ks)
#pragma unroll
                for (int ft = 0; ft < 3; ++ft) c[ft] = mm16(A[ks][ft], B[ks], c[ft]);
            u32 w[6];
#pragma unroll
            for (int ft = 0; ft < 3; ++ft) { w[2 * ft] = pk(c[ft][0], c[ft][1]); w[2 * ft + 1] = pk(c[ft][2], c[ft][3]); }
            const __amdgpu_buffer_rsrc_t rk = mkrs(ws + W_KV + ((L * 16 + p) * 16 + rg) * 2816 + hh * 1408, 1408);
            if (dt == 0) {
                __builtin_amdgcn_raw_buffer_store_b128((v4u){w[0], w[1], w[2], w[3]}, rk, l16, 0, SC1);
                if (ln < 16) __builtin_amdgcn_raw_buffer_store_b64((v2u){w[4], w[5]}, rk, ln * 8 + 1024, 0, SC1);
            } else {
                const u32 s0 = __builtin_amdgcn_update_dpp(0u, w[2], 0x113, 0xf, 0xf, true), s1 = __builtin_amdgcn_update_dpp(0u, w[3], 0x113, 0xf, 0xf, true);
                const u32 t0 = __builtin_amdgcn_update_dpp(0u, w[4], 0x116, 0xf, 0xf, true), t1 = __builtin_amdgcn_update_dpp(0u, w[5], 0x116, 0xf, 0xf, true);
                const v2u v = row < 3 ? (v2u){w[0], w[1]} : row < 6 ? (v2u){s0, s1} : row < 9 ? (v2u){t0, t1} : (v2u){0, 0};
                const int slot = row < 6 ? kg * 6 + row : 24 + row - 6;
                if (row < 6 || (kg == 0 && row < 14)) __builtin_amdgcn_raw_buffer_store_b64(v, rk, slot * 8 + 1152, 0, SC1);
            }
        }
        FENCE();
        STAMP(6 + 12 * L);
        u32 pq[20];
        v4u kp[16];
        const __amdgpu_buffer_rsrc_t rkv = mkrs(ws + W_KV + (L * 16 + p) * 16 * 2816, 16 * 2816);
        const u32 kvo = (wave * 44 + (ln < 44 ? ln : ln - 20)) * 16;
        {
            f16v aQ = {};
            v4u cQ[5];
#pragma unroll
            for (int s = 0; s < 36; ++s) {
                aQ = mm32(rQ[s & 3], xf[s], aQ);
                if (s + 4 < 36) rQ[s & 3] = lds4(aQ_ + (s + 4) * 1024); else cQ[s - 32] = lds4(aCS + 128 + (s - 32) * 16);
                if (s == 31) cQ[4] = lds4(aCS + 208);
                FENCE();
            }
            {
                u32 so = 0;
#pragma unroll
                for (int i = 0; i < 16; ++i) { OPQS(so); kp[i] = __builtin_amdgcn_raw_buffer_load_b128(rkv, kvo, so, SC1); so += 2816; }
            }
            FENCE();
            float t[18]; u32 ph[18];
#pragma unroll
            for (int i = 0; i < 18; ++i) t[i] = __builtin_fmaf(-mean, __uint_as_float(cQ[i >> 2][i & 3]), i < 16 ? aQ[i] : qQ[i - 16]);
            hedgehog(t, al, ph);
#pragma unroll
            for (int i = 0; i < 18; ++i) { pq[i] = ph[i]; OPQV(pq[i]); }
            pq[18] = 0; pq[19] = 0;
        }
        FENCE();
        STAMP(7 + 12 * L);
        {
            u32 m = 0;
#pragma unroll
            for (int i = 0; i < 16; ++i) m = mx3(m, kp[i].x, kp[i].z);
            if (__builtin_expect(bad(m), 0)) {
#pragma unroll 1
                for (int sp = 0; sp < SPIN; ++sp) {
                    __builtin_amdgcn_s_sleep(2);
                    asm volatile("" ::: "memory");
                    u32 so = 0;
#pragma unroll
                    for (int i = 0; i < 16; ++i) { OPQS(so); kp[i] = __builtin_amdgcn_raw_buffer_load_b128(rkv, kvo, so, SC1); so += 2816; }
                    m = 0;
#pragma unroll
                    for (int i = 0; i < 16; ++i) m = mx3(m, kp[i].x, kp[i].z);
                    if (!bad(m)) break;
                }
            }
            float a[8] = {};
            u32 one_lo = 0x00003f80u, one_hi = 0x3f800000u;
            OPQS(one_lo); OPQS(one_hi);
#pragma unroll
            for (int i = 0; i < 16; ++i)
#pragma unroll
                for (int j = 0; j < 4; ++j) {
                    const u32 kw = kp[i][j];
                    a[2 * j] = __builtin_amdgcn_fdot2_f32_bf16(__builtin_bit_cast(b2, kw), __builtin_bit_cast(b2, one_lo), a[2 * j], false);
                    a[2 * j + 1] = __builtin_amdgcn_fdot2_f32_bf16(__builtin_bit_cast(b2, kw), __builtin_bit_cast(b2, one_hi), a[2 * j + 1], false);
                }
            const u32 o0 = kvw & 0xffffu, o1 = kvw >> 16;
            if (o0 != 0xffffu) *(LAS v2u*)(lb + L_KVA + o0) = (v2u){pk(a[0], a[1]), pk(a[2], a[3])};
            if (o1 != 0xffffu) *(LAS v2u*)(lb + L_KVA + o1) = (v2u){pk(a[4], a[5]), pk(a[6], a[7])};
        }
        barrier();
        STAMP(8 + 12 * L);
        {
            const int hr = (tl >> 2) & 1, d = (tl & 3) + 4 * (tl >> 3);
            const u32 aF = lb + L_KVA + (h == hr ? (hr * 19 + d) * 80 : 38 * 80);
            const u32 aQ4 = lb + L_KVA + (kg == gq && rq < 3 ? ((gq >> 1) * 19 + 16 + rq) * 80 : 38 * 80);
            v4u A[5], Aq[5];
#pragma unroll
            for (int c = 0; c < 5; ++c) { Aq[c] = lds4(aQ4 + 16 * c); A[c] = lds4(aF + 16 * c); }
            FENCE();
            f16v o = {}; f4 oq = {};
#pragma unroll
            for (int c = 0; c < 5; ++c) oq = mm16(Aq[c], (v4u){pq[4 * c], pq[4 * c + 1], pq[4 * c + 2], pq[4 * c + 3]}, oq);
#pragma unroll
            for (int c = 0; c < 5; ++c) o = mm32(A[c], (v4u){pq[4 * c], pq[4 * c + 1], pq[4 * c + 2], pq[4 * c + 3]}, o);
            const float rd = __builtin_amdgcn_rcpf(oq[2] + 1e-6f);
            const __amdgpu_buffer_rsrc_t ro = mkrs(ws + W_O + ((L * 16 + rg) * 64 + p * 4 + wave) * 2304, 2304);
            __builtin_amdgcn_raw_buffer_store_b32(pk(oq[0] * rd, oq[1] * rd), ro, l4 + 2048, 0, SC1);
            __builtin_amdgcn_raw_buffer_store_b128((v4u){pk(o[0] * rd, o[1] * rd), pk(o[2] * rd, o[3] * rd), pk(o[4] * rd, o[5] * rd), pk(o[6] * rd, o[7] * rd)}, ro, l16, 0, SC1);
            __builtin_amdgcn_raw_buffer_store_b128((v4u){pk(o[8] * rd, o[9] * rd), pk(o[10] * rd, o[11] * rd), pk(o[12] * rd, o[13] * rd), pk(o[14] * rd, o[15] * rd)}, ro, l16 + 1024, 0, SC1);
        }
        FENCE();
        STAMP(9 + 12 * L);
        if (L == 0) {
            const __amdgpu_buffer_rsrc_t ry = mkrs(ws + W_Y + (36 * p) * 4096 + (rg * 128 + wave * 32) * 2, 36 * 4096);
#pragma unroll 1
            for (int sp = 0; sp < SPIN; ++sp) {
                asm volatile("" ::: "memory");
                u32 e[18]; bool nb = false; u32 so = 0;
#pragma unroll
                for (int m = 0; m < 18; ++m) { OPQS(so); e[m] = __builtin_amdgcn_raw_buffer_load_b16(ry, h * 18 * 4096 + tl * 2, so, SC1); so += 4096; }
                FENCE();
#pragma unroll
                for (int m = 0; m < 18; ++m) nb |= e[m] == 0xffffu;
#pragma unroll
                for (int i = 0; i < 9; ++i) yv[i] = e[2 * i] | (e[2 * i + 1] << 16);
                if (__builtin_amdgcn_ballot_w64(nb) == 0) break;
                __builtin_amdgcn_s_sleep(2);
            }
        } else {
            __builtin_amdgcn_s_sleep(10);
        }
        const __amdgpu_buffer_rsrc_t rog = mkrs(ws + W_O + ((L * 16 + rg) * 64 + wave) * 2304, 64 * 2304);
        gather_issue<2304, 0, 48>(rog, l16, l4, xf, st);
        FENCE();
        float x1[18];
        {
            f16v aW = {}; f4 qW = {};
            u32 aWq = aQq + (qa && rq < 2 ? 128 : 0);
            OPQV(aWq);
            v4u r1[4];
#pragma unroll
            for (int s = 0; s < 4; ++s) r1[s] = lds4(aWq + s * QTS);
            sfor<0, 36>(LAM(SI) {
                constexpr int s = SI.v;
                if constexpr ((s & 7) == 0) { FENCE(); check8<2304, (s >> 3)>(rog, l16, l4, xf, st); FENCE(); }
                aW = mm32(wf[s], xf[s], aW);
                qW = mm16(r1[s & 3], xf[s], qW);
                if constexpr (s + 4 < 36) r1[s & 3] = lds4(aWq + (s + 4) * QTS);
                FENCE();
            });
            STAMP(10 + 12 * L);
            float s1 = 0.f, s2 = 0.f;
#pragma unroll
            for (int i = 0; i < 9; ++i) {
                const float a0 = i < 8 ? aW[2 * i] : qW[0], a1 = i < 8 ? aW[2 * i + 1] : qW[1];
                const u32 at = pk(a0, a1), w = pk(bf_lo(xr[i]) + bf_lo(at), bf_hi(xr[i]) + bf_hi(at));
                x1[2 * i] = bf_lo(w); x1[2 * i + 1] = bf_hi(w);
                s1 += x1[2 * i] + x1[2 * i + 1]; s2 += x1[2 * i] * x1[2 * i] + x1[2 * i + 1] * x1[2 * i + 1];
            }
            const __amdgpu_buffer_rsrc_t rs = mkrs(ws + W_ST + ((L * 16 + rg) * 64 + p * 4 + wave) * 256, 256);
            __builtin_amdgcn_raw_buffer_store_b32(__float_as_uint(halves_sum(s1, s2)), rs, l4, 0, SC1);
        }
        FENCE();
        STAMP(11 + 12 * L);
        __builtin_amdgcn_s_sleep(10);
        {
            const __amdgpu_buffer_rsrc_t rs = mkrs(ws + W_ST + ((L * 16 + rg) * 64 + wave) * 256, 64 * 256);
            float t;
#pragma unroll 1
            for (int sp = 0; sp < SPIN; ++sp) {
                asm volatile("" ::: "memory");
                u32 e[16], m = 0, so = 0;
#pragma unroll
                for (int i = 0; i < 16; ++i) { if ((i & 3) == 0) { OPQS(so); } e[i] = __builtin_amdgcn_raw_buffer_load_b32(rs, l4 + (i & 3) * 1024, so, SC1); if ((i & 3) == 3) so += 4096; }
                FENCE();
                t = 0.f;
#pragma unroll
                for (int i = 0; i < 16; i += 2) { m = mx3(m, e[i], e[i + 1]); t += __uint_as_float(e[i]) + __uint_as_float(e[i + 1]); }
                if (!bad(m)) break;
                __builtin_amdgcn_s_sleep(2);
            }
            STAMP(12 + 12 * L);
            auto r = __builtin_amdgcn_permlane32_swap(__float_as_uint(t), __float_as_uint(t), false, false);
            const float m1 = __uint_as_float(r[0]) * (1.f / 576.f);
            const float r1 = __builtin_amdgcn_rsqf(fmaxf(__uint_as_float(r[1]) * (1.f / 576.f) - m1 * m1, 0.f) + 1e-5f);
            float s1 = 0.f, s2 = 0.f;
#pragma unroll
            for (int i = 0; i < 9; ++i) {
                const u32 xn = pk((x1[2 * i] - m1) * r1, (x1[2 * i + 1] - m1) * r1);
                const u32 gt = pk(bf_lo(xn) * bf_lo(yv[i]), bf_hi(xn) * bf_hi(yv[i]));
                xr[i] = pk(x1[2 * i] + bf_lo(gt), x1[2 * i + 1] + bf_hi(gt));
                const float a = bf_lo(xr[i]), c = bf_hi(xr[i]);
                s1 += a + c; s2 += a * a + c * c;
            }
            if (L == 0) {
                const __amdgpu_buffer_rsrc_t rp = mkrs(ws + W_XF1 + (rg * 64 + p * 4 + wave) * 2560, 2560);
                __builtin_amdgcn_raw_buffer_store_b128((v4u){xr[0], xr[1], xr[2], xr[3]}, rp, l16, 0, SC1);
                __builtin_amdgcn_raw_buffer_store_b128((v4u){xr[4], xr[5], xr[6], xr[7]}, rp, l16 + 1024, 0, SC1);
                __builtin_amdgcn_raw_buffer_store_b32(xr[8], rp, l4 + 2048, 0, SC1);
                __builtin_amdgcn_raw_buffer_store_b32(__float_as_uint(halves_sum(s1, s2)), rp, l4 + 2304, 0, SC1);
                STAMP(13);
                __builtin_amdgcn_s_sleep(10);
                const __amdgpu_buffer_rsrc_t rn = mkrs(ws + W_XF1 + (rg * 64 + wave) * 2560, 64 * 2560);
                gather_issue<2560, 0, 64>(rn, l16, l4, xf, st);
                FENCE();
            } else {
                u32* op = (u32*)(out + (rg * 128 + wave * 32 + tl) * 576 + 36 * p + 18 * h);
#pragma unroll
                for (int i = 0; i < 9; ++i) op[i] = xr[i];
                STAMP(25);
            }
        }
    }
    STAMP_END();
}

extern "C" void kernel_launch(void* const* d_in, const int* in_sizes, int n_in, void* d_out, int out_size, void* d_ws, size_t ws_size, hipStream_t stream) {
    (void)in_sizes; (void)n_in; (void)out_size; (void)ws_size;
    hipFuncSetAttribute((const void*)k_main, hipFuncAttributeMaxDynamicSharedMemorySize, L_END);
    k_pre<<<544, 256, 0, stream>>>((const u16*)d_in[0], (const u16*)d_in[1], (const u16*)d_in[2], (const u16*)d_in[3], (const u16*)d_in[5], (const u16*)d_in[6],
                                   (const u16*)d_in[7], (const u16*)d_in[8], (char*)d_ws);
    k_main<<<256, 256, L_END, stream>>>((const u16*)d_in[8], (const u16*)d_in[9], (u16*)d_out, (char*)d_ws);
}
```

```cpp
#define VARIANT 7
#include <hip/hip_runtime.h>
#include <hip/hip_bf16.h>
#include <stdint.h>
typedef unsigned short u16;
#define S_ 2048
#define D_ 576
#define H_ 32
#define DH 18
__device__ __forceinline__ float b2f(u16 v) { return __uint_as_float(((unsigned)v) << 16); }
__device__ __forceinline__ u16 f2b(float f) { __hip_bfloat16 h = __float2bfloat16(f); return *(u16*)&h; }
__device__ __forceinline__ float rb(float f) { return b2f(f2b(f)); }

__global__ void k_u(const u16* k, const u16* v, const u16* sk, u16* uT) {
    int i = blockIdx.x * 256 + threadIdx.x; int t = i / D_, d = i % D_;
    float acc = 0.f;
    for (int j = 0; j < 4; ++j) {
        int s = t + j - 3; float kv = s >= 0 ? b2f(k[s * D_ + d]) : 0.f;
        acc = rb(acc + rb(kv * b2f(sk[d * 4 + 3 - j])));
    }
    uT[d * S_ + t] = f2b(acc * b2f(v[i]));
}
__global__ void k_conv(const u16* uT, const u16* h, const u16* Dv, u16* y) {
    __shared__ float su[S_], sh[S_];
    int d = blockIdx.x;
    for (int i = threadIdx.x; i < S_; i += 256) { su[i] = b2f(uT[d * S_ + i]); sh[i] = b2f(h[d * S_ + i]); }
    __syncthreads();
    for (int t = threadIdx.x; t < S_; t += 256) {
        float a = 0.f;
        for (int s = 0; s <= t; ++s) a += sh[t - s] * su[s];
        y[t * D_ + d] = f2b(rb(a) + rb(su[t] * b2f(Dv[d])));
    }
}
__global__ void k_ln(const u16* x, u16* xn) {
    int t = blockIdx.x; int l = threadIdx.x;
    float v[9]; float s = 0.f;
    for (int j = 0; j < 9; ++j) { v[j] = b2f(x[t * D_ + l + 64 * j]); s += v[j]; }
    for (int o = 32; o; o >>= 1) s += __shfl_xor(s, o);
    float m = s / D_; float q = 0.f;
    for (int j = 0; j < 9; ++j) { float dd = v[j] - m; q += dd * dd; }
    for (int o = 32; o; o >>= 1) q += __shfl_xor(q, o);
    float mb = rb(m), var = rb(q / D_); float sd = rb(sqrtf(rb(var + 1e-5f)));
    for (int j = 0; j < 9; ++j) xn[t * D_ + l + 64 * j] = f2b(rb(v[j] - mb) / sd);
}
__global__ void k_gemm(const u16* A, const u16* B, u16* C, const u16* res, int N, int K) {
    __shared__ float sa[16][33], sb[32][17];
    int tx = threadIdx.x & 15, ty = threadIdx.x >> 4;
    int m0 = blockIdx.y * 16, n0 = blockIdx.x * 16;
    float acc = 0.f;
    for (int k0 = 0; k0 < K; k0 += 32) {
        for (int i = threadIdx.x; i < 512; i += 256) { sa[i >> 5][i & 31] = b2f(A[(m0 + (i >> 5)) * K + k0 + (i & 31)]); sb[i >> 4][i & 15] = b2f(B[(k0 + (i >> 4)) * N + n0 + (i & 15)]); }
        __syncthreads();
        for (int kk = 0; kk < 32; ++kk) acc += sa[ty][kk] * sb[kk][tx];
        __syncthreads();
    }
    float r = rb(acc);
    if (res) r = r + b2f(res[(m0 + ty) * N + n0 + tx]);
    C[(m0 + ty) * N + n0 + tx] = f2b(r);
}
__global__ void __launch_bounds__(256) k_phi(const u16* qkv, const u16* wphi, u16* phi) {
    int i = blockIdx.x * 256 + threadIdx.x;
    int t = i % S_, h = (i / S_) % H_, w = i / (S_ * H_);
    float q[DH], p[DH];
    for (int d = 0; d < DH; ++d) q[d] = b2f(qkv[t * 1728 + w * D_ + h * DH + d]);
    float mx = -1e30f, mn = 1e30f;
    #pragma unroll
    for (int f = 0; f < DH; ++f) { float a = 0.f; for (int d = 0; d < DH; ++d) a += q[d] * b2f(wphi[(h * DH + d) * DH + f]); p[f] = rb(a); mx = fmaxf(mx, p[f]); mn = fminf(mn, p[f]); }
    float s1 = 0.f, s2 = 0.f;
    #pragma unroll
    for (int f = 0; f < DH; ++f) { s1 += expf(p[f] - mx); s2 += expf(mn - p[f]); }
    #pragma unroll
    for (int f = 0; f < DH; ++f) { phi[(size_t)i * 36 + f] = f2b(expf(p[f] - mx) / s1); phi[(size_t)i * 36 + 18 + f] = f2b(expf(mn - p[f]) / s2); }
}
__global__ void k_kv(const u16* phi, const u16* qkv, float* kv) {
    int h = blockIdx.x; int e = blockIdx.y * 64 + threadIdx.x; if (e >= 36 * 19) return;
    int f = e / 19, d = e % 19; float a = 0.f;
    const u16* pk = phi + (size_t)(H_ + h) * S_ * 36;
    for (int t = 0; t < S_; ++t) a += b2f(pk[t * 36 + f]) * (d < 18 ? b2f(qkv[t * 1728 + 2 * D_ + h * DH + d]) : 1.f);
    kv[h * 684 + e] = rb(a);
}
__global__ void k_o(const u16* phi, const float* kv, u16* o) {
    int i = blockIdx.x * 256 + threadIdx.x; int t = i / D_, c = i % D_, h = c / DH, d = c % DH;
    const u16* pq = phi + ((size_t)h * S_ + t) * 36; float n = 0.f, dn = 0.f;
    for (int f = 0; f < 36; ++f) { float p = b2f(pq[f]); n += p * kv[h * 684 + f * 19 + d]; dn += p * kv[h * 684 + f * 19 + 18]; }
    o[i] = f2b(rb(n) / rb(rb(dn) + 1e-6f));
}
__global__ void k_gate(const u16* x, const u16* xn, const u16* y, u16* out) {
    int i = blockIdx.x * 256 + threadIdx.x;
    out[i] = f2b(b2f(x[i]) + rb(b2f(xn[i]) * b2f(y[i])));
}
__global__ void k_empty(int* p) { if (p == nullptr) p[0] = 1; }
#define EXTRA 0

typedef unsigned v4u __attribute__((ext_vector_type(4)));
#define LAS __attribute__((address_space(3)))
#define GAS __attribute__((address_space(1)))
__device__ __forceinline__ unsigned long long rt() { return __builtin_amdgcn_s_memrealtime(); }
__global__ void __launch_bounds__(256, 1) k_probe(const u16* x, const u16* w, const u16* wo, const u16* bk, const u16* bv, const u16* sh, unsigned long long* stamps, unsigned* sink) {
    extern __shared__ __attribute__((aligned(16))) char smem[];
    unsigned long long t0 = rt();
    const int b = blockIdx.x, wave = __builtin_amdgcn_readfirstlane(threadIdx.x >> 6), lane = threadIdx.x & 63, tid = threadIdx.x;
    const int xc = b & 7, i = b >> 3, rg = 2 * xc + (i >> 4), hg = i & 15;
    unsigned acc = 0;
#define R4(x) x x x x
#define R16(x) R4(R4(x))
#define R256(x) R16(R16(x))
    float fa = threadIdx.x, fb = 1.0001f, fc = 0.5f, fd = 2.f, fe = 3.f;
    { unsigned long long pc; asm volatile("s_getpc_b64 %0" : "=s"(pc));
      const v4u* cp = (const v4u*)((pc & ~63ull) + (b >> 3) * 1024 + wave * 256 + (lane & 15) * 16);
      v4u pv = *cp;
      acc ^= pv.x; asm volatile("s_waitcnt vmcnt(0)" : "+v"(acc) :: "memory"); t0 = rt();
    }
    asm volatile(R256(R4("v_fma_f32 %0, %0, %4, %4\n\tv_fma_f32 %1, %1, %4, %4\n\tv_fma_f32 %2, %2, %4, %4\n\tv_fma_f32 %3, %3, %4, %4\n\t")) : "+v"(fa), "+v"(fc), "+v"(fd), "+v"(fe) : "v"(fb));
    unsigned long long t2 = rt();
    acc ^= __float_as_uint(fa + fc + fd + fe);
    acc ^= *(unsigned*)(smem + lane * 4);
    if (lane == 0) { unsigned long long* s = stamps + (b * 4 + wave) * 2; s[0] = t0; s[1] = t2; }
    if (acc == 0x12345) sink[0] = acc;
}
__global__ void k_spin(const unsigned long long* stamps) {
    unsigned long long mn = ~0ull, mx = 0;
    for (int i = threadIdx.x; i < 1024; i += 64) { mn = min(mn, stamps[2 * i]); mx = max(mx, stamps[2 * i + 1]); }
    for (int o = 32; o; o >>= 1) { mn = min(mn, (unsigned long long)__shfl_xor((long long)mn, o)); mx = max(mx, (unsigned long long)__shfl_xor((long long)mx, o)); }
    unsigned long long d = mx - mn; if (d > 3000) d = 3000;
    unsigned long long t = rt();
    while (rt() - t < d * 100) __builtin_amdgcn_s_sleep(4);
}
extern "C" void kernel_launch(void* const* d_in, const int* in_sizes, int n_in,
                              void* d_out, int out_size, void* d_ws, size_t ws_size,
                              hipStream_t stream) {
    const u16* x = (const u16*)d_in[0]; const u16* wqkv = (const u16*)d_in[1]; const u16* wphi = (const u16*)d_in[2]; const u16* wo = (const u16*)d_in[3];
    const u16* bk = (const u16*)d_in[5]; const u16* bv = (const u16*)d_in[6]; const u16* sk = (const u16*)d_in[7]; const u16* sh = (const u16*)d_in[8]; const u16* sD = (const u16*)d_in[9];
    char* ws = (char*)d_ws; const size_t MB = 1 << 20;
    u16* uT = (u16*)(ws); u16* y = (u16*)(ws + 4 * MB); u16* xn = (u16*)(ws + 8 * MB); u16* qkv = (u16*)(ws + 12 * MB);
    u16* phi = (u16*)(ws + 24 * MB); float* kv = (float*)(ws + 40 * MB); u16* o = (u16*)(ws + 44 * MB); u16* x1 = (u16*)(ws + 48 * MB); u16* x2 = (u16*)(ws + 52 * MB);
    int NE = S_ * D_ / 256;
    hipFuncSetAttribute((void*)k_probe, hipFuncAttributeMaxDynamicSharedMemorySize, 130 * 1024);
    k_probe<<<256, 256, 130 * 1024, stream>>>(x, wqkv, wo, bk, bv, sh, (unsigned long long*)(ws + 100 * MB), (unsigned*)(ws + 101 * MB));
    k_u<<<NE, 256, 0, stream>>>(bk, bv, sk, uT);
    k_conv<<<D_, 256, 0, stream>>>(uT, sh, sD, y);
    const u16* xin = x;
    for (int L = 0; L < 2; ++L) {
        k_ln<<<S_, 64, 0, stream>>>(xin, xn);
        k_gemm<<<dim3(1728 / 16, S_ / 16), 256, 0, stream>>>(xn, wqkv, qkv, nullptr, 1728, D_);
        k_phi<<<2 * H_ * S_ / 256, 256, 0, stream>>>(qkv, wphi, phi);
        k_kv<<<dim3(H_, 11), 64, 0, stream>>>(phi, qkv, kv);
        k_o<<<NE, 256, 0, stream>>>(phi, kv, o);
        k_gemm<<<dim3(D_ / 16, S_ / 16), 256, 0, stream>>>(o, wo, x1, xin, D_, D_);
        k_ln<<<S_, 64, 0, stream>>>(x1, xn);
        u16* dst = L == 0 ? x2 : (u16*)d_out;
        k_gate<<<NE, 256, 0, stream>>>(x1, xn, y, dst);
        xin = x2;
    }
    k_spin<<<1, 64, 0, stream>>>((const unsigned long long*)(ws + 100 * MB));
    for (int i = 0; i < EXTRA; ++i) k_empty<<<256, 256, 0, stream>>>((int*)d_ws);
}
```

```cpp
#define DBG
#define HDBG
#include <stdio.h>
#include <hip/hip_runtime.h>
#include <stdint.h>
#pragma clang diagnostic ignored "-Wint-to-pointer-cast"
#pragma clang diagnostic ignored "-Wunused-value"
typedef unsigned short u16;
typedef unsigned u32;
typedef u32 v4u __attribute__((ext_vector_type(4)));
typedef u32 v2u __attribute__((ext_vector_type(2)));
typedef float f4 __attribute__((ext_vector_type(4)));
typedef float f2 __attribute__((ext_vector_type(2)));
typedef float f16v __attribute__((ext_vector_type(16)));
typedef __bf16 b8 __attribute__((ext_vector_type(8)));
typedef __bf16 b2 __attribute__((ext_vector_type(2)));
typedef short s4 __attribute__((ext_vector_type(4)));
#define LAS __attribute__((address_space(3)))
#define DEV __device__ __forceinline__
#define SC1 16

constexpr u32 IMG = 130048;
constexpr u32 L_K = 0, L_V = 36864, L_QT = 73728, L_Q = 93184, L_STG = 130048, L_KVA = 159744, L_CS = 162864, L_END = 163312;
constexpr u32 QTS = 528, STGS = 232, CVS = 21216, HCS = 4160;
constexpr u32 W_IMG = 0, W_WOT = W_IMG + 16 * IMG, W_CSP = W_WOT + 16 * 36864, W_UT = W_CSP + 16 * 18 * 448,
              W_XF0 = W_UT + 576 * 4096, W_CLR = W_XF0 + 1024 * 2560, W_XF1 = W_CLR, W_O = W_XF1 + 1024 * 2560,
              W_Y = W_O + 2 * 1024 * 2304, W_KV = W_Y + 576 * 4096, W_ST = W_KV + 2 * 256 * 2816, W_END = W_ST + 2 * 1024 * 256,
              W_DBG = W_END;
constexpr u32 CLRB = (W_END - W_CLR) / 256;
static_assert(CLRB * 256 == W_END - W_CLR && CLRB % 16 == 0, "");

DEV float bf_lo(u32 v) { return __uint_as_float(v << 16); }
DEV float bf_hi(u32 v) { return __uint_as_float(v & 0xffff0000u); }
DEV u32 pk(float lo, float hi) { b2 r = __builtin_convertvector((f2){lo, hi}, b2); return __builtin_bit_cast(u32, r); }
DEV float rb(float f) { return bf_lo(pk(f, f)); }
DEV f16v mm32(v4u a, v4u b, f16v c) { return __builtin_amdgcn_mfma_f32_32x32x16_bf16(__builtin_bit_cast(b8, a), __builtin_bit_cast(b8, b), c, 0, 0, 0); }
DEV f4 mm16(v4u a, v4u b, f4 c) { return __builtin_amdgcn_mfma_f32_16x16x32_bf16(__builtin_bit_cast(b8, a), __builtin_bit_cast(b8, b), c, 0, 0, 0); }
DEV __amdgpu_buffer_rsrc_t mkrs(const void* p, u32 n) { return __builtin_amdgcn_make_buffer_rsrc((void*)p, 0, (int)n, 0x00020000); }
DEV v4u lds4(u32 a) { return *(const LAS v4u*)a; }
DEV v2u lds2(u32 a) { return *(const LAS v2u*)a; }
DEV void barrier() { asm volatile("s_waitcnt lgkmcnt(0)\n\ts_barrier" ::: "memory"); }
DEV void dma16(const void* g, u32 voff, u32 lds) {
    asm volatile("s_mov_b32 m0, %2\n\ts_nop 0\n\tglobal_load_lds_dwordx4 %0, %1" ::"v"(voff), "s"(g), "s"(lds) : "m0", "memory");
}
template <int I> struct IC { static constexpr int v = I; };
template <int B, int E, class F> DEV void sfor(F&& f) { if constexpr (B < E) { f(IC<B>{}); sfor<B + 1, E>(f); } }
#define LAM(I) [&](auto I) __attribute__((always_inline))
#define FENCE() __builtin_amdgcn_sched_barrier(0)
#define OPQV(x) asm volatile("" : "+v"(x))
#define OPQS(x) asm volatile("" : "+s"(x))
DEV float halves_sum(float a, float b) {
    auto r = __builtin_amdgcn_permlane32_swap(__float_as_uint(a), __float_as_uint(b), false, false);
    return __uint_as_float(r[0]) + __uint_as_float(r[1]);
}
#define STAMP_INIT() unsigned long long rt0_ = __builtin_amdgcn_s_memrealtime(); u32 sv_ = 0; STAMP(0)
#define STAMP(i) do { unsigned long long tp_; asm volatile("s_memtime %0\n\ts_waitcnt lgkmcnt(0)" : "=s"(tp_)); asm volatile("s_mov_b32 m0, %2\n\ts_nop 0\n\tv_writelane_b32 %0, %1, m0" : "+v"(sv_) : "s"((u32)tp_), "s"(i) : "m0"); } while (0)
#define PST(i) do { unsigned long long tp_; asm volatile("s_memrealtime %0\n\ts_waitcnt lgkmcnt(0)" : "=s"(tp_)); if (lane == 0) ((u32*)(ws + W_DBG + 1024 * 256))[(blockIdx.x * 4 + wave) * 8 + (i)] = (u32)tp_; } while (0)
#define PWAIT() asm volatile("s_waitcnt vmcnt(0)" ::: "memory")
#define STAMP_END() do { STAMP(30); unsigned long long rt1_ = __builtin_amdgcn_s_memrealtime(); \
    u32* d_ = (u32*)(ws + W_DBG) + (blockIdx.x * 4 + wave) * 64; d_[lane] = lane == 32 ? (u32)rt0_ : lane == 33 ? (u32)rt1_ : sv_; } while (0)

__global__ void __launch_bounds__(256) k_pre(const u16* __restrict__ x, const u16* __restrict__ wqkv, const u16* __restrict__ wphi, const u16* __restrict__ wo,
                                             const u16* __restrict__ bk, const u16* __restrict__ bv, const u16* __restrict__ sk, const u16* __restrict__ sh, char* __restrict__ ws) {
    __shared__ __attribute__((aligned(16))) char sm[72 * 144];
    const int tid = threadIdx.x, lane = tid & 63, wave = __builtin_amdgcn_readfirstlane(tid >> 6);
    PST(0);
    if (blockIdx.x < 288) {
        const int task = blockIdx.x * 4 + wave, P = task & 15, sec = (task >> 4) & 3, rt = task >> 6;
        const int rho = lane & 31, kh = lane >> 5, hD = (rho >> 2) & 1, r = (rho & 3) + 4 * (rho >> 3);
        const int k = rt < 16 ? 36 * rt + 18 * hD + r : 36 * (4 * (2 * (rt - 16) + (r >> 3)) + ((r & 7) >> 1)) + 18 * hD + 16 + (r & 1);
        const u16* src = sec < 3 ? wqkv + k * 1728 + (sec == 0 ? 576 : sec == 1 ? 1152 : 0) + P * 36 : wo + k * 576 + P * 36;
        v4u a0[2]; u32 a1[2]; u32 bw[2][5];
        const bool fold = (sec == 0 || sec == 2);
        const int f = rho;
#pragma unroll
        for (int hh = 0; hh < 2; ++hh) {
            const u32* s32 = (const u32*)(src + 18 * hh);
            a0[hh] = (v4u){s32[4 * kh], s32[4 * kh + 1], s32[4 * kh + 2], s32[4 * kh + 3]};
            a1[hh] = kh == 0 ? s32[8] : 0u;
            if (fold) {
                const u16* ph = wphi + (2 * P + hh) * 324 + (f < 18 ? f : 0);
#pragma unroll
                for (int j = 0; j < 4; ++j) bw[hh][j] = (u32)ph[(8 * kh + 2 * j) * 18] | ((u32)ph[(8 * kh + 2 * j + 1) * 18] << 16);
                bw[hh][4] = (u32)ph[16 * 18] | ((u32)ph[17 * 18] << 16);
            } else {
#pragma unroll
                for (int j = 0; j < 4; ++j) bw[hh][j] = (8 * kh + 2 * j == f ? 0x3f80u : 0u) | (8 * kh + 2 * j + 1 == f ? 0x3f800000u : 0u);
                bw[hh][4] = (f == 16 ? 0x3f80u : 0u) | (f == 17 ? 0x3f800000u : 0u);
            }
        }
        PWAIT(); PST(1);
#pragma unroll
        for (int hh = 0; hh < 2; ++hh) {
            f16v d = {};
            d = mm32(a0[hh], (v4u){bw[hh][0], bw[hh][1], bw[hh][2], bw[hh][3]}, d);
            d = mm32((v4u){a1[hh], 0, 0, 0}, (v4u){kh == 0 ? bw[hh][4] : 0u, 0, 0, 0}, d);
            u32 w[8]; float cs = 0.f;
#pragma unroll
            for (int j = 0; j < 8; ++j) { w[j] = pk(d[2 * j], d[2 * j + 1]); cs += bf_lo(w[j]) + bf_hi(w[j]); }
            cs = halves_sum(cs, cs);
            const int m = rho;
            char* dst; u32 st;
            if (m < 16) {
                const u32 lo = (u32)(((m & 3) + 8 * (m >> 2) + 4 * hh + 32 * kh) * 16 + 2 * rt * 1024);
                dst = sec < 3 ? ws + W_IMG + P * IMG + (sec == 0 ? L_K : sec == 1 ? L_V : L_Q) + lo : ws + W_WOT + P * 36864 + lo;
                st = 1024;
            } else {
                const u32 e = sec < 2 ? (u32)(((hh * 4 + sec * 2 + (m - 16)) * 2 + kh) * 16) : (u32)((sec == 2 ? 256 : 384) + ((hh * 2 + (m - 16)) * 2 + kh) * 16);
                dst = ws + W_IMG + P * IMG + L_QT + 2 * rt * QTS + e;
                st = QTS;
            }
            if (m < 18) {
                *(v4u*)dst = (v4u){w[0], w[1], w[2], w[3]};
                *(v4u*)(dst + st) = (v4u){w[4], w[5], w[6], w[7]};
                if (sec < 3 && kh == 0) {
                    const int e = m < 16 ? sec * 16 + m : (sec < 2 ? 48 + sec * 2 + (m - 16) : 52 + (m - 16));
                    ((float*)(ws + W_CSP))[((P * 18 + rt) * 2 + hh) * 56 + e] = cs;
                }
            }
            if (sec == 3 && hh == 0 && m == 18) *(v4u*)(ws + W_IMG + P * IMG + L_QT + (2 * rt + kh) * QTS + 512) = (v4u){0, 0, 0, 0};
            if (sec == 2 && kh == 0 && (m == 18 || m == 19)) ((float*)(ws + W_CSP))[((P * 18 + rt) * 2 + hh) * 56 + 36 + m] = 0.f;
        }
        PST(2); PWAIT(); PST(3);
        return;
    }
    const int bb = blockIdx.x - 288;
    const int tok = bb * 8 + (tid & 7), c = tid >> 3;
    const u32* xs = (const u32*)(x + tok * 576 + c * 18);
    u32 xv[9];
#pragma unroll
    for (int j = 0; j < 9; ++j) xv[j] = xs[j];
    const int tg = (bb & 7) * 4 + (bb >> 6), cg = (bb >> 3) & 7;
    const int pc = tid % 9, tq = tid / 9;
    v4u kr[7], vr[4], wr[4];
    if (tid < 144) {
        const int t0 = tg * 64 + tq * 4;
#pragma unroll
        for (int j = 0; j < 7; ++j) { const int t = t0 + j - 3; kr[j] = t >= 0 ? *(const v4u*)(bk + t * 576 + cg * 72 + pc * 8) : (v4u){0, 0, 0, 0}; }
#pragma unroll
        for (int j = 0; j < 4; ++j) { vr[j] = *(const v4u*)(bv + (t0 + j) * 576 + cg * 72 + pc * 8); wr[j] = *(const v4u*)(sk + (cg * 72 + pc * 8) * 4 + j * 8); }
    }
    {
        const v4u* ph = (const v4u*)((const char*)sh + bb * 9216);
        v4u p0 = ph[tid], p1 = ph[256 + tid], p2 = ph[512 + (tid & 63)];
        asm volatile("" ::"v"(p0), "v"(p1), "v"(p2));
    }
    {
        const __amdgpu_buffer_rsrc_t rs = mkrs(ws + W_CLR + bb * CLRB, CLRB);
#pragma unroll
        for (int i = 0; i < 12; ++i) if (i * 4096 + tid * 16 < CLRB) __builtin_amdgcn_raw_buffer_store_b128((v4u){~0u, ~0u, ~0u, ~0u}, rs, tid * 16, i * 4096, SC1);
    }
    PST(4);
    {
        float s1 = 0.f, s2 = 0.f;
#pragma unroll
        for (int j = 0; j < 9; ++j) { const float a = bf_lo(xv[j]), b = bf_hi(xv[j]); s1 += a + b; s2 += a * a + b * b; }
        s1 += __shfl_xor(s1, 8); s2 += __shfl_xor(s2, 8);
        const int h = c & 1, p = c >> 1;
        char* d = ws + W_XF0 + (((tok >> 7) * 16 + p) * 4 + ((tok >> 5) & 3)) * 2560;
        const int l = h * 32 + (tok & 31);
        *(v4u*)(d + l * 16) = (v4u){xv[0], xv[1], xv[2], xv[3]};
        *(v4u*)(d + 1024 + l * 16) = (v4u){xv[4], xv[5], xv[6], xv[7]};
        *(u32*)(d + 2048 + l * 4) = xv[8];
        *(float*)(d + 2304 + l * 4) = h ? s2 : s1;
    }
    PST(5);
    if (tid < 144) {
#pragma unroll
        for (int e = 0; e < 8; ++e) {
            const u32 w01 = wr[e >> 1][(e & 1) * 2], w23 = wr[e >> 1][(e & 1) * 2 + 1];
            const float w0 = bf_lo(w01), w1 = bf_hi(w01), w2 = bf_lo(w23), w3 = bf_hi(w23);
            float kk[7], u[4];
#pragma unroll
            for (int j = 0; j < 7; ++j) kk[j] = (e & 1) ? bf_hi(kr[j][e >> 1]) : bf_lo(kr[j][e >> 1]);
#pragma unroll
            for (int j = 0; j < 4; ++j) {
                const float ks = kk[j] * w3 + kk[j + 1] * w2 + kk[j + 2] * w1 + kk[j + 3] * w0;
                u[j] = rb(ks) * ((e & 1) ? bf_hi(vr[j][e >> 1]) : bf_lo(vr[j][e >> 1]));
            }
            *(v2u*)(sm + (pc * 8 + e) * 144 + (tq >> 1) * 16 + ((tq & 1) ? 0 : 8)) = (v2u){pk(u[3], u[2]), pk(u[1], u[0])};
        }
    }
    __syncthreads();
    for (int it = tid; it < 576; it += 256) {
        const int ch = it >> 3, q = it & 7;
        *(v4u*)(ws + W_UT + (cg * 72 + ch) * 4096 + tg * 128 + q * 16) = *(const v4u*)(sm + ch * 144 + q * 16);
    }
    PST(6); PWAIT(); PST(7);
}

constexpr int SPIN = 1 << 14;
DEV bool bad(u32 m) { return __builtin_amdgcn_ballot_w64(m == ~0u) != 0; }
DEV u32 mx3(u32 a, u32 b, u32 c) { return max(max(a, b), c); }

template <int ST, int lo, int hi>
DEV void gather_issue(__amdgpu_buffer_rsrc_t rs, u32 l16, u32 l4, v4u (&xf)[36], u32 (&st)[16]) {
    u32 so = (lo < 32 ? (lo >> 1) : ((lo - 32) & 15)) * 4 * ST;
    OPQS(so);
    sfor<lo, hi>(LAM(I) {
        constexpr int i = I.v;
        if constexpr ((i == 32 || i == 48) && i != lo) { so = 0; OPQS(so); }
        if constexpr (i < 32) xf[i] = __builtin_amdgcn_raw_buffer_load_b128(rs, l16 + (i & 1) * 1024, so, SC1);
        else if constexpr (i < 48) xf[32 + ((i - 32) >> 2)][i & 3] = __builtin_amdgcn_raw_buffer_load_b32(rs, l4 + 2048, so, SC1);
        else st[i - 48] = __builtin_amdgcn_raw_buffer_load_b32(rs, l4 + 2304, so, SC1);
        if constexpr ((i >= 32 || (i & 1)) && i + 1 < hi) { so += 4 * ST; OPQS(so); }
    });
}
template <int ST, int G>
DEV u32 group_max(v4u (&xf)[36], u32 (&st)[16]) {
    u32 m = 0;
    if constexpr (G < 4) {
#pragma unroll
        for (int i = 0; i < 8; i += 2) m = mx3(m, xf[8 * G + i].x, xf[8 * G + i + 1].x);
    } else {
#pragma unroll
        for (int i = 0; i < 4; ++i) m = mx3(mx3(m, xf[32 + i].x, xf[32 + i].y), xf[32 + i].z, xf[32 + i].w);
        if (ST == 2560) {
#pragma unroll
            for (int i = 0; i < 16; i += 2) m = mx3(m, st[i], st[i + 1]);
        }
    }
    return m;
}
template <int ST, int G>
DEV void check8(__amdgpu_buffer_rsrc_t rs, u32 l16, u32 l4, v4u (&xf)[36], u32 (&st)[16]) {
    if (__builtin_expect(bad(group_max<ST, G>(xf, st)), 0)) {
#pragma unroll 1
        for (int sp = 0; sp < SPIN; ++sp) {
            __builtin_amdgcn_s_sleep(2);
            asm volatile("" ::: "memory");
            gather_issue<ST, 8 * G, (G < 4 ? 8 * G + 8 : ST == 2560 ? 64 : 48)>(rs, l16, l4, xf, st);
            if (!bad(group_max<ST, G>(xf, st))) break;
        }
    }
}
DEV void hedgehog(const float (&t)[18], float a, u32 (&ph)[18]) {
    float mx = t[0], mn = t[0];
#pragma unroll
    for (int i = 1; i < 18; ++i) { mx = fmaxf(mx, t[i]); mn = fminf(mn, t[i]); }
    const float c1 = -a * mx, c2 = a * mn;
    float e1[18], e2[18], s1 = 0.f, s2 = 0.f;
#pragma unroll
    for (int i = 0; i < 18; ++i) {
        e1[i] = __builtin_amdgcn_exp2f(__builtin_fmaf(t[i], a, c1)); s1 += e1[i];
        e2[i] = __builtin_amdgcn_exp2f(__builtin_fmaf(t[i], -a, c2)); s2 += e2[i];
    }
    const float r1 = __builtin_amdgcn_rcpf(s1), r2 = __builtin_amdgcn_rcpf(s2);
#pragma unroll
    for (int i = 0; i < 9; ++i) { ph[i] = pk(e1[2 * i] * r1, e1[2 * i + 1] * r1); ph[9 + i] = pk(e2[2 * i] * r2, e2[2 * i + 1] * r2); }
}
DEV v4u tr2(u32 a0, u32 a1) {
    s4 lo = __builtin_amdgcn_ds_read_tr16_b64_v4i16((LAS s4*)a0), hi = __builtin_amdgcn_ds_read_tr16_b64_v4i16((LAS s4*)a1);
    v2u l = __builtin_bit_cast(v2u, lo), h = __builtin_bit_cast(v2u, hi);
    return (v4u){l.x, l.y, h.x, h.y};
}
DEV void y_store(f4 acc, u32 ua, int j, float Dv, __amdgpu_buffer_rsrc_t rs, u32 yo) {
    const v2u uu = lds2(ua + 512 * j);
    const float y0 = rb(acc[0]) + rb(bf_hi(uu.y) * Dv), y1 = rb(acc[1]) + rb(bf_lo(uu.y) * Dv), y2 = rb(acc[2]) + rb(bf_hi(uu.x) * Dv), y3 = rb(acc[3]) + rb(bf_lo(uu.x) * Dv);
    __builtin_amdgcn_raw_buffer_store_b64((v2u){pk(y0, y1), pk(y2, y3)}, rs, yo + 512 * j, 0, SC1);
}
template <int N, int n, int r>
DEV void conv_read(u32 aa, u32 ba, v4u (&A)[N], v4u (&B)[N]) {
    constexpr int i = n / 3, k = n % 3;
    if constexpr (k == 0) B[i] = lds4(ba + 64 * (8 * i - r + 7));
    else { const v2u v = lds2(aa + 2 + 64 * (r + 8 * i) + 8 * (k - 1)); if constexpr (k == 1) { A[i].x = v.x; A[i].y = v.y; } else { A[i].z = v.x; A[i].w = v.y; } }
}
constexpr int conv_ij(int N, int MASK, int n) {
    for (int d = 0; d < N; ++d) for (int i = 0; i < N; ++i) for (int j = i; j < N; ++j)
        if (((MASK >> j) & 1) && (i > j - i ? i : j - i) == d && n-- == 0) return i * 8 + j;
    return -1;
}

__global__ void __launch_bounds__(256, 1) k_main(const u16* __restrict__ sh, const u16* __restrict__ sD, u16* __restrict__ out, char* __restrict__ ws) {
    extern __shared__ __attribute__((aligned(16))) char smem[];
    const int tid = threadIdx.x, lane = tid & 63, wave = __builtin_amdgcn_readfirstlane(tid >> 6);
    const int b = blockIdx.x, rg = 2 * (b & 7) + (b >> 7), p = (b >> 3) & 15;
    u32 lb = (u32)(uintptr_t)(LAS char*)smem;
    OPQS(lb);
    STAMP_INIT();
    v4u xf[36]; u32 st[16];
    u32 l16 = lane * 16, l4 = lane * 4;
    OPQV(l16); OPQV(l4);
    const char* img = ws + W_IMG + p * IMG;
    const int ch0 = (rg * 12 + p) * 3;
    const __amdgpu_buffer_rsrc_t rx0 = mkrs(ws + W_XF0 + (rg * 64 + wave) * 2560, 64 * 2560);
    if (p < 12) {
        v4u hd[3]; v2u he[3];
#pragma unroll
        for (int c = 0; c < 3; ++c) {
            const char* g = (const char*)sh + (ch0 + c) * 4096;
            asm volatile("global_load_dwordx4 %0, %1, %2" : "=v"(hd[c]) : "v"(tid * 16), "s"(g) : "memory");
            asm volatile("global_load_dwordx2 %0, %1, %2" : "=v"(he[c]) : "v"(tid == 255 ? 0 : tid * 16 + 16), "s"(g) : "memory");
        }
#pragma unroll
        for (int c = 0; c < 3; ++c) dma16(ws + W_UT + (ch0 + c) * 4096 + wave * 1024, l16, lb + L_Q + c * CVS + 4 * HCS + 480 + wave * 1024);
#pragma unroll 1
        for (int i = wave; i < 32; i += 4) dma16(img + i * 1024, l16, lb + i * 1024);
        asm volatile("s_waitcnt vmcnt(8)" : "+v"(hd[0]), "+v"(hd[1]), "+v"(hd[2]), "+v"(he[0]), "+v"(he[1]), "+v"(he[2])::"memory");
        STAMP(26);
#pragma unroll
        for (int c = 0; c < 3; ++c) {
            const u32 d0 = hd[c].x, d1 = hd[c].y, d2 = hd[c].z, d3 = hd[c].w, d4 = he[c].x, d5 = he[c].y;
            const u32 a0 = __builtin_amdgcn_alignbit(d1, d0, 16), a1 = __builtin_amdgcn_alignbit(d2, d1, 16), a2 = __builtin_amdgcn_alignbit(d3, d2, 16),
                      a3 = __builtin_amdgcn_alignbit(d4, d3, 16), a4 = __builtin_amdgcn_alignbit(d5, d4, 16);
            u32 o = lb + L_Q + c * CVS + 32 + tid * 16;
            OPQV(o);
            *(LAS v4u*)(o) = (v4u){d0, d1, d2, d3};
            *(LAS v4u*)(o + HCS) = (v4u){a0, a1, a2, a3};
            *(LAS v4u*)(o + 2 * HCS) = (v4u){d1, d2, d3, d4};
            *(LAS v4u*)(o + 3 * HCS) = (v4u){a1, a2, a3, a4};
            if (tid == 0) {
#pragma unroll
                for (int k = 0; k < 4; ++k) *(LAS v4u*)(o - 32 + k * HCS) = (v4u){0, 0, 0, 0};
                *(LAS v4u*)(o - 16) = (v4u){0, 0, 0, 0};
                *(LAS v4u*)(o - 16 + HCS) = (v4u){0, 0, 0, d0 << 16};
                *(LAS v4u*)(o - 16 + 2 * HCS) = (v4u){0, 0, 0, d0};
                *(LAS v4u*)(o - 16 + 3 * HCS) = (v4u){0, 0, d0 << 16, a0};
            }
            if (tid >= 64 && tid < 94) *(LAS v4u*)(o + 4 * HCS - 32 - 1024) = (v4u){0, 0, 0, 0};
        }
#pragma unroll 1
        for (int i = 32 + wave; i < 91; i += 4) dma16(img + i * 1024, l16, lb + i * 1024);
        barrier();
        STAMP(1);
        const int bq = lane & 15, g = lane >> 4, cc = (bq + 1) & 3;
        const u32 aoff = lb + L_Q + cc * HCS + 2 * (bq - 8 * g + 24 - cc), boff = lb + L_Q + 4 * HCS + 480 + 2 * (16 * bq + 8 * g - 16) - 7 * 64;
        const u32 uoff = lb + L_Q + 4 * HCS + 480 + 32 * bq + 16 * (g >> 1) + ((g & 1) ? 0 : 8), yo = 32 * bq + 8 * g;
        if (wave < 3) {
            u32 aa = aoff + wave * CVS, ba = boff + wave * CVS;
            OPQV(aa); OPQV(ba);
            f4 acc[8] = {};
            v4u A[2][8], B[2][8];
            sfor<0, 24>(LAM(N_) { conv_read<8, N_.v, 0>(aa, ba, A[0], B[0]); });
            FENCE();
            sfor<0, 8 * 27>(LAM(Q_) {
                constexpr int r = Q_.v / 27, n = Q_.v % 27, i = conv_ij(8, 0xf1, n) >> 3, j = conv_ij(8, 0xf1, n) & 7;
                acc[j] = mm16(A[r & 1][i], B[r & 1][j - i], acc[j]);
                if constexpr (r < 7 && n < 24) conv_read<8, n, r + 1>(aa, ba, A[(r + 1) & 1], B[(r + 1) & 1]);
                if constexpr (n >= 19) gather_issue<2560, 8 * r + n - 19, 8 * r + n - 18>(rx0, l16, l4, xf, st);
                FENCE();
            });
            const float Dv = bf_lo(sD[ch0 + wave]);
            const __amdgpu_buffer_rsrc_t ry = mkrs(ws + W_Y + (ch0 + wave) * 4096, 4096);
            y_store(acc[0], uoff + wave * CVS, 0, Dv, ry, yo);
#pragma unroll
            for (int j = 4; j < 8; ++j) y_store(acc[j], uoff + wave * CVS, j, Dv, ry, yo);
        } else {
            gather_issue<2560, 0, 32>(rx0, l16, l4, xf, st);
            FENCE();
#pragma unroll 1
            for (int c = 0; c < 3; ++c) {
                u32 aa = aoff + c * CVS, ba = boff + c * CVS;
                OPQV(aa); OPQV(ba);
                f4 acc[4] = {};
                v4u A[2][4], B[2][4];
                sfor<0, 12>(LAM(N_) { conv_read<4, N_.v, 0>(aa, ba, A[0], B[0]); });
                FENCE();
                sfor<0, 8 * 9>(LAM(Q_) {
                    constexpr int r = Q_.v / 9, n = Q_.v % 9, i = conv_ij(4, 0xe, n) >> 3, j = conv_ij(4, 0xe, n) & 7;
                    acc[j] = mm16(A[r & 1][i], B[r & 1][j - i], acc[j]);
                    if constexpr (r < 7) { conv_read<4, n, r + 1>(aa, ba, A[(r + 1) & 1], B[(r + 1) & 1]); if constexpr (n < 3) conv_read<4, 9 + n, r + 1>(aa, ba, A[(r + 1) & 1], B[(r + 1) & 1]); }
                    FENCE();
                });
                const float Dv = bf_lo(sD[ch0 + c]);
                const __amdgpu_buffer_rsrc_t ry = mkrs(ws + W_Y + (ch0 + c) * 4096, 4096);
#pragma unroll
                for (int j = 1; j < 4; ++j) y_store(acc[j], uoff + c * CVS, j, Dv, ry, yo);
            }
            gather_issue<2560, 32, 64>(rx0, l16, l4, xf, st);
        }
    } else {
#pragma unroll 1
        for (int i = wave; i < 91; i += 4) dma16(img + i * 1024, l16, lb + i * 1024);
        gather_issue<2560, 0, 64>(rx0, l16, l4, xf, st);
    }
    STAMP(2);
    {
        float cs = 0.f;
        if (tid < 112) {
            const float* cp = (const float*)(ws + W_CSP) + p * 18 * 112 + tid;
            float cv[18];
#pragma unroll
            for (int i = 0; i < 18; ++i) cv[i] = cp[i * 112];
            FENCE();
#pragma unroll
            for (int i = 0; i < 18; ++i) cs += cv[i];
            *(LAS float*)(lb + L_CS + tid * 4) = cs;
        }
        if (tid >= 128 && tid < 166) *(LAS v2u*)(lb + L_KVA + (tid - 128) * 80 + 72) = (v2u){0, 0};
        if (tid >= 192 && tid < 197) *(LAS v4u*)(lb + L_KVA + 38 * 80 + (tid - 192) * 16) = (v4u){0, 0, 0, 0};
    }
    asm volatile("s_waitcnt vmcnt(0) lgkmcnt(0)\n\ts_barrier" ::: "memory");
#pragma unroll 1
    for (int i = wave; i < 36; i += 4) dma16(img + L_Q + i * 1024, l16, lb + L_Q + i * 1024);
    v4u wf[36];
    {
        const __amdgpu_buffer_rsrc_t rw = mkrs(ws + W_WOT + p * 36864, 36864);
        u32 so = 0;
#pragma unroll
        for (int s = 0; s < 36; ++s) { if ((s & 3) == 0) { OPQS(so); } wf[s] = __builtin_amdgcn_raw_buffer_load_b128(rw, l16 + (s & 3) * 1024, so, 0); if ((s & 3) == 3) so += 4096; }
    }
    u32 xr[9], yv[9];
    {
        const v4u t0 = __builtin_amdgcn_raw_buffer_load_b128(rx0, l16, p * 4 * 2560, 0), t1 = __builtin_amdgcn_raw_buffer_load_b128(rx0, l16 + 1024, p * 4 * 2560, 0);
        xr[8] = __builtin_amdgcn_raw_buffer_load_b32(rx0, l4 + 2048, p * 4 * 2560, 0);
        xr[0] = t0.x; xr[1] = t0.y; xr[2] = t0.z; xr[3] = t0.w; xr[4] = t1.x; xr[5] = t1.y; xr[6] = t1.z; xr[7] = t1.w;
#pragma unroll
        for (int i = 0; i < 9; ++i) yv[i] = 0;
    }
    FENCE();
    u32 kvw;
    {
        const int pi = wave * 44 + lane, hh = pi >= 88, ip = pi - 88 * hh;
        int o0, o1;
        if (ip < 64) { o0 = (ip & 15) * 80 + (ip >> 4) * 8; o1 = o0 + 32; }
        else if (ip < 72) { o0 = 2 * (ip - 64) * 80 + 64; o1 = o0 + 80; }
        else {
            const int s0 = 2 * (ip - 72), s1 = s0 + 1;
            o0 = s0 < 24 ? (16 + (s0 % 6) % 3) * 80 + ((s0 % 6) / 3) * 32 + (s0 / 6) * 8 : s0 < 27 ? (16 + s0 - 24) * 80 + 64 : -1;
            o1 = s1 < 24 ? (16 + (s1 % 6) % 3) * 80 + ((s1 % 6) / 3) * 32 + (s1 / 6) * 8 : s1 < 27 ? (16 + s1 - 24) * 80 + 64 : -1;
        }
        kvw = (u32)(o0 < 0 || lane >= 44 ? 0xffff : o0 + hh * 1520) | ((u32)(o1 < 0 || lane >= 44 ? 0xffff : o1 + hh * 1520) << 16);
        OPQV(kvw);
    }
    STAMP(3);

#pragma unroll 1
    for (int L = 0; L < 2; ++L) {
        const __amdgpu_buffer_rsrc_t rx = mkrs(ws + (L ? W_XF1 : W_XF0) + (rg * 64 + wave) * 2560, 64 * 2560);
        int ln = lane;
        OPQV(ln);
        const int tl = ln & 31, h = ln >> 5, row = ln & 15, kg = ln >> 4, gq = row >> 2, rq = row & 3;
        const bool qa = (gq & 1) == (kg & 1);
        u32 aK_ = lb + L_K + l16, aV_ = lb + L_V + l16;
        u32 aKV = lb + L_QT + (qa ? (((gq >> 1) * 4 + rq) * 2 + (kg >> 1)) * 16 : 512);
        u32 aQq = lb + L_QT + (qa && rq < 2 ? 256 + (((gq >> 1) * 2 + rq) * 2 + (kg >> 1)) * 16 : 512);
        u32 aCS = lb + L_CS + h * 224;
        OPQV(aK_); OPQV(aV_); OPQV(aKV); OPQV(aQq); OPQV(aCS);
        f16v aK = {}; f4 qKV = {}, qQ = {};
        float mean, al, rstd;
        v4u rV[4], cK[5];
        {
            v4u r0[3], r1[3], r2[3];
#pragma unroll
            for (int s = 0; s < 3; ++s) { r0[s] = lds4(aK_ + s * 1024); r1[s] = lds4(aKV + s * QTS); r2[s] = lds4(aQq + s * QTS); }
            sfor<0, 36>(LAM(SI) {
                constexpr int s = SI.v;
                if constexpr ((s & 7) == 0) { FENCE(); check8<2560, (s >> 3)>(rx, l16, l4, xf, st); FENCE(); }
                aK = mm32(r0[s % 3], xf[s], aK);
                if constexpr (s + 3 < 36) r0[s % 3] = lds4(aK_ + (s + 3) * 1024); else rV[s - 33] = lds4(aV_ + (s - 33) * 1024);
                if constexpr (s == 32) {
                    float t = 0.f;
#pragma unroll
                    for (int i = 0; i < 16; ++i) t += __uint_as_float(st[i]);
                    auto r = __builtin_amdgcn_permlane32_swap(__float_as_uint(t), __float_as_uint(t), false, false);
                    mean = __uint_as_float(r[0]) * (1.f / 576.f);
                    const float var = fmaxf(__uint_as_float(r[1]) * (1.f / 576.f) - mean * mean, 0.f);
                    rstd = __builtin_amdgcn_rsqf(var + 1e-5f);
                    al = rstd * 1.4426950408889634f;
                }
                FENCE();
                qKV = mm16(r1[s % 3], xf[s], qKV);
                if constexpr (s + 3 < 36) r1[s % 3] = lds4(aKV + (s + 3) * QTS); else cK[s - 33] = lds4(aCS + (s - 33) * 16);
                FENCE();
                qQ = mm16(r2[s % 3], xf[s], qQ);
                if constexpr (s + 3 < 36) r2[s % 3] = lds4(aQq + (s + 3) * QTS); else if constexpr (s == 33) cK[3] = lds4(aCS + 48); else if constexpr (s == 34) cK[4] = lds4(aCS + 192); else rV[3] = lds4(aV_ + 3 * 1024);
                FENCE();
            });
        }
        asm volatile("" : "+a"(aK), "+a"(qKV), "+a"(qQ));
        STAMP(4 + 12 * L);
        v4u rQ[4];
        {
            f16v aV = {};
            float t[18], e1[18], e2[18], mx, mn, c1, c2, s1 = 0.f, s2 = 0.f;
            v4u cV[4];
            u32 aS = lb + L_STG + (wave * 32 + tl) * STGS + 72 * h;
            OPQV(aS);
#pragma unroll
            for (int s = 0; s < 36; ++s) {
                aV = mm32(rV[s & 3], xf[s], aV);
                if (s + 4 < 36) rV[s & 3] = lds4(aV_ + (s + 4) * 1024); else cV[s - 32] = lds4(aCS + 64 + (s - 32) * 16);
                if (s >= 2 && s < 5) {
#pragma unroll
                    for (int i = 6 * (s - 2); i < 6 * (s - 2) + 6; ++i) t[i] = __builtin_fmaf(-mean, __uint_as_float(cK[i >> 2][i & 3]), i < 16 ? aK[i] : qKV[i - 16]);
                }
                if (s == 5) { mx = fmaxf(fmaxf(t[0], t[1]), t[2]); mn = fminf(fminf(t[0], t[1]), t[2]);
#pragma unroll
                    for (int i = 3; i < 7; i += 2) { mx = fmaxf(fmaxf(mx, t[i]), t[i + 1]); mn = fminf(fminf(mn, t[i]), t[i + 1]); } }
                if (s == 6) {
#pragma unroll
                    for (int i = 7; i < 13; i += 2) { mx = fmaxf(fmaxf(mx, t[i]), t[i + 1]); mn = fminf(fminf(mn, t[i]), t[i + 1]); } }
                if (s == 7) {
#pragma unroll
                    for (int i = 13; i < 17; i += 2) { mx = fmaxf(fmaxf(mx, t[i]), t[i + 1]); mn = fminf(fminf(mn, t[i]), t[i + 1]); }
                    mx = fmaxf(mx, t[17]); mn = fminf(mn, t[17]); c1 = -al * mx; c2 = al * mn; }
                if (s >= 8 && s < 32) {
#pragma unroll
                    for (int q = (3 * (s - 8)) / 2; q < (3 * (s - 7)) / 2; ++q) {
                        const int i = q >> 1;
                        if (q & 1) { e2[i] = __builtin_amdgcn_exp2f(__builtin_fmaf(t[i], -al, c2)); s2 += e2[i]; }
                        else { e1[i] = __builtin_amdgcn_exp2f(__builtin_fmaf(t[i], al, c1)); s1 += e1[i]; }
                    }
                }
                if (s == 32) { s1 = __builtin_amdgcn_rcpf(s1); s2 = __builtin_amdgcn_rcpf(s2); }
                if (s >= 33) {
                    const int i = s - 33;
                    *(LAS v2u*)(aS + 8 * i) = (v2u){pk(e1[4 * i] * s1, e1[4 * i + 1] * s1), pk(e1[4 * i + 2] * s1, e1[4 * i + 3] * s1)};
                }
                FENCE();
            }
            *(LAS v2u*)(aS + 24) = (v2u){pk(e1[12] * s1, e1[13] * s1), pk(e1[14] * s1, e1[15] * s1)};
            *(LAS v2u*)(aS + 32) = (v2u){pk(e1[16] * s1, e1[17] * s1), pk(e2[0] * s2, e2[1] * s2)};
#pragma unroll
            for (int i = 0; i < 4; ++i) *(LAS v2u*)(aS + 40 + 8 * i) = (v2u){pk(e2[4 * i + 2] * s2, e2[4 * i + 3] * s2), pk(e2[4 * i + 4] * s2, e2[4 * i + 5] * s2)};
            const u32 aSv = aS + 144 - 32 * h;
#pragma unroll
            for (int i = 0; i < 4; ++i)
                *(LAS v2u*)(aSv + 8 * i) = (v2u){pk(__builtin_fmaf(-mean, __uint_as_float(cV[i].x), aV[4 * i]) * rstd, __builtin_fmaf(-mean, __uint_as_float(cV[i].y), aV[4 * i + 1]) * rstd),
                                                 pk(__builtin_fmaf(-mean, __uint_as_float(cV[i].z), aV[4 * i + 2]) * rstd, __builtin_fmaf(-mean, __uint_as_float(cV[i].w), aV[4 * i + 3]) * rstd)};
            *(LAS v2u*)(aSv + 32) = (v2u){pk(__builtin_fmaf(-mean, __uint_as_float(cK[4].z), qKV[2]) * rstd, __builtin_fmaf(-mean, __uint_as_float(cK[4].w), qKV[3]) * rstd), 0x3f80u};
        }
        asm volatile("s_waitcnt vmcnt(0) lgkmcnt(0)\n\ts_barrier" ::: "memory");
        STAMP(5 + 12 * L);
        u32 aQ_ = lb + L_Q + l16;
        OPQV(aQ_);
        {
            const int hh = wave >> 1, dt = wave & 1;
            const u32 r0 = lb + L_STG + (4 * (4 * (kg & 1) + gq) + (kg >> 1)) * STGS + 8 * rq;
            u32 pa = r0 + 72 * hh, pb = r0 + 144 + 40 * hh + 32 * dt;
            OPQV(pa); OPQV(pb);
            f4 c[3] = {};
            v4u B[4], A[4][3];
#pragma unroll
            for (int ks = 0; ks < 4; ++ks) {
                B[ks] = tr2(pb + ks * 32 * STGS, pb + (ks * 32 + 2) * STGS);
#pragma unroll
                for (int ft = 0; ft < 3; ++ft) A[ks][ft] = tr2(pa + ks * 32 * STGS + 32 * ft, pa + (ks * 32 + 2) * STGS + 32 * ft);
            }
#pragma unroll
            for (int s = 0; s < 4; ++s) rQ[s] = lds4(aQ_ + s * 1024);
            FENCE();
#pragma unroll
            for (int ks = 0; ks < 4; ++ks)
#pragma unroll
                for (int ft = 0; ft < 3; ++ft) c[ft] = mm16(A[ks][ft], B[ks], c[ft]);
            u32 w[6];
#pragma unroll
            for (int ft = 0; ft < 3; ++ft) { w[2 * ft] = pk(c[ft][0], c[ft][1]); w[2 * ft + 1] = pk(c[ft][2], c[ft][3]); }
            const __amdgpu_buffer_rsrc_t rk = mkrs(ws + W_KV + ((L * 16 + p) * 16 + rg) * 2816 + hh * 1408, 1408);
            if (dt == 0) {
                __builtin_amdgcn_raw_buffer_store_b128((v4u){w[0], w[1], w[2], w[3]}, rk, l16, 0, SC1);
                if (ln < 16) __builtin_amdgcn_raw_buffer_store_b64((v2u){w[4], w[5]}, rk, ln * 8 + 1024, 0, SC1);
            } else {
                const u32 s0 = __builtin_amdgcn_update_dpp(0u, w[2], 0x113, 0xf, 0xf, true), s1 = __builtin_amdgcn_update_dpp(0u, w[3], 0x113, 0xf, 0xf, true);
                const u32 t0 = __builtin_amdgcn_update_dpp(0u, w[4], 0x116, 0xf, 0xf, true), t1 = __builtin_amdgcn_update_dpp(0u, w[5], 0x116, 0xf, 0xf, true);
                const v2u v = row < 3 ? (v2u){w[0], w[1]} : row < 6 ? (v2u){s0, s1} : row < 9 ? (v2u){t0, t1} : (v2u){0, 0};
                const int slot = row < 6 ? kg * 6 + row : 24 + row - 6;
                if (row < 6 || (kg == 0 && row < 14)) __builtin_amdgcn_raw_buffer_store_b64(v, rk, slot * 8 + 1152, 0, SC1);
            }
        }
        FENCE();
        STAMP(6 + 12 * L);
        u32 pq[20];
        v4u kp[16];
        const __amdgpu_buffer_rsrc_t rkv = mkrs(ws + W_KV + (L * 16 + p) * 16 * 2816, 16 * 2816);
        const u32 kvo = (wave * 44 + (ln < 44 ? ln : ln - 20)) * 16;
        {
            f16v aQ = {};
            v4u cQ[5];
#pragma unroll
            for (int s = 0; s < 36; ++s) {
                aQ = mm32(rQ[s & 3], xf[s], aQ);
                if (s + 4 < 36) rQ[s & 3] = lds4(aQ_ + (s + 4) * 1024); else cQ[s - 32] = lds4(aCS + 128 + (s - 32) * 16);
                if (s == 31) cQ[4] = lds4(aCS + 208);
                FENCE();
            }
            {
                u32 so = 0;
#pragma unroll
                for (int i = 0; i < 16; ++i) { OPQS(so); kp[i] = __builtin_amdgcn_raw_buffer_load_b128(rkv, kvo, so, SC1); so += 2816; }
            }
            FENCE();
            float t[18]; u32 ph[18];
#pragma unroll
            for (int i = 0; i < 18; ++i) t[i] = __builtin_fmaf(-mean, __uint_as_float(cQ[i >> 2][i & 3]), i < 16 ? aQ[i] : qQ[i - 16]);
            hedgehog(t, al, ph);
#pragma unroll
            for (int i = 0; i < 18; ++i) { pq[i] = ph[i]; OPQV(pq[i]); }
            pq[18] = 0; pq[19] = 0;
        }
        FENCE();
        STAMP(7 + 12 * L);
        {
            u32 m = 0;
#pragma unroll
            for (int i = 0; i < 16; ++i) m = mx3(m, kp[i].x, kp[i].z);
            if (__builtin_expect(bad(m), 0)) {
#pragma unroll 1
                for (int sp = 0; sp < SPIN; ++sp) {
                    __builtin_amdgcn_s_sleep(2);
                    asm volatile("" ::: "memory");
                    u32 so = 0;
#pragma unroll
                    for (int i = 0; i < 16; ++i) { OPQS(so); kp[i] = __builtin_amdgcn_raw_buffer_load_b128(rkv, kvo, so, SC1); so += 2816; }
                    m = 0;
#pragma unroll
                    for (int i = 0; i < 16; ++i) m = mx3(m, kp[i].x, kp[i].z);
                    if (!bad(m)) break;
                }
            }
            float a[8] = {};
            u32 one_lo = 0x00003f80u, one_hi = 0x3f800000u;
            OPQS(one_lo); OPQS(one_hi);
#pragma unroll
            for (int i = 0; i < 16; ++i)
#pragma unroll
                for (int j = 0; j < 4; ++j) {
                    const u32 kw = kp[i][j];
                    a[2 * j] = __builtin_amdgcn_fdot2_f32_bf16(__builtin_bit_cast(b2, kw), __builtin_bit_cast(b2, one_lo), a[2 * j], false);
                    a[2 * j + 1] = __builtin_amdgcn_fdot2_f32_bf16(__builtin_bit_cast(b2, kw), __builtin_bit_cast(b2, one_hi), a[2 * j + 1], false);
                }
            const u32 o0 = kvw & 0xffffu, o1 = kvw >> 16;
            if (o0 != 0xffffu) *(LAS v2u*)(lb + L_KVA + o0) = (v2u){pk(a[0], a[1]), pk(a[2], a[3])};
            if (o1 != 0xffffu) *(LAS v2u*)(lb + L_KVA + o1) = (v2u){pk(a[4], a[5]), pk(a[6], a[7])};
        }
        barrier();
        STAMP(8 + 12 * L);
        {
            const int hr = (tl >> 2) & 1, d = (tl & 3) + 4 * (tl >> 3);
            const u32 aF = lb + L_KVA + (h == hr ? (hr * 19 + d) * 80 : 38 * 80);
            const u32 aQ4 = lb + L_KVA + (kg == gq && rq < 3 ? ((gq >> 1) * 19 + 16 + rq) * 80 : 38 * 80);
            v4u A[5], Aq[5];
#pragma unroll
            for (int c = 0; c < 5; ++c) { Aq[c] = lds4(aQ4 + 16 * c); A[c] = lds4(aF + 16 * c); }
            FENCE();
            f16v o = {}; f4 oq = {};
#pragma unroll
            for (int c = 0; c < 5; ++c) oq = mm16(Aq[c], (v4u){pq[4 * c], pq[4 * c + 1], pq[4 * c + 2], pq[4 * c + 3]}, oq);
#pragma unroll
            for (int c = 0; c < 5; ++c) o = mm32(A[c], (v4u){pq[4 * c], pq[4 * c + 1], pq[4 * c + 2], pq[4 * c + 3]}, o);
            const float rd = __builtin_amdgcn_rcpf(oq[2] + 1e-6f);
            const __amdgpu_buffer_rsrc_t ro = mkrs(ws + W_O + ((L * 16 + rg) * 64 + p * 4 + wave) * 2304, 2304);
            __builtin_amdgcn_raw_buffer_store_b32(pk(oq[0] * rd, oq[1] * rd), ro, l4 + 2048, 0, SC1);
            __builtin_amdgcn_raw_buffer_store_b128((v4u){pk(o[0] * rd, o[1] * rd), pk(o[2] * rd, o[3] * rd), pk(o[4] * rd, o[5] * rd), pk(o[6] * rd, o[7] * rd)}, ro, l16, 0, SC1);
            __builtin_amdgcn_raw_buffer_store_b128((v4u){pk(o[8] * rd, o[9] * rd), pk(o[10] * rd, o[11] * rd), pk(o[12] * rd, o[13] * rd), pk(o[14] * rd, o[15] * rd)}, ro, l16 + 1024, 0, SC1);
        }
        FENCE();
        STAMP(9 + 12 * L);
        if (L == 0) {
            const __amdgpu_buffer_rsrc_t ry = mkrs(ws + W_Y + (36 * p) * 4096 + (rg * 128 + wave * 32) * 2, 36 * 4096);
#pragma unroll 1
            for (int sp = 0; sp < SPIN; ++sp) {
                asm volatile("" ::: "memory");
                u32 e[18]; bool nb = false; u32 so = 0;
#pragma unroll
                for (int m = 0; m < 18; ++m) { OPQS(so); e[m] = __builtin_amdgcn_raw_buffer_load_b16(ry, h * 18 * 4096 + tl * 2, so, SC1); so += 4096; }
                FENCE();
#pragma unroll
                for (int m = 0; m < 18; ++m) nb |= e[m] == 0xffffu;
#pragma unroll
                for (int i = 0; i < 9; ++i) yv[i] = e[2 * i] | (e[2 * i + 1] << 16);
                if (__builtin_amdgcn_ballot_w64(nb) == 0) break;
                __builtin_amdgcn_s_sleep(2);
            }
        } else {
            __builtin_amdgcn_s_sleep(10);
        }
        const __amdgpu_buffer_rsrc_t rog = mkrs(ws + W_O + ((L * 16 + rg) * 64 + wave) * 2304, 64 * 2304);
        gather_issue<2304, 0, 48>(rog, l16, l4, xf, st);
        FENCE();
        float x1[18];
        {
            f16v aW = {}; f4 qW = {};
            u32 aWq = aQq + (qa && rq < 2 ? 128 : 0);
            OPQV(aWq);
            v4u r1[4];
#pragma unroll
            for (int s = 0; s < 4; ++s) r1[s] = lds4(aWq + s * QTS);
            sfor<0, 36>(LAM(SI) {
                constexpr int s = SI.v;
                if constexpr ((s & 7) == 0) { FENCE(); check8<2304, (s >> 3)>(rog, l16, l4, xf, st); FENCE(); }
                aW = mm32(wf[s], xf[s], aW);
                qW = mm16(r1[s & 3], xf[s], qW);
                if constexpr (s + 4 < 36) r1[s & 3] = lds4(aWq + (s + 4) * QTS);
                FENCE();
            });
            STAMP(10 + 12 * L);
            float s1 = 0.f, s2 = 0.f;
#pragma unroll
            for (int i = 0; i < 9; ++i) {
                const float a0 = i < 8 ? aW[2 * i] : qW[0], a1 = i < 8 ? aW[2 * i + 1] : qW[1];
                const u32 at = pk(a0, a1), w = pk(bf_lo(xr[i]) + bf_lo(at), bf_hi(xr[i]) + bf_hi(at));
                x1[2 * i] = bf_lo(w); x1[2 * i + 1] = bf_hi(w);
                s1 += x1[2 * i] + x1[2 * i + 1]; s2 += x1[2 * i] * x1[2 * i] + x1[2 * i + 1] * x1[2 * i + 1];
            }
            const __amdgpu_buffer_rsrc_t rs = mkrs(ws + W_ST + ((L * 16 + rg) * 64 + p * 4 + wave) * 256, 256);
            __builtin_amdgcn_raw_buffer_store_b32(__float_as_uint(halves_sum(s1, s2)), rs, l4, 0, SC1);
        }
        FENCE();
        STAMP(11 + 12 * L);
        __builtin_amdgcn_s_sleep(10);
        {
            const __amdgpu_buffer_rsrc_t rs = mkrs(ws + W_ST + ((L * 16 + rg) * 64 + wave) * 256, 64 * 256);
            float t;
#pragma unroll 1
            for (int sp = 0; sp < SPIN; ++sp) {
                asm volatile("" ::: "memory");
                u32 e[16], m = 0, so = 0;
#pragma unroll
                for (int i = 0; i < 16; ++i) { if ((i & 3) == 0) { OPQS(so); } e[i] = __builtin_amdgcn_raw_buffer_load_b32(rs, l4 + (i & 3) * 1024, so, SC1); if ((i & 3) == 3) so += 4096; }
                FENCE();
                t = 0.f;
#pragma unroll
                for (int i = 0; i < 16; i += 2) { m = mx3(m, e[i], e[i + 1]); t += __uint_as_float(e[i]) + __uint_as_float(e[i + 1]); }
                if (!bad(m)) break;
                __builtin_amdgcn_s_sleep(2);
            }
            STAMP(12 + 12 * L);
            auto r = __builtin_amdgcn_permlane32_swap(__float_as_uint(t), __float_as_uint(t), false, false);
            const float m1 = __uint_as_float(r[0]) * (1.f / 576.f);
            const float r1 = __builtin_amdgcn_rsqf(fmaxf(__uint_as_float(r[1]) * (1.f / 576.f) - m1 * m1, 0.f) + 1e-5f);
            float s1 = 0.f, s2 = 0.f;
#pragma unroll
            for (int i = 0; i < 9; ++i) {
                const u32 xn = pk((x1[2 * i] - m1) * r1, (x1[2 * i + 1] - m1) * r1);
                const u32 gt = pk(bf_lo(xn) * bf_lo(yv[i]), bf_hi(xn) * bf_hi(yv[i]));
                xr[i] = pk(x1[2 * i] + bf_lo(gt), x1[2 * i + 1] + bf_hi(gt));
                const float a = bf_lo(xr[i]), c = bf_hi(xr[i]);
                s1 += a + c; s2 += a * a + c * c;
            }
            if (L == 0) {
                const __amdgpu_buffer_rsrc_t rp = mkrs(ws + W_XF1 + (rg * 64 + p * 4 + wave) * 2560, 2560);
                __builtin_amdgcn_raw_buffer_store_b128((v4u){xr[0], xr[1], xr[2], xr[3]}, rp, l16, 0, SC1);
                __builtin_amdgcn_raw_buffer_store_b128((v4u){xr[4], xr[5], xr[6], xr[7]}, rp, l16 + 1024, 0, SC1);
                __builtin_amdgcn_raw_buffer_store_b32(xr[8], rp, l4 + 2048, 0, SC1);
                __builtin_amdgcn_raw_buffer_store_b32(__float_as_uint(halves_sum(s1, s2)), rp, l4 + 2304, 0, SC1);
                STAMP(13);
                __builtin_amdgcn_s_sleep(10);
                const __amdgpu_buffer_rsrc_t rn = mkrs(ws + W_XF1 + (rg * 64 + wave) * 2560, 64 * 2560);
                gather_issue<2560, 0, 64>(rn, l16, l4, xf, st);
                FENCE();
            } else {
                u32* op = (u32*)(out + (rg * 128 + wave * 32 + tl) * 576 + 36 * p + 18 * h);
#pragma unroll
                for (int i = 0; i < 9; ++i) op[i] = xr[i];
                STAMP(25);
            }
        }
    }
    STAMP_END();
}

__global__ void __launch_bounds__(1024) k_rep(const char* ws, int npre) {
    __shared__ float v[1024]; __shared__ unsigned t0s, p0s; __shared__ float res[48][3]; __shared__ int cnt;
    const int i = threadIdx.x;
    const u32* s = (const u32*)(ws + W_DBG) + i * 64;
    const u32* q = (const u32*)(ws + W_DBG + 1024 * 256);
    if (i == 0) { t0s = 0x7fffffff; p0s = 0x7fffffff; }
    __syncthreads();
    const u32 ref = ((const u32*)(ws + W_DBG))[32];
    atomicMin((int*)&t0s, (int)(s[32] - ref));
    for (int j = i; j < npre * 4; j += 1024) atomicMin((int*)&p0s, (int)(q[j * 8] - ref));
    __syncthreads();
    const float fs = (float)(s[30] - s[0]) / ((s[33] - s[32]) / 100.f);
    for (int k = 0; k < 31 + 8; ++k) {
        float d;
        if (k < 31) d = (k && !s[k]) ? -1.f : ((int)(s[32] - ref) - (int)t0s) / 100.f + (float)(s[k] - s[0]) / fs;
        else { d = -1.f; for (int j = i; j < npre * 4; j += 1024) { const int e = (int)(q[j * 8 + k - 31] - ref) - (int)p0s; if (e >= 0 && e < 20000) d = fmaxf(d, e / 100.f); } }
        if (!(d >= 0.f && d < 1000.f)) d = -1.f;
        v[i] = d; if (i == 0) { cnt = 0; res[k][0] = res[k][1] = res[k][2] = -1.f; }
        __syncthreads();
        if (d >= 0.f) atomicAdd(&cnt, 1);
        __syncthreads();
        if (d >= 0.f) { int r = 0; for (int j = 0; j < 1024; ++j) r += v[j] >= 0.f && (v[j] < d || (v[j] == d && j < i)); if (r == 0) res[k][0] = d; if (r == cnt / 2) res[k][1] = d; if (r == cnt - 1) res[k][2] = d; }
        __syncthreads();
    }
    if (i == 0) {
        printf("REP gap %.2f clk %.0f |", npre ? ((int)t0s - (int)p0s) / 100.f : 0.f, fs);
        for (int k = 0; k < 39; ++k) if (res[k][2] >= 0.f) printf(" %d:%.2f/%.2f", k, res[k][1], res[k][2]);
        printf("\n");
    }
}
extern "C" void kernel_launch(void* const* d_in, const int* in_sizes, int n_in, void* d_out, int out_size, void* d_ws, size_t ws_size, hipStream_t stream) {
    (void)in_sizes; (void)n_in; (void)out_size; (void)ws_size;
    hipFuncSetAttribute((const void*)k_main, hipFuncAttributeMaxDynamicSharedMemorySize, L_END);
    k_pre<<<544, 256, 0, stream>>>((const u16*)d_in[0], (const u16*)d_in[1], (const u16*)d_in[2], (const u16*)d_in[3], (const u16*)d_in[5], (const u16*)d_in[6],
                                   (const u16*)d_in[7], (const u16*)d_in[8], (char*)d_ws);
    k_main<<<256, 256, L_END, stream>>>((const u16*)d_in[8], (const u16*)d_in[9], (u16*)d_out, (char*)d_ws);
    k_rep<<<1, 1024, 0, stream>>>((const char*)d_ws, 544);
}
```

```cpp
#include <hip/hip_runtime.h>
#include <stdint.h>
#pragma clang diagnostic ignored "-Wint-to-pointer-cast"
#pragma clang diagnostic ignored "-Wunused-value"
#pragma clang diagnostic ignored "-Winline-asm"
#if defined(__HIP_DEVICE_COMPILE__)
#pragma clang attribute push(__attribute__((target("no-packed-fp32-ops"))), apply_to = function)
#endif
typedef unsigned short u16;
typedef unsigned u32;
typedef unsigned long long u64;
typedef u32 v4u __attribute__((ext_vector_type(4)));
typedef u32 v2u __attribute__((ext_vector_type(2)));
typedef float f4 __attribute__((ext_vector_type(4)));
typedef float f2 __attribute__((ext_vector_type(2)));
typedef float f16v __attribute__((ext_vector_type(16)));
typedef __bf16 b8 __attribute__((ext_vector_type(8)));
typedef __bf16 b2 __attribute__((ext_vector_type(2)));
typedef short s4 __attribute__((ext_vector_type(4)));
#define LAS __attribute__((address_space(3)))
#define DEV __device__ __forceinline__
#define SC1 16

constexpr u32 QTS = 416, STGS = 232, CVS = 21216, HCS = 4160;
constexpr u32 L_K = 0, L_V = 36864, L_QT = 73728, L_Q = 89088, L_STG = 125952, L_KVA = 155648, L_CS = 158784, L_EX = 159232, L_END = 163328;
constexpr u32 P_KS = 0, P_VS = 10240, P_WS = 20480, P_XS = 38912, P_PH = 49152;
constexpr u32 IMG = L_STG, XS = 2560, OS = 2304;
constexpr u32 W_IMG = 0, W_WOT = W_IMG + 16 * IMG, W_WL = W_WOT + 16 * 36864, W_CSP = W_WL + 4 * 18432, W_UT = W_CSP + 16 * 8960,
              W_XF0 = W_UT + 576 * 4096, W_XF1 = W_XF0 + 1024 * XS, W_O = W_XF1 + 1024 * XS, W_Y = W_O + 2 * 1024 * OS, W_KV = W_Y + 576 * 4096,
              W_ST = W_KV + 2 * 256 * 2816, W_F = W_ST + 2 * 1024 * 256, W_F2 = W_F + 256 * 128, W_E = W_F2 + 256 * 128, W_END = W_E + 128, W_DBG = W_END;
constexpr u32 OOB = 0x7ffffff0u;
constexpr int SPIN = 1 << 14;

DEV float asf(u32 v) { return __builtin_bit_cast(float, v); }
DEV u32 asu(float v) { return __builtin_bit_cast(u32, v); }
DEV float bf_lo(u32 v) { return asf(v << 16); }
DEV float bf_hi(u32 v) { return asf(v & 0xffff0000u); }
DEV u32 pk(float lo, float hi) { b2 r = __builtin_convertvector((f2){lo, hi}, b2); return __builtin_bit_cast(u32, r); }
DEV float rb(float f) { return bf_lo(pk(f, f)); }
DEV float dot2(u32 a, u32 b, float c) { return __builtin_amdgcn_fdot2_f32_bf16(__builtin_bit_cast(b2, a), __builtin_bit_cast(b2, b), c, false); }
DEV u32 umin(u32 a, u32 b) { return a < b ? a : b; }
DEV u32 umax(u32 a, u32 b) { return a > b ? a : b; }
DEV u32 mx3(u32 a, u32 b, u32 c) { return umax(umax(a, b), c); }
template <bool Z> DEV void mm32(f16v& c, v4u a, v4u b) {
    if constexpr (Z) asm volatile("v_mfma_f32_32x32x16_bf16 %0, %1, %2, 0" : "=&v"(c) : "v"(a), "v"(b));
    else asm volatile("v_mfma_f32_32x32x16_bf16 %0, %1, %2, %0" : "+v"(c) : "v"(a), "v"(b));
}
template <bool Z> DEV void mm16(f4& c, v4u a, v4u b) {
    if constexpr (Z) asm volatile("v_mfma_f32_16x16x32_bf16 %0, %1, %2, 0" : "=&v"(c) : "v"(a), "v"(b));
    else asm volatile("v_mfma_f32_16x16x32_bf16 %0, %1, %2, %0" : "+v"(c) : "v"(a), "v"(b));
}
template <bool Z, int R> DEV void mm32b(f16v& c, v4u a) {
    if constexpr (Z) asm volatile("v_mfma_f32_32x32x16_bf16 %0, %1, a[%c2:%c3], 0" : "=&v"(c) : "v"(a), "n"(R), "n"(R + 3));
    else asm volatile("v_mfma_f32_32x32x16_bf16 %0, %1, a[%c2:%c3], %0" : "+v"(c) : "v"(a), "n"(R), "n"(R + 3));
}
template <bool Z, int R> DEV void mm16b(f4& c, v4u a) {
    if constexpr (Z) asm volatile("v_mfma_f32_16x16x32_bf16 %0, %1, a[%c2:%c3], 0" : "=&v"(c) : "v"(a), "n"(R), "n"(R + 3));
    else asm volatile("v_mfma_f32_16x16x32_bf16 %0, %1, a[%c2:%c3], %0" : "+v"(c) : "v"(a), "n"(R), "n"(R + 3));
}
template <bool Z, int R> DEV void mm32a(f16v& c, v4u b) {
    if constexpr (Z) asm volatile("v_mfma_f32_32x32x16_bf16 %0, a[%c2:%c3], %1, 0" : "=&v"(c) : "v"(b), "n"(R), "n"(R + 3));
    else asm volatile("v_mfma_f32_32x32x16_bf16 %0, a[%c2:%c3], %1, %0" : "+v"(c) : "v"(b), "n"(R), "n"(R + 3));
}
template <int N, class T> DEV void pad(T& c) { asm volatile("s_nop %1" : "+v"(c) : "n"(N)); }
template <class T> DEV void pin(T& c) { asm volatile("" : "+v"(c)); }
DEV void nop2() { asm volatile("s_nop 1"); }
template <int R> DEV u32 rda() { u32 v; asm volatile("v_accvgpr_read_b32 %0, a%c1" : "=v"(v) : "n"(R)); return v; }
template <int R> DEV void wra(u32 v) { asm volatile("v_accvgpr_write_b32 a%c1, %0" ::"v"(v), "n"(R)); }
DEV __amdgpu_buffer_rsrc_t mkrs(const void* p, u32 n) { return __builtin_amdgcn_make_buffer_rsrc((void*)p, 0, (int)n, 0x00020000); }
DEV v4u mkv(const void* p, u32 n) { return (v4u){(u32)(uintptr_t)p, (u32)((uintptr_t)p >> 32), n, 0x00020000u}; }
DEV v4u lds4(u32 a) { return *(const LAS v4u*)a; }
DEV v2u lds2(u32 a) { return *(const LAS v2u*)a; }
DEV void barrier() { asm volatile("s_waitcnt lgkmcnt(0)\n\ts_barrier" ::: "memory"); }
template <int N> DEV void waitv() { asm volatile("s_waitcnt vmcnt(%0)" ::"n"(N) : "memory"); }
DEV void dma_in(const void* g, u32 voff, u32 lds) {
    asm volatile("s_mov_b32 m0, %2\n\ts_nop 0\n\tglobal_load_lds_dwordx4 %0, %1" ::"v"(voff), "s"(g), "s"(lds) : "m0", "memory");
}
DEV void dma_ws(v4u rs, u32 voff, u32 so, u32 lds) {
    asm volatile("s_mov_b32 m0, %3\n\ts_nop 0\n\tbuffer_load_dwordx4 %0, %1, %2 offen sc1 lds" ::"v"(voff), "s"(rs), "s"(so), "s"(lds) : "m0", "memory");
}
template <int IM> DEV void ld4(v4u& d, u32 voff, v4u rs, u32 so) { asm volatile("buffer_load_dwordx4 %0, %1, %2, %3 offen offset:%4 sc1" : "+v"(d) : "v"(voff), "s"(rs), "s"(so), "n"(IM) : "memory"); }
template <int IM> DEV void ld1(u32& d, u32 voff, v4u rs, u32 so) { asm volatile("buffer_load_dword %0, %1, %2, %3 offen offset:%4 sc1" : "+v"(d) : "v"(voff), "s"(rs), "s"(so), "n"(IM) : "memory"); }
template <int IM> DEV void ld4m(v4u& d, u32 voff, v4u rs, u32 so, u64 m) {
    asm volatile("s_mov_b64 exec, %5\n\tbuffer_load_dwordx4 %0, %1, %2, %3 offen offset:%4 sc1\n\ts_mov_b64 exec, -1" : "+v"(d) : "v"(voff), "s"(rs), "s"(so), "n"(IM), "s"(m) : "memory");
}
template <int IM, int R> DEV void ld4a(u32 voff, v4u rs, u32 so) { asm volatile("buffer_load_dwordx4 a[%c4:%c5], %0, %1, %2 offen offset:%3 sc1" ::"v"(voff), "s"(rs), "s"(so), "n"(IM), "n"(R), "n"(R + 3) : "memory"); }
template <int IM, int R> DEV void ld1a(u32 voff, v4u rs, u32 so) { asm volatile("buffer_load_dword a%c4, %0, %1, %2 offen offset:%3 sc1" ::"v"(voff), "s"(rs), "s"(so), "n"(IM), "n"(R) : "memory"); }
template <int IM, int R> DEV void ld4am(u32 voff, v4u rs, u32 so, u64 m) {
    asm volatile("s_mov_b64 exec, %6\n\tbuffer_load_dwordx4 a[%c4:%c5], %0, %1, %2 offen offset:%3 sc1\n\ts_mov_b64 exec, -1" ::"v"(voff), "s"(rs), "s"(so), "n"(IM), "n"(R), "n"(R + 3), "s"(m) : "memory");
}
template <int I> struct IC { static constexpr int v = I; };
template <int B, int E, class F> DEV void sfor(F&& f) { if constexpr (B < E) { f(IC<B>{}); sfor<B + 1, E>(f); } }
#define LAM(I) [&](auto I) __attribute__((always_inline))
#define FENCE() __builtin_amdgcn_sched_barrier(0)
#define OPQV(x) asm volatile("" : "+v"(x))
#define OPQS(x) asm volatile("" : "+s"(x))
DEV float halves_sum(float a, float b) {
    auto r = __builtin_amdgcn_permlane32_swap(asu(a), asu(b), false, false);
    return asf(r[0]) + asf(r[1]);
}
DEV bool bad(u32 m) { return __builtin_amdgcn_ballot_w64(m == ~0u) != 0; }
#define STAMP_INIT()
#define STAMP(i) asm volatile("; MARK " #i)
#define STAMP_END()

DEV void hedgehog(const float (&t)[18], float a, u32 (&ph)[18]) {
    float mx = t[0], mn = t[0];
#pragma unroll
    for (int i = 1; i < 18; ++i) { mx = __builtin_fmaxf(mx, t[i]); mn = __builtin_fminf(mn, t[i]); }
    const float c1 = -a * mx, c2 = a * mn;
    float e1[18], e2[18], s1 = 0.f, s2 = 0.f;
#pragma unroll
    for (int i = 0; i < 18; ++i) {
        e1[i] = __builtin_amdgcn_exp2f(__builtin_fmaf(t[i], a, c1)); s1 += e1[i];
        e2[i] = __builtin_amdgcn_exp2f(__builtin_fmaf(t[i], -a, c2)); s2 += e2[i];
    }
    const float r1 = __builtin_amdgcn_rcpf(s1), r2 = __builtin_amdgcn_rcpf(s2);
#pragma unroll
    for (int i = 0; i < 9; ++i) { ph[i] = pk(e1[2 * i] * r1, e1[2 * i + 1] * r1); ph[9 + i] = pk(e2[2 * i] * r2, e2[2 * i + 1] * r2); }
}
DEV v4u tr2(u32 a0, u32 a1) {
    s4 lo = __builtin_amdgcn_ds_read_tr16_b64_v4i16((LAS s4*)a0), hi = __builtin_amdgcn_ds_read_tr16_b64_v4i16((LAS s4*)a1);
    v2u l = __builtin_bit_cast(v2u, lo), h = __builtin_bit_cast(v2u, hi);
    return (v4u){l.x, l.y, h.x, h.y};
}
DEV v2u y_val(f4 acc, u32 ua, float Dv) {
    const v2u uu = lds2(ua);
    const float y0 = rb(acc[0]) + rb(bf_hi(uu.y) * Dv), y1 = rb(acc[1]) + rb(bf_lo(uu.y) * Dv), y2 = rb(acc[2]) + rb(bf_hi(uu.x) * Dv), y3 = rb(acc[3]) + rb(bf_lo(uu.x) * Dv);
    return (v2u){pk(y0, y1), pk(y2, y3)};
}
DEV v4u y_pair(v2u a, v2u b) {
    auto r0 = __builtin_amdgcn_permlane16_swap(a.x, b.x, false, false), r1 = __builtin_amdgcn_permlane16_swap(a.y, b.y, false, false);
    return (v4u){r0[0], r1[0], r0[1], r1[1]};
}
template <int N, int n, int r>
DEV void conv_read(u32 aa, u32 ba, v4u (&A)[N], v4u (&B)[N]) {
    constexpr int i = n / 3, k = n % 3;
    if constexpr (k == 0) B[i] = lds4(ba + 64 * (8 * i - r + 7));
    else { const v2u v = lds2(aa + 2 + 64 * (r + 8 * i) + 8 * (k - 1)); if constexpr (k == 1) { A[i].x = v.x; A[i].y = v.y; } else { A[i].z = v.x; A[i].w = v.y; } }
}
constexpr int conv_ij(int N, int MASK, int n) {
    for (int d = 0; d < N; ++d) for (int i = 0; i < N; ++i) for (int j = i; j < N; ++j)
        if (((MASK >> j) & 1) && (i > j - i ? i : j - i) == d && n-- == 0) return i * 8 + j;
    return -1;
}
constexpr bool conv_first(int N, int MASK, int n) {
    for (int m = 0; m < n; ++m) if ((conv_ij(N, MASK, m) & 7) == (conv_ij(N, MASK, n) & 7)) return false;
    return true;
}
constexpr int NSITE = 73, SITE_END = 196;
constexpr int site_at(int q) { return q <= SITE_END ? (q * NSITE + SITE_END - 1) / SITE_END : NSITE; }

__global__ void __launch_bounds__(256, 1) k_main(const u16* __restrict__ x, const u16* __restrict__ wqkv, const u16* __restrict__ wphi, const u16* __restrict__ wo,
                                                 const u16* __restrict__ bk, const u16* __restrict__ bv, const u16* __restrict__ sk, const u16* __restrict__ sh,
                                                 const u16* __restrict__ sD, u16* __restrict__ out, char* __restrict__ ws) {
    extern __shared__ __attribute__((aligned(16))) char smem[];
    const int tid = __builtin_amdgcn_workitem_id_x(), lane = tid & 63, wave = __builtin_amdgcn_readfirstlane(tid >> 6);
    const int b = __builtin_amdgcn_workgroup_id_x(), rg = 2 * (b & 7) + (b >> 7), p = (b >> 3) & 15;
    const int Pg = p >> 2, tw = p & 3, pw = 4 * Pg + wave, ci = (rg + p) & 15;
    const bool conv = ci < 12;
    u32 lb = (u32)(uintptr_t)(LAS char*)smem;
    OPQS(lb);
    STAMP_INIT();
    asm volatile("" ::: "a255");
    u32 l16 = lane * 16, l4 = lane * 4;
    OPQV(l16); OPQV(l4);
    const __amdgpu_buffer_rsrc_t rw = mkrs(ws, W_END);
    const v4u vw = mkv(ws, W_END);
    const int ch0 = 36 * p + 3 * ci;
    u32 xr[9], kvw0, kvw1;
    u64 ep = *(const __attribute__((address_space(4))) u64*)(uintptr_t)(ws + W_E);
    {
        const int sec = p & 3, cg = 2 * Pg + (p & 1), tg = 2 * rg + ((p >> 1) & 1);
        const bool fold = (sec & 1) == 0, extra = ci == 12 || ci == 13;
        const int c = lane & 15, g = lane >> 4, rho = lane & 31, kh = lane >> 5;
        v2u sw0, sw1;
        asm volatile("global_load_dwordx2 %0, %1, %2" : "=v"(sw0) : "v"((72 * cg + 16 * wave + c) * 8), "s"(sk) : "memory");
        asm volatile("global_load_dwordx2 %0, %1, %2" : "=v"(sw1) : "v"((72 * cg + 64 + (c & 7)) * 8), "s"(sk) : "memory");
        {
            const v4u rk = mkv(bk, 2048 * 1152), rv = mkv(bv, 2048 * 1152);
#pragma unroll
            for (int i = 0; i < 5; ++i) {
                const int bi = i * 4 + wave, isv = i < 2 ? 0 : i > 2 ? 1 : wave >> 1, bj = bi - 10 * isv;
                const u32 row = (lane * 7282u + bj * 64 * 7282u) >> 16, o = row * 1008 + (l16 + (bj * 1024 + (64 * tg - 3 + 3 * isv) * 1152 + cg * 144));
                v4u rs = i < 2 ? rk : rv;
                if (i == 2) { rs.x = isv ? rv.x : rk.x; rs.y = isv ? rv.y : rk.y; }
                asm volatile("s_mov_b32 m0, %2\n\ts_nop 0\n\tbuffer_load_dwordx4 %0, %1, 0 offen lds" ::"v"(o), "s"(rs), "s"(lb + P_KS + bi * 1024) : "m0", "memory");
            }
        }
        const char* wsrc = (sec < 3 ? (const char*)wqkv + (sec == 0 ? 1152 : sec == 1 ? 2304 : 0) : (const char*)wo) + Pg * 288;
        const int wst = sec < 3 ? 3456 : 1152;
        auto w_in = [&](bool ex, u32 dst) __attribute__((always_inline)) {
#pragma unroll
            for (int i = 0; i < 3; ++i) {
                const u32 bi = umin(i * 4 + wave, 8), q = (lane * 3641u + bi * 64 * 3641u) >> 16, hD = (q >> 2) & 1;
                const u32 k = ex ? 36 * (8 * (ci - 12) + 4 * (q >> 4) + 2 * ((q >> 3) & 1) + ((q >> 1) & 1)) + 18 * hD + 16 + (q & 1) : 36 * rg + 18 * hD + (q & 3) + 4 * (q >> 3);
                dma_in(wsrc, k * wst + (l16 + bi * 1024 - q * 288), dst + bi * 1024);
            }
        };
        w_in(false, lb + P_WS);
#pragma unroll
        for (int i = 0; i < 3; ++i) {
            const int bi = umin(i * 4 + wave, 9), e = umin(bi * 64 + lane, 607), q = e / 19;
            dma_in(x, (128 * rg + 32 * tw + q) * 1152 + Pg * 288 + umin(e - q * 19, 17) * 16, lb + P_XS + bi * 1024);
        }
        {
            const v4u ones = {~0u, ~0u, ~0u, ~0u};
            const u32 so = W_O + ((rg * 4 + wave) * 16 + p) * OS;
            __builtin_amdgcn_raw_buffer_store_b128(ones, rw, l16, so, SC1);
            __builtin_amdgcn_raw_buffer_store_b128(ones, rw, l16 + 1024, so, SC1);
            __builtin_amdgcn_raw_buffer_store_b128(ones, rw, lane < 16 ? l16 + 2048 : OOB, so, SC1);
            __builtin_amdgcn_raw_buffer_store_b128(ones, rw, lane < 44 ? l16 : OOB, W_KV + (p * 16 + rg) * 2816 + wave * 704, SC1);
        }
        {
            const v4u rp = mkv(wphi, 32 * 648);
#pragma unroll
            for (int i = 0; i < 5; ++i) {
                const u32 e = i * 64 + lane, row = e / 10, col = e - row * 10, o = fold && row - 6 < 18u && col < 9 ? row * 36 + col * 4 - 216 : OOB;
#pragma unroll
                for (int hh = 0; hh < 2; ++hh)
                    asm volatile("s_mov_b32 m0, %3\n\ts_nop 0\n\tbuffer_load_dword %0, %1, %2 offen lds" ::"v"(o), "s"(rp), "s"((2 * pw + hh) * 648), "s"(lb + P_PH + wave * 2560 + hh * 1280 + i * 256) : "m0", "memory");
            }
        }
        v4u hd[3]; v2u he[3];
#pragma unroll
        for (int i = 0; i < 3; ++i) asm volatile("" : "=v"(hd[i]), "=v"(he[i]));
        if (conv) {
#pragma unroll
            for (int i = 0; i < 3; ++i) {
                const char* gp = (const char*)sh + (ch0 + i) * 4096;
                asm volatile("global_load_dwordx4 %0, %1, %2" : "+v"(hd[i]) : "v"(tid * 16), "s"(gp) : "memory");
                asm volatile("global_load_dwordx2 %0, %1, %2" : "+v"(he[i]) : "v"(tid == 255 ? 0 : tid * 16 + 16), "s"(gp) : "memory");
            }
        } else if (extra) w_in(true, lb + P_WS + 9216);
        STAMP(26);
        if (conv) waitv<31>(); else if (extra) waitv<28>(); else waitv<25>();
        asm volatile("" : "+v"(sw0), "+v"(sw1));
        v4u Bd[2][4], Bi, Sel;
        {
            const bool act = g == (c >> 3);
            u32 mk[4];
#pragma unroll
            for (int i = 0; i < 4; ++i) { mk[i] = act && i == ((c & 7) >> 1) ? ((c & 1) ? 0xffff0000u : 0xffffu) : 0u; Bi[i] = mk[i] & 0x3f803f80u; }
#pragma unroll
            for (int s = 0; s < 2; ++s)
#pragma unroll
                for (int j = 0; j < 4; ++j) {
                    const v2u sw = s ? sw1 : sw0;
                    const u32 wd = __builtin_amdgcn_perm(0u, j < 2 ? sw.y : sw.x, (j & 1) ? 0x01000100u : 0x03020302u);
#pragma unroll
                    for (int i = 0; i < 4; ++i) Bd[s][j][i] = wd & mk[i];
                    pin(Bd[s][j]);
                }
            const u32 on = g == (c >> 2) ? ((c & 1) ? 0x3f800000u : 0x3f80u) : 0u, s0 = (c & 2) ? 0u : on, s1 = (c & 2) ? on : 0u;
            Sel = (v4u){s0, s1, s0, s1};
            pin(Bi); pin(Sel);
        }
        FENCE();
        if (conv) waitv<26>(); else if (extra) waitv<23>(); else waitv<20>();
        barrier();
        STAMP(27);
        {
            u32 a0 = lb + P_KS + (8 * (c >> 2) + (c & 3)) * 144 + 32 * wave + 16 * (g & 1);
            u32 a1 = lb + P_KS + (8 * (c >> 2) + (c & 3) + 4 * (wave & 1) + 32 * (g & 1)) * 144 + 128;
            OPQV(a0); OPQV(a1);
            u32 uo[5][2];
            v4u ka[5][5]; f4 pr[5][5], sm[5];
            sfor<0, 25>(LAM(I_) {
                constexpr int T = I_.v / 5, j = I_.v % 5, im = T < 4 ? (32 * (T >> 1) + 4 * (T & 1)) * 144 : 0;
                ka[T][j] = lds4((T < 4 ? a0 : a1) + im + (j < 4 ? j * 144 : P_VS));
            });
            FENCE();
            sfor<0, 45>(LAM(I_) {
                constexpr int k = I_.v / 5, ph = I_.v % 5, T = k - ph;
                if constexpr (T >= 0 && T < 5) {
                    if constexpr (ph == 0) {
#pragma unroll
                        for (int j = 0; j < 5; ++j) mm16<true>(pr[T][j], ka[T][j], j < 4 ? Bd[T == 4][j] : Bi);
                    } else if constexpr (ph < 4) {
                        f4& a = ph == 1 ? pr[T][0] : sm[T];
                        pin(a); pin(pr[T][ph]);
                        v4u bb = {pk(a[0], a[1]), pk(a[2], a[3]), pk(pr[T][ph][0], pr[T][ph][1]), pk(pr[T][ph][2], pr[T][ph][3])};
                        pad<1>(bb);
                        mm16<true>(sm[T], Sel, bb);
                    } else {
                        pin(sm[T]); pin(pr[T][4]);
                        const u32 w0 = pk(sm[T][0], sm[T][1]), w1 = pk(sm[T][2], sm[T][3]);
                        uo[T][0] = pk(bf_hi(w1) * pr[T][4][3], bf_lo(w1) * pr[T][4][2]);
                        uo[T][1] = pk(bf_hi(w0) * pr[T][4][1], bf_lo(w0) * pr[T][4][0]);
                        pin(uo[T][0]); pin(uo[T][1]);
                    }
                    FENCE();
                }
            });
            const u32 o = (72 * cg + 16 * wave + c) * 4096 + 16 * g, so = W_UT + 128 * tg;
            __builtin_amdgcn_raw_buffer_store_b128((v4u){uo[1][0], uo[1][1], uo[0][0], uo[0][1]}, rw, o, so, SC1);
            __builtin_amdgcn_raw_buffer_store_b128((v4u){uo[3][0], uo[3][1], uo[2][0], uo[2][1]}, rw, o + 64, so, SC1);
            __builtin_amdgcn_raw_buffer_store_b64((v2u){uo[4][0], uo[4][1]}, rw, wave < 2 ? (72 * cg + 64 + (c & 7)) * 4096 + 64 * (c >> 3) + 16 * g + (wave ? 0 : 8) : OOB, so, SC1);
        }
        FENCE();
        STAMP(28);
        asm volatile("s_waitcnt vmcnt(3)\n\ts_barrier" ::: "memory");
        v4u bw[2][2];
        {
            const u32 ph = lb + P_PH + wave * 2560;
            if (!fold && lane < 36) *(LAS u16*)(ph + 240 + lane * 42 + (lane >= 18 ? 1280 - 18 * 42 : 0)) = 0x3f80;
            const u32 a = ph + (c >> 2) * 40 + 32 * (g & 1) + 8 * (c & 3), a0 = a + (8 * kh + 6 - ((4 * wave) & 7)) * 40;
#pragma unroll
            for (int hh = 0; hh < 2; ++hh) {
                bw[hh][0] = tr2(a0 + hh * 1200, a0 + hh * 1200 + 160);
                const u32 a1 = kh ? a : a0 + hh * 1200 + 640;
                bw[hh][1] = tr2(a1, kh ? a1 : a1 + 160);
            }
        }
        {
            const u32 m = rho, ib = W_IMG + pw * IMG, qb = ib + L_QT;
            const u32 bA = sec < 3 ? ib + sec * L_V + (sec >> 1) * (L_Q - 2 * L_V) : W_WOT + pw * 36864;
            const u32 bB = sec < 3 ? qb + ((sec & 1) << 6) + ((sec >> 1) << 8) : W_WL + Pg * 18432 + 64 * wave;
            const u32 sh = (0x20408080u >> (8 * sec)) & 0xff, se = sec < 3 ? 32 : 16, sk = sec < 3 ? 16 : 512;
            const u32 oA = bA + ((m & 3) + 8 * (m >> 2) + 32 * kh) * 16, oB = bB + (m & 1) * se + kh * sk;
            u32 dst[2];
            dst[0] = m < 16 ? oA : m < 18 ? oB : sec == 0 && lane < 20 ? qb + 384 + (m & 1) * 16 : OOB;
            dst[1] = m < 16 ? oA + 64 : m < 18 ? oB + sh : OOB;
            const u32 st = m < 16 ? 1024 : sec < 3 ? QTS : 256, rs_ = m < 16 ? 2048 : sec < 3 ? 2 * QTS : 1024;
            const u32 cso = sec < 3 && lane < 18 ? W_CSP + (pw * 112 + (m < 16 ? sec * 16 + m : 32 + sec * 2 + m)) * 80 : OOB;
            const u32 aw = lb + P_WS + rho * 288;
#pragma unroll 1
            for (int tk = 0; tk < (extra ? 2 : 1); ++tk) {
                const int rt = tk ? 4 + ci : rg;
                v4u wa[2][2]; f16v d[2];
#pragma unroll
                for (int hh = 0; hh < 2; ++hh) {
                    const u32 a = aw + tk * 9216 + ((72 * wave + 36 * hh) & ~15);
                    wa[hh][0] = lds4(a + 16 * kh); wa[hh][1] = lds4(a + 32);
                }
                FENCE();
                mm32<true>(d[0], wa[0][0], bw[0][0]); mm32<false>(d[0], wa[0][1], bw[0][1]);
                mm32<true>(d[1], wa[1][0], bw[1][0]); mm32<false>(d[1], wa[1][1], bw[1][1]);
                pad<9>(d[0]);
#pragma unroll
                for (int hh = 0; hh < 2; ++hh) {
                    pin(d[hh]);
                    u32 w[8]; float cs = 0.f;
#pragma unroll
                    for (int j = 0; j < 8; ++j) { w[j] = pk(d[hh][2 * j], d[hh][2 * j + 1]); cs += bf_lo(w[j]) + bf_hi(w[j]); }
                    const u32 o = dst[hh] + rt * rs_;
                    __builtin_amdgcn_raw_buffer_store_b128((v4u){w[0], w[1], w[2], w[3]}, rw, o, 0, SC1);
                    __builtin_amdgcn_raw_buffer_store_b128((v4u){w[4], w[5], w[6], w[7]}, rw, o + st, 0, SC1);
                    __builtin_amdgcn_raw_buffer_store_b32(asu(halves_sum(cs, cs)), rw, cso + hh * 56 * 80, rt * 4, SC1);
                    FENCE();
                }
            }
        }
        {
            const LAS u32* xs = (const LAS u32*)(lb + P_XS + rho * 304 + 72 * wave + 36 * kh);
            float s1 = 0.f, s2 = 0.f;
#pragma unroll
            for (int j = 0; j < 9; ++j) { xr[j] = xs[j]; const float a = bf_lo(xr[j]), q = bf_hi(xr[j]); s1 += a + q; s2 += a * a + q * q; }
            const u32 so = W_XF0 + ((rg * 4 + tw) * 16 + pw) * XS;
            __builtin_amdgcn_raw_buffer_store_b128((v4u){xr[0], xr[1], xr[2], xr[3]}, rw, l16, so, SC1);
            __builtin_amdgcn_raw_buffer_store_b128((v4u){xr[4], xr[5], xr[6], xr[7]}, rw, l16 + 1024, so, SC1);
            __builtin_amdgcn_raw_buffer_store_b32(xr[8], rw, l4 + 2048, so, SC1);
            __builtin_amdgcn_raw_buffer_store_b32(asu(halves_sum(s1, s2)), rw, l4 + 2304, so, SC1);
        }
        STAMP(29);
        if (conv) {
#pragma unroll
            for (int i = 0; i < 3; ++i) {
                asm volatile("" : "+v"(hd[i]), "+v"(he[i]));
                const u32 d0 = hd[i].x, d1 = hd[i].y, d2 = hd[i].z, d3 = hd[i].w, d4 = he[i].x, d5 = he[i].y;
                const u32 a0 = __builtin_amdgcn_alignbit(d1, d0, 16), a1 = __builtin_amdgcn_alignbit(d2, d1, 16), a2 = __builtin_amdgcn_alignbit(d3, d2, 16),
                          a3 = __builtin_amdgcn_alignbit(d4, d3, 16), a4 = __builtin_amdgcn_alignbit(d5, d4, 16);
                u32 o = lb + L_Q + i * CVS + 32 + tid * 16;
                OPQV(o);
                *(LAS v4u*)(o) = (v4u){d0, d1, d2, d3};
                *(LAS v4u*)(o + HCS) = (v4u){a0, a1, a2, a3};
                *(LAS v4u*)(o + 2 * HCS) = (v4u){d1, d2, d3, d4};
                *(LAS v4u*)(o + 3 * HCS) = (v4u){a1, a2, a3, a4};
                if (tid == 0) {
#pragma unroll
                    for (int k = 0; k < 4; ++k) *(LAS v4u*)(o - 32 + k * HCS) = (v4u){0, 0, 0, 0};
                    *(LAS v4u*)(o - 16) = (v4u){0, 0, 0, 0};
                    *(LAS v4u*)(o - 16 + HCS) = (v4u){0, 0, 0, d0 << 16};
                    *(LAS v4u*)(o - 16 + 2 * HCS) = (v4u){0, 0, 0, d0};
                    *(LAS v4u*)(o - 16 + 3 * HCS) = (v4u){0, 0, d0 << 16, a0};
                }
                if (tid >= 64 && tid < 94) *(LAS v4u*)(o + 4 * HCS - 32 - 1024) = (v4u){0, 0, 0, 0};
            }
        }
        if (tid >= 128 && tid < 166) *(LAS v2u*)(lb + L_KVA + (tid - 128) * 80 + 72) = (v2u){0, 0};
        if (tid >= 192 && tid < 197) *(LAS v4u*)(lb + L_KVA + 38 * 80 + (tid - 192) * 16) = (v4u){0, 0, 0, 0};
        asm volatile("s_waitcnt vmcnt(0) lgkmcnt(0)\n\ts_barrier" ::: "memory");
        OPQS(ep);
        ep += 1;
        const v2u epv = {(u32)ep, (u32)(ep >> 32)};
        if (tid == 0) __builtin_amdgcn_raw_buffer_store_b64(epv, rw, 0, W_F + (rg * 16 + p) * 128, SC1);
        STAMP(1);
        {
            u32 o[3];
#pragma unroll
            for (int j = 0; j < 3; ++j) {
                const u32 pi = 16 * (wave + 4 * j) + c, hh = pi >= 88, ip = pi - 88 * hh, e = g & 1, s = 2 * (ip - 72) + e;
                const u32 s6 = (s * 43) >> 8, sr = s - 6 * s6, s3 = sr >= 3;
                const u32 v0 = (ip & 15) * 80 + (ip >> 4) * 8 + 32 * e, v1 = (2 * ip + e) * 80 + 64 - 128 * 80, v2 = (16 + sr - 3 * s3) * 80 + s3 * 32 + s6 * 8, v3 = s * 80 + 64 - 8 * 80;
                u32 v = ip < 64 ? v0 : v1;
                v = ip < 72 ? v : v2;
                v = ip >= 84 ? v3 : v;
                o[j] = (ip >= 72 && s >= 27) || lane >= 32 || pi >= 176 ? 0xffffu : v + hh * 1520;
            }
            kvw0 = o[0] | (o[1] << 16); kvw1 = o[2];
            OPQV(kvw0); OPQV(kvw1);
        }
        if (wave < 2) {
            const u32 fo = W_F + (wave ? rg * 16 + c : (lane >> 2) * 16 + 4 * Pg + (lane & 3)) * 128;
#pragma unroll 1
            for (int sp = 0; sp < SPIN; ++sp) {
                asm volatile("" ::: "memory");
                const v2u f = __builtin_amdgcn_raw_buffer_load_b64(rw, fo, 0, SC1);
                if (__builtin_amdgcn_ballot_w64(f.x != epv.x || f.y != epv.y) == 0) break;
                __builtin_amdgcn_s_sleep(1);
            }
        }
        barrier();
    }
    STAMP(2);
    u32 st[16];
    {
        const u32 ib = W_IMG + p * IMG, xb = W_XF0 + (rg * 4 + wave) * 16 * XS;
        if (conv) {
#pragma unroll
            for (int i = 0; i < 3; ++i) dma_ws(vw, l16, W_UT + (ch0 + i) * 4096 + wave * 1024, lb + L_Q + i * CVS + 4 * HCS + 480 + wave * 1024);
        }
        v4u cp[5];
        sfor<0, 5>(LAM(I_) { asm volatile("" : "=v"(cp[I_.v])); ld4<16 * I_.v>(cp[I_.v], tid < 112 ? tid * 80 : OOB, vw, W_CSP + p * 8960); });
#pragma unroll 1
        for (int i = 0; i < 13; ++i) { const u32 o = i < 9 ? (i * 4 + wave) * 1024 : L_QT + umin((i - 9) * 4 + wave, 14) * 1024; dma_ws(vw, l16, ib + o, lb + o); }
        u32 xo = xb;
        auto site = LAM(N_) {
            constexpr int n = N_.v;
            if constexpr (n < 9) dma_ws(vw, l16, ib + L_V + (n * 4 + wave) * 1024, lb + L_V + (n * 4 + wave) * 1024);
            else {
                constexpr int i = (n - 9) >> 2, k = (n - 9) & 3;
                if constexpr (k == 0) ld4a<0, 8 * i>(l16, vw, xo);
                if constexpr (k == 1) ld1a<2048, 128 + i>(l4, vw, xo);
                if constexpr (k == 2) ld4a<1024, 8 * i + 4>(l16, vw, xo);
                if constexpr (k == 3) { ld1a<2304, 144 + i>(l4, vw, xo); xo += XS; OPQS(xo); }
            }
        };
        {
            asm volatile("s_waitcnt vmcnt(13)" : "+v"(cp[0]), "+v"(cp[1]), "+v"(cp[2]), "+v"(cp[3]), "+v"(cp[4])::"memory");
            float cs = 0.f;
#pragma unroll
            for (int i = 0; i < 18; ++i) { const u32 t = cp[i >> 2][i & 3]; cs += asf(t); }
            if (tid < 112) *(LAS float*)(lb + L_CS + tid * 4) = cs;
        }
        if (conv) {
            asm volatile("s_barrier" ::: "memory");
            STAMP(3);
            const int bq = lane & 15, g = lane >> 4, cc = (bq + 1) & 3;
            const u32 aoff = lb + L_Q + cc * HCS + 2 * (bq - 8 * g + 24 - cc), boff = lb + L_Q + 4 * HCS + 480 + 2 * (16 * bq + 8 * g - 16) - 7 * 64;
            const u32 uoff = lb + L_Q + 4 * HCS + 480 + 32 * bq + 16 * (g >> 1) + ((g & 1) ? 0 : 8), yo = 32 * bq + 8 * g, yp = 32 * bq + 16 * (g >> 1) + 512 * (g & 1);
            if (wave < 3) {
                u32 aa = aoff + wave * CVS, ba = boff + wave * CVS;
                OPQV(aa); OPQV(ba);
                f4 acc[8];
                v4u A[2][8], B[2][8];
                sfor<0, 24>(LAM(N_) { conv_read<8, N_.v, 0>(aa, ba, A[0], B[0]); });
                FENCE();
                sfor<0, 8 * 27>(LAM(Q_) {
                    constexpr int r = Q_.v / 27, n = Q_.v % 27, i = conv_ij(8, 0xf1, n) >> 3, j = conv_ij(8, 0xf1, n) & 7;
                    mm16<r == 0 && conv_first(8, 0xf1, n)>(acc[j], A[r & 1][i], B[r & 1][j - i]);
                    if constexpr (r < 7 && n < 24) conv_read<8, n, r + 1>(aa, ba, A[(r + 1) & 1], B[(r + 1) & 1]);
                    sfor<site_at(Q_.v), site_at(Q_.v + 1)>(site);
                    FENCE();
                });
                asm volatile("s_nop 7" : "+v"(acc[0]), "+v"(acc[4]), "+v"(acc[5]), "+v"(acc[6]), "+v"(acc[7]));
                const float Dv = bf_lo(sD[ch0 + wave]);
                const u32 so = W_Y + (ch0 + wave) * 4096, ua = uoff + wave * CVS;
                const v2u y0 = y_val(acc[0], ua, Dv);
                const v4u y4 = y_pair(y_val(acc[4], ua + 2048, Dv), y_val(acc[5], ua + 2560, Dv)), y6 = y_pair(y_val(acc[6], ua + 3072, Dv), y_val(acc[7], ua + 3584, Dv));
                asm volatile("s_waitcnt vmcnt(0)" ::"v"(y0), "v"(y4), "v"(y6) : "memory");
                __builtin_amdgcn_raw_buffer_store_b64(y0, rw, yo, so, SC1);
                __builtin_amdgcn_raw_buffer_store_b128(y4, rw, yp + 2048, so, SC1);
                __builtin_amdgcn_raw_buffer_store_b128(y6, rw, yp + 3072, so, SC1);
            } else {
                sfor<0, 3>(LAM(C_) {
                    constexpr int cn = C_.v;
                    u32 aa = aoff + cn * CVS, ba = boff + cn * CVS;
                    OPQV(aa); OPQV(ba);
                    f4 acc[4];
                    v4u A[2][4], B[2][4];
                    sfor<0, 12>(LAM(N_) { conv_read<4, N_.v, 0>(aa, ba, A[0], B[0]); });
                    FENCE();
                    sfor<0, 8 * 9>(LAM(Q_) {
                        constexpr int r = Q_.v / 9, n = Q_.v % 9, i = conv_ij(4, 0xe, n) >> 3, j = conv_ij(4, 0xe, n) & 7;
                        mm16<r == 0 && conv_first(4, 0xe, n)>(acc[j], A[r & 1][i], B[r & 1][j - i]);
                        if constexpr (r < 7) { conv_read<4, n, r + 1>(aa, ba, A[(r + 1) & 1], B[(r + 1) & 1]); if constexpr (n < 3) conv_read<4, 9 + n, r + 1>(aa, ba, A[(r + 1) & 1], B[(r + 1) & 1]); }
                        sfor<site_at(72 * cn + Q_.v), site_at(72 * cn + Q_.v + 1)>(site);
                        FENCE();
                    });
                    asm volatile("s_nop 7" : "+v"(acc[1]), "+v"(acc[2]), "+v"(acc[3]));
                    const float Dv = bf_lo(sD[ch0 + cn]);
                    const u32 so = W_Y + (ch0 + cn) * 4096, ua = uoff + cn * CVS;
                    const v2u y1 = y_val(acc[1], ua + 512, Dv);
                    const v4u y2 = y_pair(y_val(acc[2], ua + 1024, Dv), y_val(acc[3], ua + 1536, Dv));
                    if constexpr (cn == 2) asm volatile("s_waitcnt vmcnt(0)" ::"v"(y1), "v"(y2) : "memory");
                    __builtin_amdgcn_raw_buffer_store_b64(y1, rw, yo + 512, so, SC1);
                    __builtin_amdgcn_raw_buffer_store_b128(y2, rw, yp + 1024, so, SC1);
                });
            }
        } else {
            sfor<0, NSITE>(site);
            waitv<0>();
        }
        STAMP(14);
        sfor<0, 16>(LAM(I_) { st[I_.v] = rda<144 + I_.v>(); });
    }
    u32 yv[9];
#pragma unroll
    for (int i = 0; i < 9; ++i) yv[i] = 0;
    v4u wv[17];
#pragma unroll
    for (int i = 0; i < 17; ++i) asm volatile("" : "=v"(wv[i]));
    barrier();
    STAMP(15);

#pragma unroll 1
    for (int L = 0; L < 2; ++L) {
        int Ls = L;
        OPQS(Ls);
        u64 m0_ = (u64)(long long)(Ls - 1), m1_ = (u64)(long long)(-Ls);
        OPQS(m0_); OPQS(m1_);
        int ln = lane;
        OPQV(ln); OPQV(l16); OPQV(l4);
        const int tl = ln & 31, h = ln >> 5, row = ln & 15, kg = ln >> 4, gq = row >> 2, rq = row & 3;
        const bool qa = (gq & 1) == (kg & 1);
        u32 aK_ = lb + L_K + l16, aV_ = lb + L_V + l16;
        u32 aKV = lb + L_QT + (qa ? (((gq >> 1) * 4 + rq) * 2 + (kg >> 1)) * 16 : 400 - 16 * (kg >> 1));
        u32 aQq = lb + L_QT + (qa && rq < 2 ? 256 + (((gq >> 1) * 2 + rq) * 2 + (kg >> 1)) * 16 : 384);
        u32 aCS = lb + L_CS + h * 224;
        OPQV(aK_); OPQV(aV_); OPQV(aKV); OPQV(aQq); OPQV(aCS);
        const u32 wb = W_WOT + pw * 36864, wlb = W_WL + Pg * 18432 + (wave >> 1) * 9216;
        u32 wso = wb;
        auto wsite = LAM(N_) {
            constexpr int n = N_.v;
            if constexpr (n == 36) wso = wlb;
            if constexpr ((n < 36 ? n : n - 36) % 4 == 0) OPQS(wso);
            if constexpr (n < 28) ld4am<(n % 4) * 1024, 144 + 4 * n>(l16, vw, wso, m0_);
            else ld4m<((n < 36 ? n : n - 36) % 4) * 1024>(wv[n - 28], l16, vw, wso, m0_);
            if constexpr ((n < 36 ? n : n - 36) % 4 == 3) wso += 4096;
        };
        f16v aK; f4 qKV;
        float mean, al, rstd;
        v4u rV[4], cK[5];
        if (L == 0) {
            const v4u ones = {~0u, ~0u, ~0u, ~0u};
            const u32 s1 = W_XF1 + ((rg * 4 + tw) * 16 + pw) * XS, s2 = W_O + (1024 + (rg * 4 + wave) * 16 + p) * OS;
            __builtin_amdgcn_raw_buffer_store_b128(ones, rw, l16, s1, SC1);
            __builtin_amdgcn_raw_buffer_store_b128(ones, rw, l16 + 1024, s1, SC1);
            __builtin_amdgcn_raw_buffer_store_b128(ones, rw, ln < 32 ? l16 + 2048 : OOB, s1, SC1);
            __builtin_amdgcn_raw_buffer_store_b128(ones, rw, l16, s2, SC1);
            __builtin_amdgcn_raw_buffer_store_b128(ones, rw, l16 + 1024, s2, SC1);
            __builtin_amdgcn_raw_buffer_store_b128(ones, rw, ln < 16 ? l16 + 2048 : OOB, s2, SC1);
            __builtin_amdgcn_raw_buffer_store_b128(ones, rw, ln < 32 ? row * 16 + (kg & 1) * 1024 * 256 : OOB, W_ST + ((rg * 4 + tw) * 16 + pw) * 256, SC1);
            __builtin_amdgcn_raw_buffer_store_b128(ones, rw, ln < 44 ? l16 : OOB, W_KV + (256 + p * 16 + rg) * 2816 + wave * 704, SC1);
        }
#pragma unroll 1
        for (int sp = 0; sp < SPIN; ++sp) {
            u32 xo = W_XF1 + (rg * 4 + wave) * 16 * XS, sm = 0;
            OPQS(xo);
            auto xsite = LAM(N_) {
                constexpr int n = N_.v;
                ld4am<(n & 1) * 1024, 4 * n>(l16, vw, xo, m1_);
                if constexpr (n & 1) { xo += XS; OPQS(xo); }
            };
            constexpr int UP = 12;
            sfor<0, UP>(xsite);
            v4u r0[3], r1[3];
#pragma unroll
            for (int s = 0; s < 3; ++s) { r0[s] = lds4(aK_ + (32 + s) * 1024); r1[s] = lds4(aKV + (32 + s) * QTS); }
            wso = wb;
            sfor<0, 36>(LAM(SI) {
                constexpr int n = SI.v, s = n < 4 ? 32 + n : n - 4, s3 = n + 3 < 4 ? 35 + n : n - 1;
                if constexpr (n >= 4 && !(s & 1)) {
                    constexpr int issued = UP + n < 32 ? UP + n : 32;
                    if (L) asm volatile("s_waitcnt vmcnt(%0)" ::"n"(issued - s - 2) : "memory");
                    sm = mx3(sm, rda<4 * s>(), rda<4 * s + 4>());
                    FENCE();
                }
                mm32b<n == 0, 4 * s>(aK, r0[n % 3]);
                if constexpr (n + 3 < 36) r0[n % 3] = lds4(aK_ + s3 * 1024); else rV[n - 33] = lds4(aV_ + (n - 33) * 1024);
                if constexpr (UP + n < 32) xsite(IC<UP + n>{});
                if constexpr (n == 1) {
                    float t = 0.f;
#pragma unroll
                    for (int i = 0; i < 16; ++i) t += asf(st[i]);
                    auto r = __builtin_amdgcn_permlane32_swap(asu(t), asu(t), false, false);
                    mean = asf(r[0]) * (1.f / 576.f);
                    const float var = __builtin_fmaxf(asf(r[1]) * (1.f / 576.f) - mean * mean, 0.f);
                    rstd = __builtin_amdgcn_rsqf(var + 1e-5f);
                    al = rstd * 1.4426950408889634f;
                }
                FENCE();
                mm16b<n == 0, 4 * s>(qKV, r1[n % 3]);
                if constexpr (n + 3 < 36) r1[n % 3] = lds4(aKV + s3 * QTS); else cK[n - 33] = lds4(aCS + (n - 33) * 16);
                if constexpr (n == 33) cK[3] = lds4(aCS + 48);
                if constexpr (n == 34) cK[4] = lds4(aCS + 192);
                if constexpr (n == 35) rV[3] = lds4(aV_ + 3 * 1024);
                if constexpr (n >= 8 && (n & 3) != 3) wsite(IC<(n - 8) - (n - 8) / 4>{});
                FENCE();
            });
            if (!bad(sm)) break;
        }
        STAMP(4 + 12 * L);
        v4u rQ[4], rq4[3];
        {
            f16v aV;
            float t[18], e1[18], e2[18], mx, mn, c1, c2, s1 = 0.f, s2 = 0.f;
            v4u cV[4];
            u32 aS = lb + L_STG + (wave * 32 + tl) * STGS + 72 * h;
            OPQV(aS);
            u32 qo = L_Q + wave * 1024;
            sfor<0, 36>(LAM(SI) {
                constexpr int s = SI.v;
                mm32b<s == 0, 4 * s>(aV, rV[s & 3]);
                if constexpr (s == 2) { pad<7>(aK); pin(qKV); }
                if constexpr (s + 4 < 36) rV[s & 3] = lds4(aV_ + (s + 4) * 1024); else cV[s - 32] = lds4(aCS + 64 + (s - 32) * 16);
                if constexpr (s < 18 && !(s & 1)) { OPQS(qo); dma_ws(vw, l16, W_IMG + p * IMG + qo, lb + qo); qo += 4096; }
                if constexpr (s >= 2 && s < 5) {
#pragma unroll
                    for (int i = 6 * (s - 2); i < 6 * (s - 2) + 6; ++i) { const u32 cv = cK[i >> 2][i & 3]; t[i] = __builtin_fmaf(-mean, asf(cv), i < 16 ? aK[i] : qKV[i - 16]); }
                }
                if constexpr (s == 5) { mx = __builtin_fmaxf(__builtin_fmaxf(t[0], t[1]), t[2]); mn = __builtin_fminf(__builtin_fminf(t[0], t[1]), t[2]);
#pragma unroll
                    for (int i = 3; i < 7; i += 2) { mx = __builtin_fmaxf(__builtin_fmaxf(mx, t[i]), t[i + 1]); mn = __builtin_fminf(__builtin_fminf(mn, t[i]), t[i + 1]); } }
                if constexpr (s == 6) {
#pragma unroll
                    for (int i = 7; i < 13; i += 2) { mx = __builtin_fmaxf(__builtin_fmaxf(mx, t[i]), t[i + 1]); mn = __builtin_fminf(__builtin_fminf(mn, t[i]), t[i + 1]); } }
                if constexpr (s == 7) {
#pragma unroll
                    for (int i = 13; i < 17; i += 2) { mx = __builtin_fmaxf(__builtin_fmaxf(mx, t[i]), t[i + 1]); mn = __builtin_fminf(__builtin_fminf(mn, t[i]), t[i + 1]); }
                    mx = __builtin_fmaxf(mx, t[17]); mn = __builtin_fminf(mn, t[17]); c1 = -al * mx; c2 = al * mn; }
                if constexpr (s >= 8 && s < 32) {
#pragma unroll
                    for (int q = (3 * (s - 8)) / 2; q < (3 * (s - 7)) / 2; ++q) {
                        const int i = q >> 1;
                        if (q & 1) { e2[i] = __builtin_amdgcn_exp2f(__builtin_fmaf(t[i], -al, c2)); s2 += e2[i]; }
                        else { e1[i] = __builtin_amdgcn_exp2f(__builtin_fmaf(t[i], al, c1)); s1 += e1[i]; }
                    }
                }
                if constexpr (s == 32) { s1 = __builtin_amdgcn_rcpf(s1); s2 = __builtin_amdgcn_rcpf(s2); }
                if constexpr (s >= 33) {
                    constexpr int i = s - 33;
                    *(LAS v2u*)(aS + 8 * i) = (v2u){pk(e1[4 * i] * s1, e1[4 * i + 1] * s1), pk(e1[4 * i + 2] * s1, e1[4 * i + 3] * s1)};
                }
                FENCE();
            });
            *(LAS v2u*)(aS + 24) = (v2u){pk(e1[12] * s1, e1[13] * s1), pk(e1[14] * s1, e1[15] * s1)};
            *(LAS v2u*)(aS + 32) = (v2u){pk(e1[16] * s1, e1[17] * s1), pk(e2[0] * s2, e2[1] * s2)};
#pragma unroll
            for (int i = 0; i < 4; ++i) *(LAS v2u*)(aS + 40 + 8 * i) = (v2u){pk(e2[4 * i + 2] * s2, e2[4 * i + 3] * s2), pk(e2[4 * i + 4] * s2, e2[4 * i + 5] * s2)};
            FENCE();
            pin(aV);
            const u32 aSv = aS + 144 - 32 * h;
            const float mr = -mean * rstd;
#pragma unroll
            for (int i = 0; i < 4; ++i) {
                const u32 q0 = cV[i].x, q1 = cV[i].y, q2 = cV[i].z, q3 = cV[i].w;
                *(LAS v2u*)(aSv + 8 * i) = (v2u){pk(__builtin_fmaf(mr, asf(q0), aV[4 * i] * rstd), __builtin_fmaf(mr, asf(q1), aV[4 * i + 1] * rstd)),
                                                 pk(__builtin_fmaf(mr, asf(q2), aV[4 * i + 2] * rstd), __builtin_fmaf(mr, asf(q3), aV[4 * i + 3] * rstd))};
            }
            const u32 q2 = cK[4].z, q3 = cK[4].w;
            *(LAS v2u*)(aSv + 32) = (v2u){pk(__builtin_fmaf(mr, asf(q2), qKV[2] * rstd), __builtin_fmaf(mr, asf(q3), qKV[3] * rstd)), 0x3f80u};
        }
        asm volatile("s_waitcnt vmcnt(0) lgkmcnt(0)\n\ts_barrier" ::: "memory");
        if (L == 0 && conv && tid == 0) __builtin_amdgcn_raw_buffer_store_b64((v2u){(u32)ep, (u32)(ep >> 32)}, rw, 0, W_F2 + (rg * 16 + p) * 128, SC1);
        STAMP(5 + 12 * L);
        u32 aQ_ = lb + L_Q + l16;
        OPQV(aQ_);
        {
            const int hh = wave >> 1, dt = wave & 1;
            const u32 r0 = lb + L_STG + (4 * (4 * (kg & 1) + gq) + (kg >> 1)) * STGS + 8 * rq;
            u32 pa = r0 + 72 * hh, pb = r0 + 144 + 40 * hh + 32 * dt;
            OPQV(pa); OPQV(pb);
            f4 c[3];
            v4u B[4], A[4][3];
#pragma unroll
            for (int ks = 0; ks < 4; ++ks) {
                B[ks] = tr2(pb + ks * 32 * STGS, pb + (ks * 32 + 2) * STGS);
#pragma unroll
                for (int ft = 0; ft < 3; ++ft) A[ks][ft] = tr2(pa + ks * 32 * STGS + 32 * ft, pa + (ks * 32 + 2) * STGS + 32 * ft);
            }
#pragma unroll
            for (int s = 0; s < 4; ++s) rQ[s] = lds4(aQ_ + s * 1024);
#pragma unroll
            for (int s = 0; s < 3; ++s) rq4[s] = lds4(aQq + s * QTS);
            FENCE();
            sfor<0, 12>(LAM(I_) { constexpr int ks = I_.v / 3, ft = I_.v % 3; mm16<ks == 0>(c[ft], A[ks][ft], B[ks]); });
            asm volatile("s_nop 7" : "+v"(c[0]), "+v"(c[1]), "+v"(c[2]));
            u32 w[6];
#pragma unroll
            for (int ft = 0; ft < 3; ++ft) { w[2 * ft] = pk(c[ft][0], c[ft][1]); w[2 * ft + 1] = pk(c[ft][2], c[ft][3]); }
            const u32 so = W_KV + ((L * 16 + p) * 16 + rg) * 2816 + hh * 1408;
            if (dt == 0) {
                __builtin_amdgcn_raw_buffer_store_b128((v4u){w[0], w[1], w[2], w[3]}, rw, l16, so, SC1);
                __builtin_amdgcn_raw_buffer_store_b64((v2u){w[4], w[5]}, rw, ln < 16 ? ln * 8 + 1024 : OOB, so, SC1);
            } else {
                const u32 s0 = __builtin_amdgcn_update_dpp(0u, w[2], 0x113, 0xf, 0xf, true), s1 = __builtin_amdgcn_update_dpp(0u, w[3], 0x113, 0xf, 0xf, true);
                const u32 t0 = __builtin_amdgcn_update_dpp(0u, w[4], 0x116, 0xf, 0xf, true), t1 = __builtin_amdgcn_update_dpp(0u, w[5], 0x116, 0xf, 0xf, true);
                const v2u v = row < 3 ? (v2u){w[0], w[1]} : row < 6 ? (v2u){s0, s1} : row < 9 ? (v2u){t0, t1} : (v2u){0, 0};
                const int slot = row < 6 ? kg * 6 + row : 24 + row - 6;
                __builtin_amdgcn_raw_buffer_store_b64(v, rw, row < 6 || (kg == 0 && row < 14) ? slot * 8 + 1152 : OOB, so, SC1);
            }
        }
        FENCE();
        STAMP(6 + 12 * L);
        u32 pq[20];
        v4u kp[12];
        const __amdgpu_buffer_rsrc_t rkv = mkrs(ws + W_KV + (L * 16 + p) * 16 * 2816, 16 * 2816);
        const u32 kvo = kg * 2816 + wave * 256 + row * 16, kv3 = wave < 3 ? kvo + 2048 : OOB;
        v2u yfl = {0, 0};
        {
            f16v aQ; f4 qQ;
            v4u cQ[5];
            sfor<0, 36>(LAM(SI) {
                constexpr int s = SI.v;
                mm32b<s == 0, 4 * s>(aQ, rQ[s & 3]);
                if constexpr (s + 4 < 36) rQ[s & 3] = lds4(aQ_ + (s + 4) * 1024); else cQ[s - 32] = lds4(aCS + 128 + (s - 32) * 16);
                if constexpr (s < 32 && (s & 3) != 3) wsite(IC<21 + s - s / 4>{});
                FENCE();
                mm16b<s == 0, 4 * s>(qQ, rq4[s % 3]);
                if constexpr (s + 3 < 36) rq4[s % 3] = lds4(aQq + (s + 3) * QTS);
                if constexpr (s == 33) cQ[4] = lds4(aCS + 208);
                FENCE();
            });
#pragma unroll
            for (int i = 0; i < 4; ++i) {
                kp[i] = __builtin_amdgcn_raw_buffer_load_b128(rkv, kvo, i * 4 * 2816, SC1);
                kp[4 + i] = __builtin_amdgcn_raw_buffer_load_b128(rkv, kvo + 1024, i * 4 * 2816, SC1);
                kp[8 + i] = __builtin_amdgcn_raw_buffer_load_b128(rkv, kv3, i * 4 * 2816, SC1);
            }
            const u32 yfo = W_F2 + ((((row < 12 ? row : 0) - pw) & 15) * 16 + pw) * 128;
            if (L == 0) yfl = __builtin_amdgcn_raw_buffer_load_b64(rw, yfo, 0, SC1);
            FENCE();
            pin(aQ); pin(qQ);
            float t[18]; u32 ph[18];
#pragma unroll
            for (int i = 0; i < 18; ++i) { const u32 cv = cQ[i >> 2][i & 3]; t[i] = __builtin_fmaf(-mean, asf(cv), i < 16 ? aQ[i] : qQ[i - 16]); }
            hedgehog(t, al, ph);
#pragma unroll
            for (int i = 0; i < 18; ++i) { pq[i] = ph[i]; OPQV(pq[i]); }
            pq[18] = 0; pq[19] = 0;
            FENCE();
            STAMP(7 + 12 * L);
            u32 m = 0;
#pragma unroll
            for (int i = 0; i < 12; ++i) m = mx3(m, kp[i].x, kp[i].z);
            if (__builtin_expect(bad(m), 0)) {
#pragma unroll 1
                for (int sp = 0; sp < SPIN; ++sp) {
                    __builtin_amdgcn_s_sleep(2);
                    asm volatile("" ::: "memory");
                    m = 0;
#pragma unroll
                    for (int i = 0; i < 4; ++i) {
                        kp[i] = __builtin_amdgcn_raw_buffer_load_b128(rkv, kvo, i * 4 * 2816, SC1);
                        kp[4 + i] = __builtin_amdgcn_raw_buffer_load_b128(rkv, kvo + 1024, i * 4 * 2816, SC1);
                        kp[8 + i] = __builtin_amdgcn_raw_buffer_load_b128(rkv, kv3, i * 4 * 2816, SC1);
                    }
#pragma unroll
                    for (int i = 0; i < 12; ++i) m = mx3(m, kp[i].x, kp[i].z);
                    if (!bad(m)) break;
                }
            }
            v4u sel;
#pragma unroll
            for (int i = 0; i < 4; ++i) sel[i] = row == 2 * i ? 0x3f80u : row == 2 * i + 1 ? 0x3f800000u : 0u;
            f4 a[3];
            pad<1>(sel);
            sfor<0, 12>(LAM(I_) { constexpr int i = I_.v / 3, j = I_.v % 3; mm16<i == 0>(a[j], sel, kp[4 * j + i]); });
            asm volatile("s_nop 7" : "+v"(a[0]), "+v"(a[1]), "+v"(a[2]));
#pragma unroll
            for (int j = 0; j < 3; ++j) {
                const u32 o = j == 0 ? kvw0 & 0xffffu : j == 1 ? kvw0 >> 16 : kvw1;
                if (o != 0xffffu) *(LAS v2u*)(lb + L_KVA + o) = (v2u){pk(a[j][0], a[j][1]), pk(a[j][2], a[j][3])};
            }
            barrier();
            STAMP(8 + 12 * L);
            if (L == 0) {
#pragma unroll 1
                for (int sp = 0; sp < SPIN; ++sp) {
                    if (__builtin_amdgcn_ballot_w64(yfl.x != (u32)ep || yfl.y != (u32)(ep >> 32)) == 0) break;
                    asm volatile("" ::: "memory");
                    yfl = __builtin_amdgcn_raw_buffer_load_b64(rw, yfo, 0, SC1);
                }
            }
        }
        u32 ye[18];
        {
            u32 so = W_Y + 36 * pw * 4096 + (rg * 128 + tw * 32) * 2;
            const u32 yo = L ? OOB : h * 18 * 4096 + tl * 2;
#pragma unroll
            for (int m = 0; m < 18; ++m) { OPQS(so); ye[m] = __builtin_amdgcn_raw_buffer_load_b16(rw, yo, so, SC1); so += 4096; }
        }
        FENCE();
        {
            const int hr = (tl >> 2) & 1, d = (tl & 3) + 4 * (tl >> 3);
            const u32 aF = lb + L_KVA + (h == hr ? (hr * 19 + d) * 80 : 38 * 80);
            const u32 aQ4 = lb + L_KVA + (kg == gq && rq < 3 ? ((gq >> 1) * 19 + 16 + rq) * 80 : 38 * 80);
            v4u A[5], Aq[5];
#pragma unroll
            for (int c = 0; c < 5; ++c) { Aq[c] = lds4(aQ4 + 16 * c); A[c] = lds4(aF + 16 * c); }
            FENCE();
            f16v o; f4 oq;
            v4u pb[5];
#pragma unroll
            for (int c = 0; c < 5; ++c) { pb[c] = (v4u){pq[4 * c], pq[4 * c + 1], pq[4 * c + 2], pq[4 * c + 3]}; pin(pb[c]); }
            nop2();
            sfor<0, 5>(LAM(C_) { mm16<C_.v == 0>(oq, Aq[C_.v], pb[C_.v]); });
            sfor<0, 5>(LAM(C_) { mm32<C_.v == 0>(o, A[C_.v], pb[C_.v]); });
            asm volatile("s_nop 11" : "+v"(o), "+v"(oq));
            const float rd = __builtin_amdgcn_rcpf(oq[2] + 1e-6f);
            const u32 so = W_O + (L * 1024 + (rg * 4 + wave) * 16 + p) * OS;
            __builtin_amdgcn_raw_buffer_store_b128((v4u){pk(o[0] * rd, o[1] * rd), pk(o[2] * rd, o[3] * rd), pk(o[4] * rd, o[5] * rd), pk(o[6] * rd, o[7] * rd)}, rw, l16, so, SC1);
            __builtin_amdgcn_raw_buffer_store_b128((v4u){pk(o[8] * rd, o[9] * rd), pk(o[10] * rd, o[11] * rd), pk(o[12] * rd, o[13] * rd), pk(o[14] * rd, o[15] * rd)}, rw, l16 + 1024, so, SC1);
            __builtin_amdgcn_raw_buffer_store_b32(pk(oq[0] * rd, oq[1] * rd), rw, l4 + 2048, so, SC1);
        }
        FENCE();
#pragma unroll
        for (int i = 0; i < 9; ++i) { yv[i] |= ye[2 * i] | (ye[2 * i + 1] << 16); OPQV(yv[i]); }
        STAMP(9 + 12 * L);
        {
            const u32 so = W_O + (L * 1024 + (rg * 4 + tw) * 16 + 4 * wave) * OS;
            v4u lo;
#pragma unroll 1
            for (int sp = 0; sp < SPIN; ++sp) {
                asm volatile("" : "+v"(l4)::"memory");
#pragma unroll
                for (int i = 0; i < 4; ++i) lo[i] = __builtin_amdgcn_raw_buffer_load_b32(rw, l4 + 2048, so + i * OS, SC1);
                if (!bad(mx3(umax(lo.x, lo.y), lo.z, lo.w))) break;
                __builtin_amdgcn_s_sleep(1);
            }
            v4u og[8];
#pragma unroll 1
            for (int sp = 0; sp < SPIN; ++sp) {
                asm volatile("" : "+v"(l16)::"memory");
#pragma unroll
                for (int i = 0; i < 8; ++i) og[i] = __builtin_amdgcn_raw_buffer_load_b128(rw, l16 + (i & 1) * 1024, so + (i >> 1) * OS, SC1);
                FENCE();
                u32 m = 0;
#pragma unroll
                for (int i = 0; i < 8; i += 2) m = mx3(m, og[i].x, og[i + 1].x);
                if (!bad(m)) break;
            }
            const u32 ao = aQ_ + wave * 8192;
#pragma unroll
            for (int i = 0; i < 8; ++i) *(LAS v4u*)(ao + i * 1024) = og[i];
            *(LAS v4u*)(aQ_ + (32 + wave) * 1024) = lo;
            barrier();
        }
        STAMP(10 + 12 * L);
        float x1[18];
        {
            f16v aW; f4 qW;
            u32 aB = lb + L_Q + ((wave >> 1) * 18 + (kg & 1)) * 1024 + ((kg >> 1) * 32 + 16 * (wave & 1) + row) * 16;
            OPQV(aB);
            v4u rb_[4], rl[9];
#pragma unroll
            for (int i = 0; i < 9; ++i) rl[i] = lds4(aB + i * 2048);
#pragma unroll
            for (int s = 0; s < 4; ++s) rb_[s] = lds4(aQ_ + s * 1024);
            FENCE();
            sfor<0, 9>(LAM(I_) { mm16<I_.v == 0>(qW, wv[8 + I_.v], rl[I_.v]); });
            FENCE();
            sfor<0, 36>(LAM(SI) {
                constexpr int s = SI.v;
                if constexpr (s < 28) mm32a<s == 0, 144 + 4 * s>(aW, rb_[s & 3]); else mm32<false>(aW, wv[s - 28], rb_[s & 3]);
                if constexpr (s + 4 < 36) rb_[s & 3] = lds4(aQ_ + (s + 4) * 1024);
                if constexpr (s == 3) pad<1>(qW);
                if constexpr (s == 3) *(LAS f4*)(lb + L_EX + wave * 1024 + l16) = qW;
                FENCE();
            });
            barrier();
            const u32 ae = lb + L_EX + (kg & 1) * 1024 + (wave * 16 + row) * 16 + 8 * h;
            const v2u e0 = lds2(ae), e1 = lds2(ae + 2048);
            STAMP(11 + 12 * L);
            pad<9>(aW);
            float s1 = 0.f, s2 = 0.f;
#pragma unroll
            for (int i = 0; i < 9; ++i) {
                const float a0 = i < 8 ? aW[2 * i] : asf(e0.x) + asf(e1.x), a1 = i < 8 ? aW[2 * i + 1] : asf(e0.y) + asf(e1.y);
                const u32 at = pk(a0, a1), w = pk(bf_lo(xr[i]) + bf_lo(at), bf_hi(xr[i]) + bf_hi(at));
                x1[2 * i] = bf_lo(w); x1[2 * i + 1] = bf_hi(w);
                s1 += x1[2 * i] + x1[2 * i + 1]; s2 += x1[2 * i] * x1[2 * i] + x1[2 * i + 1] * x1[2 * i + 1];
            }
            __builtin_amdgcn_raw_buffer_store_b32(asu(halves_sum(s1, s2)), rw, l4, W_ST + (L * 1024 + (rg * 4 + tw) * 16 + pw) * 256, SC1);
        }
        FENCE();
        {
            float t;
#pragma unroll 1
            for (int sp = 0; sp < SPIN; ++sp) {
                asm volatile("" : "+v"(l4)::"memory");
                u32 e[16], m = 0;
#pragma unroll
                for (int i = 0; i < 16; ++i) e[i] = __builtin_amdgcn_raw_buffer_load_b32(rw, l4 + i * 256, W_ST + (L * 1024 + (rg * 4 + tw) * 16) * 256, SC1);
                FENCE();
                t = 0.f;
#pragma unroll
                for (int i = 0; i < 16; i += 2) { m = mx3(m, e[i], e[i + 1]); t += asf(e[i]) + asf(e[i + 1]); }
                if (!bad(m)) break;
                __builtin_amdgcn_s_sleep(1);
            }
            STAMP(12 + 12 * L);
            auto r = __builtin_amdgcn_permlane32_swap(asu(t), asu(t), false, false);
            const float m1 = asf(r[0]) * (1.f / 576.f);
            const float r1 = __builtin_amdgcn_rsqf(__builtin_fmaxf(asf(r[1]) * (1.f / 576.f) - m1 * m1, 0.f) + 1e-5f);
            float s1 = 0.f, s2 = 0.f;
#pragma unroll
            for (int i = 0; i < 9; ++i) {
                const u32 xn = pk((x1[2 * i] - m1) * r1, (x1[2 * i + 1] - m1) * r1);
                const u32 gt = pk(bf_lo(xn) * bf_lo(yv[i]), bf_hi(xn) * bf_hi(yv[i]));
                xr[i] = pk(x1[2 * i] + bf_lo(gt), x1[2 * i + 1] + bf_hi(gt));
                const float a = bf_lo(xr[i]), c = bf_hi(xr[i]);
                s1 += a + c; s2 += a * a + c * c;
            }
            if (L == 0) {
                const u32 so = W_XF1 + ((rg * 4 + tw) * 16 + pw) * XS;
                __builtin_amdgcn_raw_buffer_store_b128((v4u){xr[0], xr[1], xr[2], xr[3]}, rw, l16, so, SC1);
                __builtin_amdgcn_raw_buffer_store_b128((v4u){xr[4], xr[5], xr[6], xr[7]}, rw, l16 + 1024, so, SC1);
                __builtin_amdgcn_raw_buffer_store_b32(xr[8], rw, l4 + 2048, so, SC1);
                __builtin_amdgcn_raw_buffer_store_b32(asu(halves_sum(s1, s2)), rw, l4 + 2304, so, SC1);
                STAMP(13);
                u32 lf[16];
#pragma unroll 1
                for (int sp = 0; sp < SPIN; ++sp) {
                    asm volatile("" : "+v"(l4)::"memory");
                    u32 xo = W_XF1 + (rg * 4 + wave) * 16 * XS, m = 0;
#pragma unroll
                    for (int i = 0; i < 16; ++i) {
                        OPQS(xo);
                        st[i] = __builtin_amdgcn_raw_buffer_load_b32(rw, l4 + 2304, xo, SC1);
                        lf[i] = __builtin_amdgcn_raw_buffer_load_b32(rw, l4 + 2048, xo, SC1);
                        xo += XS;
                    }
                    FENCE();
#pragma unroll
                    for (int i = 0; i < 16; ++i) m = mx3(m, st[i], lf[i]);
                    if (!bad(m)) break;
                    __builtin_amdgcn_s_sleep(1);
                }
                sfor<0, 16>(LAM(I_) { wra<128 + I_.v>(lf[I_.v]); });
                STAMP(25);
            } else {
                u32* op = (u32*)(out + (rg * 128 + tw * 32 + tl) * 576 + 36 * pw + 18 * h);
#pragma unroll
                for (int i = 0; i < 9; ++i) op[i] = xr[i];
                if (b == 0 && tid == 0) __builtin_amdgcn_raw_buffer_store_b64((v2u){(u32)ep, (u32)(ep >> 32)}, rw, 0, W_E, SC1);
            }
        }
    }
    STAMP_END();
}
#if defined(__HIP_DEVICE_COMPILE__)
#pragma clang attribute pop
#endif

extern "C" void kernel_launch(void* const* d_in, const int* in_sizes, int n_in, void* d_out, int out_size, void* d_ws, size_t ws_size, hipStream_t stream) {
    (void)in_sizes; (void)n_in; (void)out_size; (void)ws_size;
    hipFuncSetAttribute((const void*)k_main, hipFuncAttributeMaxDynamicSharedMemorySize, L_END);
    k_main<<<256, 256, L_END, stream>>>((const u16*)d_in[0], (const u16*)d_in[1], (const u16*)d_in[2], (const u16*)d_in[3], (const u16*)d_in[5], (const u16*)d_in[6],
                                        (const u16*)d_in[7], (const u16*)d_in[8], (const u16*)d_in[9], (u16*)d_out, (char*)d_ws);
}
```

```cpp
#include <hip/hip_runtime.h>
#include <stdint.h>
#pragma clang diagnostic ignored "-Wint-to-pointer-cast"
#pragma clang diagnostic ignored "-Wunused-value"
#pragma clang diagnostic ignored "-Winline-asm"
#if defined(__HIP_DEVICE_COMPILE__)
#pragma clang attribute push(__attribute__((target("no-packed-fp32-ops"))), apply_to = function)
#endif
typedef unsigned short u16;
typedef unsigned u32;
typedef unsigned long long u64;
typedef u32 v4u __attribute__((ext_vector_type(4)));
typedef u32 v2u __attribute__((ext_vector_type(2)));
typedef float f4 __attribute__((ext_vector_type(4)));
typedef float f2 __attribute__((ext_vector_type(2)));
typedef float f16v __attribute__((ext_vector_type(16)));
typedef __bf16 b8 __attribute__((ext_vector_type(8)));
typedef __bf16 b2 __attribute__((ext_vector_type(2)));
typedef short s4 __attribute__((ext_vector_type(4)));
#define LAS __attribute__((address_space(3)))
#define DEV __device__ __forceinline__
#define SC1 16

constexpr u32 QTS = 416, STGS = 232, CVS = 21216, HCS = 4160;
constexpr u32 L_K = 0, L_V = 36864, L_QT = 73728, L_Q = 89088, L_STG = 125952, L_KVA = 155648, L_CS = 158784, L_EX = 159232, L_END = 163328;
constexpr u32 P_KS = 0, P_VS = 10240, P_WS = 20480, P_XS = 31232, P_PH = 42496, L_Z = L_KVA + 38 * 80;
constexpr u32 IMG = L_STG, XS = 2560, OS = 2304;
constexpr u32 WOG = 4 * 36864 + 18432;
constexpr u32 W_IMG = 0, W_WOT = W_IMG + 16 * IMG, W_CSP = W_WOT + 4 * WOG, W_UT = W_CSP + 16 * 7168,
              W_XF0 = W_UT + 576 * 4096, W_XF1 = W_XF0 + 1024 * XS, W_O = W_XF1 + 1024 * XS, W_Y = W_O + 2 * 1024 * OS, W_KV = W_Y + 576 * 4096,
              W_ST = W_KV + 2 * 256 * 2816, W_F = W_ST + 2 * 1024 * 256, W_F2 = W_F + 256 * 128, W_E = W_F2 + 256 * 128, W_END = W_E + 128, W_DBG = W_END;
constexpr u32 OOB = 0x7ffffff0u;
constexpr int SPIN = 1 << 14;

DEV float asf(u32 v) { return __builtin_bit_cast(float, v); }
DEV u32 asu(float v) { return __builtin_bit_cast(u32, v); }
DEV float bf_lo(u32 v) { return asf(v << 16); }
DEV float bf_hi(u32 v) { return asf(v & 0xffff0000u); }
DEV u32 pk(float lo, float hi) { b2 r = __builtin_convertvector((f2){lo, hi}, b2); return __builtin_bit_cast(u32, r); }
DEV float rb(float f) { return bf_lo(pk(f, f)); }
DEV float dot2(u32 a, u32 b, float c) { return __builtin_amdgcn_fdot2_f32_bf16(__builtin_bit_cast(b2, a), __builtin_bit_cast(b2, b), c, false); }
DEV u32 umin(u32 a, u32 b) { return a < b ? a : b; }
DEV u32 umax(u32 a, u32 b) { return a > b ? a : b; }
DEV u32 mx3(u32 a, u32 b, u32 c) { return umax(umax(a, b), c); }
template <bool Z> DEV void mm32(f16v& c, v4u a, v4u b) {
    if constexpr (Z) asm volatile("v_mfma_f32_32x32x16_bf16 %0, %1, %2, 0" : "=&v"(c) : "v"(a), "v"(b));
    else asm volatile("v_mfma_f32_32x32x16_bf16 %0, %1, %2, %0" : "+v"(c) : "v"(a), "v"(b));
}
template <bool Z> DEV void mm16(f4& c, v4u a, v4u b) {
    if constexpr (Z) asm volatile("v_mfma_f32_16x16x32_bf16 %0, %1, %2, 0" : "=&v"(c) : "v"(a), "v"(b));
    else asm volatile("v_mfma_f32_16x16x32_bf16 %0, %1, %2, %0" : "+v"(c) : "v"(a), "v"(b));
}
template <bool Z, int R> DEV void mm32b(f16v& c, v4u a) {
    if constexpr (Z) asm volatile("v_mfma_f32_32x32x16_bf16 %0, %1, a[%c2:%c3], 0" : "=&v"(c) : "v"(a), "n"(R), "n"(R + 3));
    else asm volatile("v_mfma_f32_32x32x16_bf16 %0, %1, a[%c2:%c3], %0" : "+v"(c) : "v"(a), "n"(R), "n"(R + 3));
}
template <bool Z, int R> DEV void mm16b(f4& c, v4u a) {
    if constexpr (Z) asm volatile("v_mfma_f32_16x16x32_bf16 %0, %1, a[%c2:%c3], 0" : "=&v"(c) : "v"(a), "n"(R), "n"(R + 3));
    else asm volatile("v_mfma_f32_16x16x32_bf16 %0, %1, a[%c2:%c3], %0" : "+v"(c) : "v"(a), "n"(R), "n"(R + 3));
}
template <bool Z, int R> DEV void mm32a(f16v& c, v4u b) {
    if constexpr (Z) asm volatile("v_mfma_f32_32x32x16_bf16 %0, a[%c2:%c3], %1, 0" : "=&v"(c) : "v"(b), "n"(R), "n"(R + 3));
    else asm volatile("v_mfma_f32_32x32x16_bf16 %0, a[%c2:%c3], %1, %0" : "+v"(c) : "v"(b), "n"(R), "n"(R + 3));
}
template <int N, class T> DEV void pad(T& c) { asm volatile("s_nop %1" : "+v"(c) : "n"(N)); }
template <class T> DEV void pin(T& c) { asm volatile("" : "+v"(c)); }
DEV void nop2() { asm volatile("s_nop 1"); }
template <int R> DEV u32 rda() { u32 v; asm volatile("v_accvgpr_read_b32 %0, a%c1" : "=v"(v) : "n"(R)); return v; }
template <int R> DEV void wra(u32 v) { asm volatile("v_accvgpr_write_b32 a%c1, %0" ::"v"(v), "n"(R)); }
DEV __amdgpu_buffer_rsrc_t mkrs(const void* p, u32 n) { return __builtin_amdgcn_make_buffer_rsrc((void*)p, 0, (int)n, 0x00020000); }
DEV v4u mkv(const void* p, u32 n) { return (v4u){(u32)(uintptr_t)p, (u32)((uintptr_t)p >> 32), n, 0x00020000u}; }
DEV v4u lds4(u32 a) { return *(const LAS v4u*)a; }
DEV v2u lds2(u32 a) { return *(const LAS v2u*)a; }
DEV void barrier() { asm volatile("s_waitcnt lgkmcnt(0)\n\ts_barrier" ::: "memory"); }
template <int N> DEV void waitv() { asm volatile("s_waitcnt vmcnt(%0)" ::"n"(N) : "memory"); }
DEV void dma_in(const void* g, u32 voff, u32 lds) {
    asm volatile("s_mov_b32 m0, %2\n\ts_nop 0\n\tglobal_load_lds_dwordx4 %0, %1" ::"v"(voff), "s"(g), "s"(lds) : "m0", "memory");
}
DEV void dma_b(v4u rs, u32 voff, u32 lds) {
    asm volatile("s_mov_b32 m0, %2\n\ts_nop 0\n\tbuffer_load_dwordx4 %0, %1, 0 offen lds" ::"v"(voff), "s"(rs), "s"(lds) : "m0", "memory");
}
DEV void dma_s(v4u rs, u32 voff, u32 so, u32 lds) {
    asm volatile("s_mov_b32 m0, %3\n\ts_nop 0\n\tbuffer_load_dwordx4 %0, %1, %2 offen lds" ::"v"(voff), "s"(rs), "s"(so), "s"(lds) : "m0", "memory");
}
DEV void dma_ws(v4u rs, u32 voff, u32 so, u32 lds) {
    asm volatile("s_mov_b32 m0, %3\n\ts_nop 0\n\tbuffer_load_dwordx4 %0, %1, %2 offen sc1 lds" ::"v"(voff), "s"(rs), "s"(so), "s"(lds) : "m0", "memory");
}
template <int IM> DEV void gld4(v4u& d, u32 voff, const void* g) { asm volatile("global_load_dwordx4 %0, %1, %2 offset:%3" : "=v"(d) : "v"(voff), "s"(g), "n"(IM) : "memory"); }
DEV void gld2(v2u& d, u32 voff, const void* g) { asm volatile("global_load_dwordx2 %0, %1, %2" : "=v"(d) : "v"(voff), "s"(g) : "memory"); }
DEV void mm16k(f4& c, v2u a, v2u b) { asm volatile("v_mfma_f32_16x16x16_bf16 %0, %1, %2, 0" : "=&v"(c) : "v"(a), "v"(b)); }
DEV void mm16n(f4& c, v4u a, v4u b) { asm volatile("s_nop 1\n\tv_mfma_f32_16x16x32_bf16 %0, %1, %2, 0" : "=&v"(c) : "v"(a), "v"(b)); }
template <class A, class B> DEV void pin2(A& a, B& b) { asm volatile("" : "+v"(a), "+v"(b)); }
template <int IM> DEV void ld4(v4u& d, u32 voff, v4u rs, u32 so) { asm volatile("buffer_load_dwordx4 %0, %1, %2, %3 offen offset:%4 sc1" : "+v"(d) : "v"(voff), "s"(rs), "s"(so), "n"(IM) : "memory"); }
template <int IM> DEV void ld1(u32& d, u32 voff, v4u rs, u32 so) { asm volatile("buffer_load_dword %0, %1, %2, %3 offen offset:%4 sc1" : "+v"(d) : "v"(voff), "s"(rs), "s"(so), "n"(IM) : "memory"); }
template <int IM> DEV void ld4m(v4u& d, u32 voff, v4u rs, u32 so, u64 m) {
    asm volatile("s_mov_b64 exec, %5\n\tbuffer_load_dwordx4 %0, %1, %2, %3 offen offset:%4 sc1\n\ts_mov_b64 exec, -1" : "+v"(d) : "v"(voff), "s"(rs), "s"(so), "n"(IM), "s"(m) : "memory");
}
template <int IM, int R> DEV void ld4a(u32 voff, v4u rs, u32 so) { asm volatile("buffer_load_dwordx4 a[%c4:%c5], %0, %1, %2 offen offset:%3 sc1" ::"v"(voff), "s"(rs), "s"(so), "n"(IM), "n"(R), "n"(R + 3) : "memory"); }
template <int IM, int R> DEV void ld1a(u32 voff, v4u rs, u32 so) { asm volatile("buffer_load_dword a%c4, %0, %1, %2 offen offset:%3 sc1" ::"v"(voff), "s"(rs), "s"(so), "n"(IM), "n"(R) : "memory"); }
template <int IM, int R> DEV void ld4am(u32 voff, v4u rs, u32 so, u64 m) {
    asm volatile("s_mov_b64 exec, %6\n\tbuffer_load_dwordx4 a[%c4:%c5], %0, %1, %2 offen offset:%3 sc1\n\ts_mov_b64 exec, -1" ::"v"(voff), "s"(rs), "s"(so), "n"(IM), "n"(R), "n"(R + 3), "s"(m) : "memory");
}
template <int I> struct IC { static constexpr int v = I; };
template <int B, int E, class F> DEV void sfor(F&& f) { if constexpr (B < E) { f(IC<B>{}); sfor<B + 1, E>(f); } }
#define LAM(I) [&](auto I) __attribute__((always_inline))
#define FENCE() __builtin_amdgcn_sched_barrier(0)
#define OPQV(x) asm volatile("" : "+v"(x))
#define OPQS(x) asm volatile("" : "+s"(x))
DEV float halves_sum(float a, float b) {
    auto r = __builtin_amdgcn_permlane32_swap(asu(a), asu(b), false, false);
    return asf(r[0]) + asf(r[1]);
}
DEV bool bad(u32 m) { return __builtin_amdgcn_ballot_w64(m == ~0u) != 0; }
#define STAMP_INIT()
#define STAMP(i) asm volatile("; MARK " #i)
#define STAMP_END()

DEV void hedgehog(const float (&t)[18], float a, u32 (&ph)[18]) {
    float mx = t[0], mn = t[0];
#pragma unroll
    for (int i = 1; i < 18; ++i) { mx = __builtin_fmaxf(mx, t[i]); mn = __builtin_fminf(mn, t[i]); }
    const float c1 = -a * mx, c2 = a * mn;
    float e1[18], e2[18], s1 = 0.f, s2 = 0.f;
#pragma unroll
    for (int i = 0; i < 18; ++i) {
        e1[i] = __builtin_amdgcn_exp2f(__builtin_fmaf(t[i], a, c1)); s1 += e1[i];
        e2[i] = __builtin_amdgcn_exp2f(__builtin_fmaf(t[i], -a, c2)); s2 += e2[i];
    }
    const float r1 = __builtin_amdgcn_rcpf(s1), r2 = __builtin_amdgcn_rcpf(s2);
#pragma unroll
    for (int i = 0; i < 9; ++i) { ph[i] = pk(e1[2 * i] * r1, e1[2 * i + 1] * r1); ph[9 + i] = pk(e2[2 * i] * r2, e2[2 * i + 1] * r2); }
}
DEV v4u tr2(u32 a0, u32 a1) {
    s4 lo = __builtin_amdgcn_ds_read_tr16_b64_v4i16((LAS s4*)a0), hi = __builtin_amdgcn_ds_read_tr16_b64_v4i16((LAS s4*)a1);
    v2u l = __builtin_bit_cast(v2u, lo), h = __builtin_bit_cast(v2u, hi);
    return (v4u){l.x, l.y, h.x, h.y};
}
DEV v2u y_val(f4 acc, u32 ua, float Dv) {
    const v2u uu = lds2(ua);
    const float y0 = rb(acc[0]) + rb(bf_hi(uu.y) * Dv), y1 = rb(acc[1]) + rb(bf_lo(uu.y) * Dv), y2 = rb(acc[2]) + rb(bf_hi(uu.x) * Dv), y3 = rb(acc[3]) + rb(bf_lo(uu.x) * Dv);
    return (v2u){pk(y0, y1), pk(y2, y3)};
}
DEV v4u y_pair(v2u a, v2u b) {
    auto r0 = __builtin_amdgcn_permlane16_swap(a.x, b.x, false, false), r1 = __builtin_amdgcn_permlane16_swap(a.y, b.y, false, false);
    return (v4u){r0[0], r1[0], r0[1], r1[1]};
}
template <int N, int n, int r>
DEV void conv_read(u32 aa, u32 ba, v4u (&A)[N], v4u (&B)[N]) {
    constexpr int i = n / 3, k = n % 3;
    if constexpr (k == 0) B[i] = lds4(ba + 64 * (8 * i - r + 7));
    else { const v2u v = lds2(aa + 2 + 64 * (r + 8 * i) + 8 * (k - 1)); if constexpr (k == 1) { A[i].x = v.x; A[i].y = v.y; } else { A[i].z = v.x; A[i].w = v.y; } }
}
constexpr int conv_ij(int N, int MASK, int n) {
    for (int d = 0; d < N; ++d) for (int i = 0; i < N; ++i) for (int j = i; j < N; ++j)
        if (((MASK >> j) & 1) && (i > j - i ? i : j - i) == d && n-- == 0) return i * 8 + j;
    return -1;
}
constexpr bool conv_first(int N, int MASK, int n) {
    for (int m = 0; m < n; ++m) if ((conv_ij(N, MASK, m) & 7) == (conv_ij(N, MASK, n) & 7)) return false;
    return true;
}
constexpr int u_step(int I) { int n = 0; for (int i = 0; i < I; ++i) n += i / 5 - i % 5 >= 0 && i / 5 - i % 5 < 5; return n; }
constexpr int NSITE = 73, SITE_END = 196;
constexpr int site_at(int q) { return q <= SITE_END ? (q * NSITE + SITE_END - 1) / SITE_END : NSITE; }

__global__ void __launch_bounds__(256, 1) k_main(const u16* __restrict__ x, const u16* __restrict__ wqkv, const u16* __restrict__ wphi, const u16* __restrict__ wo,
                                                 const u16* __restrict__ bk, const u16* __restrict__ bv, const u16* __restrict__ sk, const u16* __restrict__ sh,
                                                 const u16* __restrict__ sD, u16* __restrict__ out, char* __restrict__ ws) {
    const int tid = __builtin_amdgcn_workitem_id_x(), lane = tid & 63, wave = __builtin_amdgcn_readfirstlane(tid >> 6);
    const int b = __builtin_amdgcn_workgroup_id_x(), rg = 2 * (b & 7) + (b >> 7), p = (b >> 3) & 15;
    const int Pg = p >> 2, tw = p & 3, pw = 4 * Pg + wave, ci = (rg + p) & 15;
    const bool conv = ci < 12;
    constexpr u32 lb = 0;
    STAMP_INIT();
    asm volatile("" ::: "a255");
    u32 l16 = lane * 16, l4 = lane * 4;
    OPQV(l16); OPQV(l4);
    const int ch0 = 36 * p + 3 * ci;
    u32 xr[9], kvw0, kvw1;
    asm volatile("" : "+s"(x), "+s"(wqkv), "+s"(wphi), "+s"(wo), "+s"(bk), "+s"(bv), "+s"(sk), "+s"(sh), "+s"(sD), "+s"(out), "+s"(ws));
    STAMP(40);
    u64 ep;
    asm volatile("s_load_dwordx2 %0, %1, %2" : "=s"(ep) : "s"(ws), "s"(W_E));
    const __amdgpu_buffer_rsrc_t rw = mkrs(ws, W_END);
    const v4u vw = mkv(ws, W_END);
    {
        const int sec = p & 3, cg = 2 * Pg + (p & 1), tg = 2 * rg + ((p >> 1) & 1);
        const int c = lane & 15;
        v2u sw0, sw1;
        {
            const char* sp = (const char*)sk + cg * 576;
            asm volatile("global_load_dwordx2 %0, %1, %2" : "=v"(sw0) : "v"(c * 8 + wave * 128), "s"(sp) : "memory");
            asm volatile("global_load_dwordx2 %0, %1, %2 offset:512" : "=v"(sw1) : "v"((c & 7) * 8), "s"(sp) : "memory");
            const v4u rk = mkv(bk, 2048 * 1152), rv = mkv(bv, 2048 * 1152);
            const u32 lo = ((lane * 7282u) >> 16) * 1008 + l16, t0 = (64 * tg + 7 * wave) * 1152 + cg * 144;
#pragma unroll
            for (int i = 0; i < 5; ++i) {
                const int isv = i < 2 ? 0 : i > 2 ? 1 : wave >> 1;
                v4u rs = i < 2 ? rk : rv;
                if (i == 2) { rs.x = isv ? rv.x : rk.x; rs.y = isv ? rv.y : rk.y; }
                dma_b(rs, lo + (t0 + (28 * i - 3 - 67 * isv) * 1152), P_KS + wave * 1008 + i * 4032 + isv * (P_VS - 10080));
            }
        }
        FENCE();
        STAMP(41);
        {
            const u32 wst = sec < 3 ? 3456 : 1152;
            const v4u rq = mkv(sec < 3 ? wqkv : wo, 576 * wst), rp = mkv(wphi, sec & 1 ? 0 : 32 * 648), rx = mkv(x, 2048 * 1152);
            const u32 lw = ((lane * 3641u) >> 16) * (wst - 288) + l16, w0 = (36 * rg + 3 * wave) * wst + (sec < 2 ? (sec + 1) * 1152 : 0) + Pg * 288;
#pragma unroll
            for (int i = 0; i < 3; ++i) dma_b(rq, lw + (w0 + 12 * i * wst), P_WS + wave * 864 + i * 3456);
            const u32 lx = ((lane * 3450u) >> 16) * 848 + l16 + ((128 * rg + 32 * tw + 3 * wave) * 1152 + Pg * 288);
#pragma unroll
            for (int i = 0; i < 3; ++i) dma_b(rx, lx + 12 * i * 1152, P_XS + wave * 912 + i * 3648);
            const u32 lp = l16 - ((lane * 21846u) >> 16) * 12 + (Pg * 5184 + wave * 756);
#pragma unroll
            for (int i = 0; i < 2; ++i) dma_b(rp, lp + i * 3024, P_PH + wave * 1008 + i * 4032);
        }
        FENCE();
        STAMP(44);
        const v4u z4 = {0, 0, 0, 0};
        u32 mk0, mk1, ua0, ua1, uo0, uo1, aw[4], ap[8], dst[2], ds, dr, cso, axs, aid, azc;
        v2u Bi; v4u Sel;
        {
            const u32 g = lane >> 4, m = lane & 31, kh = lane >> 5, s3 = sec == 3;
            const u32 hm = g == (c >> 2) ? ((c & 1) ? 0xffff0000u : 0xffffu) : 0u;
            mk0 = (c & 2) ? 0u : hm; mk1 = (c & 2) ? hm : 0u;
            Bi = (v2u){mk0 & 0x3f803f80u, mk1 & 0x3f803f80u}; Sel = (v4u){Bi.x, Bi.y, Bi.x, Bi.y};
            const u32 r0 = (8 * (c >> 2) + (c & 3)) * 144;
            ua0 = r0 + 8 * g + (P_KS + 32 * wave);
            ua1 = r0 + 32 * 144 * kh + 8 * (g & 1) + (P_KS + 128 + 576 * (wave & 1));
            uo0 = c * 4096 + 16 * g;
            uo1 = (c & 7) * 4096 + 64 * (c >> 3) + 16 * g + (wave < 2 ? 64 * 4096 + 8 - 8 * wave : OOB - (1u << 20));
            pin2(mk0, mk1); pin2(Bi, Sel); pin2(ua0, ua1); pin2(uo0, uo1);
            const u32 hD = (m >> 2) & 1, ra = (18 * hD + (m & 3) + 4 * (m >> 3)) * 288 + 16 * kh, rl = (18 * hD + (m & 1)) * 288 + 16 * kh;
            const u32 d0 = 8 * kh + (c >> 2), pa = d0 * 48 + 32 * (g & 1) + 8 * (c & 3);
#pragma unroll
            for (int hh = 0; hh < 2; ++hh) {
                const u32 co = P_WS + ((72 * wave + 36 * hh) & ~15u), sf = ((4 * wave) & 7) + 2 * hh;
                aw[hh] = ra + co; aw[2 + hh] = rl + (co + 16 * 288);
#pragma unroll
                for (int q = 0; q < 4; ++q) {
                    const u32 k = 16 * (q >> 1) + 4 * (q & 1) - sf;
                    ap[4 * hh + q] = d0 + k < 18u ? pa + (P_PH + ((2 * wave + hh) * 18 + k) * 48) : L_Z;
                }
                pin2(aw[hh], aw[2 + hh]); pin2(ap[4 * hh], ap[4 * hh + 1]); pin2(ap[4 * hh + 2], ap[4 * hh + 3]);
            }
            const u32 ib = W_IMG + wave * IMG;
            const u32 bA = s3 ? W_WOT + wave * 36864 : ib + sec * L_V + (sec >> 1) * (L_Q - 2 * L_V);
            const u32 bB = s3 ? W_WOT + 4 * 36864 + 64 * wave : ib + L_QT + ((sec & 1) << 6) + ((sec >> 1) << 8);
            const u32 oA = ((m & 3) + 8 * (m >> 2) + 32 * kh) * 16 + bA, oB = m < 18 ? (m & 1) * (32 - 16 * s3) + kh * (16 + 496 * s3) + bB : OOB;
            const bool lo = m < 16;
            dst[0] = lo ? oA : oB;
            dst[1] = dst[0] + (lo ? 64 : (0x20408080u >> (8 * sec)) & 0xff);
            ds = lo ? 1024 : QTS - (QTS - 256) * s3;
            dr = lo ? 2048 : 2 * QTS + (1024 - 2 * QTS) * s3;
            cso = lane < 18 - 18 * s3 ? m * 64 + (lo ? sec * 1024 : (32 + sec * 2) * 64) + (W_CSP + wave * 7168) : OOB;
            axs = m * 304 + 36 * kh + (P_XS + 72 * wave);
            aid = lane < 36 * (sec & 1) ? lane * 50 + (lane >= 18 ? 864 - 900 : 0) + (P_PH + wave * 1728) : L_END - 16;
            azc = lane < (sec ? 0 : 6) ? (lane & 1) * 16 + ((lane >> 1) + (lane < 4 ? 2 * rg : 30 + (rg & 3))) * QTS + (ib + L_QT + 384) : OOB;
            pin2(dst[0], dst[1]); pin2(ds, dr); pin2(cso, axs); pin2(aid, azc);
        }
        if (tid < 38) *(LAS v2u*)(L_KVA + tid * 80 + 72) = (v2u){0, 0};
        if (tid < 5) *(LAS v4u*)(L_Z + tid * 16) = z4;
        if (wave && lane < 30) *(LAS v4u*)(L_Q + (wave - 1) * CVS + 4 * HCS + l16) = z4;
        FENCE();
        STAMP(26);
        asm volatile("s_waitcnt vmcnt(13)" : "+v"(sw0), "+v"(sw1)::"memory");
        v2u Bd[2][4];
#pragma unroll
        for (int s = 0; s < 2; ++s)
#pragma unroll
            for (int j = 0; j < 4; ++j) {
                const v2u sw = s ? sw1 : sw0;
                const u32 wd = __builtin_amdgcn_perm(0u, j < 2 ? sw.y : sw.x, (j & 1) ? 0x01000100u : 0x03020302u);
                Bd[s][j] = (v2u){wd & mk0, wd & mk1};
                pin(Bd[s][j]);
            }
        FENCE();
        waitv<8>();
        barrier();
        STAMP(27);
        {
            u32 uo[5][2];
            v2u ka[5][5]; f4 pr[5][5], sm[5];
            const u32 so = W_UT + 128 * tg + cg * 72 * 4096;
            v4u ones = {~0u, ~0u, ~0u, ~0u};
            pin(ones);
            auto late = LAM(N_) {
                constexpr int n = N_.v;
                const u32 oo = W_O + ((rg * 4 + wave) * 16 + p) * OS;
                if constexpr (n == 1) __builtin_amdgcn_raw_buffer_store_b128(ones, rw, l16, oo, SC1);
                if constexpr (n == 3) __builtin_amdgcn_raw_buffer_store_b128(ones, rw, l16 + 1024, oo, SC1);
                if constexpr (n == 5) __builtin_amdgcn_raw_buffer_store_b128(ones, rw, lane < 16 ? l16 + 2048 : OOB, oo, SC1);
                if constexpr (n == 7) __builtin_amdgcn_raw_buffer_store_b128(ones, rw, lane < 44 ? l16 : OOB, W_KV + (p * 16 + rg) * 2816 + wave * 704, SC1);
            };
            auto rd = LAM(T_) {
                constexpr int T = T_.v, im = T < 4 ? (32 * (T >> 1) + 4 * (T & 1)) * 144 : 0;
#pragma unroll
                for (int j = 0; j < 5; ++j) ka[T][j] = lds2((T < 4 ? ua0 : ua1) + im + (j < 4 ? j * 144 : P_VS));
            };
            rd(IC<0>{}); rd(IC<1>{}); rd(IC<2>{});
            FENCE();
            sfor<0, 45>(LAM(I_) {
                constexpr int k = I_.v / 5, ph = I_.v % 5, T = k - ph;
                if constexpr (T >= 0 && T < 5) {
                    if constexpr (ph == 0) {
#pragma unroll
                        for (int j = 0; j < 5; ++j) mm16k(pr[T][j], ka[T][j], j < 4 ? Bd[T == 4][j] : Bi);
                        if constexpr (T + 3 < 5) rd(IC<T + 3>{});
                    } else if constexpr (ph < 4) {
                        f4& a = ph == 1 ? pr[T][0] : sm[T];
                        pin2(a, pr[T][ph]);
                        const v4u bb = {pk(a[0], a[1]), pk(a[2], a[3]), pk(pr[T][ph][0], pr[T][ph][1]), pk(pr[T][ph][2], pr[T][ph][3])};
                        mm16n(sm[T], Sel, bb);
                    } else {
                        pin2(sm[T], pr[T][4]);
                        const u32 w0 = pk(sm[T][0], sm[T][1]), w1 = pk(sm[T][2], sm[T][3]);
                        uo[T][0] = pk(bf_hi(w1) * pr[T][4][3], bf_lo(w1) * pr[T][4][2]);
                        uo[T][1] = pk(bf_hi(w0) * pr[T][4][1], bf_lo(w0) * pr[T][4][0]);
                        if constexpr (T == 1 || T == 3) __builtin_amdgcn_raw_buffer_store_b128((v4u){uo[T][0], uo[T][1], uo[T - 1][0], uo[T - 1][1]}, rw, uo0 + 32 * (T - 1), so + wave * 65536, SC1);
                        else if constexpr (T == 4) __builtin_amdgcn_raw_buffer_store_b64((v2u){uo[4][0], uo[4][1]}, rw, uo1, so, SC1);
                        else pin2(uo[T][0], uo[T][1]);
                    }
                    late(IC<u_step(I_.v)>{});
                    FENCE();
                }
            });
        }
        STAMP(28);
        asm volatile("s_waitcnt vmcnt(7)\n\ts_barrier" ::: "memory");
        {
            u32 one2 = 0x3f803f80u;
            OPQS(one2);
            *(LAS u16*)(aid) = 0x3f80;
            v4u bw[2][2], wa[2][2][2];
#pragma unroll
            for (int hh = 0; hh < 2; ++hh) {
                bw[hh][0] = tr2(ap[4 * hh], ap[4 * hh + 1]); wa[hh][0][0] = lds4(aw[hh]);
                bw[hh][1] = tr2(ap[4 * hh + 2], ap[4 * hh + 3]); wa[hh][0][1] = lds4(aw[hh] + 32);
                wa[hh][1][0] = lds4(aw[2 + hh]); wa[hh][1][1] = lds4(aw[2 + hh] + 32);
            }
            FENCE();
#pragma unroll
            for (int j = 0; j < 9; ++j) xr[j] = *(const LAS u32*)(axs + 4 * j);
            FENCE();
            f16v d[2][2];
#pragma unroll
            for (int i = 0; i < 4; ++i) { mm32<true>(d[i >> 1][i & 1], wa[i >> 1][i & 1][0], bw[i >> 1][0]); mm32<false>(d[i >> 1][i & 1], wa[i >> 1][i & 1][1], bw[i >> 1][1]); }
            FENCE();
            const u32 sb = Pg * (sec < 3 ? 4 * IMG : WOG);
            {
                float s1 = 0.f, s2 = 0.f;
#pragma unroll
                for (int j = 0; j < 9; ++j) { s1 = dot2(xr[j], one2, s1); s2 = dot2(xr[j], xr[j], s2); }
                const u32 so = W_XF0 + ((rg * 4 + tw) * 16 + pw) * XS;
                __builtin_amdgcn_raw_buffer_store_b128((v4u){xr[0], xr[1], xr[2], xr[3]}, rw, l16, so, SC1);
                __builtin_amdgcn_raw_buffer_store_b128((v4u){xr[4], xr[5], xr[6], xr[7]}, rw, l16 + 1024, so, SC1);
                __builtin_amdgcn_raw_buffer_store_b32(xr[8], rw, l4 + 2048, so, SC1);
                __builtin_amdgcn_raw_buffer_store_b32(asu(halves_sum(s1, s2)), rw, l4 + 2304, so, SC1);
                __builtin_amdgcn_raw_buffer_store_b128(z4, rw, azc, sb, SC1);
            }
            FENCE();
            const u32 ro = rg * dr, rl = (16 + (rg >> 3)) * dr + ((rg >> 2) & 1) * ds;
#pragma unroll
            for (int hh = 0; hh < 2; ++hh) {
                asm volatile("" : "+v"(d[hh][0]), "+v"(d[hh][1]));
                u32 w[9]; float cs = 0.f;
#pragma unroll
                for (int j = 0; j < 9; ++j) { w[j] = j < 8 ? pk(d[hh][0][2 * j], d[hh][0][2 * j + 1]) : pk(d[hh][1][0], d[hh][1][1]); cs = dot2(w[j], one2, cs); }
                const u32 o = dst[hh];
                __builtin_amdgcn_raw_buffer_store_b128((v4u){w[0], w[1], w[2], w[3]}, rw, o + ro, sb, SC1);
                __builtin_amdgcn_raw_buffer_store_b128((v4u){w[4], w[5], w[6], w[7]}, rw, o + ro + ds, sb, SC1);
                __builtin_amdgcn_raw_buffer_store_b32(w[8], rw, o + rl, sb + 4 * (rg & 3), SC1);
                __builtin_amdgcn_raw_buffer_store_b32(asu(halves_sum(cs, cs)), rw, cso + hh * 56 * 64, Pg * 4 * 7168 + rg * 4, SC1);
                FENCE();
            }
        }
        v4u hd[3]; v2u he[3], hz;
        {
            const char* gp = (const char*)sh + umin(ch0, 573) * 4096;
            const u32 o1 = tid * 16, o2 = (o1 + 16) & 4095;
            gld2(hz, umin(lane >> 3, 2) * 4096, gp);
#pragma unroll
            for (int i = 0; i < 3; ++i) { gld4<0>(hd[i], o1, gp + i * 4096); gld2(he[i], o2, gp + i * 4096); }
        }
        STAMP(29);
        asm volatile("s_waitcnt vmcnt(7)\n\ts_barrier" ::: "memory");
        OPQS(ep);
        ep += 1;
        const v2u epv = {(u32)ep, (u32)(ep >> 32)};
        if (tid == 0) __builtin_amdgcn_raw_buffer_store_b64(epv, rw, 0, W_F + (rg * 16 + p) * 128, SC1);
        STAMP(1);
        {
            u32 o[3];
#pragma unroll
            for (int j = 0; j < 3; ++j) {
                const u32 pi = 16 * (wave + 4 * j) + c, hh = pi >= 88, ip = pi - 88 * hh, e = (lane >> 4) & 1, s = 2 * (ip - 72) + e;
                const u32 s6 = (s * 43) >> 8, sr = s - 6 * s6, s3 = sr >= 3;
                const u32 v0 = (ip & 15) * 80 + (ip >> 4) * 8 + 32 * e, v1 = (2 * ip + e) * 80 + 64 - 128 * 80, v2 = (16 + sr - 3 * s3) * 80 + s3 * 32 + s6 * 8, v3 = s * 80 + 64 - 8 * 80;
                u32 v = ip < 64 ? v0 : v1;
                v = ip < 72 ? v : v2;
                v = ip >= 84 ? v3 : v;
                o[j] = (ip >= 72 && s >= 27) || lane >= 32 || pi >= 176 ? 0xffffu : v + hh * 1520;
            }
            kvw0 = o[0] | (o[1] << 16); kvw1 = o[2];
            OPQV(kvw0); OPQV(kvw1);
        }
        FENCE();
        if (wave) waitv<0>(); else waitv<1>();
        STAMP(47);
        if (conv) {
            u32 o = L_Q + 32 + tid * 16;
            OPQV(o);
#pragma unroll
            for (int i = 0; i < 3; ++i) {
                asm volatile("" : "+v"(hd[i]), "+v"(he[i]));
                const u32 d0 = hd[i].x, d1 = hd[i].y, d2 = hd[i].z, d3 = hd[i].w, d4 = he[i].x, d5 = he[i].y;
                const u32 a0 = __builtin_amdgcn_alignbit(d1, d0, 16), a1 = __builtin_amdgcn_alignbit(d2, d1, 16), a2 = __builtin_amdgcn_alignbit(d3, d2, 16),
                          a3 = __builtin_amdgcn_alignbit(d4, d3, 16), a4 = __builtin_amdgcn_alignbit(d5, d4, 16);
                *(LAS v4u*)(o + i * CVS) = hd[i];
                *(LAS v4u*)(o + i * CVS + HCS) = (v4u){a0, a1, a2, a3};
                *(LAS v4u*)(o + i * CVS + 2 * HCS) = (v4u){d1, d2, d3, d4};
                *(LAS v4u*)(o + i * CVS + 3 * HCS) = (v4u){a1, a2, a3, a4};
            }
            asm volatile("" : "+v"(hz));
            const u32 k = (lane >> 1) & 3;
            const u64 t = (lane & 1) && k ? (((u64)hz.y << 32) | hz.x) << (64 - 16 * k) : 0;
            if (tid < 24) *(LAS v4u*)(L_Q + (lane >> 3) * CVS + k * HCS + (lane & 1) * 16) = (v4u){0, 0, (u32)t, (u32)(t >> 32)};
        }
        if (wave < 2) {
            const u32 fo = W_F + (wave ? rg * 16 + c : (lane >> 2) * 16 + 4 * Pg + (lane & 3)) * 128;
#pragma unroll 1
            for (int sp = 0; sp < SPIN; ++sp) {
                asm volatile("" ::: "memory");
                const v2u f = __builtin_amdgcn_raw_buffer_load_b64(rw, fo, 0, SC1);
                if (__builtin_amdgcn_ballot_w64(f.x != epv.x || f.y != epv.y) == 0) break;
                __builtin_amdgcn_s_sleep(1);
            }
        }
        barrier();
    }
    STAMP(2);
    u32 st[16];
    {
        float Dq[3];
#pragma unroll
        for (int i = 0; i < 3; ++i) { const u32 cc = umin(ch0, 573) + i, t = ((const __attribute__((address_space(4))) u32*)(uintptr_t)sD)[cc >> 1]; Dq[i] = asf(cc & 1 ? t & 0xffff0000u : t << 16); }
        const u32 ib = W_IMG + p * IMG, xb = W_XF0 + (rg * 4 + wave) * 16 * XS;
        if (conv) {
#pragma unroll
            for (int i = 0; i < 3; ++i) dma_ws(vw, l16, W_UT + (ch0 + i) * 4096 + wave * 1024, lb + L_Q + i * CVS + 4 * HCS + 480 + wave * 1024);
        }
        v4u cp[4];
        sfor<0, 4>(LAM(I_) { asm volatile("" : "=v"(cp[I_.v])); ld4<16 * I_.v>(cp[I_.v], tid < 112 ? tid * 64 : OOB, vw, W_CSP + p * 7168); });
#pragma unroll 1
        for (int i = 0; i < 13; ++i) { const u32 o = i < 9 ? (i * 4 + wave) * 1024 : L_QT + umin((i - 9) * 4 + wave, 14) * 1024; dma_ws(vw, l16, ib + o, lb + o); }
        u32 xo = xb;
        auto site = LAM(N_) {
            constexpr int n = N_.v;
            if constexpr (n < 9) dma_ws(vw, l16, ib + L_V + (n * 4 + wave) * 1024, lb + L_V + (n * 4 + wave) * 1024);
            else {
                constexpr int i = (n - 9) >> 2, k = (n - 9) & 3;
                if constexpr (k == 0) ld4a<0, 8 * i>(l16, vw, xo);
                if constexpr (k == 1) ld1a<2048, 128 + i>(l4, vw, xo);
                if constexpr (k == 2) ld4a<1024, 8 * i + 4>(l16, vw, xo);
                if constexpr (k == 3) { ld1a<2304, 144 + i>(l4, vw, xo); xo += XS; OPQS(xo); }
            }
        };
        {
            asm volatile("s_waitcnt vmcnt(13)" : "+v"(cp[0]), "+v"(cp[1]), "+v"(cp[2]), "+v"(cp[3])::"memory");
            float cs = 0.f;
#pragma unroll
            for (int i = 0; i < 16; ++i) { const u32 t = cp[i >> 2][i & 3]; cs += asf(t); }
            if (tid < 112) *(LAS float*)(lb + L_CS + tid * 4) = cs;
        }
        if (conv) {
            asm volatile("s_barrier" ::: "memory");
            STAMP(3);
            const int bq = lane & 15, g = lane >> 4, cc = (bq + 1) & 3;
            const u32 aoff = lb + L_Q + cc * HCS + 2 * (bq - 8 * g + 24 - cc), boff = lb + L_Q + 4 * HCS + 480 + 2 * (16 * bq + 8 * g - 16) - 7 * 64;
            const u32 uoff = lb + L_Q + 4 * HCS + 480 + 32 * bq + 16 * (g >> 1) + ((g & 1) ? 0 : 8), yo = 32 * bq + 8 * g, yp = 32 * bq + 16 * (g >> 1) + 512 * (g & 1);
            if (wave < 3) {
                u32 aa = aoff + wave * CVS, ba = boff + wave * CVS;
                OPQV(aa); OPQV(ba);
                f4 acc[8];
                v4u A[2][8], B[2][8];
                sfor<0, 24>(LAM(N_) { conv_read<8, N_.v, 0>(aa, ba, A[0], B[0]); });
                FENCE();
                sfor<0, 8 * 27>(LAM(Q_) {
                    constexpr int r = Q_.v / 27, n = Q_.v % 27, i = conv_ij(8, 0xf1, n) >> 3, j = conv_ij(8, 0xf1, n) & 7;
                    mm16<r == 0 && conv_first(8, 0xf1, n)>(acc[j], A[r & 1][i], B[r & 1][j - i]);
                    if constexpr (r < 7 && n < 24) conv_read<8, n, r + 1>(aa, ba, A[(r + 1) & 1], B[(r + 1) & 1]);
                    sfor<site_at(Q_.v), site_at(Q_.v + 1)>(site);
                    FENCE();
                });
                asm volatile("s_nop 7" : "+v"(acc[0]), "+v"(acc[4]), "+v"(acc[5]), "+v"(acc[6]), "+v"(acc[7]));
                const float Dv = wave == 0 ? Dq[0] : wave == 1 ? Dq[1] : Dq[2];
                const u32 so = W_Y + (ch0 + wave) * 4096, ua = uoff + wave * CVS;
                const v2u y0 = y_val(acc[0], ua, Dv);
                const v4u y4 = y_pair(y_val(acc[4], ua + 2048, Dv), y_val(acc[5], ua + 2560, Dv)), y6 = y_pair(y_val(acc[6], ua + 3072, Dv), y_val(acc[7], ua + 3584, Dv));
                asm volatile("s_waitcnt vmcnt(0)" ::"v"(y0), "v"(y4), "v"(y6) : "memory");
                __builtin_amdgcn_raw_buffer_store_b64(y0, rw, yo, so, SC1);
                __builtin_amdgcn_raw_buffer_store_b128(y4, rw, yp + 2048, so, SC1);
                __builtin_amdgcn_raw_buffer_store_b128(y6, rw, yp + 3072, so, SC1);
            } else {
                sfor<0, 3>(LAM(C_) {
                    constexpr int cn = C_.v;
                    u32 aa = aoff + cn * CVS, ba = boff + cn * CVS;
                    OPQV(aa); OPQV(ba);
                    f4 acc[4];
                    v4u A[2][4], B[2][4];
                    sfor<0, 12>(LAM(N_) { conv_read<4, N_.v, 0>(aa, ba, A[0], B[0]); });
                    FENCE();
                    sfor<0, 8 * 9>(LAM(Q_) {
                        constexpr int r = Q_.v / 9, n = Q_.v % 9, i = conv_ij(4, 0xe, n) >> 3, j = conv_ij(4, 0xe, n) & 7;
                        mm16<r == 0 && conv_first(4, 0xe, n)>(acc[j], A[r & 1][i], B[r & 1][j - i]);
                        if constexpr (r < 7) { conv_read<4, n, r + 1>(aa, ba, A[(r + 1) & 1], B[(r + 1) & 1]); if constexpr (n < 3) conv_read<4, 9 + n, r + 1>(aa, ba, A[(r + 1) & 1], B[(r + 1) & 1]); }
                        sfor<site_at(72 * cn + Q_.v), site_at(72 * cn + Q_.v + 1)>(site);
                        FENCE();
                    });
                    asm volatile("s_nop 7" : "+v"(acc[1]), "+v"(acc[2]), "+v"(acc[3]));
                    const float Dv = Dq[cn];
                    const u32 so = W_Y + (ch0 + cn) * 4096, ua = uoff + cn * CVS;
                    const v2u y1 = y_val(acc[1], ua + 512, Dv);
                    const v4u y2 = y_pair(y_val(acc[2], ua + 1024, Dv), y_val(acc[3], ua + 1536, Dv));
                    if constexpr (cn == 2) asm volatile("s_waitcnt vmcnt(0)" ::"v"(y1), "v"(y2) : "memory");
                    __builtin_amdgcn_raw_buffer_store_b64(y1, rw, yo + 512, so, SC1);
                    __builtin_amdgcn_raw_buffer_store_b128(y2, rw, yp + 1024, so, SC1);
                });
            }
        } else {
            sfor<0, NSITE>(site);
            waitv<0>();
        }
        STAMP(14);
        sfor<0, 16>(LAM(I_) { st[I_.v] = rda<144 + I_.v>(); });
    }
    u32 yv[9];
#pragma unroll
    for (int i = 0; i < 9; ++i) yv[i] = 0;
    v4u wv[17];
#pragma unroll
    for (int i = 0; i < 17; ++i) asm volatile("" : "=v"(wv[i]));
    barrier();
    STAMP(15);

#pragma unroll 1
    for (int L = 0; L < 2; ++L) {
        int Ls = L;
        OPQS(Ls);
        u64 m0_ = (u64)(long long)(Ls - 1), m1_ = (u64)(long long)(-Ls);
        OPQS(m0_); OPQS(m1_);
        int ln = lane;
        OPQV(ln); OPQV(l16); OPQV(l4);
        const int tl = ln & 31, h = ln >> 5, row = ln & 15, kg = ln >> 4, gq = row >> 2, rq = row & 3;
        const bool qa = (gq & 1) == (kg & 1);
        u32 aK_ = lb + L_K + l16, aV_ = lb + L_V + l16;
        u32 aKV = lb + L_QT + (qa ? (((gq >> 1) * 4 + rq) * 2 + (kg >> 1)) * 16 : 400 - 16 * (kg >> 1));
        u32 aQq = lb + L_QT + (qa && rq < 2 ? 256 + (((gq >> 1) * 2 + rq) * 2 + (kg >> 1)) * 16 : 384);
        u32 aCS = lb + L_CS + h * 224;
        OPQV(aK_); OPQV(aV_); OPQV(aKV); OPQV(aQq); OPQV(aCS);
        const u32 wb = W_WOT + Pg * WOG + wave * 36864, wlb = W_WOT + Pg * WOG + 4 * 36864 + (wave >> 1) * 9216;
        u32 wso = wb;
        auto wsite = LAM(N_) {
            constexpr int n = N_.v;
            if constexpr (n == 36) wso = wlb;
            if constexpr ((n < 36 ? n : n - 36) % 4 == 0) OPQS(wso);
            if constexpr (n < 28) ld4am<(n % 4) * 1024, 144 + 4 * n>(l16, vw, wso, m0_);
            else ld4m<((n < 36 ? n : n - 36) % 4) * 1024>(wv[n - 28], l16, vw, wso, m0_);
            if constexpr ((n < 36 ? n : n - 36) % 4 == 3) wso += 4096;
        };
        f16v aK; f4 qKV;
        float mean, al, rstd;
        v4u rV[4], cK[5];
        if (L == 0) {
            const v4u ones = {~0u, ~0u, ~0u, ~0u};
            const u32 s1 = W_XF1 + ((rg * 4 + tw) * 16 + pw) * XS, s2 = W_O + (1024 + (rg * 4 + wave) * 16 + p) * OS;
            __builtin_amdgcn_raw_buffer_store_b128(ones, rw, l16, s1, SC1);
            __builtin_amdgcn_raw_buffer_store_b128(ones, rw, l16 + 1024, s1, SC1);
            __builtin_amdgcn_raw_buffer_store_b128(ones, rw, ln < 32 ? l16 + 2048 : OOB, s1, SC1);
            __builtin_amdgcn_raw_buffer_store_b128(ones, rw, l16, s2, SC1);
            __builtin_amdgcn_raw_buffer_store_b128(ones, rw, l16 + 1024, s2, SC1);
            __builtin_amdgcn_raw_buffer_store_b128(ones, rw, ln < 16 ? l16 + 2048 : OOB, s2, SC1);
            __builtin_amdgcn_raw_buffer_store_b128(ones, rw, ln < 32 ? row * 16 + (kg & 1) * 1024 * 256 : OOB, W_ST + ((rg * 4 + tw) * 16 + pw) * 256, SC1);
            __builtin_amdgcn_raw_buffer_store_b128(ones, rw, ln < 44 ? l16 : OOB, W_KV + (256 + p * 16 + rg) * 2816 + wave * 704, SC1);
        }
#pragma unroll 1
        for (int sp = 0; sp < SPIN; ++sp) {
            u32 xo = W_XF1 + (rg * 4 + wave) * 16 * XS, sm = 0;
            OPQS(xo);
            auto xsite = LAM(N_) {
                constexpr int n = N_.v;
                ld4am<(n & 1) * 1024, 4 * n>(l16, vw, xo, m1_);
                if constexpr (n & 1) { xo += XS; OPQS(xo); }
            };
            constexpr int UP = 12;
            sfor<0, UP>(xsite);
            v4u r0[3], r1[3];
#pragma unroll
            for (int s = 0; s < 3; ++s) { r0[s] = lds4(aK_ + (32 + s) * 1024); r1[s] = lds4(aKV + (32 + s) * QTS); }
            wso = wb;
            sfor<0, 36>(LAM(SI) {
                constexpr int n = SI.v, s = n < 4 ? 32 + n : n - 4, s3 = n + 3 < 4 ? 35 + n : n - 1;
                if constexpr (n >= 4 && !(s & 1)) {
                    constexpr int issued = UP + n < 32 ? UP + n : 32;
                    if (L) asm volatile("s_waitcnt vmcnt(%0)" ::"n"(issued - s - 2) : "memory");
                    sm = mx3(sm, rda<4 * s>(), rda<4 * s + 4>());
                    FENCE();
                }
                mm32b<n == 0, 4 * s>(aK, r0[n % 3]);
                if constexpr (n + 3 < 36) r0[n % 3] = lds4(aK_ + s3 * 1024); else rV[n - 33] = lds4(aV_ + (n - 33) * 1024);
                if constexpr (UP + n < 32) xsite(IC<UP + n>{});
                if constexpr (n == 1) {
                    float t = 0.f;
#pragma unroll
                    for (int i = 0; i < 16; ++i) t += asf(st[i]);
                    auto r = __builtin_amdgcn_permlane32_swap(asu(t), asu(t), false, false);
                    mean = asf(r[0]) * (1.f / 576.f);
                    const float var = __builtin_fmaxf(asf(r[1]) * (1.f / 576.f) - mean * mean, 0.f);
                    rstd = __builtin_amdgcn_rsqf(var + 1e-5f);
                    al = rstd * 1.4426950408889634f;
                }
                FENCE();
                mm16b<n == 0, 4 * s>(qKV, r1[n % 3]);
                if constexpr (n + 3 < 36) r1[n % 3] = lds4(aKV + s3 * QTS); else cK[n - 33] = lds4(aCS + (n - 33) * 16);
                if constexpr (n == 33) cK[3] = lds4(aCS + 48);
                if constexpr (n == 34) cK[4] = lds4(aCS + 192);
                if constexpr (n == 35) rV[3] = lds4(aV_ + 3 * 1024);
                if constexpr (n >= 8 && (n & 3) != 3) wsite(IC<(n - 8) - (n - 8) / 4>{});
                FENCE();
            });
            if (!bad(sm)) break;
        }
        STAMP(4 + 12 * L);
        v4u rQ[4], rq4[3];
        {
            f16v aV;
            float t[18], e1[18], e2[18], mx, mn, c1, c2, s1 = 0.f, s2 = 0.f;
            v4u cV[4];
            u32 aS = lb + L_STG + (wave * 32 + tl) * STGS + 72 * h;
            OPQV(aS);
            u32 qo = L_Q + wave * 1024;
            sfor<0, 36>(LAM(SI) {
                constexpr int s = SI.v;
                mm32b<s == 0, 4 * s>(aV, rV[s & 3]);
                if constexpr (s == 2) { pad<7>(aK); pin(qKV); }
                if constexpr (s + 4 < 36) rV[s & 3] = lds4(aV_ + (s + 4) * 1024); else cV[s - 32] = lds4(aCS + 64 + (s - 32) * 16);
                if constexpr (s < 18 && !(s & 1)) { OPQS(qo); dma_ws(vw, l16, W_IMG + p * IMG + qo, lb + qo); qo += 4096; }
                if constexpr (s >= 2 && s < 5) {
#pragma unroll
                    for (int i = 6 * (s - 2); i < 6 * (s - 2) + 6; ++i) { const u32 cv = cK[i >> 2][i & 3]; t[i] = __builtin_fmaf(-mean, asf(cv), i < 16 ? aK[i] : qKV[i - 16]); }
                }
                if constexpr (s == 5) { mx = __builtin_fmaxf(__builtin_fmaxf(t[0], t[1]), t[2]); mn = __builtin_fminf(__builtin_fminf(t[0], t[1]), t[2]);
#pragma unroll
                    for (int i = 3; i < 7; i += 2) { mx = __builtin_fmaxf(__builtin_fmaxf(mx, t[i]), t[i + 1]); mn = __builtin_fminf(__builtin_fminf(mn, t[i]), t[i + 1]); } }
                if constexpr (s == 6) {
#pragma unroll
                    for (int i = 7; i < 13; i += 2) { mx = __builtin_fmaxf(__builtin_fmaxf(mx, t[i]), t[i + 1]); mn = __builtin_fminf(__builtin_fminf(mn, t[i]), t[i + 1]); } }
                if constexpr (s == 7) {
#pragma unroll
                    for (int i = 13; i < 17; i += 2) { mx = __builtin_fmaxf(__builtin_fmaxf(mx, t[i]), t[i + 1]); mn = __builtin_fminf(__builtin_fminf(mn, t[i]), t[i + 1]); }
                    mx = __builtin_fmaxf(mx, t[17]); mn = __builtin_fminf(mn, t[17]); c1 = -al * mx; c2 = al * mn; }
                if constexpr (s >= 8 && s < 32) {
#pragma unroll
                    for (int q = (3 * (s - 8)) / 2; q < (3 * (s - 7)) / 2; ++q) {
                        const int i = q >> 1;
                        if (q & 1) { e2[i] = __builtin_amdgcn_exp2f(__builtin_fmaf(t[i], -al, c2)); s2 += e2[i]; }
                        else { e1[i] = __builtin_amdgcn_exp2f(__builtin_fmaf(t[i], al, c1)); s1 += e1[i]; }
                    }
                }
                if constexpr (s == 32) { s1 = __builtin_amdgcn_rcpf(s1); s2 = __builtin_amdgcn_rcpf(s2); }
                if constexpr (s >= 33) {
                    constexpr int i = s - 33;
                    *(LAS v2u*)(aS + 8 * i) = (v2u){pk(e1[4 * i] * s1, e1[4 * i + 1] * s1), pk(e1[4 * i + 2] * s1, e1[4 * i + 3] * s1)};
                }
                FENCE();
            });
            *(LAS v2u*)(aS + 24) = (v2u){pk(e1[12] * s1, e1[13] * s1), pk(e1[14] * s1, e1[15] * s1)};
            *(LAS v2u*)(aS + 32) = (v2u){pk(e1[16] * s1, e1[17] * s1), pk(e2[0] * s2, e2[1] * s2)};
#pragma unroll
            for (int i = 0; i < 4; ++i) *(LAS v2u*)(aS + 40 + 8 * i) = (v2u){pk(e2[4 * i + 2] * s2, e2[4 * i + 3] * s2), pk(e2[4 * i + 4] * s2, e2[4 * i + 5] * s2)};
            FENCE();
            pin(aV);
            const u32 aSv = aS + 144 - 32 * h;
            const float mr = -mean * rstd;
#pragma unroll
            for (int i = 0; i < 4; ++i) {
                const u32 q0 = cV[i].x, q1 = cV[i].y, q2 = cV[i].z, q3 = cV[i].w;
                *(LAS v2u*)(aSv + 8 * i) = (v2u){pk(__builtin_fmaf(mr, asf(q0), aV[4 * i] * rstd), __builtin_fmaf(mr, asf(q1), aV[4 * i + 1] * rstd)),
                                                 pk(__builtin_fmaf(mr, asf(q2), aV[4 * i + 2] * rstd), __builtin_fmaf(mr, asf(q3), aV[4 * i + 3] * rstd))};
            }
            const u32 q2 = cK[4].z, q3 = cK[4].w;
            *(LAS v2u*)(aSv + 32) = (v2u){pk(__builtin_fmaf(mr, asf(q2), qKV[2] * rstd), __builtin_fmaf(mr, asf(q3), qKV[3] * rstd)), 0x3f80u};
        }
        asm volatile("s_waitcnt vmcnt(0) lgkmcnt(0)\n\ts_barrier" ::: "memory");
        if (L == 0 && conv && tid == 0) __builtin_amdgcn_raw_buffer_store_b64((v2u){(u32)ep, (u32)(ep >> 32)}, rw, 0, W_F2 + (rg * 16 + p) * 128, SC1);
        STAMP(5 + 12 * L);
        u32 aQ_ = lb + L_Q + l16;
        OPQV(aQ_);
        {
            const int hh = wave >> 1, dt = wave & 1;
            const u32 r0 = lb + L_STG + (4 * (4 * (kg & 1) + gq) + (kg >> 1)) * STGS + 8 * rq;
            u32 pa = r0 + 72 * hh, pb = r0 + 144 + 40 * hh + 32 * dt;
            OPQV(pa); OPQV(pb);
            f4 c[3];
            v4u B[4], A[4][3];
#pragma unroll
            for (int ks = 0; ks < 4; ++ks) {
                B[ks] = tr2(pb + ks * 32 * STGS, pb + (ks * 32 + 2) * STGS);
#pragma unroll
                for (int ft = 0; ft < 3; ++ft) A[ks][ft] = tr2(pa + ks * 32 * STGS + 32 * ft, pa + (ks * 32 + 2) * STGS + 32 * ft);
            }
#pragma unroll
            for (int s = 0; s < 4; ++s) rQ[s] = lds4(aQ_ + s * 1024);
#pragma unroll
            for (int s = 0; s < 3; ++s) rq4[s] = lds4(aQq + s * QTS);
            FENCE();
            sfor<0, 12>(LAM(I_) { constexpr int ks = I_.v / 3, ft = I_.v % 3; mm16<ks == 0>(c[ft], A[ks][ft], B[ks]); });
            asm volatile("s_nop 7" : "+v"(c[0]), "+v"(c[1]), "+v"(c[2]));
            u32 w[6];
#pragma unroll
            for (int ft = 0; ft < 3; ++ft) { w[2 * ft] = pk(c[ft][0], c[ft][1]); w[2 * ft + 1] = pk(c[ft][2], c[ft][3]); }
            const u32 so = W_KV + ((L * 16 + p) * 16 + rg) * 2816 + hh * 1408;
            if (dt == 0) {
                __builtin_amdgcn_raw_buffer_store_b128((v4u){w[0], w[1], w[2], w[3]}, rw, l16, so, SC1);
                __builtin_amdgcn_raw_buffer_store_b64((v2u){w[4], w[5]}, rw, ln < 16 ? ln * 8 + 1024 : OOB, so, SC1);
            } else {
                const u32 s0 = __builtin_amdgcn_update_dpp(0u, w[2], 0x113, 0xf, 0xf, true), s1 = __builtin_amdgcn_update_dpp(0u, w[3], 0x113, 0xf, 0xf, true);
                const u32 t0 = __builtin_amdgcn_update_dpp(0u, w[4], 0x116, 0xf, 0xf, true), t1 = __builtin_amdgcn_update_dpp(0u, w[5], 0x116, 0xf, 0xf, true);
                const v2u v = row < 3 ? (v2u){w[0], w[1]} : row < 6 ? (v2u){s0, s1} : row < 9 ? (v2u){t0, t1} : (v2u){0, 0};
                const int slot = row < 6 ? kg * 6 + row : 24 + row - 6;
                __builtin_amdgcn_raw_buffer_store_b64(v, rw, row < 6 || (kg == 0 && row < 14) ? slot * 8 + 1152 : OOB, so, SC1);
            }
        }
        FENCE();
        STAMP(6 + 12 * L);
        u32 pq[20];
        v4u kp[12];
        const __amdgpu_buffer_rsrc_t rkv = mkrs(ws + W_KV + (L * 16 + p) * 16 * 2816, 16 * 2816);
        const u32 kvo = kg * 2816 + wave * 256 + row * 16, kv3 = wave < 3 ? kvo + 2048 : OOB;
        v2u yfl = {0, 0};
        {
            f16v aQ; f4 qQ;
            v4u cQ[5];
            sfor<0, 36>(LAM(SI) {
                constexpr int s = SI.v;
                mm32b<s == 0, 4 * s>(aQ, rQ[s & 3]);
                if constexpr (s + 4 < 36) rQ[s & 3] = lds4(aQ_ + (s + 4) * 1024); else cQ[s - 32] = lds4(aCS + 128 + (s - 32) * 16);
                if constexpr (s < 32 && (s & 3) != 3) wsite(IC<21 + s - s / 4>{});
                FENCE();
                mm16b<s == 0, 4 * s>(qQ, rq4[s % 3]);
                if constexpr (s + 3 < 36) rq4[s % 3] = lds4(aQq + (s + 3) * QTS);
                if constexpr (s == 33) cQ[4] = lds4(aCS + 208);
                FENCE();
            });
#pragma unroll
            for (int i = 0; i < 4; ++i) {
                kp[i] = __builtin_amdgcn_raw_buffer_load_b128(rkv, kvo, i * 4 * 2816, SC1);
                kp[4 + i] = __builtin_amdgcn_raw_buffer_load_b128(rkv, kvo + 1024, i * 4 * 2816, SC1);
                kp[8 + i] = __builtin_amdgcn_raw_buffer_load_b128(rkv, kv3, i * 4 * 2816, SC1);
            }
            const u32 yfo = W_F2 + ((((row < 12 ? row : 0) - pw) & 15) * 16 + pw) * 128;
            if (L == 0) yfl = __builtin_amdgcn_raw_buffer_load_b64(rw, yfo, 0, SC1);
            FENCE();
            pin(aQ); pin(qQ);
            float t[18]; u32 ph[18];
#pragma unroll
            for (int i = 0; i < 18; ++i) { const u32 cv = cQ[i >> 2][i & 3]; t[i] = __builtin_fmaf(-mean, asf(cv), i < 16 ? aQ[i] : qQ[i - 16]); }
            hedgehog(t, al, ph);
#pragma unroll
            for (int i = 0; i < 18; ++i) { pq[i] = ph[i]; OPQV(pq[i]); }
            pq[18] = 0; pq[19] = 0;
            FENCE();
            STAMP(7 + 12 * L);
            u32 m = 0;
#pragma unroll
            for (int i = 0; i < 12; ++i) m = mx3(m, kp[i].x, kp[i].z);
            if (__builtin_expect(bad(m), 0)) {
#pragma unroll 1
                for (int sp = 0; sp < SPIN; ++sp) {
                    __builtin_amdgcn_s_sleep(2);
                    asm volatile("" ::: "memory");
                    m = 0;
#pragma unroll
                    for (int i = 0; i < 4; ++i) {
                        kp[i] = __builtin_amdgcn_raw_buffer_load_b128(rkv, kvo, i * 4 * 2816, SC1);
                        kp[4 + i] = __builtin_amdgcn_raw_buffer_load_b128(rkv, kvo + 1024, i * 4 * 2816, SC1);
                        kp[8 + i] = __builtin_amdgcn_raw_buffer_load_b128(rkv, kv3, i * 4 * 2816, SC1);
                    }
#pragma unroll
                    for (int i = 0; i < 12; ++i) m = mx3(m, kp[i].x, kp[i].z);
                    if (!bad(m)) break;
                }
            }
            v4u sel;
#pragma unroll
            for (int i = 0; i < 4; ++i) sel[i] = row == 2 * i ? 0x3f80u : row == 2 * i + 1 ? 0x3f800000u : 0u;
            f4 a[3];
            pad<1>(sel);
            sfor<0, 12>(LAM(I_) { constexpr int i = I_.v / 3, j = I_.v % 3; mm16<i == 0>(a[j], sel, kp[4 * j + i]); });
            asm volatile("s_nop 7" : "+v"(a[0]), "+v"(a[1]), "+v"(a[2]));
#pragma unroll
            for (int j = 0; j < 3; ++j) {
                const u32 o = j == 0 ? kvw0 & 0xffffu : j == 1 ? kvw0 >> 16 : kvw1;
                if (o != 0xffffu) *(LAS v2u*)(lb + L_KVA + o) = (v2u){pk(a[j][0], a[j][1]), pk(a[j][2], a[j][3])};
            }
            barrier();
            STAMP(8 + 12 * L);
            if (L == 0) {
#pragma unroll 1
                for (int sp = 0; sp < SPIN; ++sp) {
                    if (__builtin_amdgcn_ballot_w64(yfl.x != (u32)ep || yfl.y != (u32)(ep >> 32)) == 0) break;
                    asm volatile("" ::: "memory");
                    yfl = __builtin_amdgcn_raw_buffer_load_b64(rw, yfo, 0, SC1);
                }
            }
        }
        u32 ye[18];
        {
            u32 so = W_Y + 36 * pw * 4096 + (rg * 128 + tw * 32) * 2;
            const u32 yo = L ? OOB : h * 18 * 4096 + tl * 2;
#pragma unroll
            for (int m = 0; m < 18; ++m) { OPQS(so); ye[m] = __builtin_amdgcn_raw_buffer_load_b16(rw, yo, so, SC1); so += 4096; }
        }
        FENCE();
        {
            const int hr = (tl >> 2) & 1, d = (tl & 3) + 4 * (tl >> 3);
            const u32 aF = lb + L_KVA + (h == hr ? (hr * 19 + d) * 80 : 38 * 80);
            const u32 aQ4 = lb + L_KVA + (kg == gq && rq < 3 ? ((gq >> 1) * 19 + 16 + rq) * 80 : 38 * 80);
            v4u A[5], Aq[5];
#pragma unroll
            for (int c = 0; c < 5; ++c) { Aq[c] = lds4(aQ4 + 16 * c); A[c] = lds4(aF + 16 * c); }
            FENCE();
            f16v o; f4 oq;
            v4u pb[5];
#pragma unroll
            for (int c = 0; c < 5; ++c) { pb[c] = (v4u){pq[4 * c], pq[4 * c + 1], pq[4 * c + 2], pq[4 * c + 3]}; pin(pb[c]); }
            nop2();
            sfor<0, 5>(LAM(C_) { mm16<C_.v == 0>(oq, Aq[C_.v], pb[C_.v]); });
            sfor<0, 5>(LAM(C_) { mm32<C_.v == 0>(o, A[C_.v], pb[C_.v]); });
            asm volatile("s_nop 11" : "+v"(o), "+v"(oq));
            const float rd = __builtin_amdgcn_rcpf(oq[2] + 1e-6f);
            const u32 so = W_O + (L * 1024 + (rg * 4 + wave) * 16 + p) * OS;
            __builtin_amdgcn_raw_buffer_store_b128((v4u){pk(o[0] * rd, o[1] * rd), pk(o[2] * rd, o[3] * rd), pk(o[4] * rd, o[5] * rd), pk(o[6] * rd, o[7] * rd)}, rw, l16, so, SC1);
            __builtin_amdgcn_raw_buffer_store_b128((v4u){pk(o[8] * rd, o[9] * rd), pk(o[10] * rd, o[11] * rd), pk(o[12] * rd, o[13] * rd), pk(o[14] * rd, o[15] * rd)}, rw, l16 + 1024, so, SC1);
            __builtin_amdgcn_raw_buffer_store_b32(pk(oq[0] * rd, oq[1] * rd), rw, l4 + 2048, so, SC1);
        }
        FENCE();
#pragma unroll
        for (int i = 0; i < 9; ++i) { yv[i] |= ye[2 * i] | (ye[2 * i + 1] << 16); OPQV(yv[i]); }
        STAMP(9 + 12 * L);
        {
            const u32 so = W_O + (L * 1024 + (rg * 4 + tw) * 16 + 4 * wave) * OS;
            v4u lo;
#pragma unroll 1
            for (int sp = 0; sp < SPIN; ++sp) {
                asm volatile("" : "+v"(l4)::"memory");
#pragma unroll
                for (int i = 0; i < 4; ++i) lo[i] = __builtin_amdgcn_raw_buffer_load_b32(rw, l4 + 2048, so + i * OS, SC1);
                if (!bad(mx3(umax(lo.x, lo.y), lo.z, lo.w))) break;
                __builtin_amdgcn_s_sleep(1);
            }
            v4u og[8];
#pragma unroll 1
            for (int sp = 0; sp < SPIN; ++sp) {
                asm volatile("" : "+v"(l16)::"memory");
#pragma unroll
                for (int i = 0; i < 8; ++i) og[i] = __builtin_amdgcn_raw_buffer_load_b128(rw, l16 + (i & 1) * 1024, so + (i >> 1) * OS, SC1);
                FENCE();
                u32 m = 0;
#pragma unroll
                for (int i = 0; i < 8; i += 2) m = mx3(m, og[i].x, og[i + 1].x);
                if (!bad(m)) break;
            }
            const u32 ao = aQ_ + wave * 8192;
#pragma unroll
            for (int i = 0; i < 8; ++i) *(LAS v4u*)(ao + i * 1024) = og[i];
            *(LAS v4u*)(aQ_ + (32 + wave) * 1024) = lo;
            barrier();
        }
        STAMP(10 + 12 * L);
        float x1[18];
        {
            f16v aW; f4 qW;
            u32 aB = lb + L_Q + ((wave >> 1) * 18 + (kg & 1)) * 1024 + ((kg >> 1) * 32 + 16 * (wave & 1) + row) * 16;
            OPQV(aB);
            v4u rb_[4], rl[9];
#pragma unroll
            for (int i = 0; i < 9; ++i) rl[i] = lds4(aB + i * 2048);
#pragma unroll
            for (int s = 0; s < 4; ++s) rb_[s] = lds4(aQ_ + s * 1024);
            FENCE();
            sfor<0, 9>(LAM(I_) { mm16<I_.v == 0>(qW, wv[8 + I_.v], rl[I_.v]); });
            FENCE();
            sfor<0, 36>(LAM(SI) {
                constexpr int s = SI.v;
                if constexpr (s < 28) mm32a<s == 0, 144 + 4 * s>(aW, rb_[s & 3]); else mm32<false>(aW, wv[s - 28], rb_[s & 3]);
                if constexpr (s + 4 < 36) rb_[s & 3] = lds4(aQ_ + (s + 4) * 1024);
                if constexpr (s == 3) pad<1>(qW);
                if constexpr (s == 3) *(LAS f4*)(lb + L_EX + wave * 1024 + l16) = qW;
                FENCE();
            });
            barrier();
            const u32 ae = lb + L_EX + (kg & 1) * 1024 + (wave * 16 + row) * 16 + 8 * h;
            const v2u e0 = lds2(ae), e1 = lds2(ae + 2048);
            STAMP(11 + 12 * L);
            pad<9>(aW);
            float s1 = 0.f, s2 = 0.f;
#pragma unroll
            for (int i = 0; i < 9; ++i) {
                const float a0 = i < 8 ? aW[2 * i] : asf(e0.x) + asf(e1.x), a1 = i < 8 ? aW[2 * i + 1] : asf(e0.y) + asf(e1.y);
                const u32 at = pk(a0, a1), w = pk(bf_lo(xr[i]) + bf_lo(at), bf_hi(xr[i]) + bf_hi(at));
                x1[2 * i] = bf_lo(w); x1[2 * i + 1] = bf_hi(w);
                s1 += x1[2 * i] + x1[2 * i + 1]; s2 += x1[2 * i] * x1[2 * i] + x1[2 * i + 1] * x1[2 * i + 1];
            }
            __builtin_amdgcn_raw_buffer_store_b32(asu(halves_sum(s1, s2)), rw, l4, W_ST + (L * 1024 + (rg * 4 + tw) * 16 + pw) * 256, SC1);
        }
        FENCE();
        {
            float t;
#pragma unroll 1
            for (int sp = 0; sp < SPIN; ++sp) {
                asm volatile("" : "+v"(l4)::"memory");
                u32 e[16], m = 0;
#pragma unroll
                for (int i = 0; i < 16; ++i) e[i] = __builtin_amdgcn_raw_buffer_load_b32(rw, l4 + i * 256, W_ST + (L * 1024 + (rg * 4 + tw) * 16) * 256, SC1);
                FENCE();
                t = 0.f;
#pragma unroll
                for (int i = 0; i < 16; i += 2) { m = mx3(m, e[i], e[i + 1]); t += asf(e[i]) + asf(e[i + 1]); }
                if (!bad(m)) break;
                __builtin_amdgcn_s_sleep(1);
            }
            STAMP(12 + 12 * L);
            auto r = __builtin_amdgcn_permlane32_swap(asu(t), asu(t), false, false);
            const float m1 = asf(r[0]) * (1.f / 576.f);
            const float r1 = __builtin_amdgcn_rsqf(__builtin_fmaxf(asf(r[1]) * (1.f / 576.f) - m1 * m1, 0.f) + 1e-5f);
            float s1 = 0.f, s2 = 0.f;
#pragma unroll
            for (int i = 0; i < 9; ++i) {
                const u32 xn = pk((x1[2 * i] - m1) * r1, (x1[2 * i + 1] - m1) * r1);
                const u32 gt = pk(bf_lo(xn) * bf_lo(yv[i]), bf_hi(xn) * bf_hi(yv[i]));
                xr[i] = pk(x1[2 * i] + bf_lo(gt), x1[2 * i + 1] + bf_hi(gt));
                const float a = bf_lo(xr[i]), c = bf_hi(xr[i]);
                s1 += a + c; s2 += a * a + c * c;
            }
            if (L == 0) {
                const u32 so = W_XF1 + ((rg * 4 + tw) * 16 + pw) * XS;
                __builtin_amdgcn_raw_buffer_store_b128((v4u){xr[0], xr[1], xr[2], xr[3]}, rw, l16, so, SC1);
                __builtin_amdgcn_raw_buffer_store_b128((v4u){xr[4], xr[5], xr[6], xr[7]}, rw, l16 + 1024, so, SC1);
                __builtin_amdgcn_raw_buffer_store_b32(xr[8], rw, l4 + 2048, so, SC1);
                __builtin_amdgcn_raw_buffer_store_b32(asu(halves_sum(s1, s2)), rw, l4 + 2304, so, SC1);
                STAMP(13);
                u32 lf[16];
#pragma unroll 1
                for (int sp = 0; sp < SPIN; ++sp) {
                    asm volatile("" : "+v"(l4)::"memory");
                    u32 xo = W_XF1 + (rg * 4 + wave) * 16 * XS, m = 0;
#pragma unroll
                    for (int i = 0; i < 16; ++i) {
                        OPQS(xo);
                        st[i] = __builtin_amdgcn_raw_buffer_load_b32(rw, l4 + 2304, xo, SC1);
                        lf[i] = __builtin_amdgcn_raw_buffer_load_b32(rw, l4 + 2048, xo, SC1);
                        xo += XS;
                    }
                    FENCE();
#pragma unroll
                    for (int i = 0; i < 16; ++i) m = mx3(m, st[i], lf[i]);
                    if (!bad(m)) break;
                    __builtin_amdgcn_s_sleep(1);
                }
                sfor<0, 16>(LAM(I_) { wra<128 + I_.v>(lf[I_.v]); });
                STAMP(25);
            } else {
                u32* op = (u32*)(out + (rg * 128 + tw * 32 + tl) * 576 + 36 * pw + 18 * h);
#pragma unroll
                for (int i = 0; i < 9; ++i) op[i] = xr[i];
                if (b == 0 && tid == 0) __builtin_amdgcn_raw_buffer_store_b64((v2u){(u32)ep, (u32)(ep >> 32)}, rw, 0, W_E, SC1);
            }
        }
    }
    STAMP_END();
}
#if defined(__HIP_DEVICE_COMPILE__)
#pragma clang attribute pop
#endif

extern "C" void kernel_launch(void* const* d_in, const int* in_sizes, int n_in, void* d_out, int out_size, void* d_ws, size_t ws_size, hipStream_t stream) {
    (void)in_sizes; (void)n_in; (void)out_size; (void)ws_size;
    hipFuncSetAttribute((const void*)k_main, hipFuncAttributeMaxDynamicSharedMemorySize, L_END);
    k_main<<<256, 256, L_END, stream>>>((const u16*)d_in[0], (const u16*)d_in[1], (const u16*)d_in[2], (const u16*)d_in[3], (const u16*)d_in[5], (const u16*)d_in[6],
                                        (const u16*)d_in[7], (const u16*)d_in[8], (const u16*)d_in[9], (u16*)d_out, (char*)d_ws + ((ws_size - W_END - 4096) & ~(size_t)4095));
}
```

```cpp
#include <hip/hip_runtime.h>
#include <stdint.h>
#pragma clang diagnostic ignored "-Wint-to-pointer-cast"
#pragma clang diagnostic ignored "-Wunused-value"
#pragma clang diagnostic ignored "-Winline-asm"
#if defined(__HIP_DEVICE_COMPILE__)
#pragma clang attribute push(__attribute__((target("no-packed-fp32-ops"))), apply_to = function)
#endif
typedef unsigned short u16;
typedef unsigned u32;
typedef unsigned long long u64;
typedef u32 v4u __attribute__((ext_vector_type(4)));
typedef u32 v2u __attribute__((ext_vector_type(2)));
typedef float f4 __attribute__((ext_vector_type(4)));
typedef float f2 __attribute__((ext_vector_type(2)));
typedef float f16v __attribute__((ext_vector_type(16)));
typedef __bf16 b8 __attribute__((ext_vector_type(8)));
typedef __bf16 b2 __attribute__((ext_vector_type(2)));
typedef short s4 __attribute__((ext_vector_type(4)));
#define LAS __attribute__((address_space(3)))
#define DEV __device__ __forceinline__
#define SC1 16

constexpr u32 QTS = 416, STGS = 232, CVS = 21216, HCS = 4160;
constexpr u32 L_K = 0, L_V = 36864, L_QT = 73728, L_Q = 89088, L_STG = 125952, L_KVA = 155648, L_CS = 158784, L_EX = 159232, L_END = 163328;
constexpr u32 P_KS = 0, P_VS = 10240, P_WS = 20480, P_XS = 31232, P_PH = 42496, P_LS = 55296, L_Z = L_KVA + 38 * 80;
constexpr u32 IMG = L_STG, XS = 2560, OS = 2304, YB = 576 * 256, CSB = 16 * 576;
constexpr u32 WOG = 4 * 36864 + 18432;
constexpr u32 W_IMG = 0, W_WOT = W_IMG + 16 * IMG, W_CSP = W_WOT + 4 * WOG, W_UT = W_CSP + 16 * CSB,
              W_XF0 = W_UT + 576 * 4096, W_XF1 = W_XF0 + 1024 * XS, W_O = W_XF1 + 1024 * XS, W_Y = W_O + 2 * 1024 * OS, W_KV = W_Y + 576 * 4096,
              W_ST = W_KV + 2 * 256 * 2816, W_F = W_ST + 2 * 1024 * 256, W_F2 = W_F + 256 * 128, W_E = W_F2 + 256 * 128, W_END = W_E + 128, W_DBG = W_END;
constexpr u32 OOB = 0x7ffffff0u;
constexpr int SPIN = 1 << 14;
#define OSL 2

DEV float asf(u32 v) { return __builtin_bit_cast(float, v); }
DEV u32 asu(float v) { return __builtin_bit_cast(u32, v); }
DEV float bf_lo(u32 v) { return asf(v << 16); }
DEV float bf_hi(u32 v) { return asf(v & 0xffff0000u); }
DEV u32 pk(float lo, float hi) { b2 r = __builtin_convertvector((f2){lo, hi}, b2); return __builtin_bit_cast(u32, r); }
DEV float rb(float f) { return bf_lo(pk(f, f)); }
DEV float dot2(u32 a, u32 b, float c) { return __builtin_amdgcn_fdot2_f32_bf16(__builtin_bit_cast(b2, a), __builtin_bit_cast(b2, b), c, false); }
DEV u32 umin(u32 a, u32 b) { return a < b ? a : b; }
DEV u32 umax(u32 a, u32 b) { return a > b ? a : b; }
DEV u32 mx3(u32 a, u32 b, u32 c) { return umax(umax(a, b), c); }
template <bool Z> DEV void mm32(f16v& c, v4u a, v4u b) {
    if constexpr (Z) asm volatile("v_mfma_f32_32x32x16_bf16 %0, %1, %2, 0" : "=&v"(c) : "v"(a), "v"(b));
    else asm volatile("v_mfma_f32_32x32x16_bf16 %0, %1, %2, %0" : "+v"(c) : "v"(a), "v"(b));
}
template <bool Z> DEV void mm16(f4& c, v4u a, v4u b) {
    if constexpr (Z) asm volatile("v_mfma_f32_16x16x32_bf16 %0, %1, %2, 0" : "=&v"(c) : "v"(a), "v"(b));
    else asm volatile("v_mfma_f32_16x16x32_bf16 %0, %1, %2, %0" : "+v"(c) : "v"(a), "v"(b));
}
template <bool Z, int R> DEV void mm32b(f16v& c, v4u a) {
    if constexpr (Z) asm volatile("v_mfma_f32_32x32x16_bf16 %0, %1, a[%c2:%c3], 0" : "=&v"(c) : "v"(a), "n"(R), "n"(R + 3));
    else asm volatile("v_mfma_f32_32x32x16_bf16 %0, %1, a[%c2:%c3], %0" : "+v"(c) : "v"(a), "n"(R), "n"(R + 3));
}
template <bool Z, int R> DEV void mm16b(f4& c, v4u a) {
    if constexpr (Z) asm volatile("v_mfma_f32_16x16x32_bf16 %0, %1, a[%c2:%c3], 0" : "=&v"(c) : "v"(a), "n"(R), "n"(R + 3));
    else asm volatile("v_mfma_f32_16x16x32_bf16 %0, %1, a[%c2:%c3], %0" : "+v"(c) : "v"(a), "n"(R), "n"(R + 3));
}
template <bool Z, int R> DEV void mm32a(f16v& c, v4u b) {
    if constexpr (Z) asm volatile("v_mfma_f32_32x32x16_bf16 %0, a[%c2:%c3], %1, 0" : "=&v"(c) : "v"(b), "n"(R), "n"(R + 3));
    else asm volatile("v_mfma_f32_32x32x16_bf16 %0, a[%c2:%c3], %1, %0" : "+v"(c) : "v"(b), "n"(R), "n"(R + 3));
}
template <int N, class T> DEV void pad(T& c) { asm volatile("s_nop %1" : "+v"(c) : "n"(N)); }
template <class T> DEV void pin(T& c) { asm volatile("" : "+v"(c)); }
DEV void nop2() { asm volatile("s_nop 1"); }
template <int R> DEV u32 rda() { u32 v; asm volatile("v_accvgpr_read_b32 %0, a%c1" : "=v"(v) : "n"(R)); return v; }
template <int R> DEV void wra(u32 v) { asm volatile("v_accvgpr_write_b32 a%c1, %0" ::"v"(v), "n"(R)); }
__device__ void bst4(v4u, v4u, u32, u32, int) __asm("llvm.amdgcn.raw.buffer.store.v4i32");
__device__ void bst2(v2u, v4u, u32, u32, int) __asm("llvm.amdgcn.raw.buffer.store.v2i32");
__device__ void bst1(u32, v4u, u32, u32, int) __asm("llvm.amdgcn.raw.buffer.store.i32");
__device__ v4u bld4(v4u, u32, u32, int) __asm("llvm.amdgcn.raw.buffer.load.v4i32");
__device__ v2u bld2(v4u, u32, u32, int) __asm("llvm.amdgcn.raw.buffer.load.v2i32");
__device__ u32 bld1(v4u, u32, u32, int) __asm("llvm.amdgcn.raw.buffer.load.i32");
struct Buf { const void* p; u32 n, f; };
struct Args { Buf k, v, w[2], ph[2], x, ws, out; const void *sk, *sh, *sD; };
template <class T> DEV void sld(T& d, const void* ka, u32 off) {
    if constexpr (sizeof(T) == 16) asm volatile("s_load_dwordx4 %0, %1, %2" : "=s"(d) : "s"(ka), "s"(off));
    else asm volatile("s_load_dwordx2 %0, %1, %2" : "=s"(d) : "s"(ka), "s"(off));
}
template <int O, class T> DEV void sldi(T& d, const void* ka) {
    if constexpr (sizeof(T) == 16) asm volatile("s_load_dwordx4 %0, %1, %2" : "=s"(d) : "s"(ka), "n"(O));
    else asm volatile("s_load_dwordx2 %0, %1, %2" : "=s"(d) : "s"(ka), "n"(O));
}
DEV v4u lds4(u32 a) { return *(const LAS v4u*)a; }
DEV v2u lds2(u32 a) { return *(const LAS v2u*)a; }
DEV void barrier() { asm volatile("s_waitcnt lgkmcnt(0)\n\ts_barrier" ::: "memory"); }
template <int N> DEV void waitv() { asm volatile("s_waitcnt vmcnt(%0)" ::"n"(N) : "memory"); }
DEV void dma_in(const void* g, u32 voff, u32 lds) {
    asm volatile("s_mov_b32 m0, %2\n\ts_nop 0\n\tglobal_load_lds_dwordx4 %0, %1" ::"v"(voff), "s"(g), "s"(lds) : "m0", "memory");
}
DEV void dma_b(v4u rs, u32 voff, u32 lds) {
    asm volatile("s_mov_b32 m0, %2\n\ts_nop 0\n\tbuffer_load_dwordx4 %0, %1, 0 offen lds" ::"v"(voff), "s"(rs), "s"(lds) : "m0", "memory");
}
DEV void dma_s(v4u rs, u32 voff, u32 so, u32 lds) {
    asm volatile("s_mov_b32 m0, %3\n\ts_nop 0\n\tbuffer_load_dwordx4 %0, %1, %2 offen lds" ::"v"(voff), "s"(rs), "s"(so), "s"(lds) : "m0", "memory");
}
DEV void dma_ws(v4u rs, u32 voff, u32 so, u32 lds) {
    asm volatile("s_mov_b32 m0, %3\n\ts_nop 0\n\tbuffer_load_dwordx4 %0, %1, %2 offen sc1 lds" ::"v"(voff), "s"(rs), "s"(so), "s"(lds) : "m0", "memory");
}
template <int IM> DEV void gld4(v4u& d, u32 voff, const void* g) { asm volatile("global_load_dwordx4 %0, %1, %2 offset:%3" : "=v"(d) : "v"(voff), "s"(g), "n"(IM) : "memory"); }
DEV void gld2(v2u& d, u32 voff, const void* g) { asm volatile("global_load_dwordx2 %0, %1, %2" : "=v"(d) : "v"(voff), "s"(g) : "memory"); }
DEV void mm16k(f4& c, v2u a, v2u b) { asm volatile("v_mfma_f32_16x16x16_bf16 %0, %1, %2, 0" : "=&v"(c) : "v"(a), "v"(b)); }
DEV void mm16n(f4& c, v4u a, v4u b) { asm volatile("s_nop 1\n\tv_mfma_f32_16x16x32_bf16 %0, %1, %2, 0" : "=&v"(c) : "v"(a), "v"(b)); }
template <class A, class B> DEV void pin2(A& a, B& b) { asm volatile("" : "+v"(a), "+v"(b)); }
template <int IM> DEV void ld4(v4u& d, u32 voff, v4u rs, u32 so) { asm volatile("buffer_load_dwordx4 %0, %1, %2, %3 offen offset:%4 sc1" : "+v"(d) : "v"(voff), "s"(rs), "s"(so), "n"(IM) : "memory"); }
template <int IM> DEV void ld1(u32& d, u32 voff, v4u rs, u32 so) { asm volatile("buffer_load_dword %0, %1, %2, %3 offen offset:%4 sc1" : "+v"(d) : "v"(voff), "s"(rs), "s"(so), "n"(IM) : "memory"); }
template <int IM> DEV void ld4m(v4u& d, u32 voff, v4u rs, u32 so, u64 m) {
    asm volatile("s_mov_b64 exec, %5\n\tbuffer_load_dwordx4 %0, %1, %2, %3 offen offset:%4 sc1\n\ts_mov_b64 exec, -1" : "+v"(d) : "v"(voff), "s"(rs), "s"(so), "n"(IM), "s"(m) : "memory");
}
template <int IM, int R> DEV void ld4a(u32 voff, v4u rs, u32 so) { asm volatile("buffer_load_dwordx4 a[%c4:%c5], %0, %1, %2 offen offset:%3 sc1" ::"v"(voff), "s"(rs), "s"(so), "n"(IM), "n"(R), "n"(R + 3) : "memory"); }
template <int IM, int R> DEV void ld1a(u32 voff, v4u rs, u32 so) { asm volatile("buffer_load_dword a%c4, %0, %1, %2 offen offset:%3 sc1" ::"v"(voff), "s"(rs), "s"(so), "n"(IM), "n"(R) : "memory"); }
template <int IM, int R> DEV void ld4am(u32 voff, v4u rs, u32 so, u64 m) {
    asm volatile("s_mov_b64 exec, %6\n\tbuffer_load_dwordx4 a[%c4:%c5], %0, %1, %2 offen offset:%3 sc1\n\ts_mov_b64 exec, -1" ::"v"(voff), "s"(rs), "s"(so), "n"(IM), "n"(R), "n"(R + 3), "s"(m) : "memory");
}
template <int IM> DEV void ld2(v2u& d, u32 voff, v4u rs, u32 so) { asm volatile("buffer_load_dwordx2 %0, %1, %2, %3 offen offset:%4 sc1" : "=v"(d) : "v"(voff), "s"(rs), "s"(so), "n"(IM) : "memory"); }
template <int IM> DEV void ld4o(v4u& d, u32 voff, v4u rs, u32 so) { asm volatile("buffer_load_dwordx4 %0, %1, %2, %3 offen offset:%4 sc1" : "=v"(d) : "v"(voff), "s"(rs), "s"(so), "n"(IM) : "memory"); }
template <int IM> DEV void ldh(u32& d, u32 voff, v4u rs, u32 so) { asm volatile("buffer_load_short_d16_hi %0, %1, %2, %3 offen offset:%4 sc1" : "+v"(d) : "v"(voff), "s"(rs), "s"(so), "n"(IM) : "memory"); }
template <int IM> DEV void st4(v4u d, u32 voff, v4u rs, u32 so) { asm volatile("buffer_store_dwordx4 %0, %1, %2, %3 offen offset:%4 sc1" ::"v"(d), "v"(voff), "s"(rs), "s"(so), "n"(IM) : "memory"); }
DEV void setm0(u32 v) { asm volatile("s_mov_b32 m0, %0" ::"s"(v) : "m0", "memory"); }
template <int IM> DEV void dmai(v4u rs, u32 voff, u32 so) { asm volatile("buffer_load_dwordx4 %0, %1, %2 offen offset:%3 sc1 lds" ::"v"(voff), "s"(rs), "s"(so), "n"(IM) : "memory"); }
DEV u32 pka(float a, float b) { u32 r; asm volatile("v_cvt_pk_bf16_f32 %0, %1, %2" : "=v"(r) : "v"(a), "v"(b)); return r; }
template <int N> DEV void waitl() { __builtin_amdgcn_s_waitcnt(0xc07f | (N << 8)); }
DEV u32 vsel(bool c, u32 a, u32 b) { asm volatile("" : "+v"(a)); asm volatile("" : "+v"(b)); return c ? a : b; }
DEV u32 ssel(bool c, u32 a, u32 b) { asm volatile("" : "+s"(a)); asm volatile("" : "+s"(b)); return c ? a : b; }
template <int N, class T> DEV void waitp(T& d) { asm volatile("s_waitcnt vmcnt(%1)" : "+v"(d) : "n"(N) : "memory"); }
template <int N> DEV void waitbar() { asm volatile("s_waitcnt vmcnt(%0) lgkmcnt(0)\n\ts_barrier" ::"n"(N) : "memory"); }
template <int N> DEV void pad3(f4& a, f4& b, f4& c) { asm volatile("s_nop %3" : "+v"(a), "+v"(b), "+v"(c) : "n"(N)); }
template <int I> struct IC { static constexpr int v = I; };
template <int B, int E, class F> DEV void sfor(F&& f) { if constexpr (B < E) { f(IC<B>{}); sfor<B + 1, E>(f); } }
#define LAM(I) [&](auto I) __attribute__((always_inline))
#define FENCE() __builtin_amdgcn_sched_barrier(0)
#define OPQV(x) asm volatile("" : "+v"(x))
#define OPQS(x) asm volatile("" : "+s"(x))
DEV float halves_sum(float a, float b) {
    auto r = __builtin_amdgcn_permlane32_swap(asu(a), asu(b), false, false);
    return asf(r[0]) + asf(r[1]);
}
DEV bool bad(u32 m) { return __builtin_amdgcn_ballot_w64(m == ~0u) != 0; }
#define STAMP_INIT()
template <int I> DEV void mark() { asm volatile("; MARK %0" ::"n"(I)); }
#define STAMP(i) mark<(i)>()
#define STAMP_END()
#define STAMPV(i, v)

DEV void hedgehog(const float (&t)[18], float a, u32 (&ph)[18]) {
    float mx = t[0], mn = t[0];
#pragma unroll
    for (int i = 1; i < 18; ++i) { mx = __builtin_fmaxf(mx, t[i]); mn = __builtin_fminf(mn, t[i]); }
    const float c1 = -a * mx, c2 = a * mn;
    float e1[18], e2[18], s1 = 0.f, s2 = 0.f;
#pragma unroll
    for (int i = 0; i < 18; ++i) {
        e1[i] = __builtin_amdgcn_exp2f(__builtin_fmaf(t[i], a, c1)); s1 = i ? s1 + e1[i] : e1[i];
        e2[i] = __builtin_amdgcn_exp2f(__builtin_fmaf(t[i], -a, c2)); s2 = i ? s2 + e2[i] : e2[i];
    }
    const float r1 = __builtin_amdgcn_rcpf(s1), r2 = __builtin_amdgcn_rcpf(s2);
#pragma unroll
    for (int i = 0; i < 9; ++i) { ph[i] = pk(e1[2 * i] * r1, e1[2 * i + 1] * r1); ph[9 + i] = pk(e2[2 * i] * r2, e2[2 * i + 1] * r2); }
}
DEV v4u tr2(u32 a0, u32 a1) {
    s4 lo = __builtin_amdgcn_ds_read_tr16_b64_v4i16((LAS s4*)a0), hi = __builtin_amdgcn_ds_read_tr16_b64_v4i16((LAS s4*)a1);
    v2u l = __builtin_bit_cast(v2u, lo), h = __builtin_bit_cast(v2u, hi);
    return (v4u){l.x, l.y, h.x, h.y};
}
DEV v2u y_val(f4 acc) { return (v2u){pk(acc[0], acc[1]), pk(acc[2], acc[3])}; }
DEV v4u y_pair(v2u a, v2u b) {
    auto r0 = __builtin_amdgcn_permlane16_swap(a.x, b.x, false, false), r1 = __builtin_amdgcn_permlane16_swap(a.y, b.y, false, false);
    return (v4u){r0[0], r1[0], r0[1], r1[1]};
}
template <int N, int n, int r>
DEV void conv_read(u32 aa, u32 ba, v4u (&A)[N], v4u (&B)[N]) {
    constexpr int i = n / 3, k = n % 3;
    if constexpr (k == 0) B[i] = lds4(ba + 64 * (8 * i - r + 7));
    else { const v2u v = lds2(aa + 2 + 64 * (r + 8 * i) + 8 * (k - 1)); if constexpr (k == 1) { A[i].x = v.x; A[i].y = v.y; } else { A[i].z = v.x; A[i].w = v.y; } }
}
constexpr int conv_ij(int N, int MASK, int n) {
    for (int d = 0; d < N; ++d) for (int i = 0; i < N; ++i) for (int j = i; j < N; ++j)
        if (((MASK >> j) & 1) && (i > j - i ? i : j - i) == d && n-- == 0) return i * 8 + j;
    return -1;
}
constexpr int NPRE = 12, NSITE = 28 + 56 + 4, SITE_END = 200;
constexpr int site_at(int q) { return q <= SITE_END ? NPRE + (q * (NSITE - NPRE) + SITE_END - 1) / SITE_END : NSITE; }
constexpr int kstep(int n) { return n % 9 < 8 ? 8 * (n / 9) + n % 9 : 32 + n / 9; }

__global__ void __launch_bounds__(256, 1) k_main(Args) {
    const void* ka = (const void*)(uintptr_t)__builtin_amdgcn_kernarg_segment_ptr();
    v4u rk, rv, rm, rq, rp, rx, vw, ro;
    const char *sk, *sh, *sD;
    int tid = __builtin_amdgcn_workitem_id_x(), b = __builtin_amdgcn_workgroup_id_x();
    sldi<0>(rk, ka); sldi<16>(rv, ka); sldi<144>(sk, ka); sldi<112>(vw, ka); sldi<96>(rx, ka); sldi<128>(ro, ka); sldi<152>(sh, ka); sldi<160>(sD, ka);
    OPQS(b); OPQV(tid);
    int wave = __builtin_amdgcn_readfirstlane(tid >> 6);
    sld(rm, ka, (wave >> 1) * 16); sld(rq, ka, (b & 24) == 24 ? 48 : 32); sld(rp, ka, 64 + (b & 8) * 2);
    OPQS(b); OPQS(wave); OPQV(tid);
    STAMP_INIT();
    asm volatile("" ::: "a255");
    u64 ep;
    v2u sw0, sw1;
    {
        const int lane = tid & 63, c = lane & 15, cg = ((b >> 4) & 6) + ((b >> 3) & 1), tg = 4 * (b & 7) + 2 * (b >> 7) + ((b >> 4) & 1);
        u32 s0 = c * 8 + (wave * 128 + cg * 576), s1 = (c & 7) * 8 + cg * 576, vo[5], lo[5];
        const u32 l0 = ((lane * 7282u) >> 16) * 1008 + lane * 16, t0 = (64 * tg + 7 * wave) * 1152 + cg * 144;
#pragma unroll
        for (int i = 0; i < 5; ++i) {
            const int isv = i < 2 ? 0 : i > 2 ? 1 : wave >> 1;
            vo[i] = l0 + (t0 + (28 * i - 3 - 67 * isv) * 1152); lo[i] = P_KS + wave * 1008 + i * 4032 + isv * (P_VS - 10080);
            OPQV(vo[i]); OPQS(lo[i]);
        }
        pin2(s0, s1);
        FENCE();
        asm volatile("s_waitcnt lgkmcnt(0)" : "+s"(rk), "+s"(rv), "+s"(rm), "+s"(rq), "+s"(rp), "+s"(rx), "+s"(vw), "+s"(ro), "+s"(sk), "+s"(sh), "+s"(sD));
        STAMP(40);
#pragma unroll
        for (int i = 0; i < 5; ++i) dma_b(i < 2 ? rk : i > 2 ? rv : rm, vo[i], lo[i]);
        asm volatile("global_load_dwordx2 %0, %1, %2" : "=v"(sw0) : "v"(s0), "s"(sk) : "memory");
        asm volatile("global_load_dwordx2 %0, %1, %2 offset:512" : "=v"(sw1) : "v"(s1), "s"(sk) : "memory");
        sld(ep, (const void*)(((u64)vw.y << 32) | vw.x), W_E);
    }
    OPQS(b); OPQS(wave); OPQV(tid);
    const int lane = tid & 63, rg = 2 * (b & 7) + (b >> 7), p = (b >> 3) & 15;
    const int Pg = p >> 2, tw = p & 3, pw = 4 * Pg + wave, ci = (rg + p) & 15;
    const bool conv = ci < 12;
    u32 l16 = lane * 16, l4 = lane * 4;
    OPQV(l16); OPQV(l4);
    const int ch0 = 36 * p + 3 * ci;
    u32 xr[9], kvw[3], kvA, kvB, l8, l8B, l16B, cpo, csd;
    {
        const int sec = p & 3, cg = 2 * Pg + (p & 1), tg = 2 * rg + ((p >> 1) & 1);
        const int c = lane & 15;
        FENCE();
        STAMP(41);
        const v4u z4 = {0, 0, 0, 0};
        u32 mk0, mk1, ua0, ua1, uo0, uo1, aw, al_, ap, d0_, d1_, d2_, cso, axs, aid, azc;
        v2u Bi; v4u Sel;
        const bool s3 = sec == 3;
        const u32 wf_ = rg >> 2, jl = rg & 3;
        {
            const u32 g = lane >> 4, m = lane & 31, kh = lane >> 5;
            const u32 hm = g == (c >> 2) ? ((c & 1) ? 0xffff0000u : 0xffffu) : 0u;
            mk0 = (c & 2) ? 0u : hm; mk1 = (c & 2) ? hm : 0u;
            Bi = (v2u){mk0 & 0x3f803f80u, mk1 & 0x3f803f80u}; Sel = (v4u){Bi.x, Bi.y, Bi.x, Bi.y};
            const u32 r0 = (8 * (c >> 2) + (c & 3)) * 144;
            ua0 = r0 + 8 * g + (P_KS + 32 * wave);
            ua1 = r0 + 32 * 144 * kh + 8 * (g & 1) + (P_KS + 128 + 576 * (wave & 1));
            uo0 = c * 4096 + 16 * g;
            uo1 = (c & 7) * 4096 + 64 * (c >> 3) + 16 * g + (wave < 2 ? 64 * 4096 + 8 - 8 * wave : OOB - (1u << 20));
            pin2(mk0, mk1); pin2(Bi, Sel); pin2(ua0, ua1); pin2(uo0, uo1);
            const u32 hD = (m >> 2) & 1;
            aw = (18 * hD + (m & 3) + 4 * (m >> 3)) * 288 + 16 * kh + (P_WS + ((72 * wave) & ~15u));
            al_ = ((((m >> 1) & 1) + 2 * ((m >> 3) & 1)) * 1024 + (2 * hD + (m & 1)) * 96) * (wave == wf_) + 16 * kh + (wave == wf_ ? P_LS : L_Z);
            ap = (8 * kh + (c >> 2)) * 48 + 32 * (g & 1) + 8 * (c & 3) + (P_PH + (64 * wave - 4 * (wave & 1)) * 48);
            pin2(aw, al_); pin(ap);
            const u32 ib = W_IMG + pw * IMG, ob = W_WOT + Pg * WOG;
            const u32 bA = ssel(s3, ob + wave * 36864, ib + sec * L_V + (sec >> 1) * (L_Q - 2 * L_V));
            const u32 bB = ssel(s3, ob + 4 * 36864 + 128 * (wave >> 1) + 32 * (wave & 1), ib + L_QT + 128 * sec);
            const u32 dsB = s3 ? 256 : QTS, drB = s3 ? 1024 : 2 * QTS, ri = (rg - (s3 ? 0 : pw & 14)) & 15;
            const bool lo = m < 16;
            const u32 lp = vsel(lo, ((m & 3) + 8 * (m >> 2) + 32 * kh) * 16 + bA, vsel(m < 18, (m & 1) * (s3 ? 16 : 32) + kh * (s3 ? 512 : 16) + bB, OOB));
            const u32 ds = vsel(lo, 1024, dsB), dr = vsel(lo, 2048, drB);
            d0_ = lp + ri * dr; d1_ = d0_ + ds;
            d2_ = umax(lp + (16 + (jl >> 1)) * dr + (jl & 1) * ds, wave == wf_ ? 0u : OOB);
            cso = vsel(m < (s3 ? 0 : 18), (18 * kh + m) * 4 + (W_CSP + pw * CSB + rg * 576 + sec * 192), OOB);
            axs = m * 304 + 36 * kh + (P_XS + 72 * wave);
            aid = vsel(lane < 36 * (sec & 1), lane * 50 + (lane >= 18 ? 1536 - 900 : 0) + (P_PH + wave * 3072), L_END - 16);
            azc = vsel(lane < (sec ? 0 : 6), (lane & 1) * 16 + ((lane >> 1) + (lane < 4 ? 2 * rg : 30 + jl)) * QTS + (ib + L_QT + 384), OOB);
            pin2(d0_, d1_); pin2(d2_, cso); pin2(axs, aid); pin(azc);
            *(LAS v2u*)(vsel(tid < 38, tid * 80 + (L_KVA + 72), L_END - 16)) = (v2u){0, 0};
            *(LAS v4u*)(l16 + kh * 1024 + (P_PH + 1024 + wave * 3072)) = z4;
            *(LAS v4u*)(vsel(lane < (wave ? 30 : 6), l16 + ssel(wave, L_Q + 4 * HCS - CVS + wave * CVS, L_Z), L_END - 16)) = z4;
        }
        FENCE();
        STAMP(42);
        {
            const u32 wst = s3 ? 1152 : 3456, cb = (sec < 2 ? (sec + 1) * 1152 : 0) + Pg * 288;
            const u32 lw = ((lane * 3641u) >> 16) * (wst - 288) + l16, w0 = (36 * rg + 3 * wave) * wst + cb;
            const u32 r4 = (lane * 43u) >> 8, sg = ((s3 ? 0 : (4 * Pg + wf_) & 14) + 4 * jl + wave) & 15;
            const u32 ll = vsel(lane < 24, r4 * (wst - 96) + (r4 >> 1) * (16 * wst) + l16 + ((36 * sg + 16) * wst + cb + ((72 * wf_) & ~15u)), OOB);
            const u32 lx = ((lane * 3450u) >> 16) * 848 + l16 + ((128 * rg + 32 * tw + 3 * wave) * 1152 + Pg * 288);
            const u32 lp = vsel(lane < 54, l16 - ((lane * 21846u) >> 16) * 12 + (Pg * 5184 + wave * 1296), OOB);
#pragma unroll
            for (int i = 0; i < 3; ++i) dma_b(rq, lw + (w0 + 12 * i * wst), P_WS + wave * 864 + i * 3456);
            dma_b(rq, ll, P_LS + wave * 1024);
#pragma unroll
            for (int i = 0; i < 2; ++i) dma_s(rp, lp, i * 648, P_PH + wave * 3072 + i * 1536);
#pragma unroll
            for (int i = 0; i < 3; ++i) dma_s(rx, lx, 12 * i * 1152, P_XS + wave * 912 + i * 3648);
        }
        FENCE();
        STAMP(26);
        asm volatile("s_waitcnt vmcnt(9)" : "+v"(sw0), "+v"(sw1)::"memory");
        v2u Bd[2][4];
#pragma unroll
        for (int s = 0; s < 2; ++s)
#pragma unroll
            for (int j = 0; j < 4; ++j) {
                const v2u sw = s ? sw1 : sw0;
                const u32 wd = __builtin_amdgcn_perm(0u, j < 2 ? sw.y : sw.x, (j & 1) ? 0x01000100u : 0x03020302u);
                Bd[s][j] = (v2u){wd & mk0, wd & mk1};
                pin(Bd[s][j]);
            }
        u32 so_u = W_UT + 128 * tg + cg * 72 * 4096, so_u2 = so_u + wave * 65536;
        OPQS(so_u); OPQS(so_u2);
        FENCE();
        barrier();
        STAMP(27);
        {
            u32 uo[5][2];
            v2u ka[5][5]; f4 pr[5][5], sm[5];
            v4u bb[2];
#pragma unroll
            for (int T = 0; T < 5; ++T)
#pragma unroll
                for (int j = 0; j < 5; ++j) ka[T][j] = lds2((T < 4 ? ua0 + (32 * (T >> 1) + 4 * (T & 1)) * 144 : ua1) + (j < 4 ? j * 144 : P_VS));
            FENCE();
            auto P = [&](auto T_, auto J_) __attribute__((always_inline)) { constexpr int T = T_.v, j = J_.v; mm16k(pr[T][j], ka[T][j], j < 4 ? Bd[T == 4][j] : Bi); };
            sfor<0, 5>(LAM(T_) { P(T_, IC<0>{}); P(T_, IC<1>{}); });
            sfor<0, 16>(LAM(I_) {
                constexpr int I = I_.v, T = I % 5, ph = I / 5 + 1, U = (I + 4) % 5;
                if constexpr (I < 15) {
                    f4& a = ph == 1 ? pr[T][0] : sm[T];
                    const u32 w0 = pka(a[0], a[1]), w1 = pka(a[2], a[3]), w2 = pka(pr[T][ph][0], pr[T][ph][1]), w3 = pka(pr[T][ph][2], pr[T][ph][3]);
                    bb[I & 1] = (v4u){w0, w1, w2, w3};
                    P(IC<T>{}, IC<ph + 1>{});
                } else {
                    uo[0][0] = pka(sm[0][0], sm[0][1]); uo[0][1] = pka(sm[0][2], sm[0][3]);
                }
                if constexpr (I > 0) mm16<true>(sm[U], Sel, bb[(I - 1) & 1]);
            });
            sfor<0, 5>(LAM(T_) {
                constexpr int T = T_.v;
                u32 w0, w1;
                if constexpr (T) { w0 = pka(sm[T][0], sm[T][1]); w1 = pka(sm[T][2], sm[T][3]); } else { w0 = uo[0][0]; w1 = uo[0][1]; }
                pin(pr[T][4]);
                FENCE();
                uo[T][0] = pk(bf_hi(w1) * pr[T][4][3], bf_lo(w1) * pr[T][4][2]);
                uo[T][1] = pk(bf_hi(w0) * pr[T][4][1], bf_lo(w0) * pr[T][4][0]);
                if constexpr (T == 1 || T == 3) bst4((v4u){uo[T][0], uo[T][1], uo[T - 1][0], uo[T - 1][1]}, vw, uo0 + 32 * (T - 1), so_u2, SC1);
                else if constexpr (T == 4) bst2((v2u){uo[4][0], uo[4][1]}, vw, uo1, so_u, SC1);
                else pin2(uo[T][0], uo[T][1]);
                FENCE();
            });
        }
        STAMP(28);
        asm volatile("s_waitcnt vmcnt(3)\n\ts_barrier" ::: "memory");
        {
            u32 one2 = 0x3f803f80u;
            OPQS(one2);
            *(LAS u16*)(aid) = 0x3f80;
            v4u bw[2][2], wa[2][3];
            bw[0][0] = tr2(ap, ap + 192); wa[0][0] = lds4(aw); wa[1][0] = lds4(al_);
            bw[0][1] = tr2(ap + 768, ap + 960); wa[0][1] = lds4(aw + 32); wa[1][1] = lds4(al_ + 32);
            bw[1][0] = tr2(ap + 1440, ap + 1440 + 192);
            bw[1][1] = tr2(ap + 1440 + 768, ap + 1440 + 960); wa[0][2] = lds4(aw + 64); wa[1][2] = lds4(al_ + 64);
            FENCE();
#pragma unroll
            for (int j = 0; j < 9; ++j) xr[j] = *(const LAS u32*)(axs + 4 * j);
            FENCE();
            f16v d[2][2];
#pragma unroll
            for (int i = 0; i < 4; ++i) { mm32<true>(d[i >> 1][i & 1], wa[i & 1][i >> 1], bw[i >> 1][0]); mm32<false>(d[i >> 1][i & 1], wa[i & 1][(i >> 1) + 1], bw[i >> 1][1]); }
            FENCE();
            {
                float s1 = 0.f, s2 = 0.f;
#pragma unroll
                for (int j = 0; j < 9; ++j) { s1 = dot2(xr[j], one2, s1); s2 = dot2(xr[j], xr[j], s2); }
                const u32 so = W_XF0 + ((rg * 4 + tw) * 16 + pw) * XS;
                bst4((v4u){xr[0], xr[1], xr[2], xr[3]}, vw, l16, so, SC1);
                bst4((v4u){xr[4], xr[5], xr[6], xr[7]}, vw, l16 + 1024, so, SC1);
                l8 = 2 * l4;
                bst2((v2u){xr[8], asu(halves_sum(s1, s2))}, vw, l8 + 2048, so, SC1);
                bst4(z4, vw, azc, 0, SC1);
            }
            FENCE();
            float cs[2];
#pragma unroll
            for (int hh = 0; hh < 2; ++hh) {
                asm volatile("" : "+v"(d[hh][0]), "+v"(d[hh][1]));
                u32 w[12];
#pragma unroll
                for (int j = 0; j < 12; ++j) w[j] = pk(d[hh][j >> 3][2 * (j & 7)], d[hh][j >> 3][2 * (j & 7) + 1]);
                bst4((v4u){w[0], w[1], w[2], w[3]}, vw, d0_ + 64 * hh, 0, SC1);
                bst4((v4u){w[4], w[5], w[6], w[7]}, vw, d1_ + 64 * hh, 0, SC1);
                bst4((v4u){w[8], w[9], w[10], w[11]}, vw, d2_ + 64 * hh, 0, SC1);
                cs[hh] = 0.f;
#pragma unroll
                for (int j = 0; j < 12; ++j) cs[hh] = dot2(w[j], one2, cs[hh]);
                FENCE();
            }
            bst1(asu(halves_sum(cs[0], cs[1])), vw, cso, 0, SC1);
        }
        v4u hd[3]; v2u he[3], hz;
        {
            const char* gp = sh + umin(ch0, 573) * 4096;
            const u32 o1 = tid * 16, o2 = (o1 + 16) & 4095;
            gld2(hz, umin(lane >> 3, 2) * 4096, gp);
#pragma unroll
            for (int i = 0; i < 3; ++i) { gld4<0>(hd[i], o1, gp + i * 4096); gld2(he[i], o2, gp + i * 4096); }
        }
        STAMP(29);
        asm volatile("s_waitcnt vmcnt(7)\n\ts_barrier" ::: "memory");
        OPQS(ep);
        ep += 1;
        const v2u epv = {(u32)ep, (u32)(ep >> 32)};
        if (tid == 0) bst2(epv, vw, 0, W_F + (rg * 16 + p) * 128, SC1);
        STAMP(1);
        {
            const u32 e = (lane >> 4) & 1, kg = lane >> 4;
#pragma unroll
            for (int j = 0; j < 3; ++j) {
                const u32 pi = 16 * (wave + 4 * j) + c, hh = pi >= 86, ip = pi - 86 * hh, q = ip - 72, s = 2 * (ip - 84) + e;
                u32 v = vsel(ip < 64, (ip & 15) * 80 + (ip >> 4) * 8 + 32 * e, (2 * ip + e) * 80 + 64 - 128 * 80);
                v = vsel(ip < 72, v, (16 + (q >> 2)) * 80 + (q & 3) * 8 + 32 * e);
                v = vsel(ip < 84, v, s * 80 + 16 * 80 + 64);
                kvw[j] = vsel(lane >= 32 || pi >= 172 || s == 3, L_EX + l8, v + hh * 1520 + L_KVA);
                OPQV(kvw[j]);
            }
            const bool dt = wave & 1;
            kvA = dt ? vsel(c < 3, 1152 + (c * 4 + kg) * 16, OOB) : l16;
            kvB = vsel(lane < (dt ? 4 : 16), l8 + (dt ? 1344 : 1024), OOB);
            l16B = l16 + XS; l8B = l8 + XS;
            const u32 sc = (lane * 57u) >> 10, r = lane - 18 * sc, hh = r >= 9, pr = r - 9 * hh;
            cpo = vsel(lane < 54, sc * (192 - 144) + l8, OOB);
            csd = vsel(lane < 54, hh * 224 + vsel(pr < 8, sc * 64 + pr * 8, 192 + sc * 8) + L_CS, L_EX + 2048 + l8);
            pin2(kvA, kvB); pin2(l16B, l8B); pin2(cpo, csd); pin(l8);
        }
        FENCE();
        if (wave) waitv<0>(); else waitv<1>();
        STAMP(47);
        if (conv) {
            u32 o = L_Q + 32 + tid * 16;
            OPQV(o);
#pragma unroll
            for (int i = 0; i < 3; ++i) {
                asm volatile("" : "+v"(hd[i]), "+v"(he[i]));
                const u32 d0 = hd[i].x, d1 = hd[i].y, d2 = hd[i].z, d3 = hd[i].w, d4 = he[i].x, d5 = he[i].y;
                const u32 a0 = __builtin_amdgcn_alignbit(d1, d0, 16), a1 = __builtin_amdgcn_alignbit(d2, d1, 16), a2 = __builtin_amdgcn_alignbit(d3, d2, 16),
                          a3 = __builtin_amdgcn_alignbit(d4, d3, 16), a4 = __builtin_amdgcn_alignbit(d5, d4, 16);
                *(LAS v4u*)(o + i * CVS) = hd[i];
                *(LAS v4u*)(o + i * CVS + HCS) = (v4u){a0, a1, a2, a3};
                *(LAS v4u*)(o + i * CVS + 2 * HCS) = (v4u){d1, d2, d3, d4};
                *(LAS v4u*)(o + i * CVS + 3 * HCS) = (v4u){a1, a2, a3, a4};
            }
            asm volatile("" : "+v"(hz));
            const u32 k = (lane >> 1) & 3;
            const u64 t = (lane & 1) && k ? (((u64)hz.y << 32) | hz.x) << (64 - 16 * k) : 0;
            if (tid < 24) *(LAS v4u*)(L_Q + (lane >> 3) * CVS + k * HCS + (lane & 1) * 16) = (v4u){0, 0, (u32)t, (u32)(t >> 32)};
        }
    }
    v2u ls[16];
    {
        u32 D2[3];
#pragma unroll
        for (int i = 0; i < 3; ++i) { const u32 cc = umin(ch0, 573) + i, t = ((const __attribute__((address_space(4))) u32*)(uintptr_t)sD)[cc >> 1]; D2[i] = (cc & 1 ? t >> 16 : t & 0xffffu) * 0x10001u; OPQS(D2[i]); }
        const int bq = lane & 15, g = lane >> 4, cc = (bq + 1) & 3;
        u32 aa = L_Q + cc * HCS + 2 * (bq - 8 * g + 24 - cc), ba = L_Q + 4 * HCS + 480 + 2 * (16 * bq + 8 * g - 16) - 7 * 64;
        u32 yo = (bq >> 3) * YB + (bq & 7) * 32 + 8 * g, yp = (bq >> 3) * YB + (bq & 7) * 32 + 16 * (g >> 1) + (g & 1) * (2 * YB);
        v4u dm;
        {
            const u32 e = 7 - (bq & 7), hm = g == 2 + (bq >> 3) ? ((e & 1) ? 0xffff0000u : 0xffffu) : 0u;
#pragma unroll
            for (int i = 0; i < 4; ++i) dm[i] = (e >> 1) == i ? hm : 0u;
        }
        if (wave < 3) { aa += wave * CVS; ba += wave * CVS; }
        pin2(aa, ba); pin2(yo, yp); pin(dm);
        const u32 xb = W_XF0 + (rg * 4 + wave) * 16 * XS;
        u32 io = umin(22 * wave, 65) * 1024, ig = W_IMG + p * IMG + io, xo = xb + (p & 14) * XS, uo = W_UT + ch0 * 4096 + wave * 1024, co = W_CSP + p * CSB + wave * (4 * 576);
        u32 fo = W_F + (wave ? rg * 16 + bq : (lane >> 2) * 16 + 4 * Pg + (lane & 3)) * 128;
        OPQS(io); OPQS(ig); OPQS(xo); OPQS(uo); OPQS(co); OPQV(fo);
        FENCE();
        if (wave < 2) {
#pragma unroll 1
            for (int sp = 0; sp < SPIN; ++sp) {
                asm volatile("" ::: "memory");
                const v2u f = bld2(vw, fo, 0, SC1);
                if (__builtin_amdgcn_ballot_w64(f.x != (u32)ep || f.y != (u32)(ep >> 32)) == 0) break;
                __builtin_amdgcn_s_sleep(1);
            }
        }
        barrier();
        STAMP(2);
        v2u cp[4];
        auto site = LAM(N_) {
            constexpr int n = N_.v;
            if constexpr (n < 28) {
                if constexpr (n % 5 == 0) { if constexpr (n) { io += 4096; ig += 4096; OPQS(io); OPQS(ig); } setm0(io); if constexpr (n < NPRE) nop2(); }
                else dmai<(n % 5 - 1) * 1024>(vw, l16, ig);
            } else if constexpr (n < 84) {
                constexpr int i = 2 * ((n - 28) / 7), k = (n - 28) % 7;
                if constexpr (k == 0) ld4a<0, 8 * i>(l16, vw, xo);
                if constexpr (k == 1) ld4a<1024, 8 * i + 4>(l16, vw, xo);
                if constexpr (k == 2) ld2<2048>(ls[i], l8, vw, xo);
                if constexpr (k == 3) ld4a<0, 8 * i + 8>(l16B, vw, xo);
                if constexpr (k == 4) ld4a<1024, 8 * i + 12>(l16B, vw, xo);
                if constexpr (k == 5) ld2<2048>(ls[i + 1], l8B, vw, xo);
                if constexpr (k == 6 && i < 14) { xo += 2 * XS; xo = xo == xb + 16 * XS ? xb : xo; OPQS(xo); }
            } else ld2<(n - 84) * 576>(cp[n - 84], cpo, vw, co);
        };
        if (conv) {
#pragma unroll
            for (int i = 0; i < 3; ++i) dma_ws(vw, l16, uo + i * 4096, L_Q + i * CVS + 4 * HCS + 480 + wave * 1024);
            sfor<0, NPRE>(site);
            asm volatile("s_waitcnt vmcnt(%0)\n\ts_barrier" ::"n"(NPRE - (NPRE + 4) / 5) : "memory");
            STAMP(3);
            if (wave < 3) {
                f4 acc[8];
                v4u A[2][8], B[2][8];
                sfor<0, 24>(LAM(N_) { conv_read<8, N_.v, 0>(aa, ba, A[0], B[0]); });
                const u32 dd = wave == 0 ? D2[0] : wave == 1 ? D2[1] : D2[2];
                v4u ad = {dm.x & dd, dm.y & dd, dm.z & dd, dm.w & dd};
                pin(ad);
                FENCE();
                sfor<0, 5>(LAM(J_) { constexpr int j = J_.v ? J_.v + 3 : 0; mm16<true>(acc[j], ad, B[0][j]); });
                FENCE();
                sfor<0, 8 * 27>(LAM(Q_) {
                    constexpr int r = Q_.v / 27, n = Q_.v % 27, i = conv_ij(8, 0xf1, n) >> 3, j = conv_ij(8, 0xf1, n) & 7;
                    if constexpr (r && n == 0) waitl<9>();
                    if constexpr (r && n == 6) waitl<(r < 7 ? 6 : 0)>();
                    mm16<false>(acc[j], A[r & 1][i], B[r & 1][j - i]);
                    if constexpr (r < 7 && n < 24) conv_read<8, n, r + 1>(aa, ba, A[(r + 1) & 1], B[(r + 1) & 1]);
                    sfor<site_at(Q_.v), site_at(Q_.v + 1)>(site);
                    FENCE();
                });
                asm volatile("s_nop 7" : "+v"(acc[0]), "+v"(acc[4]), "+v"(acc[5]), "+v"(acc[6]), "+v"(acc[7]));
                const u32 so = W_Y + (ch0 + wave) * 256;
                const v2u y0 = y_val(acc[0]);
                const v4u y4 = y_pair(y_val(acc[4]), y_val(acc[5])), y6 = y_pair(y_val(acc[6]), y_val(acc[7]));
                asm volatile("s_waitcnt vmcnt(0)" ::"v"(y0), "v"(y4), "v"(y6) : "memory");
                bst2(y0, vw, yo, so, SC1);
                bst4(y4, vw, yp, so + 8 * YB, SC1);
                bst4(y6, vw, yp, so + 12 * YB, SC1);
            } else {
                f4 acc[2][4];
                v4u A[2][4], B[2][4], ad;
                v2u y1; v4u y2;
                sfor<0, 12>(LAM(N_) { conv_read<4, N_.v, 0>(aa, ba, A[0], B[0]); });
                FENCE();
                sfor<0, 3 * 8 * 9>(LAM(Q_) {
                    constexpr int cn = Q_.v / 72, r = Q_.v % 72 / 9, n = Q_.v % 9, i = conv_ij(4, 0xe, n) >> 3, j = conv_ij(4, 0xe, n) & 7;
                    constexpr int nc = r < 7 ? cn : cn + 1, nr = (r + 1) & 7;
                    if constexpr (r == 0 && n == 0) {
                        ad = (v4u){dm.x & D2[cn], dm.y & D2[cn], dm.z & D2[cn], dm.w & D2[cn]};
                        pin(ad);
                        waitl<0>();
                        nop2();
                        FENCE();
                        sfor<1, 4>(LAM(J_) { mm16<true>(acc[cn & 1][J_.v], ad, B[0][J_.v]); });
                    } else {
                        if constexpr (n == 0) waitl<3>();
                        if constexpr (n == 7) waitl<(nc < 3 ? 10 : 0)>();
                    }
                    mm16<false>(acc[cn & 1][j], A[r & 1][i], B[r & 1][j - i]);
                    if constexpr (nc < 3) {
                        if constexpr (n < 3) { conv_read<4, 2 * n, nr>(aa + nc * CVS, ba + nc * CVS, A[nr & 1], B[nr & 1]); conv_read<4, 2 * n + 1, nr>(aa + nc * CVS, ba + nc * CVS, A[nr & 1], B[nr & 1]); }
                        else conv_read<4, n + 3, nr>(aa + nc * CVS, ba + nc * CVS, A[nr & 1], B[nr & 1]);
                    }
                    if constexpr (cn > 0 && r == 1) {
                        const u32 so = W_Y + (ch0 + cn - 1) * 256;
                        if constexpr (n == 3) { pin(acc[(cn - 1) & 1][1]); y1 = y_val(acc[(cn - 1) & 1][1]); pin(y1); }
                        if constexpr (n == 4) bst2(y1, vw, yo, so + 2 * YB, SC1);
                        if constexpr (n == 5) { pin(acc[(cn - 1) & 1][2]); y1 = y_val(acc[(cn - 1) & 1][2]); pin(y1); }
                        if constexpr (n == 6) { pin(acc[(cn - 1) & 1][3]); y2 = y_pair(y1, y_val(acc[(cn - 1) & 1][3])); pin(y2); }
                        if constexpr (n == 7) bst4(y2, vw, yp, so + 4 * YB, SC1);
                    }
                    sfor<site_at(Q_.v), site_at(Q_.v + 1)>(site);
                    FENCE();
                });
                asm volatile("s_nop 7" : "+v"(acc[0][1]), "+v"(acc[0][2]), "+v"(acc[0][3]));
                const u32 so = W_Y + (ch0 + 2) * 256;
                y1 = y_val(acc[0][1]); y2 = y_pair(y_val(acc[0][2]), y_val(acc[0][3]));
                asm volatile("s_waitcnt vmcnt(0)" ::"v"(y1), "v"(y2) : "memory");
                bst2(y1, vw, yo, so + 2 * YB, SC1);
                bst4(y2, vw, yp, so + 4 * YB, SC1);
            }
        } else {
            sfor<0, NSITE>(LAM(N_) { site(N_); if constexpr (N_.v < 28 && N_.v % 5 == 0 && N_.v >= NPRE) nop2(); });
            waitv<0>();
        }
        STAMP(14);
        asm volatile("" : "+v"(cp[0]), "+v"(cp[1]), "+v"(cp[2]), "+v"(cp[3]));
        *(LAS f2*)(L_EX + wave * 512 + l8) = (f2){(asf(cp[0].x) + asf(cp[1].x)) + (asf(cp[2].x) + asf(cp[3].x)), (asf(cp[0].y) + asf(cp[1].y)) + (asf(cp[2].y) + asf(cp[3].y))};
        barrier();
    }
    u32 yv[9];
    v4u wv[17];
    STAMP(15);

    sfor<0, 2>(LAM(L_) {
        constexpr int L = L_.v;
        int ln = lane;
        OPQV(ln); OPQV(l16); OPQV(l4);
        const int tl = ln & 31, h = ln >> 5, row = ln & 15, kg = ln >> 4, gq = row >> 2, rq = row & 3;
        const bool qa = (gq & 1) == (kg & 1);
        u32 aV_ = L_V + l16, aQ_ = L_Q + l16;
        u32 aKV = L_QT + vsel(qa, (rq >> 1) * 128 + (gq >> 1) * 64 + (rq & 1) * 32 + (kg >> 1) * 16, 400 - 16 * (kg >> 1));
        u32 aQq, aCS, aS;
        OPQV(aV_); OPQV(aKV); OPQV(aQ_);
        u32 nretry = 0;
        const u32 wb = W_WOT + Pg * WOG + wave * 36864, wlb = W_WOT + Pg * WOG + 4 * 36864 + (wave >> 1) * 9216;
        u32 wso = wb;
        auto wsite = LAM(N_) {
            constexpr int n = N_.v, k = (n < 36 ? n : n - 36) % 4;
            if constexpr (L == 0 && n < 45) {
                if constexpr (n == 36) wso = wlb; else if constexpr (n && k == 0) wso += 4096;
                if constexpr (k == 0) OPQS(wso);
                if constexpr (n < 28) ld4a<k * 1024, 144 + 4 * n>(l16, vw, wso);
                else ld4o<k * 1024>(wv[n - 28], l16, vw, wso);
            }
        };
        f16v aK; f4 qKV;
        float mean, al, rstd;
        v4u rV[4], cK[5];
        {
            const u32 xb = W_XF1 + (rg * 4 + wave) * 16 * XS;
            u32 xo = xb + (p & 14) * XS;
            OPQS(xo);
            auto xsite = LAM(I_) {
                constexpr int i = I_.v;
                if constexpr (L == 1 && i < 16) {
                    ld4a<0, 8 * i>(i & 1 ? l16B : l16, vw, xo);
                    ld4a<1024, 8 * i + 4>(i & 1 ? l16B : l16, vw, xo);
                    ld2<2048>(ls[i], i & 1 ? l8B : l8, vw, xo);
                    if constexpr ((i & 1) && i < 15) { xo += 2 * XS; xo = xo == xb + 16 * XS ? xb : xo; OPQS(xo); }
                }
            };
            constexpr int UP = 5;
            if constexpr (L == 1) { FENCE(); waitv<0>(); STAMP(25); }
            sfor<0, UP>(xsite);
            v4u r0[3], r1[3], ones = {~0u, ~0u, ~0u, ~0u};
            f2 cq[4];
            u32 cm, cs_;
            float tsum;
#pragma unroll
            for (int s = 0; s < 3; ++s) { r0[s] = lds4(l16 + s * 1024); r1[s] = lds4(aKV + s * QTS); }
            if constexpr (L == 0) pin(ones);
            FENCE();
            sfor<0, 36>(LAM(SI) {
                constexpr int n = SI.v, s = kstep(n), s3 = kstep(n + 3), i = s >> 1;
                if constexpr (s < 32 && !(s & 1)) {
                    if constexpr (L == 1) {
                        constexpr int issued = UP + i < 16 ? UP + i : 16;
                        waitp<3 * (issued - i - 1)>(ls[i]);
                        if (__builtin_expect(bad(mx3(rda<8 * i>(), rda<8 * i + 4>(), ls[i].x)), 0)) {
                            const u32 so = xb + (((p & 14) + i) & 15) * XS;
#pragma unroll 1
                            for (int sp = 0; sp < SPIN; ++sp) {
                                __builtin_amdgcn_s_sleep(2);
                                ld4a<0, 8 * i>(l16, vw, so); ld4a<1024, 8 * i + 4>(l16, vw, so); ld2<2048>(ls[i], l8, vw, so);
                                waitp<0>(ls[i]);
                                ++nretry;
                                if (!bad(mx3(rda<8 * i>(), rda<8 * i + 4>(), ls[i].x))) break;
                            }
                        }
                    } else pin(ls[i]);
                    FENCE();
                }
                waitl<(n < 34 ? 4 : 6)>();
                mm32b<n == 0, 4 * s>(aK, r0[n % 3]);
                if constexpr (n + 3 < 36) r0[n % 3] = lds4(l16 + s3 * 1024); else rV[n - 33] = lds4(aV_ + (n - 33) * 1024);
                if constexpr (s < 32 && !(s & 1)) { xsite(IC<UP + i>{}); wra<128 + i>(ls[i].x); tsum = i ? tsum + asf(ls[i].y) : asf(ls[i].y); pin(tsum); }
                if constexpr (n == 1) { aQq = L_QT + vsel(qa && rq < 2, 256 + (gq >> 1) * 64 + (rq & 1) * 32 + (kg >> 1) * 16, 384); pin(aQq); }
                if constexpr (n == 3) { aCS = L_CS + h * 224; aS = L_STG + (wave * 32 + tl) * STGS + 72 * h; pin2(aCS, aS); }
                if constexpr (L == 0) {
                    constexpr int k = n >> 1;
                    if constexpr (n & 1) {
                        if constexpr (k == 0 || k == 3) { cs_ = W_O + ((k ? 1024 : 0) + (rg * 4 + wave) * 16 + p) * OS; OPQS(cs_); }
                        if constexpr (k == 6) { cs_ = W_XF1 + ((rg * 4 + tw) * 16 + pw) * XS; OPQS(cs_); }
                        if constexpr (k == 9) { cs_ = W_KV + (p * 16 + rg) * 2816 + wave * 704; OPQS(cs_); }
                        if constexpr (k == 10) { cs_ += 256 * 2816; OPQS(cs_); }
                        if constexpr (k == 11) { cs_ = W_ST + ((rg * 4 + tw) * 16 + pw) * 256; OPQS(cs_); }
                        if constexpr (k < 9 && k % 3 < 2) st4<(k % 3) * 1024>(ones, l16, vw, cs_);
                        else if constexpr (k < 9) st4<2048>(ones, cm, vw, cs_);
                        else if constexpr (k < 12) st4<0>(ones, cm, vw, cs_);
                        else wsite(IC<k - 12>{});
                    } else {
                        if constexpr (k == 2) { cm = vsel(ln < 16, l16, OOB); pin(cm); }
                        if constexpr (k == 8) { cm = vsel(ln < 32, l16, OOB); pin(cm); }
                        if constexpr (k == 9) { cm = vsel(ln < 44, l16, OOB); pin(cm); }
                        if constexpr (k == 11) { cm = vsel(ln < 32, row * 16 + (kg & 1) * (1024 * 256), OOB); pin(cm); }
                        if constexpr (k == 13) { cq[0].x = (cq[0].x + cq[1].x) + (cq[2].x + cq[3].x); cq[0].y = (cq[0].y + cq[1].y) + (cq[2].y + cq[3].y); pin(cq[0]); }
                    }
                }
                if constexpr (n == 34) {
                    auto r = __builtin_amdgcn_permlane32_swap(asu(tsum), asu(tsum), false, false);
                    mean = asf(r[0]) * (1.f / 576.f);
                    const float var = __builtin_fmaxf(asf(r[1]) * (1.f / 576.f) - mean * mean, 0.f);
                    rstd = __builtin_amdgcn_rsqf(var + 1e-5f);
                    al = rstd * 1.4426950408889634f;
                    pin2(mean, al); pin(rstd);
                }
                FENCE();
                mm16b<n == 0, 4 * s>(qKV, r1[n % 3]);
                if constexpr (n + 3 < 36) r1[n % 3] = lds4(aKV + s3 * QTS); else cK[n - 33] = lds4(aCS + (n - 33) * 16);
                if constexpr (n == 33) cK[3] = lds4(aCS + 48);
                if constexpr (n == 34) cK[4] = lds4(aCS + 192);
                if constexpr (n == 35) rV[3] = lds4(aV_ + 3 * 1024);
                FENCE();
                if constexpr (L == 0 && n >= 20 && n < 24) { cq[n - 20] = *(const LAS f2*)(l8 + (L_EX + (n - 20) * 512)); FENCE(); }
                if constexpr (L == 0 && n == 27) { *(LAS f2*)(csd) = cq[0]; FENCE(); }
            });
        }
        STAMP(4 + 12 * L);
        if constexpr (L == 1) STAMPV(56, nretry);
        v4u rQ[4], rq4[3];
        u32 pa, pb, kso;
        {
            f16v aV;
            float t[18], e1[18], e2[18], mx, mn, c1, c2, s1, s2;
            v4u cV[4];
            u32 qo = L_Q + wave * 9216, qg = W_IMG + p * IMG + qo;
            sfor<0, 36>(LAM(SI) {
                constexpr int s = SI.v;
                if constexpr (s >= 2 && s < 34 && !(s & 1)) waitl<2>();
                mm32b<s == 0, 4 * s>(aV, rV[s & 3]);
                if constexpr (s == 2) { pin(aK); pin(qKV); }
                if constexpr (s + 4 < 36) rV[s & 3] = lds4(aV_ + (s + 4) * 1024); else cV[s - 32] = lds4(aCS + 64 + (s - 32) * 16);
                if constexpr (s < 18) {
                    if constexpr (s == 0 || s == 8 || s == 16) { if constexpr (s) { qo += 4096; qg += 4096; } OPQS(qo); OPQS(qg); setm0(qo); }
                    if constexpr (s & 1) dmai<((s >> 1) & 3) * 1024>(vw, l16, qg);
                } else if constexpr (s & 1) wsite(IC<6 + (s - 18) / 2>{});
                if constexpr (s == 1) {
                    const u32 r0 = L_STG + (4 * (4 * (kg & 1) + gq) + (kg >> 1)) * STGS + 8 * rq;
                    pa = r0 + 72 * (wave >> 1); pb = r0 + (144 + 40 * (wave >> 1) + 32 * (wave & 1));
                    kso = W_KV + ((L * 16 + p) * 16 + rg) * 2816 + (wave >> 1) * 1376;
                    pin2(pa, pb); OPQS(kso);
                }
                if constexpr (s >= 2 && s < 5) {
#pragma unroll
                    for (int i = 6 * (s - 2); i < 6 * (s - 2) + 6; ++i) { const u32 cv = cK[i >> 2][i & 3]; t[i] = __builtin_fmaf(-mean, asf(cv), i < 16 ? aK[i] : qKV[i - 16]); }
                }
                if constexpr (s == 5) { mx = __builtin_fmaxf(__builtin_fmaxf(t[0], t[1]), t[2]); mn = __builtin_fminf(__builtin_fminf(t[0], t[1]), t[2]);
#pragma unroll
                    for (int i = 3; i < 7; i += 2) { mx = __builtin_fmaxf(__builtin_fmaxf(mx, t[i]), t[i + 1]); mn = __builtin_fminf(__builtin_fminf(mn, t[i]), t[i + 1]); } }
                if constexpr (s == 6) {
#pragma unroll
                    for (int i = 7; i < 13; i += 2) { mx = __builtin_fmaxf(__builtin_fmaxf(mx, t[i]), t[i + 1]); mn = __builtin_fminf(__builtin_fminf(mn, t[i]), t[i + 1]); } }
                if constexpr (s == 7) {
#pragma unroll
                    for (int i = 13; i < 17; i += 2) { mx = __builtin_fmaxf(__builtin_fmaxf(mx, t[i]), t[i + 1]); mn = __builtin_fminf(__builtin_fminf(mn, t[i]), t[i + 1]); }
                    mx = __builtin_fmaxf(mx, t[17]); mn = __builtin_fminf(mn, t[17]); c1 = -al * mx; c2 = al * mn; }
                if constexpr (s >= 8 && s < 32) {
#pragma unroll
                    for (int q = (3 * (s - 8)) / 2; q < (3 * (s - 7)) / 2; ++q) {
                        const int i = q >> 1;
                        if (q & 1) { e2[i] = __builtin_amdgcn_exp2f(__builtin_fmaf(t[i], -al, c2)); s2 = i ? s2 + e2[i] : e2[i]; }
                        else { e1[i] = __builtin_amdgcn_exp2f(__builtin_fmaf(t[i], al, c1)); s1 = i ? s1 + e1[i] : e1[i]; }
                    }
                }
                if constexpr (s == 32) { s1 = __builtin_amdgcn_rcpf(s1); s2 = __builtin_amdgcn_rcpf(s2); }
                if constexpr (s >= 33) {
                    constexpr int i = s - 33;
                    *(LAS v2u*)(aS + 8 * i) = (v2u){pk(e1[4 * i] * s1, e1[4 * i + 1] * s1), pk(e1[4 * i + 2] * s1, e1[4 * i + 3] * s1)};
                }
                FENCE();
            });
            *(LAS v2u*)(aS + 24) = (v2u){pk(e1[12] * s1, e1[13] * s1), pk(e1[14] * s1, e1[15] * s1)};
            *(LAS v2u*)(aS + 32) = (v2u){pk(e1[16] * s1, e1[17] * s1), pk(e2[0] * s2, e2[1] * s2)};
#pragma unroll
            for (int i = 0; i < 4; ++i) *(LAS v2u*)(aS + 40 + 8 * i) = (v2u){pk(e2[4 * i + 2] * s2, e2[4 * i + 3] * s2), pk(e2[4 * i + 4] * s2, e2[4 * i + 5] * s2)};
            FENCE();
            pin(aV);
            const u32 aSv = aS + 144 - 32 * h;
            const float mr = -mean * rstd;
#pragma unroll
            for (int i = 0; i < 4; ++i) {
                const u32 q0 = cV[i].x, q1 = cV[i].y, q2 = cV[i].z, q3 = cV[i].w;
                *(LAS v2u*)(aSv + 8 * i) = (v2u){pk(__builtin_fmaf(mr, asf(q0), aV[4 * i] * rstd), __builtin_fmaf(mr, asf(q1), aV[4 * i + 1] * rstd)),
                                                 pk(__builtin_fmaf(mr, asf(q2), aV[4 * i + 2] * rstd), __builtin_fmaf(mr, asf(q3), aV[4 * i + 3] * rstd))};
            }
            const u32 q2 = cK[4].z, q3 = cK[4].w;
            *(LAS v2u*)(aSv + 32) = (v2u){pk(__builtin_fmaf(mr, asf(q2), qKV[2] * rstd), __builtin_fmaf(mr, asf(q3), qKV[3] * rstd)), 0x3f80u};
        }
        waitbar<(L ? 0 : 9)>();
        STAMP(5 + 12 * L);
        {
            f4 c[3];
            v4u B[4], A[4][3];
            auto rd = LAM(N_) {
                constexpr int ks = N_.v >> 2, f = N_.v & 3;
                if constexpr (f == 0) B[ks] = tr2(pb + ks * 32 * STGS, pb + (ks * 32 + 2) * STGS);
                else A[ks][f - 1] = tr2(pa + ks * 32 * STGS + 32 * (f - 1), pa + (ks * 32 + 2) * STGS + 32 * (f - 1));
            };
            sfor<0, 7>(LAM(N_) { rd(N_); FENCE(); });
            if constexpr (L == 0) { if (tid == 0) bst2((v2u){(u32)ep, (u32)(ep >> 32)}, vw, 0, W_F2 + (rg * 16 + p) * 128, SC1); }
            FENCE();
            sfor<0, 12>(LAM(I_) {
                constexpr int ks = I_.v / 3, ft = I_.v % 3;
                mm16<ks == 0>(c[ft], A[ks][ft], B[ks]);
                if constexpr (I_.v < 9) rd(IC<7 + I_.v>{});
                else { rQ[I_.v - 9] = lds4(aQ_ + (I_.v - 9) * 1024); rq4[I_.v - 9] = lds4(aQq + (I_.v - 9) * QTS); }
                FENCE();
            });
            rQ[3] = lds4(aQ_ + 3 * 1024);
            FENCE();
            pad3<5>(c[0], c[1], c[2]);
            bst4((v4u){pk(c[0][0], c[0][1]), pk(c[0][2], c[0][3]), pk(c[1][0], c[1][1]), pk(c[1][2], c[1][3])}, vw, kvA, kso, SC1);
            bst2((v2u){pk(c[2][0], c[2][1]), pk(c[2][2], c[2][3])}, vw, kvB, kso, SC1);
        }
        FENCE();
        STAMP(6 + 12 * L);
        v4u pq[5], kp[12], sel;
        u32 kvo, kv3, aF, aQ4, f2o, oso;
        u32 kb[4];
        v2u yfl;
        auto flags_ok = [&](u64 m) __attribute__((always_inline)) { return (__builtin_amdgcn_ballot_w64(yfl.x != (u32)ep || yfl.y != (u32)(ep >> 32)) & m) == 0; };
        auto flags_wait = [&](u64 m) __attribute__((always_inline)) {
            if (__builtin_expect(!flags_ok(m), 0)) {
#pragma unroll 1
                for (int sp = 0; sp < SPIN; ++sp) {
                    __builtin_amdgcn_s_sleep(1);
                    ld2<0>(yfl, f2o, vw, W_F2);
                    waitp<0>(yfl);
                    if (flags_ok(m)) break;
                }
            }
        };
        {
            f16v aQ; f4 qQ;
            v4u cQ[5];
            sfor<0, 36>(LAM(SI) {
                constexpr int s = SI.v;
                if constexpr (s >= 1 && s < 34) waitl<4>();
                mm32b<s == 0, 4 * s>(aQ, rQ[s & 3]);
                if constexpr (s + 4 < 36) rQ[s & 3] = lds4(aQ_ + (s + 4) * 1024); else cQ[s - 32] = lds4(aCS + 128 + (s - 32) * 16);
                if constexpr (s < 33 && s % 3 != 2) wsite(IC<15 + s - s / 3>{});
                if constexpr (s == 2) { kvo = kg * 2816 + wave * 256 + row * 16; kv3 = vsel(wave * 16 + row < 44, kvo + 2048, OOB); pin2(kvo, kv3); }
                if constexpr (s == 26) { for (int i = 0; i < 4; ++i) { kb[i] = W_KV + ((L * 16 + p) * 16 + 4 * i) * 2816; OPQS(kb[i]); } }
                if constexpr (s == 5 || s == 8) {
#pragma unroll
                    for (int i = (s - 5) / 3 * 2; i < (s - 5) / 3 * 2 + 2; ++i) sel[i] = row == 2 * i ? 0x3f80u : row == 2 * i + 1 ? 0x3f800000u : 0u;
                    if constexpr (s == 8) pin(sel);
                }
                if constexpr (s == 11) { const int hr = (tl >> 2) & 1, d = (tl & 3) + 4 * (tl >> 3); aF = vsel(h == hr, (hr * 19 + d) * 80 + L_KVA, L_Z); pin(aF); }
                if constexpr (s == 14) { aQ4 = vsel(kg == gq && rq < 3, ((gq >> 1) * 19 + 16 + rq) * 80 + L_KVA, L_Z); pin(aQ4); }
                if constexpr (s == 17) { oso = W_O + (L * 1024 + (rg * 4 + wave) * 16 + p) * OS; OPQS(oso); }
                if constexpr (L == 0 && s == 20) { f2o = vsel(kg == 1, rg * 16 + row, row * 16 + (kg ? pw : p)) * 128; pin(f2o); }
                if constexpr (L == 0 && s == 23) ld2<0>(yfl, f2o, vw, W_F2);
                FENCE();
                mm16b<s == 0, 4 * s>(qQ, rq4[s % 3]);
                if constexpr (s + 3 < 36) rq4[s % 3] = lds4(aQq + (s + 3) * QTS);
                if constexpr (s == 33) cQ[4] = lds4(aCS + 208);
                FENCE();
            });
            if constexpr (L == 0) { waitp<6>(yfl); flags_wait(0xffffull); }
#pragma unroll
            for (int i = 0; i < 4; ++i) {
                kp[i] = bld4(vw, kvo, kb[i], SC1);
                kp[4 + i] = bld4(vw, kvo + 1024, kb[i], SC1);
                kp[8 + i] = bld4(vw, kv3, kb[i], SC1);
            }
            FENCE();
            pin(aQ); pin(qQ);
            float t[18]; u32 ph[18];
#pragma unroll
            for (int i = 0; i < 18; ++i) { const u32 cv = cQ[i >> 2][i & 3]; t[i] = __builtin_fmaf(-mean, asf(cv), i < 16 ? aQ[i] : qQ[i - 16]); }
            hedgehog(t, al, ph);
#pragma unroll
            for (int i = 0; i < 5; ++i) { pq[i] = (v4u){ph[4 * i], ph[4 * i + 1], i < 4 ? ph[4 * i + 2] : 0u, i < 4 ? ph[4 * i + 3] : 0u}; pin(pq[i]); }
            FENCE();
            STAMP(7 + 12 * L);
            u32 m = umax(kp[0].x, kp[0].z);
#pragma unroll
            for (int i = 1; i < 12; ++i) m = mx3(m, kp[i].x, kp[i].z);
            nretry = 0;
            if (__builtin_expect(bad(m), 0)) {
#pragma unroll 1
                for (int sp = 0; sp < SPIN; ++sp) {
                    __builtin_amdgcn_s_sleep(2);
                    asm volatile("" ::: "memory");
                    m = 0;
                    ++nretry;
#pragma unroll
                    for (int i = 0; i < 4; ++i) {
                        kp[i] = bld4(vw, kvo, kb[i], SC1);
                        kp[4 + i] = bld4(vw, kvo + 1024, kb[i], SC1);
                        kp[8 + i] = bld4(vw, kv3, kb[i], SC1);
                    }
#pragma unroll
                    for (int i = 0; i < 12; ++i) m = mx3(m, kp[i].x, kp[i].z);
                    if (!bad(m)) break;
                }
            }
            STAMPV(57 + L, nretry);
            f4 a[3];
            sfor<0, 12>(LAM(I_) { constexpr int i = I_.v / 3, j = I_.v % 3; mm16<i == 0>(a[j], sel, kp[4 * j + i]); });
            pad3<7>(a[0], a[1], a[2]);
#pragma unroll
            for (int j = 0; j < 3; ++j) *(LAS v2u*)(kvw[j]) = (v2u){pk(a[j][0], a[j][1]), pk(a[j][2], a[j][3])};
            barrier();
            STAMP(8 + 12 * L);
        }
        {
            v4u A[5], Aq[5];
#pragma unroll
            for (int c = 0; c < 5; ++c) Aq[c] = lds4(aQ4 + 16 * c);
#pragma unroll
            for (int c = 0; c < 5; ++c) A[c] = lds4(aF + 16 * c);
            FENCE();
            f16v o; f4 oq;
            float rd; u32 ol;
            sfor<0, 5>(LAM(C_) { mm16<C_.v == 0>(oq, Aq[C_.v], pq[C_.v]); });
            sfor<0, 5>(LAM(C_) {
                mm32<C_.v == 0>(o, A[C_.v], pq[C_.v]);
                if constexpr (C_.v == 3) { pad<4>(oq); rd = __builtin_amdgcn_rcpf(oq[2] + 1e-6f); pin(rd); }
                if constexpr (C_.v == 4) { ol = pk(oq[0] * rd, oq[1] * rd); pin(ol); }
                FENCE();
            });
            pad<8>(o);
            bst4((v4u){pk(o[0] * rd, o[1] * rd), pk(o[2] * rd, o[3] * rd), pk(o[4] * rd, o[5] * rd), pk(o[6] * rd, o[7] * rd)}, vw, l16, oso, SC1);
            bst4((v4u){pk(o[8] * rd, o[9] * rd), pk(o[10] * rd, o[11] * rd), pk(o[12] * rd, o[13] * rd), pk(o[14] * rd, o[15] * rd)}, vw, l16 + 1024, oso, SC1);
            bst1(ol, vw, l4 + 2048, oso, SC1);
        }
        FENCE();
        STAMP(9 + 12 * L);
        u32 yf[18], aB, ae, ao;
        f16v aW; float xl0, xl1;
        if constexpr (L == 0) {
            sfor<37, 45>(wsite);
            flags_wait(~0ull);
            u32 so = W_Y + rg * YB + 36 * pw * 256 + tw * 64, yo = h * 18 * 256 + tl * 2;
            OPQS(so); OPQV(yo);
            sfor<0, 18>(LAM(M_) { constexpr int m = M_.v; yf[m] = 0; if constexpr (m == 16) so += 4096; ldh<(m & 15) * 256>(yf[m], yo, vw, so); });
        } else {
#pragma unroll
            for (int i = 0; i < 9; ++i) { yf[2 * i] = yv[i] << 16; yf[2 * i + 1] = yv[i] & 0xffff0000u; pin2(yf[2 * i], yf[2 * i + 1]); }
        }
#pragma unroll
        for (int i = 0; i < 8; ++i) { aW[2 * i] = bf_lo(xr[i]); aW[2 * i + 1] = bf_hi(xr[i]); }
        xl0 = bf_lo(xr[8]); xl1 = bf_hi(xr[8]);
        aB = L_Q + ((wave >> 1) * 18 + (kg & 1)) * 1024 + ((kg >> 1) * 32 + 16 * (wave & 1) + row) * 16;
        ae = L_EX + (kg & 1) * 1024 + ((2 * (wave >> 1) + h) * 16 + row) * 16 + 8 * (wave & 1);
        ao = aQ_ + wave * 8192;
        pin(aW); pin2(xl0, xl1); pin2(aB, ae); pin(ao);
        {
            u32 so[4];
#pragma unroll
            for (int i = 0; i < 4; ++i) { so[i] = W_O + (L * 1024 + (rg * 4 + tw) * 16 + 4 * wave + i) * OS; OPQS(so[i]); }
            v4u lo, og[8];
            FENCE();
            waitv<0>();
            __builtin_amdgcn_s_sleep(OSL);
            auto ldo = [&]() __attribute__((always_inline)) {
#pragma unroll
                for (int i = 0; i < 8; ++i) og[i] = bld4(vw, l16 + (i & 1) * 1024, so[i >> 1], SC1);
            };
            auto ldl = [&]() __attribute__((always_inline)) {
#pragma unroll
                for (int i = 0; i < 4; ++i) lo[i] = bld1(vw, l4 + 2048, so[i], SC1);
            };
            auto chk = [&]() __attribute__((always_inline)) {
                u32 m = mx3(umax(lo.x, lo.y), lo.z, lo.w);
#pragma unroll
                for (int i = 0; i < 8; i += 2) m = mx3(m, og[i].x, og[i + 1].x);
                return bad(m);
            };
            ldo(); ldl();
            FENCE();
            nretry = 0;
            if (__builtin_expect(chk(), 0)) {
#pragma unroll 1
                for (int sp = 0; sp < SPIN; ++sp) {
                    ++nretry;
                    if (bad(mx3(umax(lo.x, lo.y), lo.z, lo.w))) { __builtin_amdgcn_s_sleep(1); asm volatile("" ::: "memory"); ldl(); continue; }
                    asm volatile("" ::: "memory");
                    ldo();
                    if (!chk()) break;
                }
            }
            STAMPV(59 + L, nretry);
#pragma unroll
            for (int i = 0; i < 8; ++i) *(LAS v4u*)(ao + i * 1024) = og[i];
            *(LAS v4u*)(aQ_ + (32 + wave) * 1024) = lo;
            barrier();
        }
        STAMP(10 + 12 * L);
        u32 sto = W_ST + (L * 1024 + (rg * 4 + tw) * 16) * 256;
        {
            f4 qW;
            v4u rb_[4], rl[9];
            v2u e0, e1;
            float s1, s2;
#pragma unroll
            for (int i = 0; i < 4; ++i) { rl[i] = lds4(aB + i * 2048); rb_[i] = lds4(aQ_ + i * 1024); }
            FENCE();
            sfor<0, 36>(LAM(SI) {
                constexpr int s = SI.v;
                if constexpr (s < 28) mm32a<false, 144 + 4 * s>(aW, rb_[s & 3]); else mm32<false>(aW, wv[s - 28], rb_[s & 3]);
                if constexpr (s + 4 < 36) rb_[s & 3] = lds4(aQ_ + (s + 4) * 1024);
                if constexpr (s < 5) rl[4 + s] = lds4(aB + (4 + s) * 2048);
                if constexpr (s == 12) { pad<0>(qW); *(LAS f4*)(l16 + (L_EX + wave * 1024)) = qW; }
                if constexpr (s == 14) OPQS(sto);
                FENCE();
                if constexpr (s < 9) { mm16<s == 0>(qW, wv[8 + s], rl[s]); FENCE(); }
                if constexpr (s == 30) { barrier(); e0 = lds2(ae); e1 = lds2(ae + 2048); FENCE(); }
                if constexpr (s == 33) { xl0 += asf(e0.x) + asf(e1.x); xl1 += asf(e0.y) + asf(e1.y); s1 = xl0 + xl1; s2 = __builtin_fmaf(xl0, xl0, xl1 * xl1); pin2(s1, s2); FENCE(); }
            });
            STAMP(11 + 12 * L);
            pad<11>(aW);
            float s3 = aW[0], s4 = aW[0] * aW[0];
#pragma unroll
            for (int i = 1; i < 16; ++i) { if (i & 1) { s1 += aW[i]; s2 = __builtin_fmaf(aW[i], aW[i], s2); } else { s3 += aW[i]; s4 = __builtin_fmaf(aW[i], aW[i], s4); } }
            bst1(asu(halves_sum(s1 + s3, s2 + s4)), vw, l4, sto + pw * 256, SC1);
        }
        FENCE();
        {
            float t;
            u32 xso = L ? (rg * 128 + tw * 32) * 1152 + 72 * pw : W_XF1 + ((rg * 4 + tw) * 16 + pw) * XS, oo = tl * 1152 + 36 * h, one2 = 0x3f803f80u;
            OPQS(xso); OPQS(one2);
            if constexpr (L == 1) OPQV(oo);
            if constexpr (L == 0) {
#pragma unroll
                for (int i = 0; i < 9; ++i) { pin2(yf[2 * i], yf[2 * i + 1]); yv[i] = (yf[2 * i] >> 16) | yf[2 * i + 1]; pin(yv[i]); }
            }
            FENCE();
            waitv<0>();
            nretry = 0;
#pragma unroll 1
            for (int sp = 0; sp < SPIN; ++sp) {
                asm volatile("" : "+v"(l4)::"memory");
                u32 e[16];
#pragma unroll
                for (int i = 0; i < 16; ++i) e[i] = bld1(vw, l4 + i * 256, sto, SC1);
                FENCE();
                u32 m = umax(e[0], e[1]);
                t = asf(e[0]) + asf(e[1]);
#pragma unroll
                for (int i = 2; i < 16; i += 2) { m = mx3(m, e[i], e[i + 1]); t += asf(e[i]) + asf(e[i + 1]); pin2(m, t); }
                if (!bad(m)) break;
                ++nretry;
                __builtin_amdgcn_s_sleep(1);
            }
            STAMP(12 + 12 * L);
            STAMPV(61 + L, nretry);
            auto r = __builtin_amdgcn_permlane32_swap(asu(t), asu(t), false, false);
            const float m1 = asf(r[0]) * (1.f / 576.f);
            const float r1 = __builtin_amdgcn_rsqf(__builtin_fmaxf(asf(r[1]) * (1.f / 576.f) - m1 * m1, 0.f) + 1e-5f), mr = -m1 * r1;
#pragma unroll
            for (int i = 0; i < 9; ++i) {
                const float a = i < 8 ? aW[2 * i] : xl0, c = i < 8 ? aW[2 * i + 1] : xl1;
                xr[i] = pk(__builtin_fmaf(__builtin_fmaf(a, r1, mr), asf(yf[2 * i]), a), __builtin_fmaf(__builtin_fmaf(c, r1, mr), asf(yf[2 * i + 1]), c));
                if constexpr (L == 0) {
                    if (i == 3) { bst4((v4u){xr[0], xr[1], xr[2], xr[3]}, vw, l16, xso, SC1); FENCE(); }
                    if (i == 7) { bst4((v4u){xr[4], xr[5], xr[6], xr[7]}, vw, l16 + 1024, xso, SC1); FENCE(); }
                } else {
                    if (i == 3) { bst4((v4u){xr[0], xr[1], xr[2], xr[3]}, ro, oo, xso, 0); FENCE(); }
                    if (i == 7) { bst4((v4u){xr[4], xr[5], xr[6], xr[7]}, ro, oo + 16, xso, 0); FENCE(); }
                    if (i == 8) bst1(xr[8], ro, oo + 32, xso, 0);
                }
            }
            if constexpr (L == 0) {
                float s1 = 0.f, s2 = 0.f;
#pragma unroll
                for (int j = 0; j < 9; ++j) { s1 = dot2(xr[j], one2, s1); s2 = dot2(xr[j], xr[j], s2); }
                bst2((v2u){xr[8], asu(halves_sum(s1, s2))}, vw, l8 + 2048, xso, SC1);
                STAMP(13);
            } else {
                if (b == 0 && tid == 0) bst2((v2u){(u32)ep, (u32)(ep >> 32)}, vw, 0, W_E, SC1);
            }
        }
    });
    STAMP_END();
}
#if defined(__HIP_DEVICE_COMPILE__)
#pragma clang attribute pop
#endif

extern "C" void kernel_launch(void* const* d_in, const int* in_sizes, int n_in, void* d_out, int out_size, void* d_ws, size_t ws_size, hipStream_t stream) {
    (void)in_sizes; (void)n_in; (void)out_size; (void)ws_size;
    hipFuncSetAttribute((const void*)k_main, hipFuncAttributeMaxDynamicSharedMemorySize, L_END);
    const u32 f = 0x00020000u, n = 2048 * 1152;
    Args a = {{d_in[5], n, f}, {d_in[6], n, f}, {{d_in[1], 576 * 3456, f}, {d_in[3], 576 * 1152, f}}, {{d_in[2], 32 * 648, f}, {d_in[2], 0, f}}, {d_in[0], n, f}, {d_ws, W_END, f}, {d_out, n, f}, d_in[7], d_in[8], d_in[9]};
    k_main<<<256, 256, L_END, stream>>>(a);
}
```

```cpp
#include <hip/hip_runtime.h>
#include <stdint.h>
#pragma clang diagnostic ignored "-Wint-to-pointer-cast"
#pragma clang diagnostic ignored "-Wunused-value"
#pragma clang diagnostic ignored "-Winline-asm"
#if defined(__HIP_DEVICE_COMPILE__)
#pragma clang attribute push(__attribute__((target("no-packed-fp32-ops"))), apply_to = function)
#endif
typedef unsigned short u16;
typedef unsigned u32;
typedef unsigned long long u64;
typedef u32 v4u __attribute__((ext_vector_type(4)));
typedef u32 v2u __attribute__((ext_vector_type(2)));
typedef float f4 __attribute__((ext_vector_type(4)));
typedef float f2 __attribute__((ext_vector_type(2)));
typedef float f16v __attribute__((ext_vector_type(16)));
typedef __bf16 b8 __attribute__((ext_vector_type(8)));
typedef __bf16 b2 __attribute__((ext_vector_type(2)));
typedef short s4 __attribute__((ext_vector_type(4)));
#define LAS __attribute__((address_space(3)))
#define DEV __device__ __forceinline__
#define SC1 16

constexpr u32 QTS = 416, STGS = 232, CVS = 21216, HCS = 4160;
constexpr u32 L_K = 0, L_V = 36864, L_QT = 73728, L_Q = 89088, L_STG = 125952, L_KVA = 155648, L_CS = 158784, L_EX = 159232, L_END = 163328;
constexpr u32 P_KS = 0, P_VS = 10240, P_WS = 20480, P_XS = 31232, P_PH = 42496, P_LS = 55296, L_Z = L_KVA + 38 * 80;
constexpr u32 IMG = L_STG, XS = 2560, OS = 2304, YB = 576 * 256, CSB = 16 * 576;
constexpr u32 WOG = 4 * 36864 + 18432;
constexpr u32 W_IMG = 0, W_WOT = W_IMG + 16 * IMG, W_CSP = W_WOT + 4 * WOG, W_UT = W_CSP + 16 * CSB,
              W_XF0 = W_UT + 576 * 4096, W_XF1 = W_XF0 + 1024 * XS, W_O = W_XF1 + 1024 * XS, W_Y = W_O + 2 * 1024 * OS, W_KV = W_Y + 576 * 4096,
              W_ST = W_KV + 2 * 256 * 2816, W_F = W_ST + 2 * 1024 * 256, W_F2 = W_F + 256 * 128, W_E = W_F2 + 256 * 128, W_END = W_E + 128, W_DBG = W_END;
constexpr u32 OOB = 0x7ffffff0u;
constexpr int SPIN = 1 << 14;
#define OSL 2

DEV float asf(u32 v) { return __builtin_bit_cast(float, v); }
DEV u32 asu(float v) { return __builtin_bit_cast(u32, v); }
DEV float bf_lo(u32 v) { return asf(v << 16); }
DEV float bf_hi(u32 v) { return asf(v & 0xffff0000u); }
DEV u32 pk(float lo, float hi) { b2 r = __builtin_convertvector((f2){lo, hi}, b2); return __builtin_bit_cast(u32, r); }
DEV float rb(float f) { return bf_lo(pk(f, f)); }
DEV float dot2(u32 a, u32 b, float c) { return __builtin_amdgcn_fdot2_f32_bf16(__builtin_bit_cast(b2, a), __builtin_bit_cast(b2, b), c, false); }
DEV u32 umin(u32 a, u32 b) { return a < b ? a : b; }
DEV u32 umax(u32 a, u32 b) { return a > b ? a : b; }
DEV u32 mx3(u32 a, u32 b, u32 c) { return umax(umax(a, b), c); }
template <bool Z> DEV void mm32(f16v& c, v4u a, v4u b) {
    if constexpr (Z) asm volatile("v_mfma_f32_32x32x16_bf16 %0, %1, %2, 0" : "=&v"(c) : "v"(a), "v"(b));
    else asm volatile("v_mfma_f32_32x32x16_bf16 %0, %1, %2, %0" : "+v"(c) : "v"(a), "v"(b));
}
template <bool Z> DEV void mm16(f4& c, v4u a, v4u b) {
    if constexpr (Z) asm volatile("v_mfma_f32_16x16x32_bf16 %0, %1, %2, 0" : "=&v"(c) : "v"(a), "v"(b));
    else asm volatile("v_mfma_f32_16x16x32_bf16 %0, %1, %2, %0" : "+v"(c) : "v"(a), "v"(b));
}
template <bool Z, int R> DEV void mm32b(f16v& c, v4u a) {
    if constexpr (Z) asm volatile("v_mfma_f32_32x32x16_bf16 %0, %1, a[%c2:%c3], 0" : "=&v"(c) : "v"(a), "n"(R), "n"(R + 3));
    else asm volatile("v_mfma_f32_32x32x16_bf16 %0, %1, a[%c2:%c3], %0" : "+v"(c) : "v"(a), "n"(R), "n"(R + 3));
}
template <bool Z, int R> DEV void mm16b(f4& c, v4u a) {
    if constexpr (Z) asm volatile("v_mfma_f32_16x16x32_bf16 %0, %1, a[%c2:%c3], 0" : "=&v"(c) : "v"(a), "n"(R), "n"(R + 3));
    else asm volatile("v_mfma_f32_16x16x32_bf16 %0, %1, a[%c2:%c3], %0" : "+v"(c) : "v"(a), "n"(R), "n"(R + 3));
}
template <bool Z, int R> DEV void mm32a(f16v& c, v4u b) {
    if constexpr (Z) asm volatile("v_mfma_f32_32x32x16_bf16 %0, a[%c2:%c3], %1, 0" : "=&v"(c) : "v"(b), "n"(R), "n"(R + 3));
    else asm volatile("v_mfma_f32_32x32x16_bf16 %0, a[%c2:%c3], %1, %0" : "+v"(c) : "v"(b), "n"(R), "n"(R + 3));
}
template <int N, class T> DEV void pad(T& c) { asm volatile("s_nop %1" : "+v"(c) : "n"(N)); }
template <class T> DEV void pin(T& c) { asm volatile("" : "+v"(c)); }
DEV void nop2() { asm volatile("s_nop 1"); }
template <int R> DEV u32 rda() { u32 v; asm volatile("v_accvgpr_read_b32 %0, a%c1" : "=v"(v) : "n"(R)); return v; }
template <int R> DEV void wra(u32 v) { asm volatile("v_accvgpr_write_b32 a%c1, %0" ::"v"(v), "n"(R)); }
__device__ void bst4(v4u, v4u, u32, u32, int) __asm("llvm.amdgcn.raw.buffer.store.v4i32");
__device__ void bst2(v2u, v4u, u32, u32, int) __asm("llvm.amdgcn.raw.buffer.store.v2i32");
__device__ void bst1(u32, v4u, u32, u32, int) __asm("llvm.amdgcn.raw.buffer.store.i32");
__device__ v4u bld4(v4u, u32, u32, int) __asm("llvm.amdgcn.raw.buffer.load.v4i32");
__device__ v2u bld2(v4u, u32, u32, int) __asm("llvm.amdgcn.raw.buffer.load.v2i32");
__device__ u32 bld1(v4u, u32, u32, int) __asm("llvm.amdgcn.raw.buffer.load.i32");
struct Buf { const void* p; u32 n, f; };
struct Args { Buf k, v, w[2], ph[2], x, ws, out; const void *sk, *sh, *sD; };
template <class T> DEV void sld(T& d, const void* ka, u32 off) {
    if constexpr (sizeof(T) == 16) asm volatile("s_load_dwordx4 %0, %1, %2" : "=s"(d) : "s"(ka), "s"(off));
    else asm volatile("s_load_dwordx2 %0, %1, %2" : "=s"(d) : "s"(ka), "s"(off));
}
template <int O, class T> DEV void sldi(T& d, const void* ka) {
    if constexpr (sizeof(T) == 16) asm volatile("s_load_dwordx4 %0, %1, %2" : "=s"(d) : "s"(ka), "n"(O));
    else asm volatile("s_load_dwordx2 %0, %1, %2" : "=s"(d) : "s"(ka), "n"(O));
}
DEV v4u lds4(u32 a) { return *(const LAS v4u*)a; }
DEV v2u lds2(u32 a) { return *(const LAS v2u*)a; }
DEV void barrier() { asm volatile("s_waitcnt lgkmcnt(0)\n\ts_barrier" ::: "memory"); }
template <int N> DEV void waitv() { asm volatile("s_waitcnt vmcnt(%0)" ::"n"(N) : "memory"); }
DEV void dma_in(const void* g, u32 voff, u32 lds) {
    asm volatile("s_mov_b32 m0, %2\n\ts_nop 0\n\tglobal_load_lds_dwordx4 %0, %1" ::"v"(voff), "s"(g), "s"(lds) : "m0", "memory");
}
DEV void dma_b(v4u rs, u32 voff, u32 lds) {
    asm volatile("s_mov_b32 m0, %2\n\ts_nop 0\n\tbuffer_load_dwordx4 %0, %1, 0 offen lds" ::"v"(voff), "s"(rs), "s"(lds) : "m0", "memory");
}
DEV void dma_s(v4u rs, u32 voff, u32 so, u32 lds) {
    asm volatile("s_mov_b32 m0, %3\n\ts_nop 0\n\tbuffer_load_dwordx4 %0, %1, %2 offen lds" ::"v"(voff), "s"(rs), "s"(so), "s"(lds) : "m0", "memory");
}
DEV void dma_ws(v4u rs, u32 voff, u32 so, u32 lds) {
    asm volatile("s_mov_b32 m0, %3\n\ts_nop 0\n\tbuffer_load_dwordx4 %0, %1, %2 offen sc1 lds" ::"v"(voff), "s"(rs), "s"(so), "s"(lds) : "m0", "memory");
}
template <int IM> DEV void gld4(v4u& d, u32 voff, const void* g) { asm volatile("global_load_dwordx4 %0, %1, %2 offset:%3" : "=v"(d) : "v"(voff), "s"(g), "n"(IM) : "memory"); }
DEV void gld2(v2u& d, u32 voff, const void* g) { asm volatile("global_load_dwordx2 %0, %1, %2" : "=v"(d) : "v"(voff), "s"(g) : "memory"); }
DEV void mm16k(f4& c, v2u a, v2u b) { asm volatile("v_mfma_f32_16x16x16_bf16 %0, %1, %2, 0" : "=&v"(c) : "v"(a), "v"(b)); }
DEV void mm16n(f4& c, v4u a, v4u b) { asm volatile("s_nop 1\n\tv_mfma_f32_16x16x32_bf16 %0, %1, %2, 0" : "=&v"(c) : "v"(a), "v"(b)); }
template <class A, class B> DEV void pin2(A& a, B& b) { asm volatile("" : "+v"(a), "+v"(b)); }
template <int IM> DEV void ld4(v4u& d, u32 voff, v4u rs, u32 so) { asm volatile("buffer_load_dwordx4 %0, %1, %2, %3 offen offset:%4 sc1" : "+v"(d) : "v"(voff), "s"(rs), "s"(so), "n"(IM) : "memory"); }
template <int IM> DEV void ld1(u32& d, u32 voff, v4u rs, u32 so) { asm volatile("buffer_load_dword %0, %1, %2, %3 offen offset:%4 sc1" : "+v"(d) : "v"(voff), "s"(rs), "s"(so), "n"(IM) : "memory"); }
template <int IM> DEV void ld4m(v4u& d, u32 voff, v4u rs, u32 so, u64 m) {
    asm volatile("s_mov_b64 exec, %5\n\tbuffer_load_dwordx4 %0, %1, %2, %3 offen offset:%4 sc1\n\ts_mov_b64 exec, -1" : "+v"(d) : "v"(voff), "s"(rs), "s"(so), "n"(IM), "s"(m) : "memory");
}
template <int IM, int R> DEV void ld4a(u32 voff, v4u rs, u32 so) { asm volatile("buffer_load_dwordx4 a[%c4:%c5], %0, %1, %2 offen offset:%3 sc1" ::"v"(voff), "s"(rs), "s"(so), "n"(IM), "n"(R), "n"(R + 3) : "memory"); }
template <int IM, int R> DEV void ld1a(u32 voff, v4u rs, u32 so) { asm volatile("buffer_load_dword a%c4, %0, %1, %2 offen offset:%3 sc1" ::"v"(voff), "s"(rs), "s"(so), "n"(IM), "n"(R) : "memory"); }
template <int IM, int R> DEV void ld4am(u32 voff, v4u rs, u32 so, u64 m) {
    asm volatile("s_mov_b64 exec, %6\n\tbuffer_load_dwordx4 a[%c4:%c5], %0, %1, %2 offen offset:%3 sc1\n\ts_mov_b64 exec, -1" ::"v"(voff), "s"(rs), "s"(so), "n"(IM), "n"(R), "n"(R + 3), "s"(m) : "memory");
}
template <int IM> DEV void ld2(v2u& d, u32 voff, v4u rs, u32 so) { asm volatile("buffer_load_dwordx2 %0, %1, %2, %3 offen offset:%4 sc1" : "=v"(d) : "v"(voff), "s"(rs), "s"(so), "n"(IM) : "memory"); }
template <int IM> DEV void ld4o(v4u& d, u32 voff, v4u rs, u32 so) { asm volatile("buffer_load_dwordx4 %0, %1, %2, %3 offen offset:%4 sc1" : "=v"(d) : "v"(voff), "s"(rs), "s"(so), "n"(IM) : "memory"); }
template <int IM> DEV void ldh(u32& d, u32 voff, v4u rs, u32 so) { asm volatile("buffer_load_short_d16_hi %0, %1, %2, %3 offen offset:%4 sc1" : "+v"(d) : "v"(voff), "s"(rs), "s"(so), "n"(IM) : "memory"); }
template <int IM> DEV void st4(v4u d, u32 voff, v4u rs, u32 so) { asm volatile("buffer_store_dwordx4 %0, %1, %2, %3 offen offset:%4 sc1" ::"v"(d), "v"(voff), "s"(rs), "s"(so), "n"(IM) : "memory"); }
DEV void setm0(u32 v) { asm volatile("s_mov_b32 m0, %0" ::"s"(v) : "m0", "memory"); }
template <int IM> DEV void dmai(v4u rs, u32 voff, u32 so) { asm volatile("buffer_load_dwordx4 %0, %1, %2 offen offset:%3 sc1 lds" ::"v"(voff), "s"(rs), "s"(so), "n"(IM) : "memory"); }
DEV u32 pka(float a, float b) { u32 r; asm volatile("v_cvt_pk_bf16_f32 %0, %1, %2" : "=v"(r) : "v"(a), "v"(b)); return r; }
template <int N> DEV void waitl() { __builtin_amdgcn_s_waitcnt(0xc07f | (N << 8)); }
DEV u32 vsel(bool c, u32 a, u32 b) { asm volatile("" : "+v"(a)); asm volatile("" : "+v"(b)); return c ? a : b; }
DEV u32 ssel(bool c, u32 a, u32 b) { asm volatile("" : "+s"(a)); asm volatile("" : "+s"(b)); return c ? a : b; }
template <int N, class T> DEV void waitp(T& d) { asm volatile("s_waitcnt vmcnt(%1)" : "+v"(d) : "n"(N) : "memory"); }
template <int N> DEV void waitbar() { asm volatile("s_waitcnt vmcnt(%0) lgkmcnt(0)\n\ts_barrier" ::"n"(N) : "memory"); }
template <int N> DEV void pad3(f4& a, f4& b, f4& c) { asm volatile("s_nop %3" : "+v"(a), "+v"(b), "+v"(c) : "n"(N)); }
template <int I> struct IC { static constexpr int v = I; };
template <int B, int E, class F> DEV void sfor(F&& f) { if constexpr (B < E) { f(IC<B>{}); sfor<B + 1, E>(f); } }
#define LAM(I) [&](auto I) __attribute__((always_inline))
#define FENCE() __builtin_amdgcn_sched_barrier(0)
#define OPQV(x) asm volatile("" : "+v"(x))
#define OPQS(x) asm volatile("" : "+s"(x))
DEV float halves_sum(float a, float b) {
    auto r = __builtin_amdgcn_permlane32_swap(asu(a), asu(b), false, false);
    return asf(r[0]) + asf(r[1]);
}
DEV bool bad(u32 m) { return __builtin_amdgcn_ballot_w64(m == ~0u) != 0; }
#define STAMP_INIT()
template <int I> DEV void mark() { asm volatile("; MARK %0" ::"n"(I)); }
#define STAMP(i) mark<(i)>()
#define STAMP_END()
#define STAMPV(i, v)

DEV void hedgehog(const float (&t)[18], float a, u32 (&ph)[18]) {
    float mx = t[0], mn = t[0];
#pragma unroll
    for (int i = 1; i < 18; ++i) { mx = __builtin_fmaxf(mx, t[i]); mn = __builtin_fminf(mn, t[i]); }
    const float c1 = -a * mx, c2 = a * mn;
    float e1[18], e2[18], s1 = 0.f, s2 = 0.f;
#pragma unroll
    for (int i = 0; i < 18; ++i) {
        e1[i] = __builtin_amdgcn_exp2f(__builtin_fmaf(t[i], a, c1)); s1 = i ? s1 + e1[i] : e1[i];
        e2[i] = __builtin_amdgcn_exp2f(__builtin_fmaf(t[i], -a, c2)); s2 = i ? s2 + e2[i] : e2[i];
    }
    const float r1 = __builtin_amdgcn_rcpf(s1), r2 = __builtin_amdgcn_rcpf(s2);
#pragma unroll
    for (int i = 0; i < 9; ++i) { ph[i] = pk(e1[2 * i] * r1, e1[2 * i + 1] * r1); ph[9 + i] = pk(e2[2 * i] * r2, e2[2 * i + 1] * r2); }
}
DEV v4u tr2(u32 a0, u32 a1) {
    s4 lo = __builtin_amdgcn_ds_read_tr16_b64_v4i16((LAS s4*)a0), hi = __builtin_amdgcn_ds_read_tr16_b64_v4i16((LAS s4*)a1);
    v2u l = __builtin_bit_cast(v2u, lo), h = __builtin_bit_cast(v2u, hi);
    return (v4u){l.x, l.y, h.x, h.y};
}
DEV v2u y_val(f4 acc) { return (v2u){pk(acc[0], acc[1]), pk(acc[2], acc[3])}; }
DEV v4u y_pair(v2u a, v2u b) {
    auto r0 = __builtin_amdgcn_permlane16_swap(a.x, b.x, false, false), r1 = __builtin_amdgcn_permlane16_swap(a.y, b.y, false, false);
    return (v4u){r0[0], r1[0], r0[1], r1[1]};
}
template <int N, int n, int r>
DEV void conv_read(u32 aa, u32 ba, v4u (&A)[N], v4u (&B)[N]) {
    constexpr int i = n / 3, k = n % 3;
    if constexpr (k == 0) B[i] = lds4(ba + 64 * (8 * i - r + 7));
    else { const v2u v = lds2(aa + 2 + 64 * (r + 8 * i) + 8 * (k - 1)); if constexpr (k == 1) { A[i].x = v.x; A[i].y = v.y; } else { A[i].z = v.x; A[i].w = v.y; } }
}
constexpr int conv_ij(int N, int MASK, int n) {
    for (int d = 0; d < N; ++d) for (int i = 0; i < N; ++i) for (int j = i; j < N; ++j)
        if (((MASK >> j) & 1) && (i > j - i ? i : j - i) == d && n-- == 0) return i * 8 + j;
    return -1;
}
constexpr int NPRE = 12, NSITE = 28 + 56 + 4, SITE_END = 200;
constexpr int site_at(int q) { return q <= SITE_END ? NPRE + (q * (NSITE - NPRE) + SITE_END - 1) / SITE_END : NSITE; }
constexpr int kstep(int n) { return n % 9 < 8 ? 8 * (n / 9) + n % 9 : 32 + n / 9; }

__global__ void __launch_bounds__(256, 1) k_main(Args) {
    const void* ka = (const void*)(uintptr_t)__builtin_amdgcn_kernarg_segment_ptr();
    v4u rk, rv, rm, rq, rp, rx, vw, ro;
    const char *sk, *sh, *sD;
    int tid = __builtin_amdgcn_workitem_id_x(), b = __builtin_amdgcn_workgroup_id_x();
    sldi<0>(rk, ka); sldi<16>(rv, ka); sldi<144>(sk, ka); sldi<112>(vw, ka); sldi<96>(rx, ka); sldi<128>(ro, ka); sldi<152>(sh, ka); sldi<160>(sD, ka);
    OPQS(b); OPQV(tid);
    int wave = __builtin_amdgcn_readfirstlane(tid >> 6);
    sld(rm, ka, (wave >> 1) * 16); sld(rq, ka, (b & 24) == 24 ? 48 : 32); sld(rp, ka, 64 + (b & 8) * 2);
    OPQS(b); OPQS(wave); OPQV(tid);
    STAMP_INIT();
    asm volatile("" ::: "a255");
    u64 ep;
    v2u sw0, sw1;
    {
        const int lane = tid & 63, c = lane & 15, cg = ((b >> 4) & 6) + ((b >> 3) & 1), tg = 4 * (b & 7) + 2 * (b >> 7) + ((b >> 4) & 1);
        u32 s0 = c * 8 + (wave * 128 + cg * 576), s1 = (c & 7) * 8 + cg * 576, vo[5], lo[5];
        const u32 l0 = ((lane * 7282u) >> 16) * 1008 + lane * 16, t0 = (64 * tg + 7 * wave) * 1152 + cg * 144;
#pragma unroll
        for (int i = 0; i < 5; ++i) {
            const int isv = i < 2 ? 0 : i > 2 ? 1 : wave >> 1;
            vo[i] = l0 + (t0 + (28 * i - 3 - 67 * isv) * 1152); lo[i] = P_KS + wave * 1008 + i * 4032 + isv * (P_VS - 10080);
            OPQV(vo[i]); OPQS(lo[i]);
        }
        pin2(s0, s1);
        FENCE();
        asm volatile("s_waitcnt lgkmcnt(0)" : "+s"(rk), "+s"(rv), "+s"(rm), "+s"(rq), "+s"(rp), "+s"(rx), "+s"(vw), "+s"(ro), "+s"(sk), "+s"(sh), "+s"(sD));
        STAMP(40);
#pragma unroll
        for (int i = 0; i < 5; ++i) dma_b(i < 2 ? rk : i > 2 ? rv : rm, vo[i], lo[i]);
        asm volatile("global_load_dwordx2 %0, %1, %2" : "=v"(sw0) : "v"(s0), "s"(sk) : "memory");
        asm volatile("global_load_dwordx2 %0, %1, %2 offset:512" : "=v"(sw1) : "v"(s1), "s"(sk) : "memory");
        sld(ep, (const void*)(((u64)vw.y << 32) | vw.x), W_E);
        {
            u64 pc;
            asm volatile("s_getpc_b64 %0" : "=s"(pc));
            const u32 o = umin(lane * 8 + ((b >> 3) * 2048 + wave * 512), 56000);
            asm volatile("s_mov_b32 m0, %2\n\ts_nop 0\n\tglobal_load_lds_dword %0, %1" ::"v"(o), "s"(pc), "s"(L_EX + wave * 256) : "m0", "memory");
        }
    }
    OPQS(b); OPQS(wave); OPQV(tid);
    const int lane = tid & 63, rg = 2 * (b & 7) + (b >> 7), p = (b >> 3) & 15;
    const int Pg = p >> 2, tw = p & 3, pw = 4 * Pg + wave, ci = (rg + p) & 15;
    const bool conv = ci < 12;
    u32 l16 = lane * 16, l4 = lane * 4;
    OPQV(l16); OPQV(l4);
    const int ch0 = 36 * p + 3 * ci;
    u32 xr[9], kvw[3], kvA, kvB, l8, l8B, l16B, cpo, csd;
    {
        const int sec = p & 3, cg = 2 * Pg + (p & 1), tg = 2 * rg + ((p >> 1) & 1);
        const int c = lane & 15;
        FENCE();
        STAMP(41);
        const v4u z4 = {0, 0, 0, 0};
        u32 mk0, mk1, ua0, ua1, uo0, uo1, aw, al_, ap, d0_, d1_, d2_, cso, axs, aid, azc;
        v2u Bi; v4u Sel;
        const bool s3 = sec == 3;
        const u32 wf_ = rg >> 2, jl = rg & 3;
        auto wdma = [&]() __attribute__((always_inline)) {
            const u32 wst = s3 ? 1152 : 3456, cb = (sec < 2 ? (sec + 1) * 1152 : 0) + Pg * 288;
            const u32 lw = ((lane * 3641u) >> 16) * (wst - 288) + l16, w0 = (36 * rg + 3 * wave) * wst + cb;
            const u32 r4 = (lane * 43u) >> 8, sg = ((s3 ? 0 : (4 * Pg + wf_) & 14) + 4 * jl + wave) & 15;
            const u32 ll = vsel(lane < 24, r4 * (wst - 96) + (r4 >> 1) * (16 * wst) + l16 + ((36 * sg + 16) * wst + cb + ((72 * wf_) & ~15u)), OOB);
            const u32 lx = ((lane * 3450u) >> 16) * 848 + l16 + ((128 * rg + 32 * tw + 3 * wave) * 1152 + Pg * 288);
            const u32 lp = vsel(lane < 54, l16 - ((lane * 21846u) >> 16) * 12 + (Pg * 5184 + wave * 1296), OOB);
#pragma unroll
            for (int i = 0; i < 3; ++i) dma_b(rq, lw + (w0 + 12 * i * wst), P_WS + wave * 864 + i * 3456);
            dma_b(rq, ll, P_LS + wave * 1024);
#pragma unroll
            for (int i = 0; i < 2; ++i) dma_s(rp, lp, i * 648, P_PH + wave * 3072 + i * 1536);
#pragma unroll
            for (int i = 0; i < 3; ++i) dma_s(rx, lx, 12 * i * 1152, P_XS + wave * 912 + i * 3648);
        };
        {
            const u32 g = lane >> 4, m = lane & 31, kh = lane >> 5;
            const u32 hm = g == (c >> 2) ? ((c & 1) ? 0xffff0000u : 0xffffu) : 0u;
            mk0 = (c & 2) ? 0u : hm; mk1 = (c & 2) ? hm : 0u;
            Bi = (v2u){mk0 & 0x3f803f80u, mk1 & 0x3f803f80u}; Sel = (v4u){Bi.x, Bi.y, Bi.x, Bi.y};
            const u32 r0 = (8 * (c >> 2) + (c & 3)) * 144;
            ua0 = r0 + 8 * g + (P_KS + 32 * wave);
            ua1 = r0 + 32 * 144 * kh + 8 * (g & 1) + (P_KS + 128 + 576 * (wave & 1));
            uo0 = c * 4096 + 16 * g;
            uo1 = (c & 7) * 4096 + 64 * (c >> 3) + 16 * g + (wave < 2 ? 64 * 4096 + 8 - 8 * wave : OOB - (1u << 20));
            pin2(mk0, mk1); pin2(Bi, Sel); pin2(ua0, ua1); pin2(uo0, uo1);
            const u32 hD = (m >> 2) & 1;
            aw = (18 * hD + (m & 3) + 4 * (m >> 3)) * 288 + 16 * kh + (P_WS + ((72 * wave) & ~15u));
            al_ = ((((m >> 1) & 1) + 2 * ((m >> 3) & 1)) * 1024 + (2 * hD + (m & 1)) * 96) * (wave == wf_) + 16 * kh + (wave == wf_ ? P_LS : L_Z);
            ap = (8 * kh + (c >> 2)) * 48 + 32 * (g & 1) + 8 * (c & 3) + (P_PH + (64 * wave - 4 * (wave & 1)) * 48);
            pin2(aw, al_); pin(ap);
            const u32 ib = W_IMG + pw * IMG, ob = W_WOT + Pg * WOG;
            const u32 bA = ssel(s3, ob + wave * 36864, ib + sec * L_V + (sec >> 1) * (L_Q - 2 * L_V));
            const u32 bB = ssel(s3, ob + 4 * 36864 + 128 * (wave >> 1) + 32 * (wave & 1), ib + L_QT + 128 * sec);
            const u32 dsB = s3 ? 256 : QTS, drB = s3 ? 1024 : 2 * QTS, ri = (rg - (s3 ? 0 : pw & 14)) & 15;
            const bool lo = m < 16;
            const u32 lp = vsel(lo, ((m & 3) + 8 * (m >> 2) + 32 * kh) * 16 + bA, vsel(m < 18, (m & 1) * (s3 ? 16 : 32) + kh * (s3 ? 512 : 16) + bB, OOB));
            const u32 ds = vsel(lo, 1024, dsB), dr = vsel(lo, 2048, drB);
            d0_ = lp + ri * dr; d1_ = d0_ + ds;
            d2_ = umax(lp + (16 + (jl >> 1)) * dr + (jl & 1) * ds, wave == wf_ ? 0u : OOB);
            cso = vsel(m < (s3 ? 0 : 18), (18 * kh + m) * 4 + (W_CSP + pw * CSB + rg * 576 + sec * 192), OOB);
            axs = m * 304 + 36 * kh + (P_XS + 72 * wave);
            aid = vsel(lane < 36 * (sec & 1), lane * 50 + (lane >= 18 ? 1536 - 900 : 0) + (P_PH + wave * 3072), L_END - 16);
            azc = vsel(lane < (sec ? 0 : 6), (lane & 1) * 16 + ((lane >> 1) + (lane < 4 ? 2 * rg : 30 + jl)) * QTS + (ib + L_QT + 384), OOB);
            pin2(d0_, d1_); pin2(d2_, cso); pin2(axs, aid); pin(azc);
            *(LAS v2u*)(vsel(tid < 38, tid * 80 + (L_KVA + 72), L_END - 16)) = (v2u){0, 0};
            *(LAS v4u*)(l16 + kh * 1024 + (P_PH + 1024 + wave * 3072)) = z4;
            *(LAS v4u*)(vsel(lane < (wave ? 30 : 6), l16 + ssel(wave, L_Q + 4 * HCS - CVS + wave * CVS, L_Z), L_END - 16)) = z4;
        }
        FENCE();
        STAMP(42);
        wdma();
        FENCE();
        STAMP(26);
        asm volatile("s_waitcnt vmcnt(10)" : "+v"(sw0), "+v"(sw1)::"memory");
        v2u Bd[2][4];
#pragma unroll
        for (int s = 0; s < 2; ++s)
#pragma unroll
            for (int j = 0; j < 4; ++j) {
                const v2u sw = s ? sw1 : sw0;
                const u32 wd = __builtin_amdgcn_perm(0u, j < 2 ? sw.y : sw.x, (j & 1) ? 0x01000100u : 0x03020302u);
                Bd[s][j] = (v2u){wd & mk0, wd & mk1};
                pin(Bd[s][j]);
            }
        u32 so_u = W_UT + 128 * tg + cg * 72 * 4096, so_u2 = so_u + wave * 65536;
        OPQS(so_u); OPQS(so_u2);
        FENCE();
        barrier();
        STAMP(27);
        {
            u32 uo[5][2];
            v2u ka[5][5]; f4 pr[5][5], sm[5];
            v4u bb[2];
#pragma unroll
            for (int T = 0; T < 5; ++T)
#pragma unroll
                for (int j = 0; j < 5; ++j) ka[T][j] = lds2((T < 4 ? ua0 + (32 * (T >> 1) + 4 * (T & 1)) * 144 : ua1) + (j < 4 ? j * 144 : P_VS));
            FENCE();
            auto P = [&](auto T_, auto J_) __attribute__((always_inline)) { constexpr int T = T_.v, j = J_.v; mm16k(pr[T][j], ka[T][j], j < 4 ? Bd[T == 4][j] : Bi); };
            sfor<0, 5>(LAM(T_) { P(T_, IC<0>{}); P(T_, IC<1>{}); });
            sfor<0, 16>(LAM(I_) {
                constexpr int I = I_.v, T = I % 5, ph = I / 5 + 1, U = (I + 4) % 5;
                if constexpr (I < 15) {
                    f4& a = ph == 1 ? pr[T][0] : sm[T];
                    const u32 w0 = pka(a[0], a[1]), w1 = pka(a[2], a[3]), w2 = pka(pr[T][ph][0], pr[T][ph][1]), w3 = pka(pr[T][ph][2], pr[T][ph][3]);
                    bb[I & 1] = (v4u){w0, w1, w2, w3};
                    P(IC<T>{}, IC<ph + 1>{});
                } else {
                    uo[0][0] = pka(sm[0][0], sm[0][1]); uo[0][1] = pka(sm[0][2], sm[0][3]);
                }
                if constexpr (I > 0) mm16<true>(sm[U], Sel, bb[(I - 1) & 1]);
            });
            sfor<0, 5>(LAM(T_) {
                constexpr int T = T_.v;
                u32 w0, w1;
                if constexpr (T) { w0 = pka(sm[T][0], sm[T][1]); w1 = pka(sm[T][2], sm[T][3]); } else { w0 = uo[0][0]; w1 = uo[0][1]; }
                pin(pr[T][4]);
                FENCE();
                uo[T][0] = pk(bf_hi(w1) * pr[T][4][3], bf_lo(w1) * pr[T][4][2]);
                uo[T][1] = pk(bf_hi(w0) * pr[T][4][1], bf_lo(w0) * pr[T][4][0]);
                if constexpr (T == 1 || T == 3) bst4((v4u){uo[T][0], uo[T][1], uo[T - 1][0], uo[T - 1][1]}, vw, uo0 + 32 * (T - 1), so_u2, SC1);
                else if constexpr (T == 4) bst2((v2u){uo[4][0], uo[4][1]}, vw, uo1, so_u, SC1);
                else pin2(uo[T][0], uo[T][1]);
                FENCE();
            });
        }
        STAMP(28);
        asm volatile("s_waitcnt vmcnt(3)\n\ts_barrier" ::: "memory");
        {
            u32 one2 = 0x3f803f80u;
            OPQS(one2);
            *(LAS u16*)(aid) = 0x3f80;
            v4u bw[2][2], wa[2][3];
            bw[0][0] = tr2(ap, ap + 192); wa[0][0] = lds4(aw); wa[1][0] = lds4(al_);
            bw[0][1] = tr2(ap + 768, ap + 960); wa[0][1] = lds4(aw + 32); wa[1][1] = lds4(al_ + 32);
            bw[1][0] = tr2(ap + 1440, ap + 1440 + 192);
            bw[1][1] = tr2(ap + 1440 + 768, ap + 1440 + 960); wa[0][2] = lds4(aw + 64); wa[1][2] = lds4(al_ + 64);
            FENCE();
#pragma unroll
            for (int j = 0; j < 9; ++j) xr[j] = *(const LAS u32*)(axs + 4 * j);
            FENCE();
            f16v d[2][2];
#pragma unroll
            for (int i = 0; i < 4; ++i) { mm32<true>(d[i >> 1][i & 1], wa[i & 1][i >> 1], bw[i >> 1][0]); mm32<false>(d[i >> 1][i & 1], wa[i & 1][(i >> 1) + 1], bw[i >> 1][1]); }
            FENCE();
            {
                float s1 = 0.f, s2 = 0.f;
#pragma unroll
                for (int j = 0; j < 9; ++j) { s1 = dot2(xr[j], one2, s1); s2 = dot2(xr[j], xr[j], s2); }
                const u32 so = W_XF0 + ((rg * 4 + tw) * 16 + pw) * XS;
                bst4((v4u){xr[0], xr[1], xr[2], xr[3]}, vw, l16, so, SC1);
                bst4((v4u){xr[4], xr[5], xr[6], xr[7]}, vw, l16 + 1024, so, SC1);
                l8 = 2 * l4;
                bst2((v2u){xr[8], asu(halves_sum(s1, s2))}, vw, l8 + 2048, so, SC1);
                bst4(z4, vw, azc, 0, SC1);
            }
            FENCE();
            float cs[2];
#pragma unroll
            for (int hh = 0; hh < 2; ++hh) {
                asm volatile("" : "+v"(d[hh][0]), "+v"(d[hh][1]));
                u32 w[12];
#pragma unroll
                for (int j = 0; j < 12; ++j) w[j] = pk(d[hh][j >> 3][2 * (j & 7)], d[hh][j >> 3][2 * (j & 7) + 1]);
                bst4((v4u){w[0], w[1], w[2], w[3]}, vw, d0_ + 64 * hh, 0, SC1);
                bst4((v4u){w[4], w[5], w[6], w[7]}, vw, d1_ + 64 * hh, 0, SC1);
                bst4((v4u){w[8], w[9], w[10], w[11]}, vw, d2_ + 64 * hh, 0, SC1);
                cs[hh] = 0.f;
#pragma unroll
                for (int j = 0; j < 12; ++j) cs[hh] = dot2(w[j], one2, cs[hh]);
                FENCE();
            }
            bst1(asu(halves_sum(cs[0], cs[1])), vw, cso, 0, SC1);
        }
        v4u hd[3]; v2u he[3], hz;
        {
            const char* gp = sh + umin(ch0, 573) * 4096;
            const u32 o1 = tid * 16, o2 = (o1 + 16) & 4095;
            gld2(hz, umin(lane >> 3, 2) * 4096, gp);
#pragma unroll
            for (int i = 0; i < 3; ++i) { gld4<0>(hd[i], o1, gp + i * 4096); gld2(he[i], o2, gp + i * 4096); }
        }
        STAMP(29);
        asm volatile("s_waitcnt vmcnt(7)\n\ts_barrier" ::: "memory");
        OPQS(ep);
        ep += 1;
        const v2u epv = {(u32)ep, (u32)(ep >> 32)};
        if (tid == 0) bst2(epv, vw, 0, W_F + (rg * 16 + p) * 128, SC1);
        STAMP(1);
        {
            const u32 e = (lane >> 4) & 1, kg = lane >> 4;
#pragma unroll
            for (int j = 0; j < 3; ++j) {
                const u32 pi = 16 * (wave + 4 * j) + c, hh = pi >= 86, ip = pi - 86 * hh, q = ip - 72, s = 2 * (ip - 84) + e;
                u32 v = vsel(ip < 64, (ip & 15) * 80 + (ip >> 4) * 8 + 32 * e, (2 * ip + e) * 80 + 64 - 128 * 80);
                v = vsel(ip < 72, v, (16 + (q >> 2)) * 80 + (q & 3) * 8 + 32 * e);
                v = vsel(ip < 84, v, s * 80 + 16 * 80 + 64);
                kvw[j] = vsel(lane >= 32 || pi >= 172 || s == 3, L_EX + l8, v + hh * 1520 + L_KVA);
                OPQV(kvw[j]);
            }
            const bool dt = wave & 1;
            kvA = dt ? vsel(c < 3, 1152 + (c * 4 + kg) * 16, OOB) : l16;
            kvB = vsel(lane < (dt ? 4 : 16), l8 + (dt ? 1344 : 1024), OOB);
            l16B = l16 + XS; l8B = l8 + XS;
            const u32 sc = (lane * 57u) >> 10, r = lane - 18 * sc, hh = r >= 9, pr = r - 9 * hh;
            cpo = vsel(lane < 54, sc * (192 - 144) + l8, OOB);
            csd = vsel(lane < 54, hh * 224 + vsel(pr < 8, sc * 64 + pr * 8, 192 + sc * 8) + L_CS, L_EX + 2048 + l8);
            pin2(kvA, kvB); pin2(l16B, l8B); pin2(cpo, csd); pin(l8);
        }
        FENCE();
        if (wave) waitv<0>(); else waitv<1>();
        STAMP(51);
        if (conv) {
            u32 o = L_Q + 32 + tid * 16;
            OPQV(o);
#pragma unroll
            for (int i = 0; i < 3; ++i) {
                asm volatile("" : "+v"(hd[i]), "+v"(he[i]));
                const u32 d0 = hd[i].x, d1 = hd[i].y, d2 = hd[i].z, d3 = hd[i].w, d4 = he[i].x, d5 = he[i].y;
                const u32 a0 = __builtin_amdgcn_alignbit(d1, d0, 16), a1 = __builtin_amdgcn_alignbit(d2, d1, 16), a2 = __builtin_amdgcn_alignbit(d3, d2, 16),
                          a3 = __builtin_amdgcn_alignbit(d4, d3, 16), a4 = __builtin_amdgcn_alignbit(d5, d4, 16);
                *(LAS v4u*)(o + i * CVS) = hd[i];
                *(LAS v4u*)(o + i * CVS + HCS) = (v4u){a0, a1, a2, a3};
                *(LAS v4u*)(o + i * CVS + 2 * HCS) = (v4u){d1, d2, d3, d4};
                *(LAS v4u*)(o + i * CVS + 3 * HCS) = (v4u){a1, a2, a3, a4};
            }
            asm volatile("" : "+v"(hz));
            const u32 k = (lane >> 1) & 3;
            const u64 t = (lane & 1) && k ? (((u64)hz.y << 32) | hz.x) << (64 - 16 * k) : 0;
            if (tid < 24) *(LAS v4u*)(L_Q + (lane >> 3) * CVS + k * HCS + (lane & 1) * 16) = (v4u){0, 0, (u32)t, (u32)(t >> 32)};
        }
    }
    v2u ls[16];
    {
        u32 D2[3];
#pragma unroll
        for (int i = 0; i < 3; ++i) { const u32 cc = umin(ch0, 573) + i, t = ((const __attribute__((address_space(4))) u32*)(uintptr_t)sD)[cc >> 1]; D2[i] = (cc & 1 ? t >> 16 : t & 0xffffu) * 0x10001u; OPQS(D2[i]); }
        const int bq = lane & 15, g = lane >> 4, cc = (bq + 1) & 3;
        u32 aa = L_Q + cc * HCS + 2 * (bq - 8 * g + 24 - cc), ba = L_Q + 4 * HCS + 480 + 2 * (16 * bq + 8 * g - 16) - 7 * 64;
        u32 yo = (bq >> 3) * YB + (bq & 7) * 32 + 8 * g, yp = (bq >> 3) * YB + (bq & 7) * 32 + 16 * (g >> 1) + (g & 1) * (2 * YB);
        v4u dm;
        {
            const u32 e = 7 - (bq & 7), hm = g == 2 + (bq >> 3) ? ((e & 1) ? 0xffff0000u : 0xffffu) : 0u;
#pragma unroll
            for (int i = 0; i < 4; ++i) dm[i] = (e >> 1) == i ? hm : 0u;
        }
        if (wave < 3) { aa += wave * CVS; ba += wave * CVS; }
        pin2(aa, ba); pin2(yo, yp); pin(dm);
        const u32 xb = W_XF0 + (rg * 4 + wave) * 16 * XS;
        u32 io = umin(22 * wave, 65) * 1024, ig = W_IMG + p * IMG + io, xo = xb + (p & 14) * XS, uo = W_UT + ch0 * 4096 + wave * 1024, co = W_CSP + p * CSB + wave * (4 * 576);
        u32 fo = W_F + (wave ? rg * 16 + bq : (lane >> 2) * 16 + 4 * Pg + (lane & 3)) * 128;
        OPQS(io); OPQS(ig); OPQS(xo); OPQS(uo); OPQS(co); OPQV(fo);
        FENCE();
        if (wave < 2) {
#pragma unroll 1
            for (int sp = 0; sp < SPIN; ++sp) {
                asm volatile("" ::: "memory");
                const v2u f = bld2(vw, fo, 0, SC1);
                if (__builtin_amdgcn_ballot_w64(f.x != (u32)ep || f.y != (u32)(ep >> 32)) == 0) break;
                __builtin_amdgcn_s_sleep(1);
            }
        }
        barrier();
        STAMP(2);
        v2u cp[4];
        auto site = LAM(N_) {
            constexpr int n = N_.v;
            if constexpr (n < 28) {
                if constexpr (n % 5 == 0) { if constexpr (n) { io += 4096; ig += 4096; OPQS(io); OPQS(ig); } setm0(io); if constexpr (n < NPRE) nop2(); }
                else dmai<(n % 5 - 1) * 1024>(vw, l16, ig);
            } else if constexpr (n < 84) {
                constexpr int i = 2 * ((n - 28) / 7), k = (n - 28) % 7;
                if constexpr (k == 0) ld4a<0, 8 * i>(l16, vw, xo);
                if constexpr (k == 1) ld4a<1024, 8 * i + 4>(l16, vw, xo);
                if constexpr (k == 2) ld2<2048>(ls[i], l8, vw, xo);
                if constexpr (k == 3) ld4a<0, 8 * i + 8>(l16B, vw, xo);
                if constexpr (k == 4) ld4a<1024, 8 * i + 12>(l16B, vw, xo);
                if constexpr (k == 5) ld2<2048>(ls[i + 1], l8B, vw, xo);
                if constexpr (k == 6 && i < 14) { xo += 2 * XS; xo = xo == xb + 16 * XS ? xb : xo; OPQS(xo); }
            } else ld2<(n - 84) * 576>(cp[n - 84], cpo, vw, co);
        };
        if (conv) {
#pragma unroll
            for (int i = 0; i < 3; ++i) dma_ws(vw, l16, uo + i * 4096, L_Q + i * CVS + 4 * HCS + 480 + wave * 1024);
            sfor<0, NPRE>(site);
            asm volatile("s_waitcnt vmcnt(%0)\n\ts_barrier" ::"n"(NPRE - (NPRE + 4) / 5) : "memory");
            STAMP(3);
            if (wave < 3) {
                f4 acc[8];
                v4u A[2][8], B[2][8];
                sfor<0, 24>(LAM(N_) { conv_read<8, N_.v, 0>(aa, ba, A[0], B[0]); });
                const u32 dd = wave == 0 ? D2[0] : wave == 1 ? D2[1] : D2[2];
                v4u ad = {dm.x & dd, dm.y & dd, dm.z & dd, dm.w & dd};
                pin(ad);
                FENCE();
                sfor<0, 5>(LAM(J_) { constexpr int j = J_.v ? J_.v + 3 : 0; mm16<true>(acc[j], ad, B[0][j]); });
                FENCE();
                sfor<0, 8 * 27>(LAM(Q_) {
                    constexpr int r = Q_.v / 27, n = Q_.v % 27, i = conv_ij(8, 0xf1, n) >> 3, j = conv_ij(8, 0xf1, n) & 7;
                    if constexpr (r && n == 0) waitl<9>();
                    if constexpr (r && n == 6) waitl<(r < 7 ? 6 : 0)>();
                    mm16<false>(acc[j], A[r & 1][i], B[r & 1][j - i]);
                    if constexpr (r < 7 && n < 24) conv_read<8, n, r + 1>(aa, ba, A[(r + 1) & 1], B[(r + 1) & 1]);
                    sfor<site_at(Q_.v), site_at(Q_.v + 1)>(site);
                    FENCE();
                });
                asm volatile("s_nop 7" : "+v"(acc[0]), "+v"(acc[4]), "+v"(acc[5]), "+v"(acc[6]), "+v"(acc[7]));
                const u32 so = W_Y + (ch0 + wave) * 256;
                const v2u y0 = y_val(acc[0]);
                const v4u y4 = y_pair(y_val(acc[4]), y_val(acc[5])), y6 = y_pair(y_val(acc[6]), y_val(acc[7]));
                asm volatile("s_waitcnt vmcnt(0)" ::"v"(y0), "v"(y4), "v"(y6) : "memory");
                bst2(y0, vw, yo, so, SC1);
                bst4(y4, vw, yp, so + 8 * YB, SC1);
                bst4(y6, vw, yp, so + 12 * YB, SC1);
            } else {
                f4 acc[2][4];
                v4u A[2][4], B[2][4], ad;
                v2u y1; v4u y2;
                sfor<0, 12>(LAM(N_) { conv_read<4, N_.v, 0>(aa, ba, A[0], B[0]); });
                FENCE();
                sfor<0, 3 * 8 * 9>(LAM(Q_) {
                    constexpr int cn = Q_.v / 72, r = Q_.v % 72 / 9, n = Q_.v % 9, i = conv_ij(4, 0xe, n) >> 3, j = conv_ij(4, 0xe, n) & 7;
                    constexpr int nc = r < 7 ? cn : cn + 1, nr = (r + 1) & 7;
                    if constexpr (r == 0 && n == 0) {
                        ad = (v4u){dm.x & D2[cn], dm.y & D2[cn], dm.z & D2[cn], dm.w & D2[cn]};
                        pin(ad);
                        waitl<0>();
                        nop2();
                        FENCE();
                        sfor<1, 4>(LAM(J_) { mm16<true>(acc[cn & 1][J_.v], ad, B[0][J_.v]); });
                    } else {
                        if constexpr (n == 0) waitl<3>();
                        if constexpr (n == 7) waitl<(nc < 3 ? 10 : 0)>();
                    }
                    mm16<false>(acc[cn & 1][j], A[r & 1][i], B[r & 1][j - i]);
                    if constexpr (nc < 3) {
                        if constexpr (n < 3) { conv_read<4, 2 * n, nr>(aa + nc * CVS, ba + nc * CVS, A[nr & 1], B[nr & 1]); conv_read<4, 2 * n + 1, nr>(aa + nc * CVS, ba + nc * CVS, A[nr & 1], B[nr & 1]); }
                        else conv_read<4, n + 3, nr>(aa + nc * CVS, ba + nc * CVS, A[nr & 1], B[nr & 1]);
                    }
                    if constexpr (cn > 0 && r == 1) {
                        const u32 so = W_Y + (ch0 + cn - 1) * 256;
                        if constexpr (n == 3) { pin(acc[(cn - 1) & 1][1]); y1 = y_val(acc[(cn - 1) & 1][1]); pin(y1); }
                        if constexpr (n == 4) bst2(y1, vw, yo, so + 2 * YB, SC1);
                        if constexpr (n == 5) { pin(acc[(cn - 1) & 1][2]); y1 = y_val(acc[(cn - 1) & 1][2]); pin(y1); }
                        if constexpr (n == 6) { pin(acc[(cn - 1) & 1][3]); y2 = y_pair(y1, y_val(acc[(cn - 1) & 1][3])); pin(y2); }
                        if constexpr (n == 7) bst4(y2, vw, yp, so + 4 * YB, SC1);
                    }
                    sfor<site_at(Q_.v), site_at(Q_.v + 1)>(site);
                    FENCE();
                });
                asm volatile("s_nop 7" : "+v"(acc[0][1]), "+v"(acc[0][2]), "+v"(acc[0][3]));
                const u32 so = W_Y + (ch0 + 2) * 256;
                y1 = y_val(acc[0][1]); y2 = y_pair(y_val(acc[0][2]), y_val(acc[0][3]));
                asm volatile("s_waitcnt vmcnt(0)" ::"v"(y1), "v"(y2) : "memory");
                bst2(y1, vw, yo, so + 2 * YB, SC1);
                bst4(y2, vw, yp, so + 4 * YB, SC1);
            }
        } else {
            sfor<0, NSITE>(LAM(N_) { site(N_); if constexpr (N_.v < 28 && N_.v % 5 == 0 && N_.v >= NPRE) nop2(); });
            waitv<0>();
        }
        STAMP(14);
        asm volatile("" : "+v"(cp[0]), "+v"(cp[1]), "+v"(cp[2]), "+v"(cp[3]));
        *(LAS f2*)(L_EX + wave * 512 + l8) = (f2){(asf(cp[0].x) + asf(cp[1].x)) + (asf(cp[2].x) + asf(cp[3].x)), (asf(cp[0].y) + asf(cp[1].y)) + (asf(cp[2].y) + asf(cp[3].y))};
        barrier();
    }
    u32 yv[9];
    v4u wv[17];
    STAMP(15);

    sfor<0, 2>(LAM(L_) {
        constexpr int L = L_.v;
        int ln = lane;
        OPQV(ln); OPQV(l16); OPQV(l4);
        const int tl = ln & 31, h = ln >> 5, row = ln & 15, kg = ln >> 4, gq = row >> 2, rq = row & 3;
        const bool qa = (gq & 1) == (kg & 1);
        u32 aV_ = L_V + l16, aQ_ = L_Q + l16;
        u32 aKV = L_QT + vsel(qa, (rq >> 1) * 128 + (gq >> 1) * 64 + (rq & 1) * 32 + (kg >> 1) * 16, 400 - 16 * (kg >> 1));
        u32 aQq, aCS, aS;
        OPQV(aV_); OPQV(aKV); OPQV(aQ_);
        u32 nretry = 0;
        const u32 wb = W_WOT + Pg * WOG + wave * 36864, wlb = W_WOT + Pg * WOG + 4 * 36864 + (wave >> 1) * 9216;
        u32 wso = wb;
        auto wsite = LAM(N_) {
            constexpr int n = N_.v, k = (n < 36 ? n : n - 36) % 4;
            if constexpr (L == 0 && n < 45) {
                if constexpr (n == 36) wso = wlb; else if constexpr (n && k == 0) wso += 4096;
                if constexpr (k == 0) OPQS(wso);
                if constexpr (n < 28) ld4a<k * 1024, 144 + 4 * n>(l16, vw, wso);
                else ld4o<k * 1024>(wv[n - 28], l16, vw, wso);
            }
        };
        f16v aK; f4 qKV;
        float mean, al, rstd;
        v4u rV[4], cK[5];
        {
            const u32 xb = W_XF1 + (rg * 4 + wave) * 16 * XS;
            u32 xo = xb + (p & 14) * XS;
            OPQS(xo);
            auto xsite = LAM(I_) {
                constexpr int i = I_.v;
                if constexpr (L == 1 && i < 16) {
                    ld4a<0, 8 * i>(i & 1 ? l16B : l16, vw, xo);
                    ld4a<1024, 8 * i + 4>(i & 1 ? l16B : l16, vw, xo);
                    ld2<2048>(ls[i], i & 1 ? l8B : l8, vw, xo);
                    if constexpr ((i & 1) && i < 15) { xo += 2 * XS; xo = xo == xb + 16 * XS ? xb : xo; OPQS(xo); }
                }
            };
            constexpr int UP = 5;
            if constexpr (L == 1) { FENCE(); waitv<0>(); STAMP(25); }
            sfor<0, UP>(xsite);
            v4u r0[3], r1[3], ones = {~0u, ~0u, ~0u, ~0u};
            f2 cq[4];
            u32 cm, cs_;
            float tsum;
#pragma unroll
            for (int s = 0; s < 3; ++s) { r0[s] = lds4(l16 + s * 1024); r1[s] = lds4(aKV + s * QTS); }
            if constexpr (L == 0) pin(ones);
            FENCE();
            sfor<0, 36>(LAM(SI) {
                constexpr int n = SI.v, s = kstep(n), s3 = kstep(n + 3), i = s >> 1;
                if constexpr (s < 32 && !(s & 1)) {
                    if constexpr (L == 1) {
                        constexpr int issued = UP + i < 16 ? UP + i : 16;
                        waitp<3 * (issued - i - 1)>(ls[i]);
                        if (__builtin_expect(bad(mx3(rda<8 * i>(), rda<8 * i + 4>(), ls[i].x)), 0)) {
#pragma unroll 1
                            for (int sp = 0; sp < SPIN; ++sp) {
                                __builtin_amdgcn_s_sleep(2);
                                sfor<i, issued>(LAM(J_) {
                                    constexpr int j = J_.v;
                                    const u32 so = xb + (((p & 14) + j) & 15) * XS;
                                    ld4a<0, 8 * j>(l16, vw, so); ld4a<1024, 8 * j + 4>(l16, vw, so); ld2<2048>(ls[j], l8, vw, so);
                                });
                                waitp<0>(ls[i]);
                                ++nretry;
                                if (!bad(mx3(rda<8 * i>(), rda<8 * i + 4>(), ls[i].x))) break;
                            }
                        }
                    } else pin(ls[i]);
                    FENCE();
                }
                waitl<(n < 34 ? 4 : 6)>();
                mm32b<n == 0, 4 * s>(aK, r0[n % 3]);
                if constexpr (n + 3 < 36) r0[n % 3] = lds4(l16 + s3 * 1024); else rV[n - 33] = lds4(aV_ + (n - 33) * 1024);
                if constexpr (s < 32 && !(s & 1)) { xsite(IC<UP + i>{}); wra<128 + i>(ls[i].x); tsum = i ? tsum + asf(ls[i].y) : asf(ls[i].y); pin(tsum); }
                if constexpr (n == 1) { aQq = L_QT + vsel(qa && rq < 2, 256 + (gq >> 1) * 64 + (rq & 1) * 32 + (kg >> 1) * 16, 384); pin(aQq); }
                if constexpr (n == 3) { aCS = L_CS + h * 224; aS = L_STG + (wave * 32 + tl) * STGS + 72 * h; pin2(aCS, aS); }
                if constexpr (L == 0) {
                    constexpr int k = n >> 1;
                    if constexpr (n & 1) {
                        if constexpr (k == 0 || k == 3) { cs_ = W_O + ((k ? 1024 : 0) + (rg * 4 + wave) * 16 + p) * OS; OPQS(cs_); }
                        if constexpr (k == 6) { cs_ = W_XF1 + ((rg * 4 + tw) * 16 + pw) * XS; OPQS(cs_); }
                        if constexpr (k == 9) { cs_ = W_KV + (p * 16 + rg) * 2816 + wave * 704; OPQS(cs_); }
                        if constexpr (k == 10) { cs_ += 256 * 2816; OPQS(cs_); }
                        if constexpr (k == 11) { cs_ = W_ST + ((rg * 4 + tw) * 16 + pw) * 256; OPQS(cs_); }
                        if constexpr (k < 9 && k % 3 < 2) st4<(k % 3) * 1024>(ones, l16, vw, cs_);
                        else if constexpr (k < 9) st4<2048>(ones, cm, vw, cs_);
                        else if constexpr (k < 12) st4<0>(ones, cm, vw, cs_);
                        else wsite(IC<k - 12>{});
                    } else {
                        if constexpr (k == 2) { cm = vsel(ln < 16, l16, OOB); pin(cm); }
                        if constexpr (k == 8) { cm = vsel(ln < 32, l16, OOB); pin(cm); }
                        if constexpr (k == 9) { cm = vsel(ln < 44, l16, OOB); pin(cm); }
                        if constexpr (k == 11) { cm = vsel(ln < 32, row * 16 + (kg & 1) * (1024 * 256), OOB); pin(cm); }
                        if constexpr (k == 13) { cq[0].x = (cq[0].x + cq[1].x) + (cq[2].x + cq[3].x); cq[0].y = (cq[0].y + cq[1].y) + (cq[2].y + cq[3].y); pin(cq[0]); }
                    }
                }
                if constexpr (n == 34) {
                    auto r = __builtin_amdgcn_permlane32_swap(asu(tsum), asu(tsum), false, false);
                    mean = asf(r[0]) * (1.f / 576.f);
                    const float var = __builtin_fmaxf(asf(r[1]) * (1.f / 576.f) - mean * mean, 0.f);
                    rstd = __builtin_amdgcn_rsqf(var + 1e-5f);
                    al = rstd * 1.4426950408889634f;
                    pin2(mean, al); pin(rstd);
                }
                FENCE();
                mm16b<n == 0, 4 * s>(qKV, r1[n % 3]);
                if constexpr (n + 3 < 36) r1[n % 3] = lds4(aKV + s3 * QTS); else cK[n - 33] = lds4(aCS + (n - 33) * 16);
                if constexpr (n == 33) cK[3] = lds4(aCS + 48);
                if constexpr (n == 34) cK[4] = lds4(aCS + 192);
                if constexpr (n == 35) rV[3] = lds4(aV_ + 3 * 1024);
                FENCE();
                if constexpr (L == 0 && n >= 20 && n < 24) { cq[n - 20] = *(const LAS f2*)(l8 + (L_EX + (n - 20) * 512)); FENCE(); }
                if constexpr (L == 0 && n == 27) { *(LAS f2*)(csd) = cq[0]; FENCE(); }
            });
        }
        STAMP(4 + 12 * L);
        if constexpr (L == 1) STAMPV(56, nretry);
        v4u rQ[4], rq4[3];
        u32 pa, pb, kso;
        {
            f16v aV;
            float t[18], e1[18], e2[18], mx, mn, c1, c2, s1, s2;
            v4u cV[4];
            u32 qo = L_Q + wave * 9216, qg = W_IMG + p * IMG + qo;
            sfor<0, 36>(LAM(SI) {
                constexpr int s = SI.v;
                if constexpr (s >= 2 && s < 34 && !(s & 1)) waitl<2>();
                mm32b<s == 0, 4 * s>(aV, rV[s & 3]);
                if constexpr (s == 2) { pin(aK); pin(qKV); }
                if constexpr (s + 4 < 36) rV[s & 3] = lds4(aV_ + (s + 4) * 1024); else cV[s - 32] = lds4(aCS + 64 + (s - 32) * 16);
                if constexpr (s < 18) {
                    if constexpr (s == 0 || s == 8 || s == 16) { if constexpr (s) { qo += 4096; qg += 4096; } OPQS(qo); OPQS(qg); setm0(qo); }
                    if constexpr (s & 1) dmai<((s >> 1) & 3) * 1024>(vw, l16, qg);
                } else if constexpr (s & 1) wsite(IC<6 + (s - 18) / 2>{});
                if constexpr (s == 1) {
                    const u32 r0 = L_STG + (4 * (4 * (kg & 1) + gq) + (kg >> 1)) * STGS + 8 * rq;
                    pa = r0 + 72 * (wave >> 1); pb = r0 + (144 + 40 * (wave >> 1) + 32 * (wave & 1));
                    kso = W_KV + ((L * 16 + p) * 16 + rg) * 2816 + (wave >> 1) * 1376;
                    pin2(pa, pb); OPQS(kso);
                }
                if constexpr (s >= 2 && s < 5) {
#pragma unroll
                    for (int i = 6 * (s - 2); i < 6 * (s - 2) + 6; ++i) { const u32 cv = cK[i >> 2][i & 3]; t[i] = __builtin_fmaf(-mean, asf(cv), i < 16 ? aK[i] : qKV[i - 16]); }
                }
                if constexpr (s == 5) { mx = __builtin_fmaxf(__builtin_fmaxf(t[0], t[1]), t[2]); mn = __builtin_fminf(__builtin_fminf(t[0], t[1]), t[2]);
#pragma unroll
                    for (int i = 3; i < 7; i += 2) { mx = __builtin_fmaxf(__builtin_fmaxf(mx, t[i]), t[i + 1]); mn = __builtin_fminf(__builtin_fminf(mn, t[i]), t[i + 1]); } }
                if constexpr (s == 6) {
#pragma unroll
                    for (int i = 7; i < 13; i += 2) { mx = __builtin_fmaxf(__builtin_fmaxf(mx, t[i]), t[i + 1]); mn = __builtin_fminf(__builtin_fminf(mn, t[i]), t[i + 1]); } }
                if constexpr (s == 7) {
#pragma unroll
                    for (int i = 13; i < 17; i += 2) { mx = __builtin_fmaxf(__builtin_fmaxf(mx, t[i]), t[i + 1]); mn = __builtin_fminf(__builtin_fminf(mn, t[i]), t[i + 1]); }
                    mx = __builtin_fmaxf(mx, t[17]); mn = __builtin_fminf(mn, t[17]); c1 = -al * mx; c2 = al * mn; }
                if constexpr (s >= 8 && s < 32) {
#pragma unroll
                    for (int q = (3 * (s - 8)) / 2; q < (3 * (s - 7)) / 2; ++q) {
                        const int i = q >> 1;
                        if (q & 1) { e2[i] = __builtin_amdgcn_exp2f(__builtin_fmaf(t[i], -al, c2)); s2 = i ? s2 + e2[i] : e2[i]; }
                        else { e1[i] = __builtin_amdgcn_exp2f(__builtin_fmaf(t[i], al, c1)); s1 = i ? s1 + e1[i] : e1[i]; }
                    }
                }
                if constexpr (s == 32) { s1 = __builtin_amdgcn_rcpf(s1); s2 = __builtin_amdgcn_rcpf(s2); }
                if constexpr (s >= 33) {
                    constexpr int i = s - 33;
                    *(LAS v2u*)(aS + 8 * i) = (v2u){pk(e1[4 * i] * s1, e1[4 * i + 1] * s1), pk(e1[4 * i + 2] * s1, e1[4 * i + 3] * s1)};
                }
                FENCE();
            });
            *(LAS v2u*)(aS + 24) = (v2u){pk(e1[12] * s1, e1[13] * s1), pk(e1[14] * s1, e1[15] * s1)};
            *(LAS v2u*)(aS + 32) = (v2u){pk(e1[16] * s1, e1[17] * s1), pk(e2[0] * s2, e2[1] * s2)};
#pragma unroll
            for (int i = 0; i < 4; ++i) *(LAS v2u*)(aS + 40 + 8 * i) = (v2u){pk(e2[4 * i + 2] * s2, e2[4 * i + 3] * s2), pk(e2[4 * i + 4] * s2, e2[4 * i + 5] * s2)};
            FENCE();
            pin(aV);
            const u32 aSv = aS + 144 - 32 * h;
            const float mr = -mean * rstd;
#pragma unroll
            for (int i = 0; i < 4; ++i) {
                const u32 q0 = cV[i].x, q1 = cV[i].y, q2 = cV[i].z, q3 = cV[i].w;
                *(LAS v2u*)(aSv + 8 * i) = (v2u){pk(__builtin_fmaf(mr, asf(q0), aV[4 * i] * rstd), __builtin_fmaf(mr, asf(q1), aV[4 * i + 1] * rstd)),
                                                 pk(__builtin_fmaf(mr, asf(q2), aV[4 * i + 2] * rstd), __builtin_fmaf(mr, asf(q3), aV[4 * i + 3] * rstd))};
            }
            const u32 q2 = cK[4].z, q3 = cK[4].w;
            *(LAS v2u*)(aSv + 32) = (v2u){pk(__builtin_fmaf(mr, asf(q2), qKV[2] * rstd), __builtin_fmaf(mr, asf(q3), qKV[3] * rstd)), 0x3f80u};
        }
        waitbar<(L ? 0 : 9)>();
        STAMP(5 + 12 * L);
        {
            f4 c[3];
            v4u B[4], A[4][3];
            auto rd = LAM(N_) {
                constexpr int ks = N_.v >> 2, f = N_.v & 3;
                if constexpr (f == 0) B[ks] = tr2(pb + ks * 32 * STGS, pb + (ks * 32 + 2) * STGS);
                else A[ks][f - 1] = tr2(pa + ks * 32 * STGS + 32 * (f - 1), pa + (ks * 32 + 2) * STGS + 32 * (f - 1));
            };
            sfor<0, 7>(LAM(N_) { rd(N_); FENCE(); });
            if constexpr (L == 0) { if (tid == 0) bst2((v2u){(u32)ep, (u32)(ep >> 32)}, vw, 0, W_F2 + (rg * 16 + p) * 128, SC1); }
            FENCE();
            sfor<0, 12>(LAM(I_) {
                constexpr int ks = I_.v / 3, ft = I_.v % 3;
                mm16<ks == 0>(c[ft], A[ks][ft], B[ks]);
                if constexpr (I_.v < 9) rd(IC<7 + I_.v>{});
                else { rQ[I_.v - 9] = lds4(aQ_ + (I_.v - 9) * 1024); rq4[I_.v - 9] = lds4(aQq + (I_.v - 9) * QTS); }
                FENCE();
            });
            rQ[3] = lds4(aQ_ + 3 * 1024);
            FENCE();
            pad3<5>(c[0], c[1], c[2]);
            bst4((v4u){pk(c[0][0], c[0][1]), pk(c[0][2], c[0][3]), pk(c[1][0], c[1][1]), pk(c[1][2], c[1][3])}, vw, kvA, kso, SC1);
            bst2((v2u){pk(c[2][0], c[2][1]), pk(c[2][2], c[2][3])}, vw, kvB, kso, SC1);
        }
        FENCE();
        STAMP(6 + 12 * L);
        v4u pq[5], kp[12], sel;
        u32 kvo, kv3, aF, aQ4, f2o, oso;
        u32 kb[4];
        v2u yfl;
        auto flags_ok = [&](u64 m) __attribute__((always_inline)) { return (__builtin_amdgcn_ballot_w64(yfl.x != (u32)ep || yfl.y != (u32)(ep >> 32)) & m) == 0; };
        auto flags_wait = [&](u64 m) __attribute__((always_inline)) {
            if (__builtin_expect(!flags_ok(m), 0)) {
#pragma unroll 1
                for (int sp = 0; sp < SPIN; ++sp) {
                    __builtin_amdgcn_s_sleep(1);
                    ld2<0>(yfl, f2o, vw, W_F2);
                    waitp<0>(yfl);
                    if (flags_ok(m)) break;
                }
            }
        };
        {
            f16v aQ; f4 qQ;
            v4u cQ[5];
            sfor<0, 36>(LAM(SI) {
                constexpr int s = SI.v;
                if constexpr (s >= 1 && s < 34) waitl<4>();
                mm32b<s == 0, 4 * s>(aQ, rQ[s & 3]);
                if constexpr (s + 4 < 36) rQ[s & 3] = lds4(aQ_ + (s + 4) * 1024); else cQ[s - 32] = lds4(aCS + 128 + (s - 32) * 16);
                if constexpr (s < 33 && s % 3 != 2) wsite(IC<15 + s - s / 3>{});
                if constexpr (s == 2) { kvo = kg * 2816 + wave * 256 + row * 16; kv3 = vsel(wave * 16 + row < 44, kvo + 2048, OOB); pin2(kvo, kv3); }
                if constexpr (s == 26) { for (int i = 0; i < 4; ++i) { kb[i] = W_KV + ((L * 16 + p) * 16 + 4 * i) * 2816; OPQS(kb[i]); } }
                if constexpr (s == 5 || s == 8) {
#pragma unroll
                    for (int i = (s - 5) / 3 * 2; i < (s - 5) / 3 * 2 + 2; ++i) sel[i] = row == 2 * i ? 0x3f80u : row == 2 * i + 1 ? 0x3f800000u : 0u;
                    if constexpr (s == 8) pin(sel);
                }
                if constexpr (s == 11) { const int hr = (tl >> 2) & 1, d = (tl & 3) + 4 * (tl >> 3); aF = vsel(h == hr, (hr * 19 + d) * 80 + L_KVA, L_Z); pin(aF); }
                if constexpr (s == 14) { aQ4 = vsel(kg == gq && rq < 3, ((gq >> 1) * 19 + 16 + rq) * 80 + L_KVA, L_Z); pin(aQ4); }
                if constexpr (s == 17) { oso = W_O + (L * 1024 + (rg * 4 + wave) * 16 + p) * OS; OPQS(oso); }
                if constexpr (L == 0 && s == 20) { f2o = vsel(kg == 1, rg * 16 + row, row * 16 + (kg ? pw : p)) * 128; pin(f2o); }
                if constexpr (L == 0 && s == 23) ld2<0>(yfl, f2o, vw, W_F2);
                FENCE();
                mm16b<s == 0, 4 * s>(qQ, rq4[s % 3]);
                if constexpr (s + 3 < 36) rq4[s % 3] = lds4(aQq + (s + 3) * QTS);
                if constexpr (s == 33) cQ[4] = lds4(aCS + 208);
                FENCE();
            });
            STAMP(49 + L);
            if constexpr (L == 0) { waitp<6>(yfl); flags_wait(0xffffull); }
#pragma unroll
            for (int i = 0; i < 4; ++i) {
                kp[i] = bld4(vw, kvo, kb[i], SC1);
                kp[4 + i] = bld4(vw, kvo + 1024, kb[i], SC1);
                kp[8 + i] = bld4(vw, kv3, kb[i], SC1);
            }
            FENCE();
            pin(aQ); pin(qQ);
            float t[18]; u32 ph[18];
#pragma unroll
            for (int i = 0; i < 18; ++i) { const u32 cv = cQ[i >> 2][i & 3]; t[i] = __builtin_fmaf(-mean, asf(cv), i < 16 ? aQ[i] : qQ[i - 16]); }
            hedgehog(t, al, ph);
#pragma unroll
            for (int i = 0; i < 5; ++i) { pq[i] = (v4u){ph[4 * i], ph[4 * i + 1], i < 4 ? ph[4 * i + 2] : 0u, i < 4 ? ph[4 * i + 3] : 0u}; pin(pq[i]); }
            FENCE();
            STAMP(7 + 12 * L);
            u32 m = umax(kp[0].x, kp[0].z);
#pragma unroll
            for (int i = 1; i < 12; ++i) m = mx3(m, kp[i].x, kp[i].z);
            nretry = 0;
            if (__builtin_expect(bad(m), 0)) {
#pragma unroll 1
                for (int sp = 0; sp < SPIN; ++sp) {
                    __builtin_amdgcn_s_sleep(2);
                    asm volatile("" ::: "memory");
                    m = 0;
                    ++nretry;
#pragma unroll
                    for (int i = 0; i < 4; ++i) {
                        kp[i] = bld4(vw, kvo, kb[i], SC1);
                        kp[4 + i] = bld4(vw, kvo + 1024, kb[i], SC1);
                        kp[8 + i] = bld4(vw, kv3, kb[i], SC1);
                    }
#pragma unroll
                    for (int i = 0; i < 12; ++i) m = mx3(m, kp[i].x, kp[i].z);
                    if (!bad(m)) break;
                }
            }
            STAMPV(57 + L, nretry);
            f4 a[3];
            sfor<0, 12>(LAM(I_) { constexpr int i = I_.v / 3, j = I_.v % 3; mm16<i == 0>(a[j], sel, kp[4 * j + i]); });
            pad3<7>(a[0], a[1], a[2]);
#pragma unroll
            for (int j = 0; j < 3; ++j) *(LAS v2u*)(kvw[j]) = (v2u){pk(a[j][0], a[j][1]), pk(a[j][2], a[j][3])};
            barrier();
            STAMP(8 + 12 * L);
        }
        {
            v4u A[5], Aq[5];
#pragma unroll
            for (int c = 0; c < 5; ++c) Aq[c] = lds4(aQ4 + 16 * c);
#pragma unroll
            for (int c = 0; c < 5; ++c) A[c] = lds4(aF + 16 * c);
            FENCE();
            f16v o; f4 oq;
            float rd; u32 ol;
            sfor<0, 5>(LAM(C_) { mm16<C_.v == 0>(oq, Aq[C_.v], pq[C_.v]); });
            sfor<0, 5>(LAM(C_) {
                mm32<C_.v == 0>(o, A[C_.v], pq[C_.v]);
                if constexpr (C_.v == 3) { pad<4>(oq); rd = __builtin_amdgcn_rcpf(oq[2] + 1e-6f); pin(rd); }
                if constexpr (C_.v == 4) { ol = pk(oq[0] * rd, oq[1] * rd); pin(ol); }
                FENCE();
            });
            pad<8>(o);
            bst4((v4u){pk(o[0] * rd, o[1] * rd), pk(o[2] * rd, o[3] * rd), pk(o[4] * rd, o[5] * rd), pk(o[6] * rd, o[7] * rd)}, vw, l16, oso, SC1);
            bst4((v4u){pk(o[8] * rd, o[9] * rd), pk(o[10] * rd, o[11] * rd), pk(o[12] * rd, o[13] * rd), pk(o[14] * rd, o[15] * rd)}, vw, l16 + 1024, oso, SC1);
            bst1(ol, vw, l4 + 2048, oso, SC1);
        }
        FENCE();
        STAMP(9 + 12 * L);
        u32 yf[18], aB, ae, ao;
        f16v aW; float xl0, xl1;
        if constexpr (L == 0) {
            sfor<37, 45>(wsite);
            flags_wait(~0ull);
            u32 so = W_Y + rg * YB + 36 * pw * 256 + tw * 64, yo = h * 18 * 256 + tl * 2;
            OPQS(so); OPQV(yo);
            sfor<0, 18>(LAM(M_) { constexpr int m = M_.v; yf[m] = 0; if constexpr (m == 16) so += 4096; ldh<(m & 15) * 256>(yf[m], yo, vw, so); });
        } else {
#pragma unroll
            for (int i = 0; i < 9; ++i) { yf[2 * i] = yv[i] << 16; yf[2 * i + 1] = yv[i] & 0xffff0000u; pin2(yf[2 * i], yf[2 * i + 1]); }
        }
#pragma unroll
        for (int i = 0; i < 8; ++i) { aW[2 * i] = bf_lo(xr[i]); aW[2 * i + 1] = bf_hi(xr[i]); }
        xl0 = bf_lo(xr[8]); xl1 = bf_hi(xr[8]);
        aB = L_Q + ((wave >> 1) * 18 + (kg & 1)) * 1024 + ((kg >> 1) * 32 + 16 * (wave & 1) + row) * 16;
        ae = L_EX + (kg & 1) * 1024 + ((2 * (wave >> 1) + h) * 16 + row) * 16 + 8 * (wave & 1);
        ao = aQ_ + wave * 8192;
        pin(aW); pin2(xl0, xl1); pin2(aB, ae); pin(ao);
        {
            u32 so[4];
#pragma unroll
            for (int i = 0; i < 4; ++i) { so[i] = W_O + (L * 1024 + (rg * 4 + tw) * 16 + 4 * wave + i) * OS; OPQS(so[i]); }
            v4u lo, og[8];
            FENCE();
            STAMP(43 + 3 * L);
            waitv<0>();
            STAMP(44 + 3 * L);
            __builtin_amdgcn_s_sleep(OSL);
            auto ldo = [&]() __attribute__((always_inline)) {
#pragma unroll
                for (int i = 0; i < 8; ++i) og[i] = bld4(vw, l16 + (i & 1) * 1024, so[i >> 1], SC1);
            };
            auto ldl = [&]() __attribute__((always_inline)) {
#pragma unroll
                for (int i = 0; i < 4; ++i) lo[i] = bld1(vw, l4 + 2048, so[i], SC1);
            };
            auto chk = [&]() __attribute__((always_inline)) {
                u32 m = mx3(umax(lo.x, lo.y), lo.z, lo.w);
#pragma unroll
                for (int i = 0; i < 8; i += 2) m = mx3(m, og[i].x, og[i + 1].x);
                return bad(m);
            };
            ldo(); ldl();
            FENCE();
            nretry = 0;
            if (__builtin_expect(chk(), 0)) {
#pragma unroll 1
                for (int sp = 0; sp < SPIN; ++sp) {
                    ++nretry;
                    if (bad(mx3(umax(lo.x, lo.y), lo.z, lo.w))) { __builtin_amdgcn_s_sleep(1); asm volatile("" ::: "memory"); ldl(); continue; }
                    asm volatile("" ::: "memory");
                    ldo();
                    if (!chk()) break;
                }
            }
            STAMPV(59 + L, nretry);
            STAMP(45 + 3 * L);
#pragma unroll
            for (int i = 0; i < 8; ++i) *(LAS v4u*)(ao + i * 1024) = og[i];
            *(LAS v4u*)(aQ_ + (32 + wave) * 1024) = lo;
            barrier();
        }
        STAMP(10 + 12 * L);
        u32 sto = W_ST + (L * 1024 + (rg * 4 + tw) * 16) * 256;
        {
            f4 qW;
            v4u rb_[4], rl[9];
            v2u e0, e1;
            float s1, s2;
#pragma unroll
            for (int i = 0; i < 4; ++i) { rl[i] = lds4(aB + i * 2048); rb_[i] = lds4(aQ_ + i * 1024); }
            FENCE();
            sfor<0, 36>(LAM(SI) {
                constexpr int s = SI.v;
                if constexpr (s < 28) mm32a<false, 144 + 4 * s>(aW, rb_[s & 3]); else mm32<false>(aW, wv[s - 28], rb_[s & 3]);
                if constexpr (s + 4 < 36) rb_[s & 3] = lds4(aQ_ + (s + 4) * 1024);
                if constexpr (s < 5) rl[4 + s] = lds4(aB + (4 + s) * 2048);
                if constexpr (s == 12) { pad<0>(qW); *(LAS f4*)(l16 + (L_EX + wave * 1024)) = qW; }
                if constexpr (s == 14) OPQS(sto);
                FENCE();
                if constexpr (s < 9) { mm16<s == 0>(qW, wv[8 + s], rl[s]); FENCE(); }
                if constexpr (s == 30) { barrier(); e0 = lds2(ae); e1 = lds2(ae + 2048); FENCE(); }
                if constexpr (s == 33) { xl0 += asf(e0.x) + asf(e1.x); xl1 += asf(e0.y) + asf(e1.y); s1 = xl0 + xl1; s2 = __builtin_fmaf(xl0, xl0, xl1 * xl1); pin2(s1, s2); FENCE(); }
            });
            STAMP(11 + 12 * L);
            pad<11>(aW);
            float s3 = aW[0], s4 = aW[0] * aW[0];
#pragma unroll
            for (int i = 1; i < 16; ++i) { if (i & 1) { s1 += aW[i]; s2 = __builtin_fmaf(aW[i], aW[i], s2); } else { s3 += aW[i]; s4 = __builtin_fmaf(aW[i], aW[i], s4); } }
            bst1(asu(halves_sum(s1 + s3, s2 + s4)), vw, l4, sto + pw * 256, SC1);
        }
        FENCE();
        {
            float t;
            u32 xso = L ? (rg * 128 + tw * 32) * 1152 + 72 * pw : W_XF1 + ((rg * 4 + tw) * 16 + pw) * XS, oo = tl * 1152 + 36 * h, one2 = 0x3f803f80u;
            OPQS(xso); OPQS(one2);
            if constexpr (L == 1) OPQV(oo);
            if constexpr (L == 0) {
#pragma unroll
                for (int i = 0; i < 9; ++i) { pin2(yf[2 * i], yf[2 * i + 1]); yv[i] = (yf[2 * i] >> 16) | yf[2 * i + 1]; pin(yv[i]); }
            }
            FENCE();
            waitv<0>();
            nretry = 0;
#pragma unroll 1
            for (int sp = 0; sp < SPIN; ++sp) {
                asm volatile("" : "+v"(l4)::"memory");
                u32 e[16];
#pragma unroll
                for (int i = 0; i < 16; ++i) e[i] = bld1(vw, l4 + i * 256, sto, SC1);
                FENCE();
                u32 m = umax(e[0], e[1]);
                t = asf(e[0]) + asf(e[1]);
#pragma unroll
                for (int i = 2; i < 16; i += 2) { m = mx3(m, e[i], e[i + 1]); t += asf(e[i]) + asf(e[i + 1]); pin2(m, t); }
                if (!bad(m)) break;
                ++nretry;
                __builtin_amdgcn_s_sleep(1);
            }
            STAMP(12 + 12 * L);
            STAMPV(61 + L, nretry);
            auto r = __builtin_amdgcn_permlane32_swap(asu(t), asu(t), false, false);
            const float m1 = asf(r[0]) * (1.f / 576.f);
            const float r1 = __builtin_amdgcn_rsqf(__builtin_fmaxf(asf(r[1]) * (1.f / 576.f) - m1 * m1, 0.f) + 1e-5f), mr = -m1 * r1;
#pragma unroll
            for (int i = 0; i < 9; ++i) {
                const float a = i < 8 ? aW[2 * i] : xl0, c = i < 8 ? aW[2 * i + 1] : xl1;
                xr[i] = pk(__builtin_fmaf(__builtin_fmaf(a, r1, mr), asf(yf[2 * i]), a), __builtin_fmaf(__builtin_fmaf(c, r1, mr), asf(yf[2 * i + 1]), c));
                if constexpr (L == 0) {
                    if (i == 3) { bst4((v4u){xr[0], xr[1], xr[2], xr[3]}, vw, l16, xso, SC1); FENCE(); }
                    if (i == 7) { bst4((v4u){xr[4], xr[5], xr[6], xr[7]}, vw, l16 + 1024, xso, SC1); FENCE(); }
                } else {
                    if (i == 3) { bst4((v4u){xr[0], xr[1], xr[2], xr[3]}, ro, oo, xso, 0); FENCE(); }
                    if (i == 7) { bst4((v4u){xr[4], xr[5], xr[6], xr[7]}, ro, oo + 16, xso, 0); FENCE(); }
                    if (i == 8) bst1(xr[8], ro, oo + 32, xso, 0);
                }
            }
            if constexpr (L == 0) {
                float s1 = 0.f, s2 = 0.f;
#pragma unroll
                for (int j = 0; j < 9; ++j) { s1 = dot2(xr[j], one2, s1); s2 = dot2(xr[j], xr[j], s2); }
                bst2((v2u){xr[8], asu(halves_sum(s1, s2))}, vw, l8 + 2048, xso, SC1);
                STAMP(13);
            } else {
                if (b == 0 && tid == 0) bst2((v2u){(u32)ep, (u32)(ep >> 32)}, vw, 0, W_E, SC1);
            }
        }
    });
    STAMP_END();
}
#if defined(__HIP_DEVICE_COMPILE__)
#pragma clang attribute pop
#endif

extern "C" void kernel_launch(void* const* d_in, const int* in_sizes, int n_in, void* d_out, int out_size, void* d_ws, size_t ws_size, hipStream_t stream) {
    (void)in_sizes; (void)n_in; (void)out_size; (void)ws_size;
    hipFuncSetAttribute((const void*)k_main, hipFuncAttributeMaxDynamicSharedMemorySize, L_END);
    const u32 f = 0x00020000u, n = 2048 * 1152;
    Args a = {{d_in[5], n, f}, {d_in[6], n, f}, {{d_in[1], 576 * 3456, f}, {d_in[3], 576 * 1152, f}}, {{d_in[2], 32 * 648, f}, {d_in[2], 0, f}}, {d_in[0], n, f}, {d_ws, W_END, f}, {d_out, n, f}, d_in[7], d_in[8], d_in[9]};
    k_main<<<256, 256, L_END, stream>>>(a);
}
```

```cpp
#include <hip/hip_runtime.h>
#include <stdint.h>
#pragma clang diagnostic ignored "-Wint-to-pointer-cast"
#pragma clang diagnostic ignored "-Wunused-value"
#pragma clang diagnostic ignored "-Winline-asm"
#if defined(__HIP_DEVICE_COMPILE__)
#pragma clang attribute push(__attribute__((target("no-packed-fp32-ops"))), apply_to = function)
#endif
typedef unsigned short u16;
typedef unsigned u32;
typedef unsigned long long u64;
typedef u32 v4u __attribute__((ext_vector_type(4)));
typedef u32 v2u __attribute__((ext_vector_type(2)));
typedef float f4 __attribute__((ext_vector_type(4)));
typedef float f2 __attribute__((ext_vector_type(2)));
typedef float f16v __attribute__((ext_vector_type(16)));
typedef __bf16 b8 __attribute__((ext_vector_type(8)));
typedef __bf16 b2 __attribute__((ext_vector_type(2)));
typedef short s4 __attribute__((ext_vector_type(4)));
#define LAS __attribute__((address_space(3)))
#define DEV __device__ __forceinline__
#define SC1 16

constexpr u32 QTS = 416, STGS = 232, CVS = 21216, HCS = 4160;
constexpr u32 L_K = 0, L_V = 36864, L_QT = 73728, L_Q = 89088, L_STG = 125952, L_KVA = 155648, L_CS = 158784, L_EX = 159232, L_END = 163328;
constexpr u32 P_KS = 0, P_VS = 10240, P_WS = 20480, P_XS = 31232, P_PH = 42496, P_LS = 55296, L_Z = L_KVA + 38 * 80;
constexpr u32 IMG = L_STG, XS = 2560, OS = 2304, YB = 576 * 256, CSB = 16 * 576;
constexpr u32 WOG = 4 * 36864 + 18432;
constexpr u32 W_IMG = 0, W_WOT = W_IMG + 16 * IMG, W_CSP = W_WOT + 4 * WOG, W_UT = W_CSP + 16 * CSB,
              W_XF0 = W_UT + 576 * 4096, W_XF1 = W_XF0 + 1024 * XS, W_O = W_XF1 + 1024 * XS, W_Y = W_O + 2 * 1024 * OS, W_KV = W_Y + 576 * 4096,
              W_ST = W_KV + 2 * 256 * 2816, W_F = W_ST + 2 * 1024 * 256, W_F2 = W_F + 256 * 128, W_E = W_F2 + 256 * 128, W_END = W_E + 128, W_DBG = W_END;
constexpr u32 OOB = 0x7ffffff0u;
constexpr int SPIN = 1 << 14;
constexpr u32 CODE = 64000;
#define OSL 2

DEV float asf(u32 v) { return __builtin_bit_cast(float, v); }
DEV u32 asu(float v) { return __builtin_bit_cast(u32, v); }
DEV float bf_lo(u32 v) { return asf(v << 16); }
DEV float bf_hi(u32 v) { return asf(v & 0xffff0000u); }
DEV u32 pk(float lo, float hi) { b2 r = __builtin_convertvector((f2){lo, hi}, b2); return __builtin_bit_cast(u32, r); }
DEV float rb(float f) { return bf_lo(pk(f, f)); }
DEV float dot2(u32 a, u32 b, float c) { return __builtin_amdgcn_fdot2_f32_bf16(__builtin_bit_cast(b2, a), __builtin_bit_cast(b2, b), c, false); }
DEV u32 umin(u32 a, u32 b) { return a < b ? a : b; }
DEV u32 umax(u32 a, u32 b) { return a > b ? a : b; }
DEV u32 mx3(u32 a, u32 b, u32 c) { return umax(umax(a, b), c); }
template <bool Z> DEV void mm32(f16v& c, v4u a, v4u b) {
    if constexpr (Z) asm volatile("v_mfma_f32_32x32x16_bf16 %0, %1, %2, 0" : "=&v"(c) : "v"(a), "v"(b));
    else asm volatile("v_mfma_f32_32x32x16_bf16 %0, %1, %2, %0" : "+v"(c) : "v"(a), "v"(b));
}
template <bool Z> DEV void mm16(f4& c, v4u a, v4u b) {
    if constexpr (Z) asm volatile("v_mfma_f32_16x16x32_bf16 %0, %1, %2, 0" : "=&v"(c) : "v"(a), "v"(b));
    else asm volatile("v_mfma_f32_16x16x32_bf16 %0, %1, %2, %0" : "+v"(c) : "v"(a), "v"(b));
}
template <bool Z, int R> DEV void mm32b(f16v& c, v4u a) {
    if constexpr (Z) asm volatile("v_mfma_f32_32x32x16_bf16 %0, %1, a[%c2:%c3], 0" : "=&v"(c) : "v"(a), "n"(R), "n"(R + 3));
    else asm volatile("v_mfma_f32_32x32x16_bf16 %0, %1, a[%c2:%c3], %0" : "+v"(c) : "v"(a), "n"(R), "n"(R + 3));
}
template <bool Z, int R> DEV void mm16b(f4& c, v4u a) {
    if constexpr (Z) asm volatile("v_mfma_f32_16x16x32_bf16 %0, %1, a[%c2:%c3], 0" : "=&v"(c) : "v"(a), "n"(R), "n"(R + 3));
    else asm volatile("v_mfma_f32_16x16x32_bf16 %0, %1, a[%c2:%c3], %0" : "+v"(c) : "v"(a), "n"(R), "n"(R + 3));
}
template <bool Z, int R> DEV void mm32a(f16v& c, v4u b) {
    if constexpr (Z) asm volatile("v_mfma_f32_32x32x16_bf16 %0, a[%c2:%c3], %1, 0" : "=&v"(c) : "v"(b), "n"(R), "n"(R + 3));
    else asm volatile("v_mfma_f32_32x32x16_bf16 %0, a[%c2:%c3], %1, %0" : "+v"(c) : "v"(b), "n"(R), "n"(R + 3));
}
template <int N, class T> DEV void pad(T& c) { asm volatile("s_nop %1" : "+v"(c) : "n"(N)); }
template <class T> DEV void pin(T& c) { asm volatile("" : "+v"(c)); }
DEV void nop2() { asm volatile("s_nop 1"); }
template <int R> DEV u32 rda() { u32 v; asm volatile("v_accvgpr_read_b32 %0, a%c1" : "=v"(v) : "n"(R)); return v; }
template <int R> DEV void wra(u32 v) { asm volatile("v_accvgpr_write_b32 a%c1, %0" ::"v"(v), "n"(R)); }
__device__ void bst4(v4u, v4u, u32, u32, int) __asm("llvm.amdgcn.raw.buffer.store.v4i32");
__device__ void bst2(v2u, v4u, u32, u32, int) __asm("llvm.amdgcn.raw.buffer.store.v2i32");
__device__ void bst1(u32, v4u, u32, u32, int) __asm("llvm.amdgcn.raw.buffer.store.i32");
__device__ v4u bld4(v4u, u32, u32, int) __asm("llvm.amdgcn.raw.buffer.load.v4i32");
__device__ v2u bld2(v4u, u32, u32, int) __asm("llvm.amdgcn.raw.buffer.load.v2i32");
__device__ u32 bld1(v4u, u32, u32, int) __asm("llvm.amdgcn.raw.buffer.load.i32");
struct Buf { const void* p; u32 n, f; };
struct Args { Buf k, v, w[2], ph[2], x, ws, out; const void *sk, *sh, *sD; };
template <class T> DEV void sld(T& d, const void* ka, u32 off) {
    if constexpr (sizeof(T) == 16) asm volatile("s_load_dwordx4 %0, %1, %2" : "=s"(d) : "s"(ka), "s"(off));
    else asm volatile("s_load_dwordx2 %0, %1, %2" : "=s"(d) : "s"(ka), "s"(off));
}
template <int O, class T> DEV void sldi(T& d, const void* ka) {
    if constexpr (sizeof(T) == 16) asm volatile("s_load_dwordx4 %0, %1, %2" : "=s"(d) : "s"(ka), "n"(O));
    else asm volatile("s_load_dwordx2 %0, %1, %2" : "=s"(d) : "s"(ka), "n"(O));
}
DEV v4u lds4(u32 a) { return *(const LAS v4u*)a; }
DEV v2u lds2(u32 a) { return *(const LAS v2u*)a; }
DEV v2u lds2v(u32 a) { return *(const volatile LAS v2u*)a; }
DEV void barrier() { asm volatile("s_waitcnt lgkmcnt(0)\n\ts_barrier" ::: "memory"); }
template <int N> DEV void waitv() { asm volatile("s_waitcnt vmcnt(%0)" ::"n"(N) : "memory"); }
DEV void dma_in(const void* g, u32 voff, u32 lds) {
    asm volatile("s_mov_b32 m0, %2\n\ts_nop 0\n\tglobal_load_lds_dwordx4 %0, %1" ::"v"(voff), "s"(g), "s"(lds) : "m0", "memory");
}
DEV void dma_b(v4u rs, u32 voff, u32 lds) {
    asm volatile("s_mov_b32 m0, %2\n\ts_nop 0\n\tbuffer_load_dwordx4 %0, %1, 0 offen lds" ::"v"(voff), "s"(rs), "s"(lds) : "m0", "memory");
}
DEV void dma_s(v4u rs, u32 voff, u32 so, u32 lds) {
    asm volatile("s_mov_b32 m0, %3\n\ts_nop 0\n\tbuffer_load_dwordx4 %0, %1, %2 offen lds" ::"v"(voff), "s"(rs), "s"(so), "s"(lds) : "m0", "memory");
}
DEV void dma_ws(v4u rs, u32 voff, u32 so, u32 lds) {
    asm volatile("s_mov_b32 m0, %3\n\ts_nop 0\n\tbuffer_load_dwordx4 %0, %1, %2 offen sc1 lds" ::"v"(voff), "s"(rs), "s"(so), "s"(lds) : "m0", "memory");
}
template <int IM> DEV void gld4(v4u& d, u32 voff, const void* g) { asm volatile("global_load_dwordx4 %0, %1, %2 offset:%3" : "=v"(d) : "v"(voff), "s"(g), "n"(IM) : "memory"); }
DEV void gld2(v2u& d, u32 voff, const void* g) { asm volatile("global_load_dwordx2 %0, %1, %2" : "=v"(d) : "v"(voff), "s"(g) : "memory"); }
DEV void mm16k(f4& c, v2u a, v2u b) { asm volatile("v_mfma_f32_16x16x16_bf16 %0, %1, %2, 0" : "=&v"(c) : "v"(a), "v"(b)); }
DEV void mm16n(f4& c, v4u a, v4u b) { asm volatile("s_nop 1\n\tv_mfma_f32_16x16x32_bf16 %0, %1, %2, 0" : "=&v"(c) : "v"(a), "v"(b)); }
template <class A, class B> DEV void pin2(A& a, B& b) { asm volatile("" : "+v"(a), "+v"(b)); }
template <int IM> DEV void ld4(v4u& d, u32 voff, v4u rs, u32 so) { asm volatile("buffer_load_dwordx4 %0, %1, %2, %3 offen offset:%4 sc1" : "+v"(d) : "v"(voff), "s"(rs), "s"(so), "n"(IM) : "memory"); }
template <int IM> DEV void ld1(u32& d, u32 voff, v4u rs, u32 so) { asm volatile("buffer_load_dword %0, %1, %2, %3 offen offset:%4 sc1" : "+v"(d) : "v"(voff), "s"(rs), "s"(so), "n"(IM) : "memory"); }
template <int IM> DEV void ld4m(v4u& d, u32 voff, v4u rs, u32 so, u64 m) {
    asm volatile("s_mov_b64 exec, %5\n\tbuffer_load_dwordx4 %0, %1, %2, %3 offen offset:%4 sc1\n\ts_mov_b64 exec, -1" : "+v"(d) : "v"(voff), "s"(rs), "s"(so), "n"(IM), "s"(m) : "memory");
}
template <int IM, int R> DEV void ld4a(u32 voff, v4u rs, u32 so) { asm volatile("buffer_load_dwordx4 a[%c4:%c5], %0, %1, %2 offen offset:%3 sc1" ::"v"(voff), "s"(rs), "s"(so), "n"(IM), "n"(R), "n"(R + 3) : "memory"); }
template <int IM, int R> DEV void ld1a(u32 voff, v4u rs, u32 so) { asm volatile("buffer_load_dword a%c4, %0, %1, %2 offen offset:%3 sc1" ::"v"(voff), "s"(rs), "s"(so), "n"(IM), "n"(R) : "memory"); }
template <int IM, int R> DEV void ld4am(u32 voff, v4u rs, u32 so, u64 m) {
    asm volatile("s_mov_b64 exec, %6\n\tbuffer_load_dwordx4 a[%c4:%c5], %0, %1, %2 offen offset:%3 sc1\n\ts_mov_b64 exec, -1" ::"v"(voff), "s"(rs), "s"(so), "n"(IM), "n"(R), "n"(R + 3), "s"(m) : "memory");
}
template <int IM> DEV void ld2(v2u& d, u32 voff, v4u rs, u32 so) { asm volatile("buffer_load_dwordx2 %0, %1, %2, %3 offen offset:%4 sc1" : "=v"(d) : "v"(voff), "s"(rs), "s"(so), "n"(IM) : "memory"); }
DEV void ld2p(v2u& d, u32 voff, v4u rs) { asm volatile("buffer_load_dwordx2 %0, %1, %2, 0 offen sc1" : "+v"(d) : "v"(voff), "s"(rs) : "memory"); }
template <int IM> DEV void ld4o(v4u& d, u32 voff, v4u rs, u32 so) { asm volatile("buffer_load_dwordx4 %0, %1, %2, %3 offen offset:%4 sc1" : "=v"(d) : "v"(voff), "s"(rs), "s"(so), "n"(IM) : "memory"); }
template <int IM> DEV void ldh(u32& d, u32 voff, v4u rs, u32 so) { asm volatile("buffer_load_short_d16_hi %0, %1, %2, %3 offen offset:%4 sc1" : "+v"(d) : "v"(voff), "s"(rs), "s"(so), "n"(IM) : "memory"); }
template <int IM> DEV void st4(v4u d, u32 voff, v4u rs, u32 so) { asm volatile("buffer_store_dwordx4 %0, %1, %2, %3 offen offset:%4 sc1" ::"v"(d), "v"(voff), "s"(rs), "s"(so), "n"(IM) : "memory"); }
DEV void setm0(u32 v) { asm volatile("s_mov_b32 m0, %0" ::"s"(v) : "m0", "memory"); }
template <int IM> DEV void dmai(v4u rs, u32 voff, u32 so) { asm volatile("buffer_load_dwordx4 %0, %1, %2 offen offset:%3 sc1 lds" ::"v"(voff), "s"(rs), "s"(so), "n"(IM) : "memory"); }
DEV u32 pka(float a, float b) { u32 r; asm volatile("v_cvt_pk_bf16_f32 %0, %1, %2" : "=v"(r) : "v"(a), "v"(b)); return r; }
template <int N> DEV void waitl() { __builtin_amdgcn_s_waitcnt(0xc07f | (N << 8)); }
DEV u32 vsel(bool c, u32 a, u32 b) { asm volatile("" : "+v"(a)); asm volatile("" : "+v"(b)); return c ? a : b; }
DEV u32 ssel(bool c, u32 a, u32 b) { asm volatile("" : "+s"(a)); asm volatile("" : "+s"(b)); return c ? a : b; }
template <int N, class T> DEV void waitp(T& d) { asm volatile("s_waitcnt vmcnt(%1)" : "+v"(d) : "n"(N) : "memory"); }
template <int N> DEV void waitbar() { asm volatile("s_waitcnt vmcnt(%0) lgkmcnt(0)\n\ts_barrier" ::"n"(N) : "memory"); }
template <int N> DEV void pad3(f4& a, f4& b, f4& c) { asm volatile("s_nop %3" : "+v"(a), "+v"(b), "+v"(c) : "n"(N)); }
template <int I> struct IC { static constexpr int v = I; };
template <int B, int E, class F> DEV void sfor(F&& f) { if constexpr (B < E) { f(IC<B>{}); sfor<B + 1, E>(f); } }
#define LAM(I) [&](auto I) __attribute__((always_inline))
#define FENCE() __builtin_amdgcn_sched_barrier(0)
#define OPQV(x) asm volatile("" : "+v"(x))
#define OPQS(x) asm volatile("" : "+s"(x))
DEV float halves_sum(float a, float b) {
    auto r = __builtin_amdgcn_permlane32_swap(asu(a), asu(b), false, false);
    return asf(r[0]) + asf(r[1]);
}
DEV bool bad(u32 m) { return __builtin_amdgcn_ballot_w64(m == ~0u) != 0; }
#define STAMP_INIT()
template <int I> DEV void mark() { asm volatile("; MARK %0" ::"n"(I)); }
#define STAMP(i) mark<(i)>()
#define STAMP_END()
#define STAMPV(i, v)

DEV void hedgehog(const float (&t)[18], float a, u32 (&ph)[18]) {
    float mx = t[0], mn = t[0];
#pragma unroll
    for (int i = 1; i < 18; ++i) { mx = __builtin_fmaxf(mx, t[i]); mn = __builtin_fminf(mn, t[i]); }
    const float c1 = -a * mx, c2 = a * mn;
    float e1[18], e2[18], s1 = 0.f, s2 = 0.f;
#pragma unroll
    for (int i = 0; i < 18; ++i) {
        e1[i] = __builtin_amdgcn_exp2f(__builtin_fmaf(t[i], a, c1)); s1 = i ? s1 + e1[i] : e1[i];
        e2[i] = __builtin_amdgcn_exp2f(__builtin_fmaf(t[i], -a, c2)); s2 = i ? s2 + e2[i] : e2[i];
    }
    const float r1 = __builtin_amdgcn_rcpf(s1), r2 = __builtin_amdgcn_rcpf(s2);
#pragma unroll
    for (int i = 0; i < 9; ++i) { ph[i] = pk(e1[2 * i] * r1, e1[2 * i + 1] * r1); ph[9 + i] = pk(e2[2 * i] * r2, e2[2 * i + 1] * r2); }
}
DEV v4u tr2(u32 a0, u32 a1) {
    s4 lo = __builtin_amdgcn_ds_read_tr16_b64_v4i16((LAS s4*)a0), hi = __builtin_amdgcn_ds_read_tr16_b64_v4i16((LAS s4*)a1);
    v2u l = __builtin_bit_cast(v2u, lo), h = __builtin_bit_cast(v2u, hi);
    return (v4u){l.x, l.y, h.x, h.y};
}
DEV v2u y_val(f4 acc) { return (v2u){pk(acc[0], acc[1]), pk(acc[2], acc[3])}; }
DEV v4u y_pair(v2u a, v2u b) {
    auto r0 = __builtin_amdgcn_permlane16_swap(a.x, b.x, false, false), r1 = __builtin_amdgcn_permlane16_swap(a.y, b.y, false, false);
    return (v4u){r0[0], r1[0], r0[1], r1[1]};
}
template <int N, int n, int r>
DEV void conv_read(u32 aa, u32 ba, v4u (&A)[N], v4u (&B)[N]) {
    constexpr int i = n / 3, k = n % 3;
    if constexpr (k == 0) B[i] = lds4(ba + 64 * (8 * i - r + 7));
    else { const v2u v = lds2(aa + 2 + 64 * (r + 8 * i) + 8 * (k - 1)); if constexpr (k == 1) { A[i].x = v.x; A[i].y = v.y; } else { A[i].z = v.x; A[i].w = v.y; } }
}
constexpr int conv_ij(int N, int MASK, int n) {
    for (int d = 0; d < N; ++d) for (int i = 0; i < N; ++i) for (int j = i; j < N; ++j)
        if (((MASK >> j) & 1) && (i > j - i ? i : j - i) == d && n-- == 0) return i * 8 + j;
    return -1;
}
constexpr int NPRE = 12, NSITE = 28 + 56 + 4, SITE_END = 200;
constexpr int site_at(int q) { return q <= SITE_END ? NPRE + (q * (NSITE - NPRE) + SITE_END - 1) / SITE_END : NSITE; }
constexpr int kstep(int n) { return n % 9 < 8 ? 8 * (n / 9) + n % 9 : 32 + n / 9; }

__global__ void __launch_bounds__(256, 1) k_main(Args) {
    const void* ka = (const void*)(uintptr_t)__builtin_amdgcn_kernarg_segment_ptr();
    v4u rk, rv, rm, rq, rp, rx, vw, ro;
    const char *sk, *sh, *sD;
    int tid = __builtin_amdgcn_workitem_id_x(), b = __builtin_amdgcn_workgroup_id_x();
    sldi<0>(rk, ka); sldi<16>(rv, ka); sldi<144>(sk, ka); sldi<112>(vw, ka); sldi<96>(rx, ka); sldi<128>(ro, ka); sldi<152>(sh, ka); sldi<160>(sD, ka);
    OPQS(b); OPQV(tid);
    int wave = __builtin_amdgcn_readfirstlane(tid >> 6);
    sld(rm, ka, (wave >> 1) * 16); sld(rq, ka, (b & 24) == 24 ? 48 : 32); sld(rp, ka, 64 + (b & 8) * 2);
    OPQS(b); OPQS(wave); OPQV(tid);
    {
        u64 pc;
        asm volatile("s_getpc_b64 %0" : "=s"(pc));
        const u32 o = umin(tid * 8 + (b >> 3) * 2048, CODE - 512);
        asm volatile("s_mov_b32 m0, %2\n\ts_nop 0\n\tglobal_load_lds_dword %0, %1" ::"v"(o), "s"(pc), "s"(L_EX) : "m0", "memory");
    }
    STAMP_INIT();
    asm volatile("" ::: "a255");
    u64 ep;
    v2u sw0, sw1;
    {
        const int lane = tid & 63, c = lane & 15, cg = ((b >> 4) & 6) + ((b >> 3) & 1), tg = 4 * (b & 7) + 2 * (b >> 7) + ((b >> 4) & 1);
        u32 s0 = c * 8 + (wave * 128 + cg * 576), s1 = (c & 7) * 8 + cg * 576, vo[5], lo[5];
        const u32 l0 = ((lane * 7282u) >> 16) * 1008 + lane * 16, t0 = (64 * tg + 7 * wave) * 1152 + cg * 144;
#pragma unroll
        for (int i = 0; i < 5; ++i) {
            const int isv = i < 2 ? 0 : i > 2 ? 1 : wave >> 1;
            vo[i] = l0 + (t0 + (28 * i - 3 - 67 * isv) * 1152); lo[i] = P_KS + wave * 1008 + i * 4032 + isv * (P_VS - 10080);
            OPQV(vo[i]); OPQS(lo[i]);
        }
        pin2(s0, s1);
        FENCE();
        asm volatile("s_waitcnt lgkmcnt(0)" : "+s"(rk), "+s"(rv), "+s"(rm), "+s"(rq), "+s"(rp), "+s"(rx), "+s"(vw), "+s"(ro), "+s"(sk), "+s"(sh), "+s"(sD));
        STAMP(40);
#pragma unroll
        for (int i = 0; i < 5; ++i) dma_b(i < 2 ? rk : i > 2 ? rv : rm, vo[i], lo[i]);
        asm volatile("global_load_dwordx2 %0, %1, %2" : "=v"(sw0) : "v"(s0), "s"(sk) : "memory");
        asm volatile("global_load_dwordx2 %0, %1, %2 offset:512" : "=v"(sw1) : "v"(s1), "s"(sk) : "memory");
        sld(ep, (const void*)(((u64)vw.y << 32) | vw.x), W_E);
    }
    OPQS(b); OPQS(wave); OPQV(tid);
    const int lane = tid & 63, rg = 2 * (b & 7) + (b >> 7), p = (b >> 3) & 15;
    const int Pg = p >> 2, tw = p & 3, pw = 4 * Pg + wave, ci = (rg + p) & 15;
    const bool conv = ci < 12;
    u32 l16 = lane * 16, l4 = lane * 4;
    OPQV(l16); OPQV(l4);
    const int ch0 = 36 * p + 3 * ci;
    u32 xr[9], kvw[3], kvA, kvB, l8, l8B, l16B, cpo, csd;
    {
        const int sec = p & 3, cg = 2 * Pg + (p & 1), tg = 2 * rg + ((p >> 1) & 1);
        const int c = lane & 15;
        FENCE();
        STAMP(41);
        const v4u z4 = {0, 0, 0, 0};
        u32 mk0, mk1, ua0, ua1, uo0, uo1, aw, al_, ap, d0_, d1_, d2_, cso, axs, aid, azc;
        v2u Bi; v4u Sel;
        const bool s3 = sec == 3;
        const u32 wf_ = rg >> 2, jl = rg & 3;
        auto wdma = [&]() __attribute__((always_inline)) {
            const u32 wst = s3 ? 1152 : 3456, cb = (sec < 2 ? (sec + 1) * 1152 : 0) + Pg * 288;
            const u32 lw = ((lane * 3641u) >> 16) * (wst - 288) + l16, w0 = (36 * rg + 3 * wave) * wst + cb;
            const u32 r4 = (lane * 43u) >> 8, sg = ((s3 ? 0 : (4 * Pg + wf_) & 14) + 4 * jl + wave) & 15;
            const u32 ll = vsel(lane < 24, r4 * (wst - 96) + (r4 >> 1) * (16 * wst) + l16 + ((36 * sg + 16) * wst + cb + ((72 * wf_) & ~15u)), OOB);
            const u32 lx = ((lane * 3450u) >> 16) * 848 + l16 + ((128 * rg + 32 * tw + 3 * wave) * 1152 + Pg * 288);
            const u32 lp = vsel(lane < 54, l16 - ((lane * 21846u) >> 16) * 12 + (Pg * 5184 + wave * 1296), OOB);
#pragma unroll
            for (int i = 0; i < 3; ++i) dma_b(rq, lw + (w0 + 12 * i * wst), P_WS + wave * 864 + i * 3456);
            dma_b(rq, ll, P_LS + wave * 1024);
#pragma unroll
            for (int i = 0; i < 2; ++i) dma_s(rp, lp, i * 648, P_PH + wave * 3072 + i * 1536);
#pragma unroll
            for (int i = 0; i < 3; ++i) dma_s(rx, lx, 12 * i * 1152, P_XS + wave * 912 + i * 3648);
        };
        {
            const u32 g = lane >> 4, m = lane & 31, kh = lane >> 5;
            const u32 hm = g == (c >> 2) ? ((c & 1) ? 0xffff0000u : 0xffffu) : 0u;
            mk0 = (c & 2) ? 0u : hm; mk1 = (c & 2) ? hm : 0u;
            Bi = (v2u){mk0 & 0x3f803f80u, mk1 & 0x3f803f80u}; Sel = (v4u){Bi.x, Bi.y, Bi.x, Bi.y};
            const u32 r0 = (8 * (c >> 2) + (c & 3)) * 144;
            ua0 = r0 + 8 * g + (P_KS + 32 * wave);
            ua1 = r0 + 32 * 144 * kh + 8 * (g & 1) + (P_KS + 128 + 576 * (wave & 1));
            uo0 = c * 4096 + 16 * g;
            uo1 = (c & 7) * 4096 + 64 * (c >> 3) + 16 * g + (wave < 2 ? 64 * 4096 + 8 - 8 * wave : OOB - (1u << 20));
            pin2(mk0, mk1); pin2(Bi, Sel); pin2(ua0, ua1); pin2(uo0, uo1);
            const u32 hD = (m >> 2) & 1;
            aw = (18 * hD + (m & 3) + 4 * (m >> 3)) * 288 + 16 * kh + (P_WS + ((72 * wave) & ~15u));
            al_ = ((((m >> 1) & 1) + 2 * ((m >> 3) & 1)) * 1024 + (2 * hD + (m & 1)) * 96) * (wave == wf_) + 16 * kh + (wave == wf_ ? P_LS : L_Z);
            ap = (8 * kh + (c >> 2)) * 48 + 32 * (g & 1) + 8 * (c & 3) + (P_PH + (64 * wave - 4 * (wave & 1)) * 48);
            pin2(aw, al_); pin(ap);
            const u32 ib = W_IMG + pw * IMG, ob = W_WOT + Pg * WOG;
            const u32 bA = ssel(s3, ob + wave * 36864, ib + sec * L_V + (sec >> 1) * (L_Q - 2 * L_V));
            const u32 bB = ssel(s3, ob + 4 * 36864 + 128 * (wave >> 1) + 32 * (wave & 1), ib + L_QT + 128 * sec);
            const u32 dsB = s3 ? 256 : QTS, drB = s3 ? 1024 : 2 * QTS, ri = (rg - (s3 ? 0 : pw & 14)) & 15;
            const bool lo = m < 16;
            const u32 lp = vsel(lo, ((m & 3) + 8 * (m >> 2) + 32 * kh) * 16 + bA, vsel(m < 18, (m & 1) * (s3 ? 16 : 32) + kh * (s3 ? 512 : 16) + bB, OOB));
            const u32 ds = vsel(lo, 1024, dsB), dr = vsel(lo, 2048, drB);
            d0_ = lp + ri * dr; d1_ = d0_ + ds;
            d2_ = umax(lp + (16 + (jl >> 1)) * dr + (jl & 1) * ds, wave == wf_ ? 0u : OOB);
            cso = vsel(m < (s3 ? 0 : 18), (18 * kh + m) * 4 + (W_CSP + pw * CSB + rg * 576 + sec * 192), OOB);
            axs = m * 304 + 36 * kh + (P_XS + 72 * wave);
            aid = vsel(lane < 36 * (sec & 1), lane * 50 + (lane >= 18 ? 1536 - 900 : 0) + (P_PH + wave * 3072), L_END - 16);
            azc = vsel(lane < (sec ? 0 : 6), (lane & 1) * 16 + ((lane >> 1) + (lane < 4 ? 2 * rg : 30 + jl)) * QTS + (ib + L_QT + 384), OOB);
            pin2(d0_, d1_); pin2(d2_, cso); pin2(axs, aid); pin(azc);
            *(LAS v2u*)(vsel(tid < 38, tid * 80 + (L_KVA + 72), L_END - 16)) = (v2u){0, 0};
            *(LAS v4u*)(l16 + kh * 1024 + (P_PH + 1024 + wave * 3072)) = z4;
            *(LAS v4u*)(vsel(lane < (wave ? 30 : 6), l16 + ssel(wave, L_Q + 4 * HCS - CVS + wave * CVS, L_Z), L_END - 16)) = z4;
        }
        FENCE();
        STAMP(42);
        wdma();
        FENCE();
        STAMP(26);
        asm volatile("s_waitcnt vmcnt(9)" : "+v"(sw0), "+v"(sw1)::"memory");
        v2u Bd[2][4];
#pragma unroll
        for (int s = 0; s < 2; ++s)
#pragma unroll
            for (int j = 0; j < 4; ++j) {
                const v2u sw = s ? sw1 : sw0;
                const u32 wd = __builtin_amdgcn_perm(0u, j < 2 ? sw.y : sw.x, (j & 1) ? 0x01000100u : 0x03020302u);
                Bd[s][j] = (v2u){wd & mk0, wd & mk1};
                pin(Bd[s][j]);
            }
        u32 so_u = W_UT + 128 * tg + cg * 72 * 4096, so_u2 = so_u + wave * 65536;
        OPQS(so_u); OPQS(so_u2);
        FENCE();
        barrier();
        STAMP(27);
        {
            u32 uo[5][2];
            v2u ka[5][5]; f4 pr[5][5], sm[5];
            v4u bb[2];
#pragma unroll
            for (int n = 0; n < 25; ++n) {
                const int T = n < 10 ? n >> 1 : n % 5, j = n < 10 ? n & 1 : n / 5;
                ka[T][j] = lds2v((T < 4 ? ua0 + (32 * (T >> 1) + 4 * (T & 1)) * 144 : ua1) + (j < 4 ? j * 144 : P_VS));
            }
            FENCE();
            auto P = [&](auto T_, auto J_) __attribute__((always_inline)) { constexpr int T = T_.v, j = J_.v; mm16k(pr[T][j], ka[T][j], j < 4 ? Bd[T == 4][j] : Bi); };
            sfor<0, 5>(LAM(T_) { P(T_, IC<0>{}); P(T_, IC<1>{}); });
            STAMP(52);
            sfor<0, 16>(LAM(I_) {
                constexpr int I = I_.v, T = I % 5, ph = I / 5 + 1, U = (I + 4) % 5;
                if constexpr (I < 15) {
                    f4& a = ph == 1 ? pr[T][0] : sm[T];
                    const u32 w0 = pka(a[0], a[1]), w1 = pka(a[2], a[3]), w2 = pka(pr[T][ph][0], pr[T][ph][1]), w3 = pka(pr[T][ph][2], pr[T][ph][3]);
                    bb[I & 1] = (v4u){w0, w1, w2, w3};
                    P(IC<T>{}, IC<ph + 1>{});
                } else {
                    uo[0][0] = pka(sm[0][0], sm[0][1]); uo[0][1] = pka(sm[0][2], sm[0][3]);
                }
                if constexpr (I > 0) mm16<true>(sm[U], Sel, bb[(I - 1) & 1]);
            });
            STAMP(53);
            sfor<0, 5>(LAM(T_) {
                constexpr int T = T_.v;
                u32 w0, w1;
                if constexpr (T) { w0 = pka(sm[T][0], sm[T][1]); w1 = pka(sm[T][2], sm[T][3]); } else { w0 = uo[0][0]; w1 = uo[0][1]; }
                pin(pr[T][4]);
                FENCE();
                uo[T][0] = pk(bf_hi(w1) * pr[T][4][3], bf_lo(w1) * pr[T][4][2]);
                uo[T][1] = pk(bf_hi(w0) * pr[T][4][1], bf_lo(w0) * pr[T][4][0]);
                if constexpr (T == 1 || T == 3) bst4((v4u){uo[T][0], uo[T][1], uo[T - 1][0], uo[T - 1][1]}, vw, uo0 + 32 * (T - 1), so_u2, SC1);
                else if constexpr (T == 4) bst2((v2u){uo[4][0], uo[4][1]}, vw, uo1, so_u, SC1);
                else pin2(uo[T][0], uo[T][1]);
                FENCE();
            });
        }
        STAMP(28);
        asm volatile("s_waitcnt vmcnt(3)\n\ts_barrier" ::: "memory");
        STAMP(54);
        {
            u32 one2 = 0x3f803f80u;
            OPQS(one2);
            *(LAS u16*)(aid) = 0x3f80;
            v4u bw[2][2], wa[2][3];
            bw[0][0] = tr2(ap, ap + 192); wa[0][0] = lds4(aw); wa[1][0] = lds4(al_);
            bw[0][1] = tr2(ap + 768, ap + 960); wa[0][1] = lds4(aw + 32); wa[1][1] = lds4(al_ + 32);
            bw[1][0] = tr2(ap + 1440, ap + 1440 + 192);
            bw[1][1] = tr2(ap + 1440 + 768, ap + 1440 + 960); wa[0][2] = lds4(aw + 64); wa[1][2] = lds4(al_ + 64);
            FENCE();
#pragma unroll
            for (int j = 0; j < 9; ++j) xr[j] = *(const LAS u32*)(axs + 4 * j);
            FENCE();
            f16v d[2][2];
#pragma unroll
            for (int i = 0; i < 4; ++i) { mm32<true>(d[i >> 1][i & 1], wa[i & 1][i >> 1], bw[i >> 1][0]); mm32<false>(d[i >> 1][i & 1], wa[i & 1][(i >> 1) + 1], bw[i >> 1][1]); }
            FENCE();
            STAMP(55);
            {
                float s1 = 0.f, s2 = 0.f;
#pragma unroll
                for (int j = 0; j < 9; ++j) { s1 = dot2(xr[j], one2, s1); s2 = dot2(xr[j], xr[j], s2); }
                const u32 so = W_XF0 + ((rg * 4 + tw) * 16 + pw) * XS;
                bst4((v4u){xr[0], xr[1], xr[2], xr[3]}, vw, l16, so, SC1);
                bst4((v4u){xr[4], xr[5], xr[6], xr[7]}, vw, l16 + 1024, so, SC1);
                l8 = 2 * l4;
                bst2((v2u){xr[8], asu(halves_sum(s1, s2))}, vw, l8 + 2048, so, SC1);
                bst4(z4, vw, azc, 0, SC1);
            }
            FENCE();
            float cs[2];
#pragma unroll
            for (int hh = 0; hh < 2; ++hh) {
                asm volatile("" : "+v"(d[hh][0]), "+v"(d[hh][1]));
                u32 w[12];
#pragma unroll
                for (int j = 0; j < 12; ++j) w[j] = pk(d[hh][j >> 3][2 * (j & 7)], d[hh][j >> 3][2 * (j & 7) + 1]);
                bst4((v4u){w[0], w[1], w[2], w[3]}, vw, d0_ + 64 * hh, 0, SC1);
                bst4((v4u){w[4], w[5], w[6], w[7]}, vw, d1_ + 64 * hh, 0, SC1);
                bst4((v4u){w[8], w[9], w[10], w[11]}, vw, d2_ + 64 * hh, 0, SC1);
                cs[hh] = 0.f;
#pragma unroll
                for (int j = 0; j < 12; ++j) cs[hh] = dot2(w[j], one2, cs[hh]);
                FENCE();
            }
            bst1(asu(halves_sum(cs[0], cs[1])), vw, cso, 0, SC1);
        }
        v4u hd[3]; v2u he[3], hz;
        {
            const char* gp = sh + umin(ch0, 573) * 4096;
            const u32 o1 = tid * 16, o2 = (o1 + 16) & 4095;
            gld2(hz, umin(lane >> 3, 2) * 4096, gp);
#pragma unroll
            for (int i = 0; i < 3; ++i) { gld4<0>(hd[i], o1, gp + i * 4096); gld2(he[i], o2, gp + i * 4096); }
        }
        STAMP(29);
        asm volatile("s_waitcnt vmcnt(7)\n\ts_barrier" ::: "memory");
        OPQS(ep);
        ep += 1;
        const v2u epv = {(u32)ep, (u32)(ep >> 32)};
        if (tid == 0) bst2(epv, vw, 0, W_F + (rg * 16 + p) * 128, SC1);
        STAMP(1);
        {
            const u32 e = (lane >> 4) & 1, kg = lane >> 4;
#pragma unroll
            for (int j = 0; j < 3; ++j) {
                const u32 pi = 16 * (wave + 4 * j) + c, hh = pi >= 86, ip = pi - 86 * hh, q = ip - 72, s = 2 * (ip - 84) + e;
                u32 v = vsel(ip < 64, (ip & 15) * 80 + (ip >> 4) * 8 + 32 * e, (2 * ip + e) * 80 + 64 - 128 * 80);
                v = vsel(ip < 72, v, (16 + (q >> 2)) * 80 + (q & 3) * 8 + 32 * e);
                v = vsel(ip < 84, v, s * 80 + 16 * 80 + 64);
                kvw[j] = vsel(lane >= 32 || pi >= 172 || s == 3, L_EX + l8, v + hh * 1520 + L_KVA);
                OPQV(kvw[j]);
            }
            const bool dt = wave & 1;
            kvA = dt ? vsel(c < 3, 1152 + (c * 4 + kg) * 16, OOB) : l16;
            kvB = vsel(lane < (dt ? 4 : 16), l8 + (dt ? 1344 : 1024), OOB);
            l16B = l16 + XS; l8B = l8 + XS;
            const u32 sc = (lane * 57u) >> 10, r = lane - 18 * sc, hh = r >= 9, pr = r - 9 * hh;
            cpo = vsel(lane < 54, sc * (192 - 144) + l8, OOB);
            csd = vsel(lane < 54, hh * 224 + vsel(pr < 8, sc * 64 + pr * 8, 192 + sc * 8) + L_CS, L_EX + 2048 + l8);
            pin2(kvA, kvB); pin2(l16B, l8B); pin2(cpo, csd); pin(l8);
        }
        FENCE();
        if (wave) waitv<0>(); else waitv<1>();
        STAMP(51);
        if (conv) {
            u32 o = L_Q + 32 + tid * 16;
            OPQV(o);
#pragma unroll
            for (int i = 0; i < 3; ++i) {
                asm volatile("" : "+v"(hd[i]), "+v"(he[i]));
                const u32 d0 = hd[i].x, d1 = hd[i].y, d2 = hd[i].z, d3 = hd[i].w, d4 = he[i].x, d5 = he[i].y;
                const u32 a0 = __builtin_amdgcn_alignbit(d1, d0, 16), a1 = __builtin_amdgcn_alignbit(d2, d1, 16), a2 = __builtin_amdgcn_alignbit(d3, d2, 16),
                          a3 = __builtin_amdgcn_alignbit(d4, d3, 16), a4 = __builtin_amdgcn_alignbit(d5, d4, 16);
                *(LAS v4u*)(o + i * CVS) = hd[i];
                *(LAS v4u*)(o + i * CVS + HCS) = (v4u){a0, a1, a2, a3};
                *(LAS v4u*)(o + i * CVS + 2 * HCS) = (v4u){d1, d2, d3, d4};
                *(LAS v4u*)(o + i * CVS + 3 * HCS) = (v4u){a1, a2, a3, a4};
            }
            asm volatile("" : "+v"(hz));
            const u32 k = (lane >> 1) & 3;
            const u64 t = (lane & 1) && k ? (((u64)hz.y << 32) | hz.x) << (64 - 16 * k) : 0;
            if (tid < 24) *(LAS v4u*)(L_Q + (lane >> 3) * CVS + k * HCS + (lane & 1) * 16) = (v4u){0, 0, (u32)t, (u32)(t >> 32)};
        }
    }
    v2u ls[16];
    {
        u32 D2[3];
#pragma unroll
        for (int i = 0; i < 3; ++i) { const u32 cc = umin(ch0, 573) + i, t = ((const __attribute__((address_space(4))) u32*)(uintptr_t)sD)[cc >> 1]; D2[i] = (cc & 1 ? t >> 16 : t & 0xffffu) * 0x10001u; OPQS(D2[i]); }
        const int bq = lane & 15, g = lane >> 4, cc = (bq + 1) & 3;
        u32 aa = L_Q + cc * HCS + 2 * (bq - 8 * g + 24 - cc), ba = L_Q + 4 * HCS + 480 + 2 * (16 * bq + 8 * g - 16) - 7 * 64;
        u32 yo = (bq >> 3) * YB + (bq & 7) * 32 + 8 * g, yp = (bq >> 3) * YB + (bq & 7) * 32 + 16 * (g >> 1) + (g & 1) * (2 * YB);
        v4u dm;
        {
            const u32 e = 7 - (bq & 7), hm = g == 2 + (bq >> 3) ? ((e & 1) ? 0xffff0000u : 0xffffu) : 0u;
#pragma unroll
            for (int i = 0; i < 4; ++i) dm[i] = (e >> 1) == i ? hm : 0u;
        }
        if (wave < 3) { aa += wave * CVS; ba += wave * CVS; }
        pin2(aa, ba); pin2(yo, yp); pin(dm);
        const u32 xb = W_XF0 + (rg * 4 + wave) * 16 * XS;
        u32 io = umin(22 * wave, 65) * 1024, ig = W_IMG + p * IMG + io, xo = xb + (p & 14) * XS, uo = W_UT + ch0 * 4096 + wave * 1024, co = W_CSP + p * CSB + wave * (4 * 576);
        u32 fo = W_F + (wave ? rg * 16 + bq : (lane >> 2) * 16 + 4 * Pg + (lane & 3)) * 128;
        OPQS(io); OPQS(ig); OPQS(xo); OPQS(uo); OPQS(co); OPQV(fo);
        FENCE();
        if (wave < 2) {
#pragma unroll 1
            for (int sp = 0; sp < SPIN; ++sp) {
                asm volatile("" ::: "memory");
                const v2u f = bld2(vw, fo, 0, SC1);
                if (__builtin_amdgcn_ballot_w64(f.x != (u32)ep || f.y != (u32)(ep >> 32)) == 0) break;
                __builtin_amdgcn_s_sleep(1);
            }
        }
        barrier();
        STAMP(2);
        v2u cp[4];
        auto site = LAM(N_) {
            constexpr int n = N_.v;
            if constexpr (n < 28) {
                if constexpr (n % 5 == 0) { if constexpr (n) { io += 4096; ig += 4096; OPQS(io); OPQS(ig); } setm0(io); if constexpr (n < NPRE) nop2(); }
                else dmai<(n % 5 - 1) * 1024>(vw, l16, ig);
            } else if constexpr (n < 84) {
                constexpr int i = 2 * ((n - 28) / 7), k = (n - 28) % 7;
                if constexpr (k == 0) ld4a<0, 8 * i>(l16, vw, xo);
                if constexpr (k == 1) ld4a<1024, 8 * i + 4>(l16, vw, xo);
                if constexpr (k == 2) ld2<2048>(ls[i], l8, vw, xo);
                if constexpr (k == 3) ld4a<0, 8 * i + 8>(l16B, vw, xo);
                if constexpr (k == 4) ld4a<1024, 8 * i + 12>(l16B, vw, xo);
                if constexpr (k == 5) ld2<2048>(ls[i + 1], l8B, vw, xo);
                if constexpr (k == 6 && i < 14) { xo += 2 * XS; xo = xo == xb + 16 * XS ? xb : xo; OPQS(xo); }
            } else ld2<(n - 84) * 576>(cp[n - 84], cpo, vw, co);
        };
        if (conv) {
#pragma unroll
            for (int i = 0; i < 3; ++i) dma_ws(vw, l16, uo + i * 4096, L_Q + i * CVS + 4 * HCS + 480 + wave * 1024);
            sfor<0, NPRE>(site);
            asm volatile("s_waitcnt vmcnt(%0)\n\ts_barrier" ::"n"(NPRE - (NPRE + 4) / 5) : "memory");
            STAMP(3);
            if (wave < 3) {
                f4 acc[8];
                v4u A[2][8], B[2][8];
                sfor<0, 24>(LAM(N_) { conv_read<8, N_.v, 0>(aa, ba, A[0], B[0]); });
                const u32 dd = wave == 0 ? D2[0] : wave == 1 ? D2[1] : D2[2];
                v4u ad = {dm.x & dd, dm.y & dd, dm.z & dd, dm.w & dd};
                pin(ad);
                FENCE();
                sfor<0, 5>(LAM(J_) { constexpr int j = J_.v ? J_.v + 3 : 0; mm16<true>(acc[j], ad, B[0][j]); });
                FENCE();
                sfor<0, 8 * 27>(LAM(Q_) {
                    constexpr int r = Q_.v / 27, n = Q_.v % 27, i = conv_ij(8, 0xf1, n) >> 3, j = conv_ij(8, 0xf1, n) & 7;
                    if constexpr (r && n == 0) waitl<9>();
                    if constexpr (r && n == 6) waitl<(r < 7 ? 6 : 0)>();
                    mm16<false>(acc[j], A[r & 1][i], B[r & 1][j - i]);
                    if constexpr (r < 7 && n < 24) conv_read<8, n, r + 1>(aa, ba, A[(r + 1) & 1], B[(r + 1) & 1]);
                    sfor<site_at(Q_.v), site_at(Q_.v + 1)>(site);
                    FENCE();
                });
                asm volatile("s_nop 7" : "+v"(acc[0]), "+v"(acc[4]), "+v"(acc[5]), "+v"(acc[6]), "+v"(acc[7]));
                const u32 so = W_Y + (ch0 + wave) * 256;
                const v2u y0 = y_val(acc[0]);
                const v4u y4 = y_pair(y_val(acc[4]), y_val(acc[5])), y6 = y_pair(y_val(acc[6]), y_val(acc[7]));
                asm volatile("s_waitcnt vmcnt(0)" ::"v"(y0), "v"(y4), "v"(y6) : "memory");
                bst2(y0, vw, yo, so, SC1);
                bst4(y4, vw, yp, so + 8 * YB, SC1);
                bst4(y6, vw, yp, so + 12 * YB, SC1);
            } else {
                f4 acc[2][4];
                v4u A[2][4], B[2][4], ad;
                v2u y1; v4u y2;
                sfor<0, 12>(LAM(N_) { conv_read<4, N_.v, 0>(aa, ba, A[0], B[0]); });
                FENCE();
                sfor<0, 3 * 8 * 9>(LAM(Q_) {
                    constexpr int cn = Q_.v / 72, r = Q_.v % 72 / 9, n = Q_.v % 9, i = conv_ij(4, 0xe, n) >> 3, j = conv_ij(4, 0xe, n) & 7;
                    constexpr int nc = r < 7 ? cn : cn + 1, nr = (r + 1) & 7;
                    if constexpr (r == 0 && n == 0) {
                        ad = (v4u){dm.x & D2[cn], dm.y & D2[cn], dm.z & D2[cn], dm.w & D2[cn]};
                        pin(ad);
                        waitl<0>();
                        nop2();
                        FENCE();
                        sfor<1, 4>(LAM(J_) { mm16<true>(acc[cn & 1][J_.v], ad, B[0][J_.v]); });
                    } else {
                        if constexpr (n == 0) waitl<3>();
                        if constexpr (n == 7) waitl<(nc < 3 ? 10 : 0)>();
                    }
                    mm16<false>(acc[cn & 1][j], A[r & 1][i], B[r & 1][j - i]);
                    if constexpr (nc < 3) {
                        if constexpr (n < 3) { conv_read<4, 2 * n, nr>(aa + nc * CVS, ba + nc * CVS, A[nr & 1], B[nr & 1]); conv_read<4, 2 * n + 1, nr>(aa + nc * CVS, ba + nc * CVS, A[nr & 1], B[nr & 1]); }
                        else conv_read<4, n + 3, nr>(aa + nc * CVS, ba + nc * CVS, A[nr & 1], B[nr & 1]);
                    }
                    if constexpr (cn > 0 && r == 1) {
                        const u32 so = W_Y + (ch0 + cn - 1) * 256;
                        if constexpr (n == 3) { pin(acc[(cn - 1) & 1][1]); y1 = y_val(acc[(cn - 1) & 1][1]); pin(y1); }
                        if constexpr (n == 4) bst2(y1, vw, yo, so + 2 * YB, SC1);
                        if constexpr (n == 5) { pin(acc[(cn - 1) & 1][2]); y1 = y_val(acc[(cn - 1) & 1][2]); pin(y1); }
                        if constexpr (n == 6) { pin(acc[(cn - 1) & 1][3]); y2 = y_pair(y1, y_val(acc[(cn - 1) & 1][3])); pin(y2); }
                        if constexpr (n == 7) bst4(y2, vw, yp, so + 4 * YB, SC1);
                    }
                    sfor<site_at(Q_.v), site_at(Q_.v + 1)>(site);
                    FENCE();
                });
                asm volatile("s_nop 7" : "+v"(acc[0][1]), "+v"(acc[0][2]), "+v"(acc[0][3]));
                const u32 so = W_Y + (ch0 + 2) * 256;
                y1 = y_val(acc[0][1]); y2 = y_pair(y_val(acc[0][2]), y_val(acc[0][3]));
                asm volatile("s_waitcnt vmcnt(0)" ::"v"(y1), "v"(y2) : "memory");
                bst2(y1, vw, yo, so + 2 * YB, SC1);
                bst4(y2, vw, yp, so + 4 * YB, SC1);
            }
        } else {
            sfor<0, NSITE>(LAM(N_) { site(N_); if constexpr (N_.v < 28 && N_.v % 5 == 0 && N_.v >= NPRE) nop2(); });
            waitv<0>();
        }
        STAMP(14);
        asm volatile("" : "+v"(cp[0]), "+v"(cp[1]), "+v"(cp[2]), "+v"(cp[3]));
        *(LAS f2*)(L_EX + wave * 512 + l8) = (f2){(asf(cp[0].x) + asf(cp[1].x)) + (asf(cp[2].x) + asf(cp[3].x)), (asf(cp[0].y) + asf(cp[1].y)) + (asf(cp[2].y) + asf(cp[3].y))};
        barrier();
    }
    u32 yv[9];
    v4u wv[17];
    STAMP(15);

    sfor<0, 2>(LAM(L_) {
        constexpr int L = L_.v;
        int ln = lane;
        OPQV(ln); OPQV(l16); OPQV(l4);
        const int tl = ln & 31, h = ln >> 5, row = ln & 15, kg = ln >> 4, gq = row >> 2, rq = row & 3;
        const bool qa = (gq & 1) == (kg & 1);
        u32 aV_ = L_V + l16, aQ_ = L_Q + l16;
        u32 aKV = L_QT + vsel(qa, (rq >> 1) * 128 + (gq >> 1) * 64 + (rq & 1) * 32 + (kg >> 1) * 16, 400 - 16 * (kg >> 1));
        u32 aQq, aCS, aS;
        OPQV(aV_); OPQV(aKV); OPQV(aQ_);
        u32 nretry = 0;
        const u32 wb = W_WOT + Pg * WOG + wave * 36864, wlb = W_WOT + Pg * WOG + 4 * 36864 + (wave >> 1) * 9216;
        u32 wso = wb;
        auto wsite = LAM(N_) {
            constexpr int n = N_.v, k = (n < 36 ? n : n - 36) % 4;
            if constexpr (L == 0 && n < 45) {
                if constexpr (n == 36) wso = wlb; else if constexpr (n && k == 0) wso += 4096;
                if constexpr (k == 0) OPQS(wso);
                if constexpr (n < 28) ld4a<k * 1024, 144 + 4 * n>(l16, vw, wso);
                else ld4o<k * 1024>(wv[n - 28], l16, vw, wso);
            }
        };
        f16v aK; f4 qKV;
        float mean, al, rstd;
        v4u rV[4], cK[5];
        {
            const u32 xb = W_XF1 + (rg * 4 + wave) * 16 * XS;
            u32 xo = xb + (p & 14) * XS;
            OPQS(xo);
            auto xsite = LAM(I_) {
                constexpr int i = I_.v;
                if constexpr (L == 1 && i < 16) {
                    ld4a<0, 8 * i>(i & 1 ? l16B : l16, vw, xo);
                    ld4a<1024, 8 * i + 4>(i & 1 ? l16B : l16, vw, xo);
                    ld2<2048>(ls[i], i & 1 ? l8B : l8, vw, xo);
                    if constexpr ((i & 1) && i < 15) { xo += 2 * XS; xo = xo == xb + 16 * XS ? xb : xo; OPQS(xo); }
                }
            };
            constexpr int UP = 5;
            if constexpr (L == 1) { FENCE(); waitv<0>(); STAMP(25); }
            sfor<0, UP>(xsite);
            v4u r0[3], r1[3], ones = {~0u, ~0u, ~0u, ~0u};
            f2 cq[4];
            u32 cm, cs_;
            float tsum;
#pragma unroll
            for (int s = 0; s < 3; ++s) { r0[s] = lds4(l16 + s * 1024); r1[s] = lds4(aKV + s * QTS); }
            if constexpr (L == 0) pin(ones);
            FENCE();
            sfor<0, 36>(LAM(SI) {
                constexpr int n = SI.v, s = kstep(n), s3 = kstep(n + 3), i = s >> 1;
                if constexpr (s < 32 && !(s & 1)) {
                    if constexpr (L == 1) {
                        constexpr int issued = UP + i < 16 ? UP + i : 16;
                        waitp<3 * (issued - i - 1)>(ls[i]);
                        if (__builtin_expect(bad(mx3(rda<8 * i>(), rda<8 * i + 4>(), ls[i].x)), 0)) {
#pragma unroll 1
                            for (int sp = 0; sp < SPIN; ++sp) {
                                __builtin_amdgcn_s_sleep(2);
                                sfor<i, issued>(LAM(J_) {
                                    constexpr int j = J_.v;
                                    const u32 so = xb + (((p & 14) + j) & 15) * XS;
                                    ld4a<0, 8 * j>(l16, vw, so); ld4a<1024, 8 * j + 4>(l16, vw, so); ld2<2048>(ls[j], l8, vw, so);
                                });
                                waitp<0>(ls[i]);
                                ++nretry;
                                if (!bad(mx3(rda<8 * i>(), rda<8 * i + 4>(), ls[i].x))) break;
                            }
                        }
                    } else pin(ls[i]);
                    FENCE();
                }
                waitl<(n < 34 ? 4 : 6)>();
                mm32b<n == 0, 4 * s>(aK, r0[n % 3]);
                if constexpr (n + 3 < 36) r0[n % 3] = lds4(l16 + s3 * 1024); else rV[n - 33] = lds4(aV_ + (n - 33) * 1024);
                if constexpr (s < 32 && !(s & 1)) { xsite(IC<UP + i>{}); wra<128 + i>(ls[i].x); tsum = i ? tsum + asf(ls[i].y) : asf(ls[i].y); pin(tsum); }
                if constexpr (n == 1) { aQq = L_QT + vsel(qa && rq < 2, 256 + (gq >> 1) * 64 + (rq & 1) * 32 + (kg >> 1) * 16, 384); pin(aQq); }
                if constexpr (n == 3) { aCS = L_CS + h * 224; aS = L_STG + (wave * 32 + tl) * STGS + 72 * h; pin2(aCS, aS); }
                if constexpr (L == 0) {
                    constexpr int k = n >> 1;
                    if constexpr (n & 1) {
                        if constexpr (k == 0 || k == 3) { cs_ = W_O + ((k ? 1024 : 0) + (rg * 4 + wave) * 16 + p) * OS; OPQS(cs_); }
                        if constexpr (k == 6) { cs_ = W_XF1 + ((rg * 4 + tw) * 16 + pw) * XS; OPQS(cs_); }
                        if constexpr (k == 9) { cs_ = W_KV + (p * 16 + rg) * 2816 + wave * 704; OPQS(cs_); }
                        if constexpr (k == 10) { cs_ += 256 * 2816; OPQS(cs_); }
                        if constexpr (k == 11) { cs_ = W_ST + ((rg * 4 + tw) * 16 + pw) * 256; OPQS(cs_); }
                        if constexpr (k < 9 && k % 3 < 2) st4<(k % 3) * 1024>(ones, l16, vw, cs_);
                        else if constexpr (k < 9) st4<2048>(ones, cm, vw, cs_);
                        else if constexpr (k < 12) st4<0>(ones, cm, vw, cs_);
                        else wsite(IC<k - 12>{});
                    } else {
                        if constexpr (k == 2) { cm = vsel(ln < 16, l16, OOB); pin(cm); }
                        if constexpr (k == 8) { cm = vsel(ln < 32, l16, OOB); pin(cm); }
                        if constexpr (k == 9) { cm = vsel(ln < 44, l16, OOB); pin(cm); }
                        if constexpr (k == 11) { cm = vsel(ln < 32, row * 16 + (kg & 1) * (1024 * 256), OOB); pin(cm); }
                        if constexpr (k == 13) { cq[0].x = (cq[0].x + cq[1].x) + (cq[2].x + cq[3].x); cq[0].y = (cq[0].y + cq[1].y) + (cq[2].y + cq[3].y); pin(cq[0]); }
                    }
                }
                if constexpr (n == 34) {
                    auto r = __builtin_amdgcn_permlane32_swap(asu(tsum), asu(tsum), false, false);
                    mean = asf(r[0]) * (1.f / 576.f);
                    const float var = __builtin_fmaxf(asf(r[1]) * (1.f / 576.f) - mean * mean, 0.f);
                    rstd = __builtin_amdgcn_rsqf(var + 1e-5f);
                    al = rstd * 1.4426950408889634f;
                    pin2(mean, al); pin(rstd);
                }
                FENCE();
                mm16b<n == 0, 4 * s>(qKV, r1[n % 3]);
                if constexpr (n + 3 < 36) r1[n % 3] = lds4(aKV + s3 * QTS); else cK[n - 33] = lds4(aCS + (n - 33) * 16);
                if constexpr (n == 33) cK[3] = lds4(aCS + 48);
                if constexpr (n == 34) cK[4] = lds4(aCS + 192);
                if constexpr (n == 35) rV[3] = lds4(aV_ + 3 * 1024);
                FENCE();
                if constexpr (L == 0 && n >= 20 && n < 24) { cq[n - 20] = *(const LAS f2*)(l8 + (L_EX + (n - 20) * 512)); FENCE(); }
                if constexpr (L == 0 && n == 27) { *(LAS f2*)(csd) = cq[0]; FENCE(); }
            });
        }
        STAMP(4 + 12 * L);
        if constexpr (L == 1) STAMPV(56, nretry);
        v4u rQ[4], rq4[3];
        u32 pa, pb, kso;
        {
            f16v aV;
            float t[18], e1[18], e2[18], mx, mn, c1, c2, s1, s2;
            v4u cV[4];
            u32 qo = L_Q + wave * 9216, qg = W_IMG + p * IMG + qo;
            sfor<0, 36>(LAM(SI) {
                constexpr int s = SI.v;
                if constexpr (s >= 2 && s < 34 && !(s & 1)) waitl<2>();
                mm32b<s == 0, 4 * s>(aV, rV[s & 3]);
                if constexpr (s == 2) { pin(aK); pin(qKV); }
                if constexpr (s + 4 < 36) rV[s & 3] = lds4(aV_ + (s + 4) * 1024); else cV[s - 32] = lds4(aCS + 64 + (s - 32) * 16);
                if constexpr (s < 18) {
                    if constexpr (s == 0 || s == 8 || s == 16) { if constexpr (s) { qo += 4096; qg += 4096; } OPQS(qo); OPQS(qg); setm0(qo); }
                    if constexpr (s & 1) dmai<((s >> 1) & 3) * 1024>(vw, l16, qg);
                } else if constexpr (s & 1) wsite(IC<6 + (s - 18) / 2>{});
                if constexpr (s == 1) {
                    const u32 r0 = L_STG + (4 * (4 * (kg & 1) + gq) + (kg >> 1)) * STGS + 8 * rq;
                    pa = r0 + 72 * (wave >> 1); pb = r0 + (144 + 40 * (wave >> 1) + 32 * (wave & 1));
                    kso = W_KV + ((L * 16 + p) * 16 + rg) * 2816 + (wave >> 1) * 1376;
                    pin2(pa, pb); OPQS(kso);
                }
                if constexpr (s >= 2 && s < 5) {
#pragma unroll
                    for (int i = 6 * (s - 2); i < 6 * (s - 2) + 6; ++i) { const u32 cv = cK[i >> 2][i & 3]; t[i] = __builtin_fmaf(-mean, asf(cv), i < 16 ? aK[i] : qKV[i - 16]); }
                }
                if constexpr (s == 5) { mx = __builtin_fmaxf(__builtin_fmaxf(t[0], t[1]), t[2]); mn = __builtin_fminf(__builtin_fminf(t[0], t[1]), t[2]);
#pragma unroll
                    for (int i = 3; i < 7; i += 2) { mx = __builtin_fmaxf(__builtin_fmaxf(mx, t[i]), t[i + 1]); mn = __builtin_fminf(__builtin_fminf(mn, t[i]), t[i + 1]); } }
                if constexpr (s == 6) {
#pragma unroll
                    for (int i = 7; i < 13; i += 2) { mx = __builtin_fmaxf(__builtin_fmaxf(mx, t[i]), t[i + 1]); mn = __builtin_fminf(__builtin_fminf(mn, t[i]), t[i + 1]); } }
                if constexpr (s == 7) {
#pragma unroll
                    for (int i = 13; i < 17; i += 2) { mx = __builtin_fmaxf(__builtin_fmaxf(mx, t[i]), t[i + 1]); mn = __builtin_fminf(__builtin_fminf(mn, t[i]), t[i + 1]); }
                    mx = __builtin_fmaxf(mx, t[17]); mn = __builtin_fminf(mn, t[17]); c1 = -al * mx; c2 = al * mn; }
                if constexpr (s >= 8 && s < 32) {
#pragma unroll
                    for (int q = (3 * (s - 8)) / 2; q < (3 * (s - 7)) / 2; ++q) {
                        const int i = q >> 1;
                        if (q & 1) { e2[i] = __builtin_amdgcn_exp2f(__builtin_fmaf(t[i], -al, c2)); s2 = i ? s2 + e2[i] : e2[i]; }
                        else { e1[i] = __builtin_amdgcn_exp2f(__builtin_fmaf(t[i], al, c1)); s1 = i ? s1 + e1[i] : e1[i]; }
                    }
                }
                if constexpr (s == 32) { s1 = __builtin_amdgcn_rcpf(s1); s2 = __builtin_amdgcn_rcpf(s2); }
                if constexpr (s >= 33) {
                    constexpr int i = s - 33;
                    *(LAS v2u*)(aS + 8 * i) = (v2u){pk(e1[4 * i] * s1, e1[4 * i + 1] * s1), pk(e1[4 * i + 2] * s1, e1[4 * i + 3] * s1)};
                }
                FENCE();
            });
            *(LAS v2u*)(aS + 24) = (v2u){pk(e1[12] * s1, e1[13] * s1), pk(e1[14] * s1, e1[15] * s1)};
            *(LAS v2u*)(aS + 32) = (v2u){pk(e1[16] * s1, e1[17] * s1), pk(e2[0] * s2, e2[1] * s2)};
#pragma unroll
            for (int i = 0; i < 4; ++i) *(LAS v2u*)(aS + 40 + 8 * i) = (v2u){pk(e2[4 * i + 2] * s2, e2[4 * i + 3] * s2), pk(e2[4 * i + 4] * s2, e2[4 * i + 5] * s2)};
            FENCE();
            pin(aV);
            const u32 aSv = aS + 144 - 32 * h;
            const float mr = -mean * rstd;
#pragma unroll
            for (int i = 0; i < 4; ++i) {
                const u32 q0 = cV[i].x, q1 = cV[i].y, q2 = cV[i].z, q3 = cV[i].w;
                *(LAS v2u*)(aSv + 8 * i) = (v2u){pk(__builtin_fmaf(mr, asf(q0), aV[4 * i] * rstd), __builtin_fmaf(mr, asf(q1), aV[4 * i + 1] * rstd)),
                                                 pk(__builtin_fmaf(mr, asf(q2), aV[4 * i + 2] * rstd), __builtin_fmaf(mr, asf(q3), aV[4 * i + 3] * rstd))};
            }
            const u32 q2 = cK[4].z, q3 = cK[4].w;
            *(LAS v2u*)(aSv + 32) = (v2u){pk(__builtin_fmaf(mr, asf(q2), qKV[2] * rstd), __builtin_fmaf(mr, asf(q3), qKV[3] * rstd)), 0x3f80u};
        }
        waitbar<(L ? 0 : 9)>();
        STAMP(5 + 12 * L);
        {
            f4 c[3];
            v4u B[4], A[4][3];
            auto rd = LAM(N_) {
                constexpr int ks = N_.v >> 2, f = N_.v & 3;
                if constexpr (f == 0) B[ks] = tr2(pb + ks * 32 * STGS, pb + (ks * 32 + 2) * STGS);
                else A[ks][f - 1] = tr2(pa + ks * 32 * STGS + 32 * (f - 1), pa + (ks * 32 + 2) * STGS + 32 * (f - 1));
            };
            sfor<0, 7>(LAM(N_) { rd(N_); FENCE(); });
            if constexpr (L == 0) { if (tid == 0) bst2((v2u){(u32)ep, (u32)(ep >> 32)}, vw, 0, W_F2 + (rg * 16 + p) * 128, SC1); }
            FENCE();
            sfor<0, 12>(LAM(I_) {
                constexpr int ks = I_.v / 3, ft = I_.v % 3;
                mm16<ks == 0>(c[ft], A[ks][ft], B[ks]);
                if constexpr (I_.v < 9) rd(IC<7 + I_.v>{});
                else { rQ[I_.v - 9] = lds4(aQ_ + (I_.v - 9) * 1024); rq4[I_.v - 9] = lds4(aQq + (I_.v - 9) * QTS); }
                FENCE();
            });
            rQ[3] = lds4(aQ_ + 3 * 1024);
            FENCE();
            pad3<5>(c[0], c[1], c[2]);
            bst4((v4u){pk(c[0][0], c[0][1]), pk(c[0][2], c[0][3]), pk(c[1][0], c[1][1]), pk(c[1][2], c[1][3])}, vw, kvA, kso, SC1);
            bst2((v2u){pk(c[2][0], c[2][1]), pk(c[2][2], c[2][3])}, vw, kvB, kso, SC1);
        }
        FENCE();
        STAMP(6 + 12 * L);
        v4u pq[5], kp[12], sel;
        u32 kvo, kv3, aF, aQ4, f2o, oso;
        u32 kb[4];
        v2u yfl;
        auto flags_ok = [&](u64 m) __attribute__((always_inline)) { return (__builtin_amdgcn_ballot_w64(yfl.x != (u32)ep || yfl.y != (u32)(ep >> 32)) & m) == 0; };
        auto flags_wait = [&](u64 m) __attribute__((always_inline)) {
            if (__builtin_expect(!flags_ok(m), 0)) {
#pragma unroll 1
                for (int sp = 0; sp < SPIN; ++sp) {
                    __builtin_amdgcn_s_sleep(1);
                    ld2<0>(yfl, f2o, vw, W_F2);
                    waitp<0>(yfl);
                    if (flags_ok(m)) break;
                }
            }
        };
        {
            f16v aQ; f4 qQ;
            v4u cQ[5];
            sfor<0, 36>(LAM(SI) {
                constexpr int s = SI.v;
                if constexpr (s >= 1 && s < 34) waitl<4>();
                mm32b<s == 0, 4 * s>(aQ, rQ[s & 3]);
                if constexpr (s + 4 < 36) rQ[s & 3] = lds4(aQ_ + (s + 4) * 1024); else cQ[s - 32] = lds4(aCS + 128 + (s - 32) * 16);
                if constexpr (s < 33 && s % 3 != 2) wsite(IC<15 + s - s / 3>{});
                if constexpr (s == 2) { kvo = kg * 2816 + wave * 256 + row * 16; kv3 = vsel(wave * 16 + row < 44, kvo + 2048, OOB); pin2(kvo, kv3); }
                if constexpr (s == 26) { for (int i = 0; i < 4; ++i) { kb[i] = W_KV + ((L * 16 + p) * 16 + 4 * i) * 2816; OPQS(kb[i]); } }
                if constexpr (s == 5 || s == 8) {
#pragma unroll
                    for (int i = (s - 5) / 3 * 2; i < (s - 5) / 3 * 2 + 2; ++i) sel[i] = row == 2 * i ? 0x3f80u : row == 2 * i + 1 ? 0x3f800000u : 0u;
                    if constexpr (s == 8) pin(sel);
                }
                if constexpr (s == 11) { const int hr = (tl >> 2) & 1, d = (tl & 3) + 4 * (tl >> 3); aF = vsel(h == hr, (hr * 19 + d) * 80 + L_KVA, L_Z); pin(aF); }
                if constexpr (s == 14) { aQ4 = vsel(kg == gq && rq < 3, ((gq >> 1) * 19 + 16 + rq) * 80 + L_KVA, L_Z); pin(aQ4); }
                if constexpr (s == 17) { oso = W_O + (L * 1024 + (rg * 4 + wave) * 16 + p) * OS; OPQS(oso); }
                if constexpr (L == 0 && s == 20) { f2o = vsel(kg == 1, rg * 16 + row, row * 16 + (kg ? pw : p)) * 128; pin(f2o); }
                if constexpr (L == 0 && s == 23) ld2<0>(yfl, f2o, vw, W_F2);
                FENCE();
                mm16b<s == 0, 4 * s>(qQ, rq4[s % 3]);
                if constexpr (s + 3 < 36) rq4[s % 3] = lds4(aQq + (s + 3) * QTS);
                if constexpr (s == 33) cQ[4] = lds4(aCS + 208);
                FENCE();
            });
            STAMP(49 + L);
            if constexpr (L == 0) { waitp<6>(yfl); flags_wait(0xffffull); }
#pragma unroll
            for (int i = 0; i < 4; ++i) {
                kp[i] = bld4(vw, kvo, kb[i], SC1);
                kp[4 + i] = bld4(vw, kvo + 1024, kb[i], SC1);
                kp[8 + i] = bld4(vw, kv3, kb[i], SC1);
            }
            FENCE();
            pin(aQ); pin(qQ);
            float t[18]; u32 ph[18];
#pragma unroll
            for (int i = 0; i < 18; ++i) { const u32 cv = cQ[i >> 2][i & 3]; t[i] = __builtin_fmaf(-mean, asf(cv), i < 16 ? aQ[i] : qQ[i - 16]); }
            hedgehog(t, al, ph);
#pragma unroll
            for (int i = 0; i < 5; ++i) { pq[i] = (v4u){ph[4 * i], ph[4 * i + 1], i < 4 ? ph[4 * i + 2] : 0u, i < 4 ? ph[4 * i + 3] : 0u}; pin(pq[i]); }
            FENCE();
            STAMP(7 + 12 * L);
            u32 m = umax(kp[0].x, kp[0].z);
#pragma unroll
            for (int i = 1; i < 12; ++i) m = mx3(m, kp[i].x, kp[i].z);
            nretry = 0;
            if (__builtin_expect(bad(m), 0)) {
#pragma unroll 1
                for (int sp = 0; sp < SPIN; ++sp) {
                    __builtin_amdgcn_s_sleep(2);
                    asm volatile("" ::: "memory");
                    m = 0;
                    ++nretry;
#pragma unroll
                    for (int i = 0; i < 4; ++i) {
                        kp[i] = bld4(vw, kvo, kb[i], SC1);
                        kp[4 + i] = bld4(vw, kvo + 1024, kb[i], SC1);
                        kp[8 + i] = bld4(vw, kv3, kb[i], SC1);
                    }
#pragma unroll
                    for (int i = 0; i < 12; ++i) m = mx3(m, kp[i].x, kp[i].z);
                    if (!bad(m)) break;
                }
            }
            STAMPV(57 + L, nretry);
            f4 a[3];
            sfor<0, 12>(LAM(I_) { constexpr int i = I_.v / 3, j = I_.v % 3; mm16<i == 0>(a[j], sel, kp[4 * j + i]); });
            pad3<7>(a[0], a[1], a[2]);
#pragma unroll
            for (int j = 0; j < 3; ++j) *(LAS v2u*)(kvw[j]) = (v2u){pk(a[j][0], a[j][1]), pk(a[j][2], a[j][3])};
            barrier();
            STAMP(8 + 12 * L);
        }
        {
            v4u A[5], Aq[5];
#pragma unroll
            for (int c = 0; c < 5; ++c) Aq[c] = lds4(aQ4 + 16 * c);
#pragma unroll
            for (int c = 0; c < 5; ++c) A[c] = lds4(aF + 16 * c);
            FENCE();
            f16v o; f4 oq;
            float rd; u32 ol;
            sfor<0, 5>(LAM(C_) { mm16<C_.v == 0>(oq, Aq[C_.v], pq[C_.v]); });
            sfor<0, 5>(LAM(C_) {
                mm32<C_.v == 0>(o, A[C_.v], pq[C_.v]);
                if constexpr (C_.v == 3) { pad<4>(oq); rd = __builtin_amdgcn_rcpf(oq[2] + 1e-6f); pin(rd); }
                if constexpr (C_.v == 4) { ol = pk(oq[0] * rd, oq[1] * rd); pin(ol); }
                FENCE();
            });
            pad<8>(o);
            bst4((v4u){pk(o[0] * rd, o[1] * rd), pk(o[2] * rd, o[3] * rd), pk(o[4] * rd, o[5] * rd), pk(o[6] * rd, o[7] * rd)}, vw, l16, oso, SC1);
            bst4((v4u){pk(o[8] * rd, o[9] * rd), pk(o[10] * rd, o[11] * rd), pk(o[12] * rd, o[13] * rd), pk(o[14] * rd, o[15] * rd)}, vw, l16 + 1024, oso, SC1);
            bst1(ol, vw, l4 + 2048, oso, SC1);
        }
        FENCE();
        STAMP(9 + 12 * L);
        u32 yf[18], aB, ae, ao;
        f16v aW; float xl0, xl1;
        if constexpr (L == 0) {
            sfor<37, 45>(wsite);
            flags_wait(~0ull);
            u32 so = W_Y + rg * YB + 36 * pw * 256 + tw * 64, yo = h * 18 * 256 + tl * 2;
            OPQS(so); OPQV(yo);
            sfor<0, 18>(LAM(M_) { constexpr int m = M_.v; yf[m] = 0; if constexpr (m == 16) so += 4096; ldh<(m & 15) * 256>(yf[m], yo, vw, so); });
        } else {
#pragma unroll
            for (int i = 0; i < 9; ++i) { yf[2 * i] = yv[i] << 16; yf[2 * i + 1] = yv[i] & 0xffff0000u; pin2(yf[2 * i], yf[2 * i + 1]); }
        }
#pragma unroll
        for (int i = 0; i < 8; ++i) { aW[2 * i] = bf_lo(xr[i]); aW[2 * i + 1] = bf_hi(xr[i]); }
        xl0 = bf_lo(xr[8]); xl1 = bf_hi(xr[8]);
        aB = L_Q + ((wave >> 1) * 18 + (kg & 1)) * 1024 + ((kg >> 1) * 32 + 16 * (wave & 1) + row) * 16;
        ae = L_EX + (kg & 1) * 1024 + ((2 * (wave >> 1) + h) * 16 + row) * 16 + 8 * (wave & 1);
        ao = aQ_ + wave * 8192;
        pin(aW); pin2(xl0, xl1); pin2(aB, ae); pin(ao);
        {
            u32 so[4];
#pragma unroll
            for (int i = 0; i < 4; ++i) { so[i] = W_O + (L * 1024 + (rg * 4 + tw) * 16 + 4 * wave + i) * OS; OPQS(so[i]); }
            v4u lo, og[8];
            FENCE();
            STAMP(43 + 3 * L);
            waitv<0>();
            STAMP(44 + 3 * L);
            __builtin_amdgcn_s_sleep(OSL);
            auto ldo = [&]() __attribute__((always_inline)) {
#pragma unroll
                for (int i = 0; i < 8; ++i) og[i] = bld4(vw, l16 + (i & 1) * 1024, so[i >> 1], SC1);
            };
            auto ldl = [&]() __attribute__((always_inline)) {
#pragma unroll
                for (int i = 0; i < 4; ++i) lo[i] = bld1(vw, l4 + 2048, so[i], SC1);
            };
            auto chk = [&]() __attribute__((always_inline)) {
                u32 m = mx3(umax(lo.x, lo.y), lo.z, lo.w);
#pragma unroll
                for (int i = 0; i < 8; i += 2) m = mx3(m, og[i].x, og[i + 1].x);
                return bad(m);
            };
            ldo(); ldl();
            FENCE();
            nretry = 0;
            if (__builtin_expect(chk(), 0)) {
#pragma unroll 1
                for (int sp = 0; sp < SPIN; ++sp) {
                    ++nretry;
                    if (bad(mx3(umax(lo.x, lo.y), lo.z, lo.w))) { __builtin_amdgcn_s_sleep(1); asm volatile("" ::: "memory"); ldl(); continue; }
                    asm volatile("" ::: "memory");
                    ldo();
                    if (!chk()) break;
                }
            }
            STAMPV(59 + L, nretry);
            STAMP(45 + 3 * L);
#pragma unroll
            for (int i = 0; i < 8; ++i) *(LAS v4u*)(ao + i * 1024) = og[i];
            *(LAS v4u*)(aQ_ + (32 + wave) * 1024) = lo;
            barrier();
        }
        STAMP(10 + 12 * L);
        u32 sto = W_ST + (L * 1024 + (rg * 4 + tw) * 16) * 256;
        {
            f4 qW;
            v4u rb_[4], rl[9];
            v2u e0, e1;
            float s1, s2;
#pragma unroll
            for (int i = 0; i < 4; ++i) { rl[i] = lds4(aB + i * 2048); rb_[i] = lds4(aQ_ + i * 1024); }
            FENCE();
            sfor<0, 36>(LAM(SI) {
                constexpr int s = SI.v;
                if constexpr (s < 28) mm32a<false, 144 + 4 * s>(aW, rb_[s & 3]); else mm32<false>(aW, wv[s - 28], rb_[s & 3]);
                if constexpr (s + 4 < 36) rb_[s & 3] = lds4(aQ_ + (s + 4) * 1024);
                if constexpr (s < 5) rl[4 + s] = lds4(aB + (4 + s) * 2048);
                if constexpr (s == 12) { pad<0>(qW); *(LAS f4*)(l16 + (L_EX + wave * 1024)) = qW; }
                if constexpr (s == 14) OPQS(sto);
                FENCE();
                if constexpr (s < 9) { mm16<s == 0>(qW, wv[8 + s], rl[s]); FENCE(); }
                if constexpr (s == 30) { barrier(); e0 = lds2(ae); e1 = lds2(ae + 2048); FENCE(); }
                if constexpr (s == 33) { xl0 += asf(e0.x) + asf(e1.x); xl1 += asf(e0.y) + asf(e1.y); s1 = xl0 + xl1; s2 = __builtin_fmaf(xl0, xl0, xl1 * xl1); pin2(s1, s2); FENCE(); }
            });
            STAMP(11 + 12 * L);
            pad<11>(aW);
            float s3 = aW[0], s4 = aW[0] * aW[0];
#pragma unroll
            for (int i = 1; i < 16; ++i) { if (i & 1) { s1 += aW[i]; s2 = __builtin_fmaf(aW[i], aW[i], s2); } else { s3 += aW[i]; s4 = __builtin_fmaf(aW[i], aW[i], s4); } }
            bst1(asu(halves_sum(s1 + s3, s2 + s4)), vw, l4, sto + pw * 256, SC1);
        }
        FENCE();
        {
            float t;
            u32 xso = L ? (rg * 128 + tw * 32) * 1152 + 72 * pw : W_XF1 + ((rg * 4 + tw) * 16 + pw) * XS, oo = tl * 1152 + 36 * h, one2 = 0x3f803f80u;
            OPQS(xso); OPQS(one2);
            if constexpr (L == 1) OPQV(oo);
            if constexpr (L == 0) {
#pragma unroll
                for (int i = 0; i < 9; ++i) { pin2(yf[2 * i], yf[2 * i + 1]); yv[i] = (yf[2 * i] >> 16) | yf[2 * i + 1]; pin(yv[i]); }
            }
            FENCE();
            waitv<0>();
            nretry = 0;
#pragma unroll 1
            for (int sp = 0; sp < SPIN; ++sp) {
                asm volatile("" : "+v"(l4)::"memory");
                u32 e[16];
#pragma unroll
                for (int i = 0; i < 16; ++i) e[i] = bld1(vw, l4 + i * 256, sto, SC1);
                FENCE();
                u32 m = umax(e[0], e[1]);
                t = asf(e[0]) + asf(e[1]);
#pragma unroll
                for (int i = 2; i < 16; i += 2) { m = mx3(m, e[i], e[i + 1]); t += asf(e[i]) + asf(e[i + 1]); pin2(m, t); }
                if (!bad(m)) break;
                ++nretry;
                __builtin_amdgcn_s_sleep(1);
            }
            STAMP(12 + 12 * L);
            STAMPV(61 + L, nretry);
            auto r = __builtin_amdgcn_permlane32_swap(asu(t), asu(t), false, false);
            const float m1 = asf(r[0]) * (1.f / 576.f);
            const float r1 = __builtin_amdgcn_rsqf(__builtin_fmaxf(asf(r[1]) * (1.f / 576.f) - m1 * m1, 0.f) + 1e-5f), mr = -m1 * r1;
#pragma unroll
            for (int i = 0; i < 9; ++i) {
                const float a = i < 8 ? aW[2 * i] : xl0, c = i < 8 ? aW[2 * i + 1] : xl1;
                xr[i] = pk(__builtin_fmaf(__builtin_fmaf(a, r1, mr), asf(yf[2 * i]), a), __builtin_fmaf(__builtin_fmaf(c, r1, mr), asf(yf[2 * i + 1]), c));
                if constexpr (L == 0) {
                    if (i == 3) { bst4((v4u){xr[0], xr[1], xr[2], xr[3]}, vw, l16, xso, SC1); FENCE(); }
                    if (i == 7) { bst4((v4u){xr[4], xr[5], xr[6], xr[7]}, vw, l16 + 1024, xso, SC1); FENCE(); }
                } else {
                    if (i == 3) { bst4((v4u){xr[0], xr[1], xr[2], xr[3]}, ro, oo, xso, 0); FENCE(); }
                    if (i == 7) { bst4((v4u){xr[4], xr[5], xr[6], xr[7]}, ro, oo + 16, xso, 0); FENCE(); }
                    if (i == 8) bst1(xr[8], ro, oo + 32, xso, 0);
                }
            }
            if constexpr (L == 0) {
                float s1 = 0.f, s2 = 0.f;
#pragma unroll
                for (int j = 0; j < 9; ++j) { s1 = dot2(xr[j], one2, s1); s2 = dot2(xr[j], xr[j], s2); }
                bst2((v2u){xr[8], asu(halves_sum(s1, s2))}, vw, l8 + 2048, xso, SC1);
                STAMP(13);
            } else {
                if (b == 0 && tid == 0) bst2((v2u){(u32)ep, (u32)(ep >> 32)}, vw, 0, W_E, SC1);
            }
        }
    });
    STAMP_END();
}
#if defined(__HIP_DEVICE_COMPILE__)
#pragma clang attribute pop
#endif

extern "C" void kernel_launch(void* const* d_in, const int* in_sizes, int n_in, void* d_out, int out_size, void* d_ws, size_t ws_size, hipStream_t stream) {
    (void)in_sizes; (void)n_in; (void)out_size; (void)ws_size;
    hipFuncSetAttribute((const void*)k_main, hipFuncAttributeMaxDynamicSharedMemorySize, L_END);
    const u32 f = 0x00020000u, n = 2048 * 1152;
    Args a = {{d_in[5], n, f}, {d_in[6], n, f}, {{d_in[1], 576 * 3456, f}, {d_in[3], 576 * 1152, f}}, {{d_in[2], 32 * 648, f}, {d_in[2], 0, f}}, {d_in[0], n, f}, {d_ws, W_END, f}, {d_out, n, f}, d_in[7], d_in[8], d_in[9]};
    k_main<<<256, 256, L_END, stream>>>(a);
}
```

```cpp
#include <hip/hip_runtime.h>
#include <stdint.h>
#pragma clang diagnostic ignored "-Wint-to-pointer-cast"
#pragma clang diagnostic ignored "-Wunused-value"
#pragma clang diagnostic ignored "-Winline-asm"
#if defined(__HIP_DEVICE_COMPILE__)
#pragma clang attribute push(__attribute__((target("no-packed-fp32-ops"))), apply_to = function)
#endif
typedef unsigned short u16;
typedef unsigned u32;
typedef unsigned long long u64;
typedef u32 v4u __attribute__((ext_vector_type(4)));
typedef u32 v2u __attribute__((ext_vector_type(2)));
typedef float f4 __attribute__((ext_vector_type(4)));
typedef float f2 __attribute__((ext_vector_type(2)));
typedef float f16v __attribute__((ext_vector_type(16)));
typedef __bf16 b8 __attribute__((ext_vector_type(8)));
typedef __bf16 b2 __attribute__((ext_vector_type(2)));
typedef short s4 __attribute__((ext_vector_type(4)));
#define LAS __attribute__((address_space(3)))
#define DEV __device__ __forceinline__
#define SC1 16

constexpr u32 QTS = 416, STGS = 232, CVS = 21216, HCS = 4160;
constexpr u32 L_K = 0, L_V = 36864, L_QT = 73728, L_Q = 89088, L_STG = 125952, L_KVA = 155648, L_CS = 158784, L_EX = 159232, L_END = 163328;
constexpr u32 P_KS = 0, P_VS = 10240, P_WS = 20480, P_XS = 31232, P_PH = 42496, P_LS = 55296, L_Z = L_KVA + 38 * 80;
constexpr u32 IMG = L_STG, XS = 2560, OS = 2304, YB = 576 * 256, CSB = 16 * 576;
constexpr u32 WOG = 4 * 36864 + 18432;
constexpr u32 W_IMG = 0, W_WOT = W_IMG + 16 * IMG, W_CSP = W_WOT + 4 * WOG, W_UT = W_CSP + 16 * CSB,
              W_XF0 = W_UT + 576 * 4096, W_XF1 = W_XF0 + 1024 * XS, W_O = W_XF1 + 1024 * XS, W_Y = W_O + 2 * 1024 * OS, W_KV = W_Y + 576 * 4096,
              W_ST = W_KV + 2 * 256 * 2816, W_F = W_ST + 2 * 1024 * 256, W_F2 = W_F + 256 * 128, W_E = W_F2 + 256 * 128, W_END = W_E + 128, W_DBG = W_END;
constexpr u32 OOB = 0x7ffffff0u;
constexpr int SPIN = 1 << 14;
constexpr u32 CODE = 61000;
#define STOLD
#define OSL 8
#define RSX 1
#define RSK 1
#define RSO 1
#define RSS 1
#define XSL 9
#define SSL 0

DEV float asf(u32 v) { return __builtin_bit_cast(float, v); }
DEV u32 asu(float v) { return __builtin_bit_cast(u32, v); }
DEV float bf_lo(u32 v) { return asf(v << 16); }
DEV float bf_hi(u32 v) { return asf(v & 0xffff0000u); }
DEV u32 pk(float lo, float hi) { b2 r = __builtin_convertvector((f2){lo, hi}, b2); return __builtin_bit_cast(u32, r); }
DEV float rb(float f) { return bf_lo(pk(f, f)); }
DEV float dot2(u32 a, u32 b, float c) { return __builtin_amdgcn_fdot2_f32_bf16(__builtin_bit_cast(b2, a), __builtin_bit_cast(b2, b), c, false); }
DEV u32 umin(u32 a, u32 b) { return a < b ? a : b; }
DEV u32 umax(u32 a, u32 b) { return a > b ? a : b; }
DEV u32 mx3(u32 a, u32 b, u32 c) { return umax(umax(a, b), c); }
template <bool Z> DEV void mm32(f16v& c, v4u a, v4u b) {
    if constexpr (Z) asm volatile("v_mfma_f32_32x32x16_bf16 %0, %1, %2, 0" : "=&v"(c) : "v"(a), "v"(b));
    else asm volatile("v_mfma_f32_32x32x16_bf16 %0, %1, %2, %0" : "+v"(c) : "v"(a), "v"(b));
}
template <bool Z> DEV void mm16(f4& c, v4u a, v4u b) {
    if constexpr (Z) asm volatile("v_mfma_f32_16x16x32_bf16 %0, %1, %2, 0" : "=&v"(c) : "v"(a), "v"(b));
    else asm volatile("v_mfma_f32_16x16x32_bf16 %0, %1, %2, %0" : "+v"(c) : "v"(a), "v"(b));
}
template <bool Z, int R> DEV void mm32b(f16v& c, v4u a) {
    if constexpr (Z) asm volatile("v_mfma_f32_32x32x16_bf16 %0, %1, a[%c2:%c3], 0" : "=&v"(c) : "v"(a), "n"(R), "n"(R + 3));
    else asm volatile("v_mfma_f32_32x32x16_bf16 %0, %1, a[%c2:%c3], %0" : "+v"(c) : "v"(a), "n"(R), "n"(R + 3));
}
template <bool Z, int R> DEV void mm16b(f4& c, v4u a) {
    if constexpr (Z) asm volatile("v_mfma_f32_16x16x32_bf16 %0, %1, a[%c2:%c3], 0" : "=&v"(c) : "v"(a), "n"(R), "n"(R + 3));
    else asm volatile("v_mfma_f32_16x16x32_bf16 %0, %1, a[%c2:%c3], %0" : "+v"(c) : "v"(a), "n"(R), "n"(R + 3));
}
template <bool Z, int R> DEV void mm32a(f16v& c, v4u b) {
    if constexpr (Z) asm volatile("v_mfma_f32_32x32x16_bf16 %0, a[%c2:%c3], %1, 0" : "=&v"(c) : "v"(b), "n"(R), "n"(R + 3));
    else asm volatile("v_mfma_f32_32x32x16_bf16 %0, a[%c2:%c3], %1, %0" : "+v"(c) : "v"(b), "n"(R), "n"(R + 3));
}
template <int N, class T> DEV void pad(T& c) { asm volatile("s_nop %1" : "+v"(c) : "n"(N)); }
template <class T> DEV void pin(T& c) { asm volatile("" : "+v"(c)); }
DEV void nop2() { asm volatile("s_nop 1"); }
template <int R> DEV u32 rda() { u32 v; asm volatile("v_accvgpr_read_b32 %0, a%c1" : "=v"(v) : "n"(R)); return v; }
template <int R> DEV void wra(u32 v) { asm volatile("v_accvgpr_write_b32 a%c1, %0" ::"v"(v), "n"(R)); }
__device__ void bst4(v4u, v4u, u32, u32, int) __asm("llvm.amdgcn.raw.buffer.store.v4i32");
__device__ void bst2(v2u, v4u, u32, u32, int) __asm("llvm.amdgcn.raw.buffer.store.v2i32");
__device__ void bst1(u32, v4u, u32, u32, int) __asm("llvm.amdgcn.raw.buffer.store.i32");
__device__ v4u bld4(v4u, u32, u32, int) __asm("llvm.amdgcn.raw.buffer.load.v4i32");
__device__ v2u bld2(v4u, u32, u32, int) __asm("llvm.amdgcn.raw.buffer.load.v2i32");
__device__ u32 bld1(v4u, u32, u32, int) __asm("llvm.amdgcn.raw.buffer.load.i32");
struct Buf { const void* p; u32 n, f; };
struct Args { Buf k, v, w[2], ph[2], x, ws, out; const void *sk, *sh, *sD; };
template <class T> DEV void sld(T& d, const void* ka, u32 off) {
    if constexpr (sizeof(T) == 16) asm volatile("s_load_dwordx4 %0, %1, %2" : "=s"(d) : "s"(ka), "s"(off));
    else asm volatile("s_load_dwordx2 %0, %1, %2" : "=s"(d) : "s"(ka), "s"(off));
}
template <int O, class T> DEV void sldi(T& d, const void* ka) {
    if constexpr (sizeof(T) == 16) asm volatile("s_load_dwordx4 %0, %1, %2" : "=s"(d) : "s"(ka), "n"(O));
    else asm volatile("s_load_dwordx2 %0, %1, %2" : "=s"(d) : "s"(ka), "n"(O));
}
DEV v4u lds4(u32 a) { return *(const LAS v4u*)a; }
DEV v2u lds2(u32 a) { return *(const LAS v2u*)a; }
DEV v2u lds2v(u32 a) { return *(const volatile LAS v2u*)a; }
DEV void barrier() { asm volatile("s_waitcnt lgkmcnt(0)\n\ts_barrier" ::: "memory"); }
template <int N> DEV void waitv() { asm volatile("s_waitcnt vmcnt(%0)" ::"n"(N) : "memory"); }
DEV void dma_in(const void* g, u32 voff, u32 lds) {
    asm volatile("s_mov_b32 m0, %2\n\ts_nop 0\n\tglobal_load_lds_dwordx4 %0, %1" ::"v"(voff), "s"(g), "s"(lds) : "m0", "memory");
}
DEV void dma_b(v4u rs, u32 voff, u32 lds) {
    asm volatile("s_mov_b32 m0, %2\n\ts_nop 0\n\tbuffer_load_dwordx4 %0, %1, 0 offen lds" ::"v"(voff), "s"(rs), "s"(lds) : "m0", "memory");
}
DEV void dma_s(v4u rs, u32 voff, u32 so, u32 lds) {
    asm volatile("s_mov_b32 m0, %3\n\ts_nop 0\n\tbuffer_load_dwordx4 %0, %1, %2 offen lds" ::"v"(voff), "s"(rs), "s"(so), "s"(lds) : "m0", "memory");
}
DEV void dma_ws(v4u rs, u32 voff, u32 so, u32 lds) {
    asm volatile("s_mov_b32 m0, %3\n\ts_nop 0\n\tbuffer_load_dwordx4 %0, %1, %2 offen sc1 lds" ::"v"(voff), "s"(rs), "s"(so), "s"(lds) : "m0", "memory");
}
template <int IM> DEV void gld4(v4u& d, u32 voff, const void* g) { asm volatile("global_load_dwordx4 %0, %1, %2 offset:%3" : "=v"(d) : "v"(voff), "s"(g), "n"(IM) : "memory"); }
DEV void gld2(v2u& d, u32 voff, const void* g) { asm volatile("global_load_dwordx2 %0, %1, %2" : "=v"(d) : "v"(voff), "s"(g) : "memory"); }
DEV void mm16k(f4& c, v2u a, v2u b) { asm volatile("v_mfma_f32_16x16x16_bf16 %0, %1, %2, 0" : "=&v"(c) : "v"(a), "v"(b)); }
DEV void mm16n(f4& c, v4u a, v4u b) { asm volatile("s_nop 1\n\tv_mfma_f32_16x16x32_bf16 %0, %1, %2, 0" : "=&v"(c) : "v"(a), "v"(b)); }
template <class A, class B> DEV void pin2(A& a, B& b) { asm volatile("" : "+v"(a), "+v"(b)); }
template <int IM> DEV void ld4(v4u& d, u32 voff, v4u rs, u32 so) { asm volatile("buffer_load_dwordx4 %0, %1, %2, %3 offen offset:%4 sc1" : "+v"(d) : "v"(voff), "s"(rs), "s"(so), "n"(IM) : "memory"); }
template <int IM> DEV void ld1(u32& d, u32 voff, v4u rs, u32 so) { asm volatile("buffer_load_dword %0, %1, %2, %3 offen offset:%4 sc1" : "+v"(d) : "v"(voff), "s"(rs), "s"(so), "n"(IM) : "memory"); }
template <int IM> DEV void ld4m(v4u& d, u32 voff, v4u rs, u32 so, u64 m) {
    asm volatile("s_mov_b64 exec, %5\n\tbuffer_load_dwordx4 %0, %1, %2, %3 offen offset:%4 sc1\n\ts_mov_b64 exec, -1" : "+v"(d) : "v"(voff), "s"(rs), "s"(so), "n"(IM), "s"(m) : "memory");
}
template <int IM, int R> DEV void ld4a(u32 voff, v4u rs, u32 so) { asm volatile("buffer_load_dwordx4 a[%c4:%c5], %0, %1, %2 offen offset:%3 sc1" ::"v"(voff), "s"(rs), "s"(so), "n"(IM), "n"(R), "n"(R + 3) : "memory"); }
template <int IM, int R> DEV void ld4v(u32 voff, v4u rs, u32 so) { asm volatile("buffer_load_dwordx4 v[%c4:%c5], %0, %1, %2 offen offset:%3 sc1" ::"v"(voff), "s"(rs), "s"(so), "n"(IM), "n"(R), "n"(R + 3) : "memory"); }
template <int R> DEV u32 mx3v(u32 a, u32 b) { u32 r; asm volatile("v_max3_u32 %0, %1, %2, v%c3" : "=v"(r) : "v"(a), "v"(b), "n"(R)); return r; }
template <int R> DEV u32 mx3vv(u32 a) { u32 r; asm volatile("v_max3_u32 %0, %1, v%c2, v%c3" : "=v"(r) : "v"(a), "n"(R), "n"(R + 2)); return r; }
template <int R, int A> DEV void swapw() { asm volatile("v_permlane32_swap_b32 v%c0, v%c1\n\tv_accvgpr_write_b32 a%c2, v%c0\n\tv_accvgpr_write_b32 a%c3, v%c1" ::"n"(R), "n"(R + 2), "n"(A), "n"(A + 1)); }
template <int R, int R2> DEV void addv() { asm volatile("v_add_f32 v%c0, v%c0, v%c1" ::"n"(R), "n"(R2)); }
template <int R> DEV float swapsum() { float r; asm volatile("s_nop 1\n\tv_permlane32_swap_b32 v%c1, v%c2\n\tv_add_f32 %0, v%c1, v%c2" : "=v"(r) : "n"(R + 1), "n"(R + 3)); return r; }
template <int IM, int R> DEV void ld1a(u32 voff, v4u rs, u32 so) { asm volatile("buffer_load_dword a%c4, %0, %1, %2 offen offset:%3 sc1" ::"v"(voff), "s"(rs), "s"(so), "n"(IM), "n"(R) : "memory"); }
template <int IM, int R> DEV void ld4am(u32 voff, v4u rs, u32 so, u64 m) {
    asm volatile("s_mov_b64 exec, %6\n\tbuffer_load_dwordx4 a[%c4:%c5], %0, %1, %2 offen offset:%3 sc1\n\ts_mov_b64 exec, -1" ::"v"(voff), "s"(rs), "s"(so), "n"(IM), "n"(R), "n"(R + 3), "s"(m) : "memory");
}
template <int IM> DEV void ld2(v2u& d, u32 voff, v4u rs, u32 so) { asm volatile("buffer_load_dwordx2 %0, %1, %2, %3 offen offset:%4 sc1" : "=v"(d) : "v"(voff), "s"(rs), "s"(so), "n"(IM) : "memory"); }
template <int IM> DEV void ld4o(v4u& d, u32 voff, v4u rs, u32 so) { asm volatile("buffer_load_dwordx4 %0, %1, %2, %3 offen offset:%4 sc1" : "=v"(d) : "v"(voff), "s"(rs), "s"(so), "n"(IM) : "memory"); }
template <int IM> DEV void ldh(u32& d, u32 voff, v4u rs, u32 so) { asm volatile("buffer_load_short_d16_hi %0, %1, %2, %3 offen offset:%4 sc1" : "+v"(d) : "v"(voff), "s"(rs), "s"(so), "n"(IM) : "memory"); }
template <int IM> DEV void st4(v4u d, u32 voff, v4u rs, u32 so) { asm volatile("buffer_store_dwordx4 %0, %1, %2, %3 offen offset:%4 sc1" ::"v"(d), "v"(voff), "s"(rs), "s"(so), "n"(IM) : "memory"); }
DEV void setm0(u32 v) { asm volatile("s_mov_b32 m0, %0" ::"s"(v) : "m0", "memory"); }
template <int IM> DEV void dmai(v4u rs, u32 voff, u32 so) { asm volatile("buffer_load_dwordx4 %0, %1, %2 offen offset:%3 sc1 lds" ::"v"(voff), "s"(rs), "s"(so), "n"(IM) : "memory"); }
DEV u32 pka(float a, float b) { u32 r; asm volatile("v_cvt_pk_bf16_f32 %0, %1, %2" : "=v"(r) : "v"(a), "v"(b)); return r; }
template <int N> DEV void waitl() { __builtin_amdgcn_s_waitcnt(0xc07f | (N << 8)); }
DEV u32 vsel(bool c, u32 a, u32 b) { asm volatile("" : "+v"(a)); asm volatile("" : "+v"(b)); return c ? a : b; }
DEV u32 ssel(bool c, u32 a, u32 b) { asm volatile("" : "+s"(a)); asm volatile("" : "+s"(b)); return c ? a : b; }
template <int N, class T> DEV void waitp(T& d) { asm volatile("s_waitcnt vmcnt(%1)" : "+v"(d) : "n"(N) : "memory"); }
template <int N> DEV void waitbar() { asm volatile("s_waitcnt vmcnt(%0) lgkmcnt(0)\n\ts_barrier" ::"n"(N) : "memory"); }
template <int N> DEV void pad3(f4& a, f4& b, f4& c) { asm volatile("s_nop %3" : "+v"(a), "+v"(b), "+v"(c) : "n"(N)); }
template <int I> struct IC { static constexpr int v = I; };
template <int B, int E, class F> DEV void sfor(F&& f) { if constexpr (B < E) { f(IC<B>{}); sfor<B + 1, E>(f); } }
#define LAM(I) [&](auto I) __attribute__((always_inline))
#define FENCE() __builtin_amdgcn_sched_barrier(0)
#define OPQV(x) asm volatile("" : "+v"(x))
#define OPQS(x) asm volatile("" : "+s"(x))
DEV float halves_sum(float a, float b) {
    auto r = __builtin_amdgcn_permlane32_swap(asu(a), asu(b), false, false);
    return asf(r[0]) + asf(r[1]);
}
DEV bool bad(u32 m) { return __builtin_amdgcn_ballot_w64(m == ~0u) != 0; }
#define STAMP_INIT()
template <int I> DEV void mark() { asm volatile("; MARK %0" ::"n"(I)); }
#define STAMP(i) mark<(i)>()
#define STAMP_END()
#define STAMPV(i, v)

DEV void hedgehog(const float (&t)[18], float a, u32 (&ph)[18]) {
    float mx = t[0], mn = t[0];
#pragma unroll
    for (int i = 1; i < 18; ++i) { mx = __builtin_fmaxf(mx, t[i]); mn = __builtin_fminf(mn, t[i]); }
    const float c1 = -a * mx, c2 = a * mn;
    float e1[18], e2[18], s1 = 0.f, s2 = 0.f;
#pragma unroll
    for (int i = 0; i < 18; ++i) {
        e1[i] = __builtin_amdgcn_exp2f(__builtin_fmaf(t[i], a, c1)); s1 = i ? s1 + e1[i] : e1[i];
        e2[i] = __builtin_amdgcn_exp2f(__builtin_fmaf(t[i], -a, c2)); s2 = i ? s2 + e2[i] : e2[i];
    }
    const float r1 = __builtin_amdgcn_rcpf(s1), r2 = __builtin_amdgcn_rcpf(s2);
#pragma unroll
    for (int i = 0; i < 9; ++i) { ph[i] = pk(e1[2 * i] * r1, e1[2 * i + 1] * r1); ph[9 + i] = pk(e2[2 * i] * r2, e2[2 * i + 1] * r2); }
}
DEV v4u tr2(u32 a0, u32 a1) {
    s4 lo = __builtin_amdgcn_ds_read_tr16_b64_v4i16((LAS s4*)a0), hi = __builtin_amdgcn_ds_read_tr16_b64_v4i16((LAS s4*)a1);
    v2u l = __builtin_bit_cast(v2u, lo), h = __builtin_bit_cast(v2u, hi);
    return (v4u){l.x, l.y, h.x, h.y};
}
DEV v2u y_val(f4 acc) { return (v2u){pk(acc[0], acc[1]), pk(acc[2], acc[3])}; }
DEV v4u y_pair(v2u a, v2u b) {
    auto r0 = __builtin_amdgcn_permlane16_swap(a.x, b.x, false, false), r1 = __builtin_amdgcn_permlane16_swap(a.y, b.y, false, false);
    return (v4u){r0[0], r1[0], r0[1], r1[1]};
}
template <int N, int n, int r>
DEV void conv_read(u32 aa, u32 ba, v4u (&A)[N], v4u (&B)[N]) {
    constexpr int i = n / 3, k = n % 3;
    if constexpr (k == 0) B[i] = lds4(ba + 64 * (8 * i - r + 7));
    else { const v2u v = lds2(aa + 2 + 64 * (r + 8 * i) + 8 * (k - 1)); if constexpr (k == 1) { A[i].x = v.x; A[i].y = v.y; } else { A[i].z = v.x; A[i].w = v.y; } }
}
constexpr int conv_ij(int N, int MASK, int n) {
    for (int d = 0; d < N; ++d) for (int i = 0; i < N; ++i) for (int j = i; j < N; ++j)
        if (((MASK >> j) & 1) && (i > j - i ? i : j - i) == d && n-- == 0) return i * 8 + j;
    return -1;
}
#define NPRE_ 10
#define SEND_ 212
#define UPA_ 8
#define UPO_ 12
constexpr int NPRE = NPRE_, NSITE = 13 + 4 + 40 + 9, SITE_END = SEND_;
constexpr int site_at(int q) { return q <= SITE_END ? NPRE + (q * (NSITE - NPRE) + SITE_END - 1) / SITE_END : NSITE; }
constexpr int xissued(int n) { return UPO_ + (5 * n + 3) / 4 < 32 ? UPO_ + (5 * n + 3) / 4 : 32; }
constexpr int kstep(int n) { return n % 9 < 8 ? 8 * (n / 9) + n % 9 : 32 + n / 9; }

__global__ void __launch_bounds__(256, 1) k_main(Args) {
    const void* ka = (const void*)(uintptr_t)__builtin_amdgcn_kernarg_segment_ptr();
    v4u rk, rv, rm, rq, rp, rx, vw, ro;
    const char *sk, *sh, *sD;
    int tid = __builtin_amdgcn_workitem_id_x(), b = __builtin_amdgcn_workgroup_id_x();
    sldi<0>(rk, ka); sldi<16>(rv, ka); sldi<144>(sk, ka); sldi<112>(vw, ka); sldi<96>(rx, ka); sldi<128>(ro, ka); sldi<152>(sh, ka); sldi<160>(sD, ka);
    OPQS(b); OPQV(tid);
    int wave = __builtin_amdgcn_readfirstlane(tid >> 6);
    sld(rm, ka, (wave >> 1) * 16); sld(rq, ka, (b & 24) == 24 ? 48 : 32); sld(rp, ka, 64 + (b & 8) * 2);
    OPQS(b); OPQS(wave); OPQV(tid);
    {
        u64 pc;
        asm volatile("s_getpc_b64 %0" : "=s"(pc));
        const u32 o = umin(tid * 8 + (b >> 3) * 2048, CODE - 512);
        asm volatile("s_mov_b32 m0, %2\n\ts_nop 0\n\tglobal_load_lds_dword %0, %1" ::"v"(o), "s"(pc), "s"(L_EX) : "m0", "memory");
    }
    STAMP_INIT();
    asm volatile("" ::: "a255", "v255");
    u64 ep;
    v2u sw0, sw1;
    {
        const int lane = tid & 63, c = lane & 15, cg = ((b >> 4) & 6) + ((b >> 3) & 1), tg = 4 * (b & 7) + 2 * (b >> 7) + ((b >> 4) & 1);
        u32 s0 = c * 8 + (wave * 128 + cg * 576), s1 = (c & 7) * 8 + cg * 576, vo[5], lo[5];
        const u32 l0 = ((lane * 7282u) >> 16) * 1008 + lane * 16, t0 = (64 * tg + 7 * wave) * 1152 + cg * 144;
#pragma unroll
        for (int i = 0; i < 5; ++i) {
            const int isv = i < 2 ? 0 : i > 2 ? 1 : wave >> 1;
            vo[i] = l0 + (t0 + (28 * i - 3 - 67 * isv) * 1152); lo[i] = P_KS + wave * 1008 + i * 4032 + isv * (P_VS - 10080);
            OPQV(vo[i]); OPQS(lo[i]);
        }
        pin2(s0, s1);
        FENCE();
        asm volatile("s_waitcnt lgkmcnt(0)" : "+s"(rk), "+s"(rv), "+s"(rm), "+s"(rq), "+s"(rp), "+s"(rx), "+s"(vw), "+s"(ro), "+s"(sk), "+s"(sh), "+s"(sD));
        STAMP(40);
#pragma unroll
        for (int i = 0; i < 5; ++i) dma_b(i < 2 ? rk : i > 2 ? rv : rm, vo[i], lo[i]);
        asm volatile("global_load_dwordx2 %0, %1, %2" : "=v"(sw0) : "v"(s0), "s"(sk) : "memory");
        asm volatile("global_load_dwordx2 %0, %1, %2 offset:512" : "=v"(sw1) : "v"(s1), "s"(sk) : "memory");
        sld(ep, (const void*)(((u64)vw.y << 32) | vw.x), W_E);
    }
    OPQS(b); OPQS(wave); OPQV(tid);
    const int lane = tid & 63, rg = 2 * (b & 7) + (b >> 7), p = (b >> 3) & 15;
    const int Pg = p >> 2, tw = p & 3, pw = 4 * Pg + wave, ci = (rg + p) & 15;
    const bool conv = ci < 12;
    u32 l16 = lane * 16, l4 = lane * 4;
    OPQV(l16); OPQV(l4);
    const int ch0 = 36 * p + 3 * ci;
    u32 xr[9], kvw[3], kvA, kvB, l8, lP, l16B, cpo, csd;
    {
        const int sec = p & 3, cg = 2 * Pg + (p & 1), tg = 2 * rg + ((p >> 1) & 1);
        const int c = lane & 15;
        FENCE();
        STAMP(41);
        const v4u z4 = {0, 0, 0, 0};
        u32 mk0, mk1, ua0, ua1, uo0, uo1, aw, al_, ap, d0_, d1_, d2_, cso, axs, aid, azc;
        v2u Bi; v4u Sel;
        const bool s3 = sec == 3;
        const u32 wf_ = rg >> 2, jl = rg & 3;
        auto wdma = [&]() __attribute__((always_inline)) {
            const u32 wst = s3 ? 1152 : 3456, cb = (sec < 2 ? (sec + 1) * 1152 : 0) + Pg * 288;
            const u32 lw = ((lane * 3641u) >> 16) * (wst - 288) + l16, w0 = (36 * rg + 3 * wave) * wst + cb;
            const u32 r4 = (lane * 43u) >> 8, sg = ((s3 ? 0 : (4 * Pg + wf_) & 14) + 4 * jl + wave) & 15;
            const u32 ll = vsel(lane < 24, r4 * (wst - 96) + (r4 >> 1) * (16 * wst) + l16 + ((36 * sg + 16) * wst + cb + ((72 * wf_) & ~15u)), OOB);
            const u32 lx = ((lane * 3450u) >> 16) * 848 + l16 + ((128 * rg + 32 * tw + 3 * wave) * 1152 + Pg * 288);
            const u32 lp = vsel(lane < 54, l16 - ((lane * 21846u) >> 16) * 12 + (Pg * 5184 + wave * 1296), OOB);
#pragma unroll
            for (int i = 0; i < 3; ++i) dma_b(rq, lw + (w0 + 12 * i * wst), P_WS + wave * 864 + i * 3456);
            dma_b(rq, ll, P_LS + wave * 1024);
#pragma unroll
            for (int i = 0; i < 2; ++i) dma_s(rp, lp, i * 648, P_PH + wave * 3072 + i * 1536);
#pragma unroll
            for (int i = 0; i < 3; ++i) dma_s(rx, lx, 12 * i * 1152, P_XS + wave * 912 + i * 3648);
        };
        {
            const u32 g = lane >> 4, m = lane & 31, kh = lane >> 5;
            const u32 hm = g == (c >> 2) ? ((c & 1) ? 0xffff0000u : 0xffffu) : 0u;
            mk0 = (c & 2) ? 0u : hm; mk1 = (c & 2) ? hm : 0u;
            Bi = (v2u){mk0 & 0x3f803f80u, mk1 & 0x3f803f80u}; Sel = (v4u){Bi.x, Bi.y, Bi.x, Bi.y};
            const u32 r0 = (8 * (c >> 2) + (c & 3)) * 144;
            ua0 = r0 + 8 * g + (P_KS + 32 * wave);
            ua1 = r0 + 32 * 144 * kh + 8 * (g & 1) + (P_KS + 128 + 576 * (wave & 1));
            uo0 = c * 4096 + 16 * g;
            uo1 = (c & 7) * 4096 + 64 * (c >> 3) + 16 * g + (wave < 2 ? 64 * 4096 + 8 - 8 * wave : OOB - (1u << 20));
            pin2(mk0, mk1); pin2(Bi, Sel); pin2(ua0, ua1); pin2(uo0, uo1);
            const u32 hD = (m >> 2) & 1;
            aw = (18 * hD + (m & 3) + 4 * (m >> 3)) * 288 + 16 * kh + (P_WS + ((72 * wave) & ~15u));
            al_ = ((((m >> 1) & 1) + 2 * ((m >> 3) & 1)) * 1024 + (2 * hD + (m & 1)) * 96) * (wave == wf_) + 16 * kh + (wave == wf_ ? P_LS : L_Z);
            ap = (8 * kh + (c >> 2)) * 48 + 32 * (g & 1) + 8 * (c & 3) + (P_PH + (64 * wave - 4 * (wave & 1)) * 48);
            pin2(aw, al_); pin(ap);
            const u32 ib = W_IMG + pw * IMG, ob = W_WOT + Pg * WOG;
            const u32 bA = ssel(s3, ob + wave * 36864, ib + sec * L_V + (sec >> 1) * (L_Q - 2 * L_V));
            const u32 bB = ssel(s3, ob + 4 * 36864 + 128 * (wave >> 1) + 32 * (wave & 1), ib + L_QT + 128 * sec);
            const u32 dsB = s3 ? 256 : QTS, drB = s3 ? 1024 : 2 * QTS, ri = (rg - (s3 ? 0 : pw & 14)) & 15;
            const bool lo = m < 16;
            const u32 lp = vsel(lo, ((m & 3) + 8 * (m >> 2) + 32 * kh) * 16 + bA, vsel(m < 18, (m & 1) * (s3 ? 16 : 32) + kh * (s3 ? 512 : 16) + bB, OOB));
            const u32 ds = vsel(lo, 1024, dsB), dr = vsel(lo, 2048, drB);
            d0_ = lp + ri * dr; d1_ = d0_ + ds;
            d2_ = umax(lp + (16 + (jl >> 1)) * dr + (jl & 1) * ds, wave == wf_ ? 0u : OOB);
            cso = vsel(m < (s3 ? 0 : 18), (18 * kh + m) * 4 + (W_CSP + pw * CSB + rg * 576 + sec * 192), OOB);
            axs = m * 304 + 36 * kh + (P_XS + 72 * wave);
            aid = vsel(lane < 36 * (sec & 1), lane * 50 + (lane >= 18 ? 1536 - 900 : 0) + (P_PH + wave * 3072), L_END - 16);
            azc = vsel(lane < (sec ? 0 : 6), (lane & 1) * 16 + ((lane >> 1) + (lane < 4 ? 2 * rg : 30 + jl)) * QTS + (ib + L_QT + 384), OOB);
            pin2(d0_, d1_); pin2(d2_, cso); pin2(axs, aid); pin(azc);
            *(LAS v2u*)(vsel(tid < 38, tid * 80 + (L_KVA + 72), L_END - 16)) = (v2u){0, 0};
            *(LAS v4u*)(l16 + kh * 1024 + (P_PH + 1024 + wave * 3072)) = z4;
            *(LAS v4u*)(vsel(lane < (wave ? 30 : 6), l16 + ssel(wave, L_Q + 4 * HCS - CVS + wave * CVS, L_Z), L_END - 16)) = z4;
        }
        FENCE();
        STAMP(42);
        wdma();
        FENCE();
        STAMP(26);
        asm volatile("s_waitcnt vmcnt(9)" : "+v"(sw0), "+v"(sw1)::"memory");
        v2u Bd[2][4];
#pragma unroll
        for (int s = 0; s < 2; ++s)
#pragma unroll
            for (int j = 0; j < 4; ++j) {
                const v2u sw = s ? sw1 : sw0;
                const u32 wd = __builtin_amdgcn_perm(0u, j < 2 ? sw.y : sw.x, (j & 1) ? 0x01000100u : 0x03020302u);
                Bd[s][j] = (v2u){wd & mk0, wd & mk1};
                pin(Bd[s][j]);
            }
        u32 so_u = W_UT + 128 * tg + cg * 72 * 4096, so_u2 = so_u + wave * 65536;
        OPQS(so_u); OPQS(so_u2);
        FENCE();
        barrier();
        STAMP(27);
        {
            u32 uo[5][2];
            v2u ka[5][5]; f4 pr[5][5], sm[5];
            v4u bb[2];
#pragma unroll
            for (int n = 0; n < 25; ++n) {
                const int T = n < 10 ? n >> 1 : n % 5, j = n < 10 ? n & 1 : n / 5;
                ka[T][j] = lds2v((T < 4 ? ua0 + (32 * (T >> 1) + 4 * (T & 1)) * 144 : ua1) + (j < 4 ? j * 144 : P_VS));
            }
            FENCE();
            auto P = [&](auto T_, auto J_) __attribute__((always_inline)) { constexpr int T = T_.v, j = J_.v; mm16k(pr[T][j], ka[T][j], j < 4 ? Bd[T == 4][j] : Bi); };
            sfor<0, 5>(LAM(T_) { P(T_, IC<0>{}); P(T_, IC<1>{}); });
            STAMP(52);
            sfor<0, 16>(LAM(I_) {
                constexpr int I = I_.v, T = I % 5, ph = I / 5 + 1, U = (I + 4) % 5;
                if constexpr (I < 15) {
                    f4& a = ph == 1 ? pr[T][0] : sm[T];
                    const u32 w0 = pka(a[0], a[1]), w1 = pka(a[2], a[3]), w2 = pka(pr[T][ph][0], pr[T][ph][1]), w3 = pka(pr[T][ph][2], pr[T][ph][3]);
                    bb[I & 1] = (v4u){w0, w1, w2, w3};
                    P(IC<T>{}, IC<ph + 1>{});
                } else {
                    uo[0][0] = pka(sm[0][0], sm[0][1]); uo[0][1] = pka(sm[0][2], sm[0][3]);
                }
                if constexpr (I > 0) mm16<true>(sm[U], Sel, bb[(I - 1) & 1]);
            });
            STAMP(53);
            sfor<0, 5>(LAM(T_) {
                constexpr int T = T_.v;
                u32 w0, w1;
                if constexpr (T) { w0 = pka(sm[T][0], sm[T][1]); w1 = pka(sm[T][2], sm[T][3]); } else { w0 = uo[0][0]; w1 = uo[0][1]; }
                pin(pr[T][4]);
                FENCE();
                uo[T][0] = pk(bf_hi(w1) * pr[T][4][3], bf_lo(w1) * pr[T][4][2]);
                uo[T][1] = pk(bf_hi(w0) * pr[T][4][1], bf_lo(w0) * pr[T][4][0]);
                if constexpr (T == 1 || T == 3) bst4((v4u){uo[T][0], uo[T][1], uo[T - 1][0], uo[T - 1][1]}, vw, uo0 + 32 * (T - 1), so_u2, SC1);
                else if constexpr (T == 4) bst2((v2u){uo[4][0], uo[4][1]}, vw, uo1, so_u, SC1);
                else pin2(uo[T][0], uo[T][1]);
                FENCE();
            });
        }
        STAMP(28);
        asm volatile("s_waitcnt vmcnt(3)\n\ts_barrier" ::: "memory");
        STAMP(54);
        {
            u32 one2 = 0x3f803f80u;
            OPQS(one2);
            *(LAS u16*)(aid) = 0x3f80;
            v4u bw[2][2], wa[2][3];
            bw[0][0] = tr2(ap, ap + 192); wa[0][0] = lds4(aw); wa[1][0] = lds4(al_);
            bw[0][1] = tr2(ap + 768, ap + 960); wa[0][1] = lds4(aw + 32); wa[1][1] = lds4(al_ + 32);
            bw[1][0] = tr2(ap + 1440, ap + 1440 + 192);
            bw[1][1] = tr2(ap + 1440 + 768, ap + 1440 + 960); wa[0][2] = lds4(aw + 64); wa[1][2] = lds4(al_ + 64);
            FENCE();
#pragma unroll
            for (int j = 0; j < 9; ++j) xr[j] = *(const LAS u32*)(axs + 4 * j);
            FENCE();
            f16v d[2][2];
#pragma unroll
            for (int i = 0; i < 4; ++i) { mm32<true>(d[i >> 1][i & 1], wa[i & 1][i >> 1], bw[i >> 1][0]); mm32<false>(d[i >> 1][i & 1], wa[i & 1][(i >> 1) + 1], bw[i >> 1][1]); }
            FENCE();
            STAMP(55);
            {
                float s1 = 0.f, s2 = 0.f;
#pragma unroll
                for (int j = 0; j < 9; ++j) { s1 = dot2(xr[j], one2, s1); s2 = dot2(xr[j], xr[j], s2); }
                const u32 so = W_XF0 + ((rg * 4 + tw) * 16 + pw) * XS;
                bst4((v4u){xr[0], xr[1], xr[2], xr[3]}, vw, l16, so, SC1);
                bst4((v4u){xr[4], xr[5], xr[6], xr[7]}, vw, l16 + 1024, so, SC1);
                l8 = 2 * l4;
                bst2((v2u){xr[8], asu(halves_sum(s1, s2))}, vw, (lane & 31) * 16 + (lane >> 5) * 8 + 2048, so, SC1);
                bst4(z4, vw, azc, 0, SC1);
            }
            FENCE();
            float cs[2];
#pragma unroll
            for (int hh = 0; hh < 2; ++hh) {
                asm volatile("" : "+v"(d[hh][0]), "+v"(d[hh][1]));
                u32 w[12];
#pragma unroll
                for (int j = 0; j < 12; ++j) w[j] = pk(d[hh][j >> 3][2 * (j & 7)], d[hh][j >> 3][2 * (j & 7) + 1]);
                bst4((v4u){w[0], w[1], w[2], w[3]}, vw, d0_ + 64 * hh, 0, SC1);
                bst4((v4u){w[4], w[5], w[6], w[7]}, vw, d1_ + 64 * hh, 0, SC1);
                bst4((v4u){w[8], w[9], w[10], w[11]}, vw, d2_ + 64 * hh, 0, SC1);
                cs[hh] = 0.f;
#pragma unroll
                for (int j = 0; j < 12; ++j) cs[hh] = dot2(w[j], one2, cs[hh]);
                FENCE();
            }
            bst1(asu(halves_sum(cs[0], cs[1])), vw, cso, 0, SC1);
        }
        v4u hd[3]; v2u he[3], hz;
        {
            const char* gp = sh + umin(ch0, 573) * 4096;
            const u32 o1 = tid * 16, o2 = (o1 + 16) & 4095;
            gld2(hz, umin(lane >> 3, 2) * 4096, gp);
#pragma unroll
            for (int i = 0; i < 3; ++i) { gld4<0>(hd[i], o1, gp + i * 4096); gld2(he[i], o2, gp + i * 4096); }
        }
        STAMP(29);
        asm volatile("s_waitcnt vmcnt(7)\n\ts_barrier" ::: "memory");
        OPQS(ep);
        ep += 1;
        const v2u epv = {(u32)ep, (u32)(ep >> 32)};
        if (tid == 0) bst2(epv, vw, 0, W_F + (rg * 16 + p) * 128, SC1);
        STAMP(1);
        {
            const u32 e = (lane >> 4) & 1, kg = lane >> 4;
#pragma unroll
            for (int j = 0; j < 3; ++j) {
                const u32 pi = 16 * (wave + 4 * j) + c, hh = pi >= 86, ip = pi - 86 * hh, q = ip - 72, s = 2 * (ip - 84) + e;
                u32 v = vsel(ip < 64, (ip & 15) * 80 + (ip >> 4) * 8 + 32 * e, (2 * ip + e) * 80 + 64 - 128 * 80);
                v = vsel(ip < 72, v, (16 + (q >> 2)) * 80 + (q & 3) * 8 + 32 * e);
                v = vsel(ip < 84, v, s * 80 + 16 * 80 + 64);
                kvw[j] = vsel(lane >= 32 || pi >= 172 || s == 3, L_EX + l8, v + hh * 1520 + L_KVA);
                OPQV(kvw[j]);
            }
            const bool dt = wave & 1;
            kvA = dt ? vsel(c < 3, 1152 + (c * 4 + kg) * 16, OOB) : l16;
            kvB = vsel(lane < (dt ? 4 : 16), l8 + (dt ? 1344 : 1024), OOB);
            l16B = l16 + XS; lP = (lane & 31) * 16 + (lane >> 5) * XS;
            const u32 sc = (lane * 57u) >> 10, r = lane - 18 * sc, hh = r >= 9, pr = r - 9 * hh;
            cpo = vsel(lane < 54, sc * (192 - 144) + l8, OOB);
            csd = vsel(lane < 54, hh * 224 + vsel(pr < 8, sc * 64 + pr * 8, 192 + sc * 8) + L_CS, L_EX + 2048 + l8);
            pin2(kvA, kvB); pin2(l16B, lP); pin2(cpo, csd); pin(l8);
        }
        FENCE();
        if (wave) waitv<0>(); else waitv<1>();
        STAMP(51);
        if (conv) {
            u32 o = L_Q + 32 + tid * 16;
            OPQV(o);
#pragma unroll
            for (int i = 0; i < 3; ++i) {
                asm volatile("" : "+v"(hd[i]), "+v"(he[i]));
                const u32 d0 = hd[i].x, d1 = hd[i].y, d2 = hd[i].z, d3 = hd[i].w, d4 = he[i].x, d5 = he[i].y;
                const u32 a0 = __builtin_amdgcn_alignbit(d1, d0, 16), a1 = __builtin_amdgcn_alignbit(d2, d1, 16), a2 = __builtin_amdgcn_alignbit(d3, d2, 16),
                          a3 = __builtin_amdgcn_alignbit(d4, d3, 16), a4 = __builtin_amdgcn_alignbit(d5, d4, 16);
                *(LAS v4u*)(o + i * CVS) = hd[i];
                *(LAS v4u*)(o + i * CVS + HCS) = (v4u){a0, a1, a2, a3};
                *(LAS v4u*)(o + i * CVS + 2 * HCS) = (v4u){d1, d2, d3, d4};
                *(LAS v4u*)(o + i * CVS + 3 * HCS) = (v4u){a1, a2, a3, a4};
            }
            asm volatile("" : "+v"(hz));
            const u32 k = (lane >> 1) & 3;
            const u64 t = (lane & 1) && k ? (((u64)hz.y << 32) | hz.x) << (64 - 16 * k) : 0;
            if (tid < 24) *(LAS v4u*)(L_Q + (lane >> 3) * CVS + k * HCS + (lane & 1) * 16) = (v4u){0, 0, (u32)t, (u32)(t >> 32)};
        }
    }
    auto xop = [&](auto Q_, u32 so) __attribute__((always_inline)) {
        constexpr int j = Q_.v / 5, k = Q_.v % 5;
        if constexpr (k == 0) ld4a<0, 16 * j>(l16, vw, so);
        if constexpr (k == 1) ld4a<1024, 16 * j + 4>(l16, vw, so);
        if constexpr (k == 2) ld4v<2048, 224 + 4 * j>(lP, vw, so);
        if constexpr (k == 3) ld4a<0, 16 * j + 8>(l16B, vw, so);
        if constexpr (k == 4) ld4a<1024, 16 * j + 12>(l16B, vw, so);
    };
    {
        u32 D2[3];
#pragma unroll
        for (int i = 0; i < 3; ++i) { const u32 cc = umin(ch0, 573) + i, t = ((const __attribute__((address_space(4))) u32*)(uintptr_t)sD)[cc >> 1]; D2[i] = (cc & 1 ? t >> 16 : t & 0xffffu) * 0x10001u; OPQS(D2[i]); }
        const int bq = lane & 15, g = lane >> 4, cc = (bq + 1) & 3;
        u32 aa = L_Q + cc * HCS + 2 * (bq - 8 * g + 24 - cc), ba = L_Q + 4 * HCS + 480 + 2 * (16 * bq + 8 * g - 16) - 7 * 64;
        u32 yo = (bq >> 3) * YB + (bq & 7) * 32 + 8 * g, yp = (bq >> 3) * YB + (bq & 7) * 32 + 16 * (g >> 1) + (g & 1) * (2 * YB);
        v4u dm;
        {
            const u32 e = 7 - (bq & 7), hm = g == 2 + (bq >> 3) ? ((e & 1) ? 0xffff0000u : 0xffffu) : 0u;
#pragma unroll
            for (int i = 0; i < 4; ++i) dm[i] = (e >> 1) == i ? hm : 0u;
        }
        if (wave < 3) { aa += wave * CVS; ba += wave * CVS; }
        pin2(aa, ba); pin2(yo, yp); pin(dm);
        const u32 xb = W_XF0 + (rg * 4 + wave) * 16 * XS;
        u32 io = wave * 9216, ig = W_IMG + p * IMG + io, iq = umin(4 * wave, 11) * 1024 + L_QT - 8192 - io, xo = xb + (p & 14) * XS, uo = W_UT + ch0 * 4096 + wave * 1024, co = W_CSP + p * CSB + wave * (4 * 576);
        u32 fo = W_F + (wave ? rg * 16 + bq : (lane >> 2) * 16 + 4 * Pg + (lane & 3)) * 128;
        OPQS(io); OPQS(ig); OPQS(iq); OPQS(xo); OPQS(uo); OPQS(co); OPQV(fo);
        FENCE();
        if (wave < 2) {
#pragma unroll 1
            for (int sp = 0; sp < SPIN; ++sp) {
                asm volatile("" ::: "memory");
                const v2u f = bld2(vw, fo, 0, SC1);
                if (__builtin_amdgcn_ballot_w64(f.x != (u32)ep || f.y != (u32)(ep >> 32)) == 0) break;
                __builtin_amdgcn_s_sleep(1);
            }
        }
        barrier();
        STAMP(2);
        v2u cp[4];
        auto site = LAM(N_) {
            constexpr int n = N_.v, v = n - 57;
            if constexpr (n < 9) dmai<(n & 3) * 1024>(vw, l16, ig);
            else if constexpr (n < 13) dmai<(n - 9) * 1024>(vw, l16, ig);
            else if constexpr (n < 17) ld2<(n - 13) * 576>(cp[n - 13], cpo, vw, co);
            else if constexpr (n < 57) {
                if constexpr (n == 37) waitv<20>();
                xop(IC<n - 17>{}, xo);
                if constexpr ((n - 17) % 5 == 4 && n < 56) { xo += 2 * XS; xo = xo == xb + 16 * XS ? xb : xo; OPQS(xo); }
            } else dmai<(v & 3) * 1024>(vw, l16, ig);
            if constexpr (n == 3 || n == 7 || n == 60 || n == 64) { io += 4096; ig += 4096; }
            if constexpr (n == 8) { io += iq; ig += iq; }
            if constexpr (n == 12) { io += L_V - 8192 - iq; ig += L_V - 8192 - iq; }
            if constexpr (n == 3 || n == 7 || n == 8 || n == 12 || n == 60 || n == 64) { OPQS(io); OPQS(ig); setm0(io); }
        };
        auto site1 = LAM(N_) { site(N_); if constexpr (N_.v == 3 || N_.v == 7 || N_.v == 8 || N_.v == 60 || N_.v == 64) nop2(); };
        if (conv) {
#pragma unroll
            for (int i = 0; i < 3; ++i) dma_ws(vw, l16, uo + i * 4096, L_Q + i * CVS + 4 * HCS + 480 + wave * 1024);
            setm0(io); nop2();
            sfor<0, NPRE>(site1);
            asm volatile("s_waitcnt vmcnt(%0)\n\ts_barrier" ::"n"(NPRE) : "memory");
            STAMP(3);
            if (wave < 3) {
                f4 acc[8];
                v4u A[2][8], B[2][8];
                sfor<0, 24>(LAM(N_) { conv_read<8, N_.v, 0>(aa, ba, A[0], B[0]); });
                const u32 dd = wave == 0 ? D2[0] : wave == 1 ? D2[1] : D2[2];
                v4u ad = {dm.x & dd, dm.y & dd, dm.z & dd, dm.w & dd};
                pin(ad);
                FENCE();
                sfor<0, 5>(LAM(J_) { constexpr int j = J_.v ? J_.v + 3 : 0; mm16<true>(acc[j], ad, B[0][j]); });
                FENCE();
                sfor<0, 8 * 27>(LAM(Q_) {
                    constexpr int r = Q_.v / 27, n = Q_.v % 27, i = conv_ij(8, 0xf1, n) >> 3, j = conv_ij(8, 0xf1, n) & 7;
                    if constexpr (r && n == 0) waitl<9>();
                    if constexpr (r && n == 6) waitl<(r < 7 ? 6 : 0)>();
                    mm16<false>(acc[j], A[r & 1][i], B[r & 1][j - i]);
                    if constexpr (r < 7 && n < 24) conv_read<8, n, r + 1>(aa, ba, A[(r + 1) & 1], B[(r + 1) & 1]);
                    sfor<site_at(Q_.v), site_at(Q_.v + 1)>(site);
                    FENCE();
                });
                asm volatile("s_nop 7" : "+v"(acc[0]), "+v"(acc[4]), "+v"(acc[5]), "+v"(acc[6]), "+v"(acc[7]));
                const u32 so = W_Y + (ch0 + wave) * 256;
                const v2u y0 = y_val(acc[0]);
                const v4u y4 = y_pair(y_val(acc[4]), y_val(acc[5])), y6 = y_pair(y_val(acc[6]), y_val(acc[7]));
                bst2(y0, vw, yo, so, SC1);
                bst4(y4, vw, yp, so + 8 * YB, SC1);
                bst4(y6, vw, yp, so + 12 * YB, SC1);
            } else {
                f4 acc[2][4];
                v4u A[2][4], B[2][4], ad;
                v2u y1; v4u y2;
                sfor<0, 12>(LAM(N_) { conv_read<4, N_.v, 0>(aa, ba, A[0], B[0]); });
                FENCE();
                sfor<0, 3 * 8 * 9>(LAM(Q_) {
                    constexpr int cn = Q_.v / 72, r = Q_.v % 72 / 9, n = Q_.v % 9, i = conv_ij(4, 0xe, n) >> 3, j = conv_ij(4, 0xe, n) & 7;
                    constexpr int nc = r < 7 ? cn : cn + 1, nr = (r + 1) & 7;
                    if constexpr (r == 0 && n == 0) {
                        ad = (v4u){dm.x & D2[cn], dm.y & D2[cn], dm.z & D2[cn], dm.w & D2[cn]};
                        pin(ad);
                        waitl<0>();
                        nop2();
                        FENCE();
                        sfor<1, 4>(LAM(J_) { mm16<true>(acc[cn & 1][J_.v], ad, B[0][J_.v]); });
                    } else {
                        if constexpr (n == 0) waitl<3>();
                        if constexpr (n == 7) waitl<(nc < 3 ? 10 : 0)>();
                    }
                    mm16<false>(acc[cn & 1][j], A[r & 1][i], B[r & 1][j - i]);
                    if constexpr (nc < 3) {
                        if constexpr (n < 3) { conv_read<4, 2 * n, nr>(aa + nc * CVS, ba + nc * CVS, A[nr & 1], B[nr & 1]); conv_read<4, 2 * n + 1, nr>(aa + nc * CVS, ba + nc * CVS, A[nr & 1], B[nr & 1]); }
                        else conv_read<4, n + 3, nr>(aa + nc * CVS, ba + nc * CVS, A[nr & 1], B[nr & 1]);
                    }
                    if constexpr (cn > 0 && r == 1) {
                        const u32 so = W_Y + (ch0 + cn - 1) * 256;
                        if constexpr (n == 3) { pin(acc[(cn - 1) & 1][1]); y1 = y_val(acc[(cn - 1) & 1][1]); pin(y1); }
                        if constexpr (n == 4) bst2(y1, vw, yo, so + 2 * YB, SC1);
                        if constexpr (n == 5) { pin(acc[(cn - 1) & 1][2]); y1 = y_val(acc[(cn - 1) & 1][2]); pin(y1); }
                        if constexpr (n == 6) { pin(acc[(cn - 1) & 1][3]); y2 = y_pair(y1, y_val(acc[(cn - 1) & 1][3])); pin(y2); }
                        if constexpr (n == 7) bst4(y2, vw, yp, so + 4 * YB, SC1);
                    }
                    sfor<site_at(Q_.v), site_at(Q_.v + 1)>(site);
                    FENCE();
                });
                asm volatile("s_nop 7" : "+v"(acc[0][1]), "+v"(acc[0][2]), "+v"(acc[0][3]));
                const u32 so = W_Y + (ch0 + 2) * 256;
                y1 = y_val(acc[0][1]); y2 = y_pair(y_val(acc[0][2]), y_val(acc[0][3]));
                bst2(y1, vw, yo, so + 2 * YB, SC1);
                bst4(y2, vw, yp, so + 4 * YB, SC1);
            }
        } else {
            setm0(io); nop2();
            sfor<0, NSITE>(site1);
        }
        STAMP(14);
        asm volatile("" : "+v"(cp[0]), "+v"(cp[1]), "+v"(cp[2]), "+v"(cp[3]));
        *(LAS f2*)(L_EX + wave * 512 + l8) = (f2){(asf(cp[0].x) + asf(cp[1].x)) + (asf(cp[2].x) + asf(cp[3].x)), (asf(cp[0].y) + asf(cp[1].y)) + (asf(cp[2].y) + asf(cp[3].y))};
        barrier();
    }
    u32 yv[9];
    v4u wv[17];
    STAMP(15);

    sfor<0, 2>(LAM(L_) {
        constexpr int L = L_.v;
        int ln = lane;
        OPQV(ln); OPQV(l16); OPQV(l4);
        const int tl = ln & 31, h = ln >> 5, row = ln & 15, kg = ln >> 4, gq = row >> 2, rq = row & 3;
        const bool qa = (gq & 1) == (kg & 1);
        u32 aV_ = L_V + l16, aQ_ = L_Q + l16;
        u32 aKV = L_QT + vsel(qa, (rq >> 1) * 128 + (gq >> 1) * 64 + (rq & 1) * 32 + (kg >> 1) * 16, 400 - 16 * (kg >> 1));
        u32 aQq, aCS, aS;
        OPQV(aV_); OPQV(aKV); OPQV(aQ_);
        u32 nretry = 0;
        const u32 wb = W_WOT + Pg * WOG + wave * 36864, wlb = W_WOT + Pg * WOG + 4 * 36864 + (wave >> 1) * 9216;
        u32 wso = wb;
        auto wsite = LAM(N_) {
            constexpr int n = N_.v, k = (n < 36 ? n : n - 36) % 4;
            if constexpr (L == 0 && n < 45) {
                if constexpr (n == 36) wso = wlb; else if constexpr (n && k == 0) wso += 4096;
                if constexpr (k == 0) OPQS(wso);
                if constexpr (n < 28) ld4a<k * 1024, 144 + 4 * n>(l16, vw, wso);
                else ld4o<k * 1024>(wv[n - 28], l16, vw, wso);
            }
        };
        f16v aK; f4 qKV;
        float mean, al, rstd;
        v4u rV[4], cK[5];
        {
            const u32 xb = W_XF1 + (rg * 4 + wave) * 16 * XS;
            u32 xs[8];
            float ts;
            auto xsite = LAM(Q_) { if constexpr (L == 1) xop(IC<Q_.v / 4 * 5 + Q_.v % 4 + (Q_.v % 4 > 1)>{}, xs[Q_.v / 4]); };
            auto stat = [&]() __attribute__((always_inline)) {
                auto r = __builtin_amdgcn_permlane32_swap(asu(ts), asu(ts), false, false);
                mean = asf(r[0]) * (1.f / 576.f);
                const float var = __builtin_fmaxf(asf(r[1]) * (1.f / 576.f) - mean * mean, 0.f);
                rstd = __builtin_amdgcn_rsqf(var + 1e-5f);
                al = rstd * 1.4426950408889634f;
                pin2(mean, al); pin(rstd);
            };
            if constexpr (L == 1) {
#pragma unroll
                for (int j = 0; j < 8; ++j) { xs[j] = xb + (((p & 14) + 2 * j) & 15) * XS; OPQS(xs[j]); }
                FENCE();
                __builtin_amdgcn_s_sleep(XSL);
                sfor<0, 8>(LAM(J_) { xop(IC<5 * J_.v + 2>{}, xs[J_.v]); });
                sfor<0, UPA_>(xsite);
                waitv<UPA_>();
                u32 m, m2;
                asm volatile("v_max3_u32 %0, v224, v226, v228\n\tv_max3_u32 %1, v230, v232, v234\n\tv_max3_u32 %0, %0, v236, v238\n\tv_max3_u32 %1, %1, v240, v242\n\t"
                             "v_max3_u32 %0, %0, v244, v246\n\tv_max3_u32 %1, %1, v248, v250\n\tv_max3_u32 %0, %0, v252, v254\n\tv_max_u32 %0, %0, %1" : "=&v"(m), "=&v"(m2));
                if (__builtin_expect(bad(m), 0)) {
#pragma unroll 1
                    for (int sp = 0; sp < SPIN; ++sp) {
                        u32 mk = 0;
                        sfor<0, 8>(LAM(J_) { mk |= (u32)bad(mx3vv<224 + 4 * J_.v>(0u)) << J_.v; });
                        if (!mk) break;
                        __builtin_amdgcn_s_sleep(RSX);
                        sfor<0, 8>(LAM(J_) { if (mk >> J_.v & 1) xop(IC<5 * J_.v + 2>{}, xs[J_.v]); });
                        waitv<0>();
                        ++nretry;
                    }
                }
                STAMP(25);
                STAMPV(63, nretry);
                nretry = 0;
                sfor<UPA_, xissued(0)>(xsite);
                sfor<0, 8>(LAM(J_) { swapw<224 + 4 * J_.v, 128 + 2 * J_.v>(); if constexpr (J_.v) { addv<225, 225 + 4 * J_.v>(); addv<227, 227 + 4 * J_.v>(); } });
                ts = swapsum<224>();
                stat();
            }
            v4u r0[3], r1[3], ones = {~0u, ~0u, ~0u, ~0u};
            f2 cq[4];
            u32 cm, cs_;
#pragma unroll
            for (int s = 0; s < 3; ++s) { r0[s] = lds4(l16 + s * 1024); r1[s] = lds4(aKV + s * QTS); }
            if constexpr (L == 0) pin(ones);
            FENCE();
            sfor<0, 36>(LAM(SI) {
                constexpr int n = SI.v, s = kstep(n), s3 = kstep(n + 3), i = s >> 1;
                if constexpr (L == 0 && s < 32 && !(s & 1)) {
                    constexpr int j = i >> 1, later = 39 - 5 * j - (i & 1 ? 4 : 1) + 9 + (n >> 1);
                    waitv<later>();
                    FENCE();
                }
                if constexpr (L == 1 && s < 32 && !(s & 3)) {
                    constexpr int j = i >> 1;
                    static_assert(xissued(n) >= 4 * j + 4, "");
                    waitv<xissued(n) - 4 * j - 4>();
                    auto late = LAM(J_) { constexpr int k = J_.v; return bad(umax(mx3(rda<16 * k>(), rda<16 * k + 4>(), rda<16 * k + 8>()), rda<16 * k + 12>())); };
                    if (__builtin_expect(late(IC<j>{}), 0)) {
#pragma unroll 1
                        for (int sp = 0; sp < SPIN; ++sp) {
                            sfor<4 * j, 4 * j + 4>(xsite);
                            waitv<0>();
                            ++nretry;
                            if (!late(IC<j>{})) break;
                        }
                    }
                    FENCE();
                }
                if constexpr (L == 0 && n == 32) asm volatile("s_waitcnt vmcnt(16)\n\ts_barrier" ::: "memory");
                waitl<(n < 34 ? 4 : 6)>();
                mm32b<n == 0, 4 * s>(aK, r0[n % 3]);
                if constexpr (n + 3 < 36) r0[n % 3] = lds4(l16 + s3 * 1024); else rV[n - 33] = lds4(aV_ + (n - 33) * 1024);
                sfor<xissued(n), xissued(n + 1)>(xsite);
                if constexpr (L == 0 && s < 32 && (s & 3) == 2) swapw<224 + 2 * (i - 1), 127 + i>();
                if constexpr (L == 0 && s > 3 && s < 31 && ((s & 3) == 3 || s == 30)) { addv<225, 225 + 4 * (i >> 1)>(); addv<227, 227 + 4 * (i >> 1)>(); }
                if constexpr (L == 0 && n == 34) ts = swapsum<224>();
                if constexpr (n == 1) { aQq = L_QT + vsel(qa && rq < 2, 256 + (gq >> 1) * 64 + (rq & 1) * 32 + (kg >> 1) * 16, 384); pin(aQq); }
                if constexpr (n == 3) { aCS = L_CS + h * 224; aS = L_STG + (wave * 32 + tl) * STGS + 72 * h; pin2(aCS, aS); }
                if constexpr (L == 0) {
                    constexpr int k = n >> 1;
                    if constexpr (n & 1) {
                        if constexpr (k == 0 || k == 3) { cs_ = W_O + ((k ? 1024 : 0) + (rg * 4 + wave) * 16 + p) * OS; OPQS(cs_); }
                        if constexpr (k == 6) { cs_ = W_XF1 + ((rg * 4 + tw) * 16 + pw) * XS; OPQS(cs_); }
                        if constexpr (k == 9) { cs_ = W_KV + (p * 16 + rg) * 2816 + wave * 704; OPQS(cs_); }
                        if constexpr (k == 10) { cs_ += 256 * 2816; OPQS(cs_); }
                        if constexpr (k == 11) { cs_ = W_ST + ((rg * 4 + tw) * 16 + pw) * 256; OPQS(cs_); }
                        if constexpr (k < 9 && k % 3 < 2) st4<(k % 3) * 1024>(ones, l16, vw, cs_);
                        else if constexpr (k < 9) st4<2048>(ones, cm, vw, cs_);
                        else if constexpr (k < 12) st4<0>(ones, cm, vw, cs_);
                        else wsite(IC<k - 12>{});
                    } else {
                        if constexpr (k == 2) { cm = vsel(ln < 16, l16, OOB); pin(cm); }
                        if constexpr (k == 8) { cm = vsel(ln < 32, l16, OOB); pin(cm); }
                        if constexpr (k == 9) { cm = vsel(ln < 44, l16, OOB); pin(cm); }
                        if constexpr (k == 11) { cm = vsel(ln < 32, row * 16 + (kg & 1) * (1024 * 256), OOB); pin(cm); }
                        if constexpr (k == 13) { cq[0].x = (cq[0].x + cq[1].x) + (cq[2].x + cq[3].x); cq[0].y = (cq[0].y + cq[1].y) + (cq[2].y + cq[3].y); pin(cq[0]); }
                    }
                }
                if constexpr (L == 0 && n == 35) stat();
                FENCE();
                mm16b<n == 0, 4 * s>(qKV, r1[n % 3]);
                if constexpr (n + 3 < 36) r1[n % 3] = lds4(aKV + s3 * QTS); else cK[n - 33] = lds4(aCS + (n - 33) * 16);
                if constexpr (n == 33) cK[3] = lds4(aCS + 48);
                if constexpr (n == 34) cK[4] = lds4(aCS + 192);
                if constexpr (n == 35) rV[3] = lds4(aV_ + 3 * 1024);
                FENCE();
                if constexpr (L == 0 && n >= 20 && n < 24) { cq[n - 20] = *(const LAS f2*)(l8 + (L_EX + (n - 20) * 512)); FENCE(); }
                if constexpr (L == 0 && n == 27) { *(LAS f2*)(csd) = cq[0]; FENCE(); }
            });
        }
        STAMP(4 + 12 * L);
        if constexpr (L == 1) STAMPV(56, nretry);
        v4u rQ[4], rq4[3];
        u32 pa, pb, kso;
        {
            f16v aV;
            float t[18], e1[18], e2[18], mx, mn, c1, c2, s1, s2;
            v4u cV[4];
            u32 qo = L_Q + wave * 9216, qg = W_IMG + p * IMG + qo;
            sfor<0, 36>(LAM(SI) {
                constexpr int s = SI.v;
                if constexpr (s >= 2 && s < 34 && !(s & 1)) waitl<2>();
                mm32b<s == 0, 4 * s>(aV, rV[s & 3]);
                if constexpr (s == 2) { pin(aK); pin(qKV); }
                if constexpr (s + 4 < 36) rV[s & 3] = lds4(aV_ + (s + 4) * 1024); else cV[s - 32] = lds4(aCS + 64 + (s - 32) * 16);
                if constexpr (s < 18) {
                    if constexpr (s == 0 || s == 8 || s == 16) { if constexpr (s) { qo += 4096; qg += 4096; } OPQS(qo); OPQS(qg); setm0(qo); }
                    if constexpr (s & 1) dmai<((s >> 1) & 3) * 1024>(vw, l16, qg);
                } else wsite(IC<s - 12>{});
                if constexpr (s == 1) {
                    const u32 r0 = L_STG + (4 * (4 * (kg & 1) + gq) + (kg >> 1)) * STGS + 8 * rq;
                    pa = r0 + 72 * (wave >> 1); pb = r0 + (144 + 40 * (wave >> 1) + 32 * (wave & 1));
                    kso = W_KV + ((L * 16 + p) * 16 + rg) * 2816 + (wave >> 1) * 1376;
                    pin2(pa, pb); OPQS(kso);
                }
                if constexpr (s >= 2 && s < 5) {
#pragma unroll
                    for (int i = 6 * (s - 2); i < 6 * (s - 2) + 6; ++i) { const u32 cv = cK[i >> 2][i & 3]; t[i] = __builtin_fmaf(-mean, asf(cv), i < 16 ? aK[i] : qKV[i - 16]); }
                }
                if constexpr (s == 5) { mx = __builtin_fmaxf(__builtin_fmaxf(t[0], t[1]), t[2]); mn = __builtin_fminf(__builtin_fminf(t[0], t[1]), t[2]);
#pragma unroll
                    for (int i = 3; i < 7; i += 2) { mx = __builtin_fmaxf(__builtin_fmaxf(mx, t[i]), t[i + 1]); mn = __builtin_fminf(__builtin_fminf(mn, t[i]), t[i + 1]); } }
                if constexpr (s == 6) {
#pragma unroll
                    for (int i = 7; i < 13; i += 2) { mx = __builtin_fmaxf(__builtin_fmaxf(mx, t[i]), t[i + 1]); mn = __builtin_fminf(__builtin_fminf(mn, t[i]), t[i + 1]); } }
                if constexpr (s == 7) {
#pragma unroll
                    for (int i = 13; i < 17; i += 2) { mx = __builtin_fmaxf(__builtin_fmaxf(mx, t[i]), t[i + 1]); mn = __builtin_fminf(__builtin_fminf(mn, t[i]), t[i + 1]); }
                    mx = __builtin_fmaxf(mx, t[17]); mn = __builtin_fminf(mn, t[17]); c1 = -al * mx; c2 = al * mn; }
                if constexpr (s >= 8 && s < 32) {
#pragma unroll
                    for (int q = (3 * (s - 8)) / 2; q < (3 * (s - 7)) / 2; ++q) {
                        const int i = q >> 1;
                        if (q & 1) { e2[i] = __builtin_amdgcn_exp2f(__builtin_fmaf(t[i], -al, c2)); s2 = i ? s2 + e2[i] : e2[i]; }
                        else { e1[i] = __builtin_amdgcn_exp2f(__builtin_fmaf(t[i], al, c1)); s1 = i ? s1 + e1[i] : e1[i]; }
                    }
                }
                if constexpr (s == 32) { s1 = __builtin_amdgcn_rcpf(s1); s2 = __builtin_amdgcn_rcpf(s2); }
                if constexpr (s >= 33) {
                    constexpr int i = s - 33;
                    *(LAS v2u*)(aS + 8 * i) = (v2u){pk(e1[4 * i] * s1, e1[4 * i + 1] * s1), pk(e1[4 * i + 2] * s1, e1[4 * i + 3] * s1)};
                }
                FENCE();
            });
            *(LAS v2u*)(aS + 24) = (v2u){pk(e1[12] * s1, e1[13] * s1), pk(e1[14] * s1, e1[15] * s1)};
            *(LAS v2u*)(aS + 32) = (v2u){pk(e1[16] * s1, e1[17] * s1), pk(e2[0] * s2, e2[1] * s2)};
#pragma unroll
            for (int i = 0; i < 4; ++i) *(LAS v2u*)(aS + 40 + 8 * i) = (v2u){pk(e2[4 * i + 2] * s2, e2[4 * i + 3] * s2), pk(e2[4 * i + 4] * s2, e2[4 * i + 5] * s2)};
            FENCE();
            pin(aV);
            const u32 aSv = aS + 144 - 32 * h;
            const float mr = -mean * rstd;
#pragma unroll
            for (int i = 0; i < 4; ++i) {
                const u32 q0 = cV[i].x, q1 = cV[i].y, q2 = cV[i].z, q3 = cV[i].w;
                *(LAS v2u*)(aSv + 8 * i) = (v2u){pk(__builtin_fmaf(mr, asf(q0), aV[4 * i] * rstd), __builtin_fmaf(mr, asf(q1), aV[4 * i + 1] * rstd)),
                                                 pk(__builtin_fmaf(mr, asf(q2), aV[4 * i + 2] * rstd), __builtin_fmaf(mr, asf(q3), aV[4 * i + 3] * rstd))};
            }
            const u32 q2 = cK[4].z, q3 = cK[4].w;
            *(LAS v2u*)(aSv + 32) = (v2u){pk(__builtin_fmaf(mr, asf(q2), qKV[2] * rstd), __builtin_fmaf(mr, asf(q3), qKV[3] * rstd)), 0x3f80u};
        }
        waitbar<(L ? 0 : 18)>();
        STAMP(5 + 12 * L);
        {
            f4 c[3];
            v4u B[4], A[4][3];
            auto rd = LAM(N_) {
                constexpr int ks = N_.v >> 2, f = N_.v & 3;
                if constexpr (f == 0) B[ks] = tr2(pb + ks * 32 * STGS, pb + (ks * 32 + 2) * STGS);
                else A[ks][f - 1] = tr2(pa + ks * 32 * STGS + 32 * (f - 1), pa + (ks * 32 + 2) * STGS + 32 * (f - 1));
            };
            sfor<0, 7>(LAM(N_) { rd(N_); FENCE(); });
            if constexpr (L == 0) { if (tid == 0) bst2((v2u){(u32)ep, (u32)(ep >> 32)}, vw, 0, W_F2 + (rg * 16 + p) * 128, SC1); }
            FENCE();
            sfor<0, 12>(LAM(I_) {
                constexpr int ks = I_.v / 3, ft = I_.v % 3;
                mm16<ks == 0>(c[ft], A[ks][ft], B[ks]);
                if constexpr (I_.v < 9) rd(IC<7 + I_.v>{});
                else { rQ[I_.v - 9] = lds4(aQ_ + (I_.v - 9) * 1024); rq4[I_.v - 9] = lds4(aQq + (I_.v - 9) * QTS); }
                FENCE();
            });
            rQ[3] = lds4(aQ_ + 3 * 1024);
            FENCE();
            pad3<5>(c[0], c[1], c[2]);
            bst4((v4u){pk(c[0][0], c[0][1]), pk(c[0][2], c[0][3]), pk(c[1][0], c[1][1]), pk(c[1][2], c[1][3])}, vw, kvA, kso, SC1);
            bst2((v2u){pk(c[2][0], c[2][1]), pk(c[2][2], c[2][3])}, vw, kvB, kso, SC1);
        }
        FENCE();
        STAMP(6 + 12 * L);
        v4u pq[5], kp[12], sel;
        u32 kvo, kv3, aF, aQ4, f2o, oso;
        u32 kb[4];
        v2u yfl;
        auto flags_ok = [&](u64 m) __attribute__((always_inline)) { return (__builtin_amdgcn_ballot_w64(yfl.x != (u32)ep || yfl.y != (u32)(ep >> 32)) & m) == 0; };
        auto flags_wait = [&](u64 m) __attribute__((always_inline)) {
            if (__builtin_expect(!flags_ok(m), 0)) {
#pragma unroll 1
                for (int sp = 0; sp < SPIN; ++sp) {
                    __builtin_amdgcn_s_sleep(1);
                    ld2<0>(yfl, f2o, vw, W_F2);
                    waitp<0>(yfl);
                    if (flags_ok(m)) break;
                }
            }
        };
        {
            f16v aQ; f4 qQ;
            v4u cQ[5];
            sfor<0, 36>(LAM(SI) {
                constexpr int s = SI.v;
                if constexpr (s >= 1 && s < 34) waitl<4>();
                mm32b<s == 0, 4 * s>(aQ, rQ[s & 3]);
                if constexpr (s + 4 < 36) rQ[s & 3] = lds4(aQ_ + (s + 4) * 1024); else cQ[s - 32] = lds4(aCS + 128 + (s - 32) * 16);
                if constexpr (s < 31 && s % 3 != 2) wsite(IC<24 + s - s / 3>{});
                if constexpr (s == 2) { kvo = kg * 2816 + wave * 256 + row * 16; kv3 = vsel(wave * 16 + row < 44, kvo + 2048, OOB); pin2(kvo, kv3); }
                if constexpr (s == 26) { for (int i = 0; i < 4; ++i) { kb[i] = W_KV + ((L * 16 + p) * 16 + 4 * i) * 2816; OPQS(kb[i]); } }
                if constexpr (s == 5 || s == 8) {
#pragma unroll
                    for (int i = (s - 5) / 3 * 2; i < (s - 5) / 3 * 2 + 2; ++i) sel[i] = row == 2 * i ? 0x3f80u : row == 2 * i + 1 ? 0x3f800000u : 0u;
                    if constexpr (s == 8) pin(sel);
                }
                if constexpr (s == 11) { const int hr = (tl >> 2) & 1, d = (tl & 3) + 4 * (tl >> 3); aF = vsel(h == hr, (hr * 19 + d) * 80 + L_KVA, L_Z); pin(aF); }
                if constexpr (s == 14) { aQ4 = vsel(kg == gq && rq < 3, ((gq >> 1) * 19 + 16 + rq) * 80 + L_KVA, L_Z); pin(aQ4); }
                if constexpr (s == 17) { oso = W_O + (L * 1024 + (rg * 4 + wave) * 16 + p) * OS; OPQS(oso); }
                if constexpr (L == 0 && s == 20) { f2o = vsel(kg == 1, rg * 16 + row, row * 16 + (kg ? pw : p)) * 128; pin(f2o); }
                if constexpr (L == 0 && s == 23) ld2<0>(yfl, f2o, vw, W_F2);
                FENCE();
                mm16b<s == 0, 4 * s>(qQ, rq4[s % 3]);
                if constexpr (s + 3 < 36) rq4[s % 3] = lds4(aQq + (s + 3) * QTS);
                if constexpr (s == 33) cQ[4] = lds4(aCS + 208);
                FENCE();
            });
            STAMP(49 + L);
            if constexpr (L == 0) { waitp<5>(yfl); flags_wait(0xffffull); }
#pragma unroll
            for (int i = 0; i < 4; ++i) {
                kp[i] = bld4(vw, kvo, kb[i], SC1);
                kp[4 + i] = bld4(vw, kvo + 1024, kb[i], SC1);
                kp[8 + i] = bld4(vw, kv3, kb[i], SC1);
            }
            FENCE();
            pin(aQ); pin(qQ);
            float t[18]; u32 ph[18];
#pragma unroll
            for (int i = 0; i < 18; ++i) { const u32 cv = cQ[i >> 2][i & 3]; t[i] = __builtin_fmaf(-mean, asf(cv), i < 16 ? aQ[i] : qQ[i - 16]); }
            hedgehog(t, al, ph);
#pragma unroll
            for (int i = 0; i < 5; ++i) { pq[i] = (v4u){ph[4 * i], ph[4 * i + 1], i < 4 ? ph[4 * i + 2] : 0u, i < 4 ? ph[4 * i + 3] : 0u}; pin(pq[i]); }
            FENCE();
            STAMP(7 + 12 * L);
            u32 m = umax(kp[0].x, kp[0].z);
#pragma unroll
            for (int i = 1; i < 12; ++i) m = mx3(m, kp[i].x, kp[i].z);
            nretry = 0;
            if (__builtin_expect(bad(m), 0)) {
#pragma unroll 1
                for (int sp = 0; sp < SPIN; ++sp) {
                    u32 mk = 0;
                    waitv<0>();
#pragma unroll
                    for (int i = 0; i < 4; ++i) {
                        pin2(kp[i], kp[4 + i]); pin(kp[8 + i]);
                        mk |= (u32)bad(mx3(mx3(kp[i].x, kp[i].z, kp[4 + i].x), mx3(kp[4 + i].z, kp[8 + i].x, kp[8 + i].z), 0u)) << i;
                    }
                    if (!mk) break;
                    __builtin_amdgcn_s_sleep(RSK);
#pragma unroll
                    for (int i = 0; i < 4; ++i)
                        if (mk >> i & 1) { ld4<0>(kp[i], kvo, vw, kb[i]); ld4<1024>(kp[4 + i], kvo, vw, kb[i]); ld4<0>(kp[8 + i], kv3, vw, kb[i]); }
                    ++nretry;
                }
            }
            STAMPV(57 + L, nretry);
            f4 a[3];
            sfor<0, 12>(LAM(I_) { constexpr int i = I_.v / 3, j = I_.v % 3; mm16<i == 0>(a[j], sel, kp[4 * j + i]); });
            pad3<7>(a[0], a[1], a[2]);
#pragma unroll
            for (int j = 0; j < 3; ++j) *(LAS v2u*)(kvw[j]) = (v2u){pk(a[j][0], a[j][1]), pk(a[j][2], a[j][3])};
            barrier();
            STAMP(8 + 12 * L);
        }
        {
            v4u A[5], Aq[5];
#pragma unroll
            for (int c = 0; c < 5; ++c) Aq[c] = lds4(aQ4 + 16 * c);
#pragma unroll
            for (int c = 0; c < 5; ++c) A[c] = lds4(aF + 16 * c);
            FENCE();
            f16v o; f4 oq;
            float rd; u32 ol;
            sfor<0, 5>(LAM(C_) { mm16<C_.v == 0>(oq, Aq[C_.v], pq[C_.v]); });
            sfor<0, 5>(LAM(C_) {
                mm32<C_.v == 0>(o, A[C_.v], pq[C_.v]);
                if constexpr (C_.v == 3) { pad<4>(oq); rd = __builtin_amdgcn_rcpf(oq[2] + 1e-6f); pin(rd); }
                if constexpr (C_.v == 4) { ol = pk(oq[0] * rd, oq[1] * rd); pin(ol); }
                FENCE();
            });
            pad<8>(o);
            bst4((v4u){pk(o[0] * rd, o[1] * rd), pk(o[2] * rd, o[3] * rd), pk(o[4] * rd, o[5] * rd), pk(o[6] * rd, o[7] * rd)}, vw, l16, oso, SC1);
            bst4((v4u){pk(o[8] * rd, o[9] * rd), pk(o[10] * rd, o[11] * rd), pk(o[12] * rd, o[13] * rd), pk(o[14] * rd, o[15] * rd)}, vw, l16 + 1024, oso, SC1);
            bst1(ol, vw, l4 + 2048, oso, SC1);
        }
        FENCE();
        STAMP(9 + 12 * L);
        u32 aB, ae, ao, yo, ys;
        f16v aW; float xl0, xl1;
        if constexpr (L == 0) {
            flags_wait(~0ull);
            ys = W_Y + rg * YB + 36 * pw * 256 + tw * 64; yo = (ln >> 2) * 256 + rq * 16;
            OPQS(ys); OPQV(yo);
        }
#pragma unroll
        for (int i = 0; i < 8; ++i) { aW[2 * i] = bf_lo(xr[i]); aW[2 * i + 1] = bf_hi(xr[i]); }
        xl0 = bf_lo(xr[8]); xl1 = bf_hi(xr[8]);
        aB = L_Q + ((wave >> 1) * 18 + (kg & 1)) * 1024 + ((kg >> 1) * 32 + 16 * (wave & 1) + row) * 16;
        ae = L_EX + (kg & 1) * 1024 + ((2 * (wave >> 1) + h) * 16 + row) * 16 + 8 * (wave & 1);
        ao = aQ_ + wave * 8192;
        pin(aW); pin2(xl0, xl1); pin2(aB, ae); pin(ao);
        {
            u32 so[4];
#pragma unroll
            for (int i = 0; i < 4; ++i) { so[i] = W_O + (L * 1024 + (rg * 4 + tw) * 16 + 4 * wave + i) * OS; OPQS(so[i]); }
            v4u og[8]; u32 lo[4];
            FENCE();
            STAMP(43 + 3 * L);
            waitv<0>();
            STAMP(44 + 3 * L);
            __builtin_amdgcn_s_sleep(OSL);
            auto ldo = [&]() __attribute__((always_inline)) {
#pragma unroll
                for (int i = 0; i < 8; ++i) og[i] = bld4(vw, l16 + (i & 1) * 1024, so[i >> 1], SC1);
            };
            auto ldl = [&]() __attribute__((always_inline)) {
#pragma unroll
                for (int i = 0; i < 4; ++i) lo[i] = bld1(vw, l4 + 2048, so[i], SC1);
            };
            auto chk = [&]() __attribute__((always_inline)) {
                u32 m = mx3(umax(lo[0], lo[1]), lo[2], lo[3]);
#pragma unroll
                for (int i = 0; i < 8; i += 2) m = mx3(m, og[i].x, og[i + 1].x);
                return bad(m);
            };
            ldo(); ldl();
            FENCE();
            nretry = 0;
            if (__builtin_expect(chk(), 0)) {
#pragma unroll 1
                for (int sp = 0; sp < SPIN; ++sp) {
                    u32 mk = 0;
                    waitv<0>();
#pragma unroll
                    for (int i = 0; i < 4; ++i) {
                        pin2(og[2 * i], og[2 * i + 1]); pin(lo[i]);
                        mk |= (u32)bad(mx3(og[2 * i].x, og[2 * i + 1].x, lo[i])) << i;
                    }
                    if (!mk) break;
                    __builtin_amdgcn_s_sleep(RSO);
#pragma unroll
                    for (int i = 0; i < 4; ++i)
                        if (mk >> i & 1) { ld4<0>(og[2 * i], l16, vw, so[i]); ld4<1024>(og[2 * i + 1], l16, vw, so[i]); ld1<2048>(lo[i], l4, vw, so[i]); }
                    ++nretry;
                }
            }
            STAMPV(59 + L, nretry);
            STAMP(45 + 3 * L);
#pragma unroll
            for (int i = 0; i < 8; ++i) *(LAS v4u*)(ao + i * 1024) = og[i];
            *(LAS v4u*)(aQ_ + (32 + wave) * 1024) = (v4u){lo[0], lo[1], lo[2], lo[3]};
            barrier();
        }
        STAMP(10 + 12 * L);
        u32 sto = W_ST + (L * 1024 + (rg * 4 + tw) * 16) * 256;
        {
            f4 qW;
            v4u rb_[4], rl[9];
            v2u e0, e1;
            float s1, s2;
            u32 le;
#pragma unroll
            for (int i = 0; i < 4; ++i) { rl[i] = lds4(aB + i * 2048); rb_[i] = lds4(aQ_ + i * 1024); }
            FENCE();
            sfor<0, 36>(LAM(SI) {
                constexpr int s = SI.v;
                if constexpr (s < 28) mm32a<false, 144 + 4 * s>(aW, rb_[s & 3]); else mm32<false>(aW, wv[s - 28], rb_[s & 3]);
                if constexpr (s + 4 < 36) rb_[s & 3] = lds4(aQ_ + (s + 4) * 1024);
                if constexpr (s < 5) rl[4 + s] = lds4(aB + (4 + s) * 2048);
                if constexpr (s == 12) { pad<0>(qW); *(LAS f4*)(l16 + (L_EX + wave * 1024)) = qW; }
                if constexpr (s == 14) OPQS(sto);
                if constexpr (s == 24) { le = l4; pin(le); }
                if constexpr (L == 0 && s >= 16 && s < 22 && !(s & 1)) dma_ws(vw, yo, ys + (s - 16) * 2048, L_STG + wave * 3072 + (s - 16) * 512);
                FENCE();
                if constexpr (s < 9) { mm16<s == 0>(qW, wv[8 + s], rl[s]); FENCE(); }
                if constexpr (s == 30) { barrier(); e0 = lds2(ae); e1 = lds2(ae + 2048); FENCE(); }
                if constexpr (s == 33) { xl0 += asf(e0.x) + asf(e1.x); xl1 += asf(e0.y) + asf(e1.y); s1 = xl0 + xl1; s2 = __builtin_fmaf(xl0, xl0, xl1 * xl1); pin2(s1, s2); FENCE(); }
            });
            STAMP(11 + 12 * L);
            pad<11>(aW);
            float s3 = aW[0], s4 = aW[0] * aW[0];
#pragma unroll
            for (int i = 1; i < 16; ++i) { if (i & 1) { s1 += aW[i]; s2 = __builtin_fmaf(aW[i], aW[i], s2); } else { s3 += aW[i]; s4 = __builtin_fmaf(aW[i], aW[i], s4); } }
            bst1(asu(halves_sum(s1 + s3, s2 + s4)), vw, le, sto + pw * 256, SC1);
        }
        FENCE();
        {
            float t;
            u32 xso = L ? (rg * 128 + tw * 32) * 1152 + 72 * pw : W_XF1 + ((rg * 4 + tw) * 16 + pw) * XS, oo = tl * 1152 + 36 * h, one2 = 0x3f803f80u;
            OPQS(xso); OPQS(one2);
            if constexpr (L == 1) OPQV(oo);
            u32 yf[18], lS;
            if constexpr (L == 0) {
                const u32 ya = (18 * h + gq) * 64 + 32 * (kg & 1) + 8 * rq + (L_STG + wave * 3072);
                lS = tl * 16 + h * 8;
                pin(lS);
                waitv<1>();
#pragma unroll
                for (int i = 0; i < 5; ++i) {
                    const v2u v = __builtin_bit_cast(v2u, __builtin_amdgcn_ds_read_tr16_b64_v4i16((LAS s4*)(ya + 256 * i)));
                    yv[2 * i] = v.x; if (i < 4) yv[2 * i + 1] = v.y;
                }
            }
#pragma unroll
            for (int i = 0; i < 9; ++i) { yf[2 * i] = yv[i] << 16; yf[2 * i + 1] = yv[i] & 0xffff0000u; pin2(yf[2 * i], yf[2 * i + 1]); }
            FENCE();
            waitv<0>();
            if constexpr (SSL) __builtin_amdgcn_s_sleep(SSL);
            nretry = 0;
#pragma unroll 1
            for (int sp = 0; sp < SPIN; ++sp) {
                asm volatile("" : "+v"(l4)::"memory");
                u32 e[16];
#pragma unroll
                for (int i = 0; i < 16; ++i) e[i] = bld1(vw, l4 + i * 256, sto, SC1);
                FENCE();
                u32 m = umax(e[0], e[1]);
                t = asf(e[0]) + asf(e[1]);
#pragma unroll
                for (int i = 2; i < 16; i += 2) { m = mx3(m, e[i], e[i + 1]); t += asf(e[i]) + asf(e[i + 1]); pin2(m, t); }
                if (!bad(m)) break;
                ++nretry;
                __builtin_amdgcn_s_sleep(RSS);
            }
            STAMP(12 + 12 * L);
            STAMPV(61 + L, nretry);
            auto r = __builtin_amdgcn_permlane32_swap(asu(t), asu(t), false, false);
            const float m1 = asf(r[0]) * (1.f / 576.f);
            const float r1 = __builtin_amdgcn_rsqf(__builtin_fmaxf(asf(r[1]) * (1.f / 576.f) - m1 * m1, 0.f) + 1e-5f), mr = -m1 * r1;
#pragma unroll
            for (int i = 0; i < 9; ++i) {
                const float a = i < 8 ? aW[2 * i] : xl0, c = i < 8 ? aW[2 * i + 1] : xl1;
                xr[i] = pk(__builtin_fmaf(__builtin_fmaf(a, r1, mr), asf(yf[2 * i]), a), __builtin_fmaf(__builtin_fmaf(c, r1, mr), asf(yf[2 * i + 1]), c));
                if constexpr (L == 0) {
                    if (i == 3) { bst4((v4u){xr[0], xr[1], xr[2], xr[3]}, vw, l16, xso, SC1); FENCE(); }
                    if (i == 7) { bst4((v4u){xr[4], xr[5], xr[6], xr[7]}, vw, l16 + 1024, xso, SC1); FENCE(); }
                } else {
                    if (i == 3) { bst4((v4u){xr[0], xr[1], xr[2], xr[3]}, ro, oo, xso, 0); FENCE(); }
                    if (i == 7) { bst4((v4u){xr[4], xr[5], xr[6], xr[7]}, ro, oo + 16, xso, 0); FENCE(); }
                    if (i == 8) bst1(xr[8], ro, oo + 32, xso, 0);
                }
            }
            if constexpr (L == 0) {
                float s1 = 0.f, s2 = 0.f;
#pragma unroll
                for (int j = 0; j < 9; ++j) { s1 = dot2(xr[j], one2, s1); s2 = dot2(xr[j], xr[j], s2); }
                bst2((v2u){xr[8], asu(halves_sum(s1, s2))}, vw, lS + 2048, xso, SC1);
                STAMP(13);
            } else {
                if (b == 0 && tid == 0) bst2((v2u){(u32)ep, (u32)(ep >> 32)}, vw, 0, W_E, SC1);
            }
        }
    });
    STAMP_END();
}
#if defined(__HIP_DEVICE_COMPILE__)
#pragma clang attribute pop
#endif

extern "C" void kernel_launch(void* const* d_in, const int* in_sizes, int n_in, void* d_out, int out_size, void* d_ws, size_t ws_size, hipStream_t stream) {
    (void)in_sizes; (void)n_in; (void)out_size; (void)ws_size;
    hipFuncSetAttribute((const void*)k_main, hipFuncAttributeMaxDynamicSharedMemorySize, L_END);
    const u32 f = 0x00020000u, n = 2048 * 1152;
    Args a = {{d_in[5], n, f}, {d_in[6], n, f}, {{d_in[1], 576 * 3456, f}, {d_in[3], 576 * 1152, f}}, {{d_in[2], 32 * 648, f}, {d_in[2], 0, f}}, {d_in[0], n, f}, {d_ws, W_END, f}, {d_out, n, f}, d_in[7], d_in[8], d_in[9]};
    k_main<<<256, 256, L_END, stream>>>(a);
}
```

```cpp
#include <hip/hip_runtime.h>
#include <stdint.h>
#pragma clang diagnostic ignored "-Wint-to-pointer-cast"
#pragma clang diagnostic ignored "-Wunused-value"
#pragma clang diagnostic ignored "-Winline-asm"
#if defined(__HIP_DEVICE_COMPILE__)
#pragma clang attribute push(__attribute__((target("no-packed-fp32-ops"))), apply_to = function)
#endif
typedef unsigned short u16;
typedef unsigned u32;
typedef unsigned long long u64;
typedef u32 v4u __attribute__((ext_vector_type(4)));
typedef u32 v2u __attribute__((ext_vector_type(2)));
typedef float f4 __attribute__((ext_vector_type(4)));
typedef float f2 __attribute__((ext_vector_type(2)));
typedef float f16v __attribute__((ext_vector_type(16)));
typedef __bf16 b8 __attribute__((ext_vector_type(8)));
typedef __bf16 b2 __attribute__((ext_vector_type(2)));
typedef short s4 __attribute__((ext_vector_type(4)));
#define LAS __attribute__((address_space(3)))
#define DEV __device__ __forceinline__
#define SC1 16

constexpr u32 QTS = 416, STGS = 232, CVS = 21216, HCS = 4160;
constexpr u32 L_K = 0, L_V = 36864, L_QT = 73728, L_Q = 89088, L_STG = 125952, L_KVA = 155648, L_CS = 158784, L_EX = 159232, L_END = 163328;
constexpr u32 P_KS = 0, P_VS = 10240, P_WS = 20480, P_XS = 31232, P_PH = 42496, P_LS = 55296, L_Z = L_KVA + 38 * 80;
constexpr u32 IMG = L_STG, XS = 2560, OS = 2304, YB = 576 * 256, CSB = 16 * 576;
constexpr u32 WOG = 4 * 36864 + 18432;
constexpr u32 W_IMG = 0, W_WOT = W_IMG + 16 * IMG, W_CSP = W_WOT + 4 * WOG, W_UT = W_CSP + 16 * CSB,
              W_XF0 = W_UT + 576 * 4096, W_XF1 = W_XF0 + 1024 * XS, W_O = W_XF1 + 1024 * XS, W_Y = W_O + 2 * 1024 * OS, W_KV = W_Y + 576 * 4096,
              W_ST = W_KV + 2 * 256 * 2816, W_F = W_ST + 2 * 1024 * 256, W_F2 = W_F + 256 * 128, W_E = W_F2 + 256 * 128, W_END = W_E + 128, W_DBG = W_END;
constexpr u32 OOB = 0x7ffffff0u;
constexpr int SPIN = 1 << 14;
#define REM_ 17000
constexpr u32 REM = REM_;
constexpr u32 CODE = 45000;
#define STOLD
#define OSL 8
#define RSX 1
#define RSK 1
#define RSO 1
#define RSS 1
#define XSL 9
#define SSL 0

DEV float asf(u32 v) { return __builtin_bit_cast(float, v); }
DEV u32 asu(float v) { return __builtin_bit_cast(u32, v); }
DEV float bf_lo(u32 v) { return asf(v << 16); }
DEV float bf_hi(u32 v) { return asf(v & 0xffff0000u); }
DEV u32 pk(float lo, float hi) { b2 r = __builtin_convertvector((f2){lo, hi}, b2); return __builtin_bit_cast(u32, r); }
DEV float rb(float f) { return bf_lo(pk(f, f)); }
DEV float dot2(u32 a, u32 b, float c) { return __builtin_amdgcn_fdot2_f32_bf16(__builtin_bit_cast(b2, a), __builtin_bit_cast(b2, b), c, false); }
DEV u32 umin(u32 a, u32 b) { return a < b ? a : b; }
DEV u32 umax(u32 a, u32 b) { return a > b ? a : b; }
DEV u32 mx3(u32 a, u32 b, u32 c) { return umax(umax(a, b), c); }
template <bool Z> DEV void mm32(f16v& c, v4u a, v4u b) {
    if constexpr (Z) asm volatile("v_mfma_f32_32x32x16_bf16 %0, %1, %2, 0" : "=&v"(c) : "v"(a), "v"(b));
    else asm volatile("v_mfma_f32_32x32x16_bf16 %0, %1, %2, %0" : "+v"(c) : "v"(a), "v"(b));
}
template <bool Z> DEV void mm16(f4& c, v4u a, v4u b) {
    if constexpr (Z) asm volatile("v_mfma_f32_16x16x32_bf16 %0, %1, %2, 0" : "=&v"(c) : "v"(a), "v"(b));
    else asm volatile("v_mfma_f32_16x16x32_bf16 %0, %1, %2, %0" : "+v"(c) : "v"(a), "v"(b));
}
template <bool Z, int R> DEV void mm32b(f16v& c, v4u a) {
    if constexpr (Z) asm volatile("v_mfma_f32_32x32x16_bf16 %0, %1, a[%c2:%c3], 0" : "=&v"(c) : "v"(a), "n"(R), "n"(R + 3));
    else asm volatile("v_mfma_f32_32x32x16_bf16 %0, %1, a[%c2:%c3], %0" : "+v"(c) : "v"(a), "n"(R), "n"(R + 3));
}
template <bool Z, int R> DEV void mm16b(f4& c, v4u a) {
    if constexpr (Z) asm volatile("v_mfma_f32_16x16x32_bf16 %0, %1, a[%c2:%c3], 0" : "=&v"(c) : "v"(a), "n"(R), "n"(R + 3));
    else asm volatile("v_mfma_f32_16x16x32_bf16 %0, %1, a[%c2:%c3], %0" : "+v"(c) : "v"(a), "n"(R), "n"(R + 3));
}
template <bool Z, int R> DEV void mm32a(f16v& c, v4u b) {
    if constexpr (Z) asm volatile("v_mfma_f32_32x32x16_bf16 %0, a[%c2:%c3], %1, 0" : "=&v"(c) : "v"(b), "n"(R), "n"(R + 3));
    else asm volatile("v_mfma_f32_32x32x16_bf16 %0, a[%c2:%c3], %1, %0" : "+v"(c) : "v"(b), "n"(R), "n"(R + 3));
}
template <int N, class T> DEV void pad(T& c) { asm volatile("s_nop %1" : "+v"(c) : "n"(N)); }
template <class T> DEV void pin(T& c) { asm volatile("" : "+v"(c)); }
DEV void nop2() { asm volatile("s_nop 1"); }
template <int R> DEV u32 rda() { u32 v; asm volatile("v_accvgpr_read_b32 %0, a%c1" : "=v"(v) : "n"(R)); return v; }
template <int R> DEV void wra(u32 v) { asm volatile("v_accvgpr_write_b32 a%c1, %0" ::"v"(v), "n"(R)); }
__device__ void bst4(v4u, v4u, u32, u32, int) __asm("llvm.amdgcn.raw.buffer.store.v4i32");
__device__ void bst2(v2u, v4u, u32, u32, int) __asm("llvm.amdgcn.raw.buffer.store.v2i32");
__device__ void bst1(u32, v4u, u32, u32, int) __asm("llvm.amdgcn.raw.buffer.store.i32");
__device__ v4u bld4(v4u, u32, u32, int) __asm("llvm.amdgcn.raw.buffer.load.v4i32");
__device__ v2u bld2(v4u, u32, u32, int) __asm("llvm.amdgcn.raw.buffer.load.v2i32");
__device__ u32 bld1(v4u, u32, u32, int) __asm("llvm.amdgcn.raw.buffer.load.i32");
struct Buf { const void* p; u32 n, f; };
struct Args { Buf k, v, w[2], ph[2], x, ws, out; const void *sk, *sh, *sD; };
template <class T> DEV void sld(T& d, const void* ka, u32 off) {
    if constexpr (sizeof(T) == 16) asm volatile("s_load_dwordx4 %0, %1, %2" : "=s"(d) : "s"(ka), "s"(off));
    else asm volatile("s_load_dwordx2 %0, %1, %2" : "=s"(d) : "s"(ka), "s"(off));
}
template <int O, class T> DEV void sldi(T& d, const void* ka) {
    if constexpr (sizeof(T) == 16) asm volatile("s_load_dwordx4 %0, %1, %2" : "=s"(d) : "s"(ka), "n"(O));
    else asm volatile("s_load_dwordx2 %0, %1, %2" : "=s"(d) : "s"(ka), "n"(O));
}
DEV v4u lds4(u32 a) { return *(const LAS v4u*)a; }
DEV v2u lds2(u32 a) { return *(const LAS v2u*)a; }
DEV v2u lds2v(u32 a) { return *(const volatile LAS v2u*)a; }
DEV void barrier() { asm volatile("s_waitcnt lgkmcnt(0)\n\ts_barrier" ::: "memory"); }
template <int N> DEV void waitv() { asm volatile("s_waitcnt vmcnt(%0)" ::"n"(N) : "memory"); }
DEV void dma_in(const void* g, u32 voff, u32 lds) {
    asm volatile("s_mov_b32 m0, %2\n\ts_nop 0\n\tglobal_load_lds_dwordx4 %0, %1" ::"v"(voff), "s"(g), "s"(lds) : "m0", "memory");
}
DEV void dma_b(v4u rs, u32 voff, u32 lds) {
    asm volatile("s_mov_b32 m0, %2\n\ts_nop 0\n\tbuffer_load_dwordx4 %0, %1, 0 offen lds" ::"v"(voff), "s"(rs), "s"(lds) : "m0", "memory");
}
DEV void dma_s(v4u rs, u32 voff, u32 so, u32 lds) {
    asm volatile("s_mov_b32 m0, %3\n\ts_nop 0\n\tbuffer_load_dwordx4 %0, %1, %2 offen lds" ::"v"(voff), "s"(rs), "s"(so), "s"(lds) : "m0", "memory");
}
DEV void dma_ws(v4u rs, u32 voff, u32 so, u32 lds) {
    asm volatile("s_mov_b32 m0, %3\n\ts_nop 0\n\tbuffer_load_dwordx4 %0, %1, %2 offen sc1 lds" ::"v"(voff), "s"(rs), "s"(so), "s"(lds) : "m0", "memory");
}
template <int IM> DEV void gld4(v4u& d, u32 voff, const void* g) { asm volatile("global_load_dwordx4 %0, %1, %2 offset:%3" : "=v"(d) : "v"(voff), "s"(g), "n"(IM) : "memory"); }
DEV void gld2(v2u& d, u32 voff, const void* g) { asm volatile("global_load_dwordx2 %0, %1, %2" : "=v"(d) : "v"(voff), "s"(g) : "memory"); }
DEV void mm16k(f4& c, v2u a, v2u b) { asm volatile("v_mfma_f32_16x16x16_bf16 %0, %1, %2, 0" : "=&v"(c) : "v"(a), "v"(b)); }
DEV void mm16n(f4& c, v4u a, v4u b) { asm volatile("s_nop 1\n\tv_mfma_f32_16x16x32_bf16 %0, %1, %2, 0" : "=&v"(c) : "v"(a), "v"(b)); }
template <class A, class B> DEV void pin2(A& a, B& b) { asm volatile("" : "+v"(a), "+v"(b)); }
template <int IM> DEV void ld4(v4u& d, u32 voff, v4u rs, u32 so) { asm volatile("buffer_load_dwordx4 %0, %1, %2, %3 offen offset:%4 sc1" : "+v"(d) : "v"(voff), "s"(rs), "s"(so), "n"(IM) : "memory"); }
template <int IM> DEV void ld1(u32& d, u32 voff, v4u rs, u32 so) { asm volatile("buffer_load_dword %0, %1, %2, %3 offen offset:%4 sc1" : "+v"(d) : "v"(voff), "s"(rs), "s"(so), "n"(IM) : "memory"); }
template <int IM> DEV void ld4m(v4u& d, u32 voff, v4u rs, u32 so, u64 m) {
    asm volatile("s_mov_b64 exec, %5\n\tbuffer_load_dwordx4 %0, %1, %2, %3 offen offset:%4 sc1\n\ts_mov_b64 exec, -1" : "+v"(d) : "v"(voff), "s"(rs), "s"(so), "n"(IM), "s"(m) : "memory");
}
template <int IM, int R> DEV void ld4a(u32 voff, v4u rs, u32 so) { asm volatile("buffer_load_dwordx4 a[%c4:%c5], %0, %1, %2 offen offset:%3 sc1" ::"v"(voff), "s"(rs), "s"(so), "n"(IM), "n"(R), "n"(R + 3) : "memory"); }
template <int IM, int R> DEV void ld4v(u32 voff, v4u rs, u32 so) { asm volatile("buffer_load_dwordx4 v[%c4:%c5], %0, %1, %2 offen offset:%3 sc1" ::"v"(voff), "s"(rs), "s"(so), "n"(IM), "n"(R), "n"(R + 3) : "memory"); }
template <int R> DEV u32 mx3v(u32 a, u32 b) { u32 r; asm volatile("v_max3_u32 %0, %1, %2, v%c3" : "=v"(r) : "v"(a), "v"(b), "n"(R)); return r; }
template <int R> DEV u32 mx3vv(u32 a) { u32 r; asm volatile("v_max3_u32 %0, %1, v%c2, v%c3" : "=v"(r) : "v"(a), "n"(R), "n"(R + 2)); return r; }
template <int R, int A> DEV void swapw() { asm volatile("v_permlane32_swap_b32 v%c0, v%c1\n\tv_accvgpr_write_b32 a%c2, v%c0\n\tv_accvgpr_write_b32 a%c3, v%c1" ::"n"(R), "n"(R + 2), "n"(A), "n"(A + 1)); }
template <int R, int R2> DEV void addv() { asm volatile("v_add_f32 v%c0, v%c0, v%c1" ::"n"(R), "n"(R2)); }
template <int R> DEV float swapsum() { float r; asm volatile("s_nop 1\n\tv_permlane32_swap_b32 v%c1, v%c2\n\tv_add_f32 %0, v%c1, v%c2" : "=v"(r) : "n"(R + 1), "n"(R + 3)); return r; }
template <int IM, int R> DEV void ld1a(u32 voff, v4u rs, u32 so) { asm volatile("buffer_load_dword a%c4, %0, %1, %2 offen offset:%3 sc1" ::"v"(voff), "s"(rs), "s"(so), "n"(IM), "n"(R) : "memory"); }
template <int IM, int R> DEV void ld4am(u32 voff, v4u rs, u32 so, u64 m) {
    asm volatile("s_mov_b64 exec, %6\n\tbuffer_load_dwordx4 a[%c4:%c5], %0, %1, %2 offen offset:%3 sc1\n\ts_mov_b64 exec, -1" ::"v"(voff), "s"(rs), "s"(so), "n"(IM), "n"(R), "n"(R + 3), "s"(m) : "memory");
}
template <int IM> DEV void ld2(v2u& d, u32 voff, v4u rs, u32 so) { asm volatile("buffer_load_dwordx2 %0, %1, %2, %3 offen offset:%4 sc1" : "=v"(d) : "v"(voff), "s"(rs), "s"(so), "n"(IM) : "memory"); }
template <int IM> DEV void ld4o(v4u& d, u32 voff, v4u rs, u32 so) { asm volatile("buffer_load_dwordx4 %0, %1, %2, %3 offen offset:%4 sc1" : "=v"(d) : "v"(voff), "s"(rs), "s"(so), "n"(IM) : "memory"); }
template <int IM> DEV void ldh(u32& d, u32 voff, v4u rs, u32 so) { asm volatile("buffer_load_short_d16_hi %0, %1, %2, %3 offen offset:%4 sc1" : "+v"(d) : "v"(voff), "s"(rs), "s"(so), "n"(IM) : "memory"); }
template <int IM> DEV void st4(v4u d, u32 voff, v4u rs, u32 so) { asm volatile("buffer_store_dwordx4 %0, %1, %2, %3 offen offset:%4 sc1" ::"v"(d), "v"(voff), "s"(rs), "s"(so), "n"(IM) : "memory"); }
DEV void setm0(u32 v) { asm volatile("s_mov_b32 m0, %0" ::"s"(v) : "m0", "memory"); }
template <int IM> DEV void dmai(v4u rs, u32 voff, u32 so) { asm volatile("buffer_load_dwordx4 %0, %1, %2 offen offset:%3 sc1 lds" ::"v"(voff), "s"(rs), "s"(so), "n"(IM) : "memory"); }
DEV u32 pka(float a, float b) { u32 r; asm volatile("v_cvt_pk_bf16_f32 %0, %1, %2" : "=v"(r) : "v"(a), "v"(b)); return r; }
template <int N> DEV void waitl() { __builtin_amdgcn_s_waitcnt(0xc07f | (N << 8)); }
DEV u32 vsel(bool c, u32 a, u32 b) { asm volatile("" : "+v"(a)); asm volatile("" : "+v"(b)); return c ? a : b; }
DEV u32 ssel(bool c, u32 a, u32 b) { asm volatile("" : "+s"(a)); asm volatile("" : "+s"(b)); return c ? a : b; }
template <int N, class T> DEV void waitp(T& d) { asm volatile("s_waitcnt vmcnt(%1)" : "+v"(d) : "n"(N) : "memory"); }
template <int N> DEV void waitbar() { asm volatile("s_waitcnt vmcnt(%0) lgkmcnt(0)\n\ts_barrier" ::"n"(N) : "memory"); }
template <int N> DEV void pad3(f4& a, f4& b, f4& c) { asm volatile("s_nop %3" : "+v"(a), "+v"(b), "+v"(c) : "n"(N)); }
template <int I> struct IC { static constexpr int v = I; };
template <int B, int E, class F> DEV void sfor(F&& f) { if constexpr (B < E) { f(IC<B>{}); sfor<B + 1, E>(f); } }
#define LAM(I) [&](auto I) __attribute__((always_inline))
#define FENCE() __builtin_amdgcn_sched_barrier(0)
#define OPQV(x) asm volatile("" : "+v"(x))
#define OPQS(x) asm volatile("" : "+s"(x))
DEV float halves_sum(float a, float b) {
    auto r = __builtin_amdgcn_permlane32_swap(asu(a), asu(b), false, false);
    return asf(r[0]) + asf(r[1]);
}
DEV bool bad(u32 m) { return __builtin_amdgcn_ballot_w64(m == ~0u) != 0; }
#define STAMP_INIT()
template <int I> DEV void mark() { asm volatile("; MARK %0" ::"n"(I)); }
#define STAMP(i) mark<(i)>()
#define STAMP_END()
#define STAMPV(i, v)

DEV void hedgehog(const float (&t)[18], float a, u32 (&ph)[18]) {
    float mx = t[0], mn = t[0];
#pragma unroll
    for (int i = 1; i < 18; ++i) { mx = __builtin_fmaxf(mx, t[i]); mn = __builtin_fminf(mn, t[i]); }
    const float c1 = -a * mx, c2 = a * mn;
    float e1[18], e2[18], s1 = 0.f, s2 = 0.f;
#pragma unroll
    for (int i = 0; i < 18; ++i) {
        e1[i] = __builtin_amdgcn_exp2f(__builtin_fmaf(t[i], a, c1)); s1 = i ? s1 + e1[i] : e1[i];
        e2[i] = __builtin_amdgcn_exp2f(__builtin_fmaf(t[i], -a, c2)); s2 = i ? s2 + e2[i] : e2[i];
    }
    const float r1 = __builtin_amdgcn_rcpf(s1), r2 = __builtin_amdgcn_rcpf(s2);
#pragma unroll
    for (int i = 0; i < 9; ++i) { ph[i] = pk(e1[2 * i] * r1, e1[2 * i + 1] * r1); ph[9 + i] = pk(e2[2 * i] * r2, e2[2 * i + 1] * r2); }
}
DEV v4u tr2(u32 a0, u32 a1) {
    s4 lo = __builtin_amdgcn_ds_read_tr16_b64_v4i16((LAS s4*)a0), hi = __builtin_amdgcn_ds_read_tr16_b64_v4i16((LAS s4*)a1);
    v2u l = __builtin_bit_cast(v2u, lo), h = __builtin_bit_cast(v2u, hi);
    return (v4u){l.x, l.y, h.x, h.y};
}
DEV v2u y_val(f4 acc) { return (v2u){pk(acc[0], acc[1]), pk(acc[2], acc[3])}; }
DEV v4u y_pair(v2u a, v2u b) {
    auto r0 = __builtin_amdgcn_permlane16_swap(a.x, b.x, false, false), r1 = __builtin_amdgcn_permlane16_swap(a.y, b.y, false, false);
    return (v4u){r0[0], r1[0], r0[1], r1[1]};
}
template <int N, int n, int r>
DEV void conv_read(u32 aa, u32 ba, v4u (&A)[N], v4u (&B)[N]) {
    constexpr int i = n / 3, k = n % 3;
    if constexpr (k == 0) B[i] = lds4(ba + 64 * (8 * i - r + 7));
    else { const v2u v = lds2(aa + 2 + 64 * (r + 8 * i) + 8 * (k - 1)); if constexpr (k == 1) { A[i].x = v.x; A[i].y = v.y; } else { A[i].z = v.x; A[i].w = v.y; } }
}
constexpr int conv_ij(int N, int MASK, int n) {
    for (int d = 0; d < N; ++d) for (int i = 0; i < N; ++i) for (int j = i; j < N; ++j)
        if (((MASK >> j) & 1) && (i > j - i ? i : j - i) == d && n-- == 0) return i * 8 + j;
    return -1;
}
#define NPRE_ 10
#define SEND_ 212
#define UPA_ 8
#define UPO_ 12
constexpr int NPRE = NPRE_, NSITE = 13 + 4 + 40 + 9, SITE_END = SEND_;
constexpr int site_at(int q) { return q <= SITE_END ? NPRE + (q * (NSITE - NPRE) + SITE_END - 1) / SITE_END : NSITE; }
constexpr int xissued(int n) { return UPO_ + (5 * n + 3) / 4 < 32 ? UPO_ + (5 * n + 3) / 4 : 32; }
constexpr int kstep(int n) { return n % 9 < 8 ? 8 * (n / 9) + n % 9 : 32 + n / 9; }

__global__ void __launch_bounds__(256, 1) k_main(Args) {
    const void* ka = (const void*)(uintptr_t)__builtin_amdgcn_kernarg_segment_ptr();
    v4u rk, rv, rm, rq, rp, rx, vw, ro;
    const char *sk, *sh, *sD;
    int tid = __builtin_amdgcn_workitem_id_x(), b = __builtin_amdgcn_workgroup_id_x();
    sldi<0>(rk, ka); sldi<16>(rv, ka); sldi<144>(sk, ka); sldi<112>(vw, ka); sldi<96>(rx, ka); sldi<128>(ro, ka); sldi<152>(sh, ka); sldi<160>(sD, ka);
    OPQS(b); OPQV(tid);
    int wave = __builtin_amdgcn_readfirstlane(tid >> 6);
    sld(rm, ka, (wave >> 1) * 16); sld(rq, ka, (b & 24) == 24 ? 48 : 32); sld(rp, ka, 64 + (b & 8) * 2);
    OPQS(b); OPQS(wave); OPQV(tid);
    {
        u64 pc;
        asm volatile("s_getpc_b64 %0" : "=s"(pc));
        const u32 o = umin(tid * 8 + (b >> 3) * 2048, CODE - 512);
        asm volatile("s_mov_b32 m0, %2\n\ts_nop 0\n\tglobal_load_lds_dword %0, %1" ::"v"(o), "s"(pc), "s"(L_EX) : "m0", "memory");
    }
    STAMP_INIT();
    asm volatile("" ::: "a255", "v255");
    u64 ep;
    v2u sw0, sw1;
    {
        const int lane = tid & 63, c = lane & 15, cg = ((b >> 4) & 6) + ((b >> 3) & 1), tg = 4 * (b & 7) + 2 * (b >> 7) + ((b >> 4) & 1);
        u32 s0 = c * 8 + (wave * 128 + cg * 576), s1 = (c & 7) * 8 + cg * 576, vo[5], lo[5];
        const u32 l0 = ((lane * 7282u) >> 16) * 1008 + lane * 16, t0 = (64 * tg + 7 * wave) * 1152 + cg * 144;
#pragma unroll
        for (int i = 0; i < 5; ++i) {
            const int isv = i < 2 ? 0 : i > 2 ? 1 : wave >> 1;
            vo[i] = l0 + (t0 + (28 * i - 3 - 67 * isv) * 1152); lo[i] = P_KS + wave * 1008 + i * 4032 + isv * (P_VS - 10080);
            OPQV(vo[i]); OPQS(lo[i]);
        }
        pin2(s0, s1);
        FENCE();
        asm volatile("s_waitcnt lgkmcnt(0)" : "+s"(rk), "+s"(rv), "+s"(rm), "+s"(rq), "+s"(rp), "+s"(rx), "+s"(vw), "+s"(ro), "+s"(sk), "+s"(sh), "+s"(sD));
        STAMP(40);
#pragma unroll
        for (int i = 0; i < 5; ++i) dma_b(i < 2 ? rk : i > 2 ? rv : rm, vo[i], lo[i]);
        asm volatile("global_load_dwordx2 %0, %1, %2" : "=v"(sw0) : "v"(s0), "s"(sk) : "memory");
        asm volatile("global_load_dwordx2 %0, %1, %2 offset:512" : "=v"(sw1) : "v"(s1), "s"(sk) : "memory");
        sld(ep, (const void*)(((u64)vw.y << 32) | vw.x), W_E);
    }
    OPQS(b); OPQS(wave); OPQV(tid);
    const int lane = tid & 63, rg = 2 * (b & 7) + (b >> 7), p = (b >> 3) & 15;
    const int Pg = p >> 2, tw = p & 3, pw = 4 * Pg + wave, ci = (rg + p) & 15;
    const bool conv = ci < 12;
    u32 l16 = lane * 16, l4 = lane * 4;
    OPQV(l16); OPQV(l4);
    const int ch0 = 36 * p + 3 * ci;
    u32 xr[9], kvw[3], kvA, kvB, l8, lP, l16B, cpo, csd;
    {
        const int sec = p & 3, cg = 2 * Pg + (p & 1), tg = 2 * rg + ((p >> 1) & 1);
        const int c = lane & 15;
        FENCE();
        STAMP(41);
        const v4u z4 = {0, 0, 0, 0};
        u32 mk0, mk1, ua0, ua1, uo0, uo1, aw, al_, ap, d0_, d1_, d2_, cso, axs, aid, azc;
        v2u Bi; v4u Sel;
        const bool s3 = sec == 3;
        const u32 wf_ = rg >> 2, jl = rg & 3;
        auto wdma = [&]() __attribute__((always_inline)) {
            const u32 wst = s3 ? 1152 : 3456, cb = (sec < 2 ? (sec + 1) * 1152 : 0) + Pg * 288;
            const u32 lw = ((lane * 3641u) >> 16) * (wst - 288) + l16, w0 = (36 * rg + 3 * wave) * wst + cb;
            const u32 r4 = (lane * 43u) >> 8, sg = ((s3 ? 0 : (4 * Pg + wf_) & 14) + 4 * jl + wave) & 15;
            const u32 ll = vsel(lane < 24, r4 * (wst - 96) + (r4 >> 1) * (16 * wst) + l16 + ((36 * sg + 16) * wst + cb + ((72 * wf_) & ~15u)), OOB);
            const u32 lx = ((lane * 3450u) >> 16) * 848 + l16 + ((128 * rg + 32 * tw + 3 * wave) * 1152 + Pg * 288);
            const u32 lp = vsel(lane < 54, l16 - ((lane * 21846u) >> 16) * 12 + (Pg * 5184 + wave * 1296), OOB);
#pragma unroll
            for (int i = 0; i < 3; ++i) dma_b(rq, lw + (w0 + 12 * i * wst), P_WS + wave * 864 + i * 3456);
            dma_b(rq, ll, P_LS + wave * 1024);
#pragma unroll
            for (int i = 0; i < 2; ++i) dma_s(rp, lp, i * 648, P_PH + wave * 3072 + i * 1536);
#pragma unroll
            for (int i = 0; i < 3; ++i) dma_s(rx, lx, 12 * i * 1152, P_XS + wave * 912 + i * 3648);
        };
        {
            const u32 g = lane >> 4, m = lane & 31, kh = lane >> 5;
            const u32 hm = g == (c >> 2) ? ((c & 1) ? 0xffff0000u : 0xffffu) : 0u;
            mk0 = (c & 2) ? 0u : hm; mk1 = (c & 2) ? hm : 0u;
            Bi = (v2u){mk0 & 0x3f803f80u, mk1 & 0x3f803f80u}; Sel = (v4u){Bi.x, Bi.y, Bi.x, Bi.y};
            const u32 r0 = (8 * (c >> 2) + (c & 3)) * 144;
            ua0 = r0 + 8 * g + (P_KS + 32 * wave);
            ua1 = r0 + 32 * 144 * kh + 8 * (g & 1) + (P_KS + 128 + 576 * (wave & 1));
            uo0 = c * 4096 + 16 * g;
            uo1 = (c & 7) * 4096 + 64 * (c >> 3) + 16 * g + (wave < 2 ? 64 * 4096 + 8 - 8 * wave : OOB - (1u << 20));
            pin2(mk0, mk1); pin2(Bi, Sel); pin2(ua0, ua1); pin2(uo0, uo1);
            const u32 hD = (m >> 2) & 1;
            aw = (18 * hD + (m & 3) + 4 * (m >> 3)) * 288 + 16 * kh + (P_WS + ((72 * wave) & ~15u));
            al_ = ((((m >> 1) & 1) + 2 * ((m >> 3) & 1)) * 1024 + (2 * hD + (m & 1)) * 96) * (wave == wf_) + 16 * kh + (wave == wf_ ? P_LS : L_Z);
            ap = (8 * kh + (c >> 2)) * 48 + 32 * (g & 1) + 8 * (c & 3) + (P_PH + (64 * wave - 4 * (wave & 1)) * 48);
            pin2(aw, al_); pin(ap);
            const u32 ib = W_IMG + pw * IMG, ob = W_WOT + Pg * WOG;
            const u32 bA = ssel(s3, ob + wave * 36864, ib + sec * L_V + (sec >> 1) * (L_Q - 2 * L_V));
            const u32 bB = ssel(s3, ob + 4 * 36864 + 128 * (wave >> 1) + 32 * (wave & 1), ib + L_QT + 128 * sec);
            const u32 dsB = s3 ? 256 : QTS, drB = s3 ? 1024 : 2 * QTS, ri = (rg - (s3 ? 0 : pw & 14)) & 15;
            const bool lo = m < 16;
            const u32 lp = vsel(lo, ((m & 3) + 8 * (m >> 2) + 32 * kh) * 16 + bA, vsel(m < 18, (m & 1) * (s3 ? 16 : 32) + kh * (s3 ? 512 : 16) + bB, OOB));
            const u32 ds = vsel(lo, 1024, dsB), dr = vsel(lo, 2048, drB);
            d0_ = lp + ri * dr; d1_ = d0_ + ds;
            d2_ = umax(lp + (16 + (jl >> 1)) * dr + (jl & 1) * ds, wave == wf_ ? 0u : OOB);
            cso = vsel(m < (s3 ? 0 : 18), (18 * kh + m) * 4 + (W_CSP + pw * CSB + rg * 576 + sec * 192), OOB);
            axs = m * 304 + 36 * kh + (P_XS + 72 * wave);
            aid = vsel(lane < 36 * (sec & 1), lane * 50 + (lane >= 18 ? 1536 - 900 : 0) + (P_PH + wave * 3072), L_END - 16);
            azc = vsel(lane < (sec ? 0 : 6), (lane & 1) * 16 + ((lane >> 1) + (lane < 4 ? 2 * rg : 30 + jl)) * QTS + (ib + L_QT + 384), OOB);
            pin2(d0_, d1_); pin2(d2_, cso); pin2(axs, aid); pin(azc);
            *(LAS v2u*)(vsel(tid < 38, tid * 80 + (L_KVA + 72), L_END - 16)) = (v2u){0, 0};
            *(LAS v4u*)(l16 + kh * 1024 + (P_PH + 1024 + wave * 3072)) = z4;
            *(LAS v4u*)(vsel(lane < (wave ? 30 : 6), l16 + ssel(wave, L_Q + 4 * HCS - CVS + wave * CVS, L_Z), L_END - 16)) = z4;
        }
        FENCE();
        STAMP(42);
        wdma();
        FENCE();
        STAMP(26);
        asm volatile("s_waitcnt vmcnt(9)" : "+v"(sw0), "+v"(sw1)::"memory");
        v2u Bd[2][4];
#pragma unroll
        for (int s = 0; s < 2; ++s)
#pragma unroll
            for (int j = 0; j < 4; ++j) {
                const v2u sw = s ? sw1 : sw0;
                const u32 wd = __builtin_amdgcn_perm(0u, j < 2 ? sw.y : sw.x, (j & 1) ? 0x01000100u : 0x03020302u);
                Bd[s][j] = (v2u){wd & mk0, wd & mk1};
                pin(Bd[s][j]);
            }
        u32 so_u = W_UT + 128 * tg + cg * 72 * 4096, so_u2 = so_u + wave * 65536;
        OPQS(so_u); OPQS(so_u2);
        FENCE();
        barrier();
        STAMP(27);
        {
            u32 uo[5][2];
            v2u ka[5][5]; f4 pr[5][5], sm[5];
            v4u bb[2];
#pragma unroll
            for (int n = 0; n < 25; ++n) {
                const int T = n < 10 ? n >> 1 : n % 5, j = n < 10 ? n & 1 : n / 5;
                ka[T][j] = lds2v((T < 4 ? ua0 + (32 * (T >> 1) + 4 * (T & 1)) * 144 : ua1) + (j < 4 ? j * 144 : P_VS));
            }
            FENCE();
            auto P = [&](auto T_, auto J_) __attribute__((always_inline)) { constexpr int T = T_.v, j = J_.v; mm16k(pr[T][j], ka[T][j], j < 4 ? Bd[T == 4][j] : Bi); };
            sfor<0, 5>(LAM(T_) { P(T_, IC<0>{}); P(T_, IC<1>{}); });
            STAMP(52);
            sfor<0, 16>(LAM(I_) {
                constexpr int I = I_.v, T = I % 5, ph = I / 5 + 1, U = (I + 4) % 5;
                if constexpr (I < 15) {
                    f4& a = ph == 1 ? pr[T][0] : sm[T];
                    const u32 w0 = pka(a[0], a[1]), w1 = pka(a[2], a[3]), w2 = pka(pr[T][ph][0], pr[T][ph][1]), w3 = pka(pr[T][ph][2], pr[T][ph][3]);
                    bb[I & 1] = (v4u){w0, w1, w2, w3};
                    P(IC<T>{}, IC<ph + 1>{});
                } else {
                    uo[0][0] = pka(sm[0][0], sm[0][1]); uo[0][1] = pka(sm[0][2], sm[0][3]);
                }
                if constexpr (I > 0) mm16<true>(sm[U], Sel, bb[(I - 1) & 1]);
            });
            STAMP(53);
            sfor<0, 5>(LAM(T_) {
                constexpr int T = T_.v;
                u32 w0, w1;
                if constexpr (T) { w0 = pka(sm[T][0], sm[T][1]); w1 = pka(sm[T][2], sm[T][3]); } else { w0 = uo[0][0]; w1 = uo[0][1]; }
                pin(pr[T][4]);
                FENCE();
                uo[T][0] = pk(bf_hi(w1) * pr[T][4][3], bf_lo(w1) * pr[T][4][2]);
                uo[T][1] = pk(bf_hi(w0) * pr[T][4][1], bf_lo(w0) * pr[T][4][0]);
                if constexpr (T == 1 || T == 3) bst4((v4u){uo[T][0], uo[T][1], uo[T - 1][0], uo[T - 1][1]}, vw, uo0 + 32 * (T - 1), so_u2, SC1);
                else if constexpr (T == 4) bst2((v2u){uo[4][0], uo[4][1]}, vw, uo1, so_u, SC1);
                else pin2(uo[T][0], uo[T][1]);
                FENCE();
            });
        }
        STAMP(28);
        asm volatile("s_waitcnt vmcnt(3)\n\ts_barrier" ::: "memory");
        STAMP(54);
        {
            u32 one2 = 0x3f803f80u;
            OPQS(one2);
            *(LAS u16*)(aid) = 0x3f80;
            v4u bw[2][2], wa[2][3];
            bw[0][0] = tr2(ap, ap + 192); wa[0][0] = lds4(aw); wa[1][0] = lds4(al_);
            bw[0][1] = tr2(ap + 768, ap + 960); wa[0][1] = lds4(aw + 32); wa[1][1] = lds4(al_ + 32);
            bw[1][0] = tr2(ap + 1440, ap + 1440 + 192);
            bw[1][1] = tr2(ap + 1440 + 768, ap + 1440 + 960); wa[0][2] = lds4(aw + 64); wa[1][2] = lds4(al_ + 64);
            FENCE();
#pragma unroll
            for (int j = 0; j < 9; ++j) xr[j] = *(const LAS u32*)(axs + 4 * j);
            FENCE();
            f16v d[2][2];
#pragma unroll
            for (int i = 0; i < 4; ++i) { mm32<true>(d[i >> 1][i & 1], wa[i & 1][i >> 1], bw[i >> 1][0]); mm32<false>(d[i >> 1][i & 1], wa[i & 1][(i >> 1) + 1], bw[i >> 1][1]); }
            FENCE();
            STAMP(55);
            {
                float s1 = 0.f, s2 = 0.f;
#pragma unroll
                for (int j = 0; j < 9; ++j) { s1 = dot2(xr[j], one2, s1); s2 = dot2(xr[j], xr[j], s2); }
                const u32 so = W_XF0 + ((rg * 4 + tw) * 16 + pw) * XS;
                bst4((v4u){xr[0], xr[1], xr[2], xr[3]}, vw, l16, so, SC1);
                bst4((v4u){xr[4], xr[5], xr[6], xr[7]}, vw, l16 + 1024, so, SC1);
                l8 = 2 * l4;
                bst2((v2u){xr[8], asu(halves_sum(s1, s2))}, vw, (lane & 31) * 16 + (lane >> 5) * 8 + 2048, so, SC1);
                bst4(z4, vw, azc, 0, SC1);
            }
            FENCE();
            float cs[2];
#pragma unroll
            for (int hh = 0; hh < 2; ++hh) {
                asm volatile("" : "+v"(d[hh][0]), "+v"(d[hh][1]));
                u32 w[12];
#pragma unroll
                for (int j = 0; j < 12; ++j) w[j] = pk(d[hh][j >> 3][2 * (j & 7)], d[hh][j >> 3][2 * (j & 7) + 1]);
                bst4((v4u){w[0], w[1], w[2], w[3]}, vw, d0_ + 64 * hh, 0, SC1);
                bst4((v4u){w[4], w[5], w[6], w[7]}, vw, d1_ + 64 * hh, 0, SC1);
                bst4((v4u){w[8], w[9], w[10], w[11]}, vw, d2_ + 64 * hh, 0, SC1);
                cs[hh] = 0.f;
#pragma unroll
                for (int j = 0; j < 12; ++j) cs[hh] = dot2(w[j], one2, cs[hh]);
                FENCE();
            }
            bst1(asu(halves_sum(cs[0], cs[1])), vw, cso, 0, SC1);
        }
        v4u hd[3]; v2u he[3], hz;
        {
            const char* gp = sh + umin(ch0, 573) * 4096;
            const u32 o1 = tid * 16, o2 = (o1 + 16) & 4095;
            gld2(hz, umin(lane >> 3, 2) * 4096, gp);
#pragma unroll
            for (int i = 0; i < 3; ++i) { gld4<0>(hd[i], o1, gp + i * 4096); gld2(he[i], o2, gp + i * 4096); }
        }
        STAMP(29);
        asm volatile("s_waitcnt vmcnt(7)\n\ts_barrier" ::: "memory");
        OPQS(ep);
        ep += 1;
        const v2u epv = {(u32)ep, (u32)(ep >> 32)};
        if (tid == 0) bst2(epv, vw, 0, W_F + (rg * 16 + p) * 128, SC1);
        STAMP(1);
        {
            const u32 e = (lane >> 4) & 1, kg = lane >> 4;
#pragma unroll
            for (int j = 0; j < 3; ++j) {
                const u32 pi = 16 * (wave + 4 * j) + c, hh = pi >= 86, ip = pi - 86 * hh, q = ip - 72, s = 2 * (ip - 84) + e;
                u32 v = vsel(ip < 64, (ip & 15) * 80 + (ip >> 4) * 8 + 32 * e, (2 * ip + e) * 80 + 64 - 128 * 80);
                v = vsel(ip < 72, v, (16 + (q >> 2)) * 80 + (q & 3) * 8 + 32 * e);
                v = vsel(ip < 84, v, s * 80 + 16 * 80 + 64);
                kvw[j] = vsel(lane >= 32 || pi >= 172 || s == 3, L_EX + l8, v + hh * 1520 + L_KVA);
                OPQV(kvw[j]);
            }
            const bool dt = wave & 1;
            kvA = dt ? vsel(c < 3, 1152 + (c * 4 + kg) * 16, OOB) : l16;
            kvB = vsel(lane < (dt ? 4 : 16), l8 + (dt ? 1344 : 1024), OOB);
            l16B = l16 + XS; lP = (lane & 31) * 16 + (lane >> 5) * XS;
            const u32 sc = (lane * 57u) >> 10, r = lane - 18 * sc, hh = r >= 9, pr = r - 9 * hh;
            cpo = vsel(lane < 54, sc * (192 - 144) + l8, OOB);
            csd = vsel(lane < 54, hh * 224 + vsel(pr < 8, sc * 64 + pr * 8, 192 + sc * 8) + L_CS, L_EX + 2048 + l8);
            pin2(kvA, kvB); pin2(l16B, lP); pin2(cpo, csd); pin(l8);
        }
        FENCE();
        if (wave) waitv<0>(); else waitv<1>();
        STAMP(51);
        if (conv) {
            u32 o = L_Q + 32 + tid * 16;
            OPQV(o);
#pragma unroll
            for (int i = 0; i < 3; ++i) {
                asm volatile("" : "+v"(hd[i]), "+v"(he[i]));
                const u32 d0 = hd[i].x, d1 = hd[i].y, d2 = hd[i].z, d3 = hd[i].w, d4 = he[i].x, d5 = he[i].y;
                const u32 a0 = __builtin_amdgcn_alignbit(d1, d0, 16), a1 = __builtin_amdgcn_alignbit(d2, d1, 16), a2 = __builtin_amdgcn_alignbit(d3, d2, 16),
                          a3 = __builtin_amdgcn_alignbit(d4, d3, 16), a4 = __builtin_amdgcn_alignbit(d5, d4, 16);
                *(LAS v4u*)(o + i * CVS) = hd[i];
                *(LAS v4u*)(o + i * CVS + HCS) = (v4u){a0, a1, a2, a3};
                *(LAS v4u*)(o + i * CVS + 2 * HCS) = (v4u){d1, d2, d3, d4};
                *(LAS v4u*)(o + i * CVS + 3 * HCS) = (v4u){a1, a2, a3, a4};
            }
            asm volatile("" : "+v"(hz));
            const u32 k = (lane >> 1) & 3;
            const u64 t = (lane & 1) && k ? (((u64)hz.y << 32) | hz.x) << (64 - 16 * k) : 0;
            if (tid < 24) *(LAS v4u*)(L_Q + (lane >> 3) * CVS + k * HCS + (lane & 1) * 16) = (v4u){0, 0, (u32)t, (u32)(t >> 32)};
        }
    }
    auto xop = [&](auto Q_, u32 so) __attribute__((always_inline)) {
        constexpr int j = Q_.v / 5, k = Q_.v % 5;
        if constexpr (k == 0) ld4a<0, 16 * j>(l16, vw, so);
        if constexpr (k == 1) ld4a<1024, 16 * j + 4>(l16, vw, so);
        if constexpr (k == 2) ld4v<2048, 224 + 4 * j>(lP, vw, so);
        if constexpr (k == 3) ld4a<0, 16 * j + 8>(l16B, vw, so);
        if constexpr (k == 4) ld4a<1024, 16 * j + 12>(l16B, vw, so);
    };
    {
        u32 D2[3];
#pragma unroll
        for (int i = 0; i < 3; ++i) { const u32 cc = umin(ch0, 573) + i, t = ((const __attribute__((address_space(4))) u32*)(uintptr_t)sD)[cc >> 1]; D2[i] = (cc & 1 ? t >> 16 : t & 0xffffu) * 0x10001u; OPQS(D2[i]); }
        const int bq = lane & 15, g = lane >> 4, cc = (bq + 1) & 3;
        u32 aa = L_Q + cc * HCS + 2 * (bq - 8 * g + 24 - cc), ba = L_Q + 4 * HCS + 480 + 2 * (16 * bq + 8 * g - 16) - 7 * 64;
        u32 yo = (bq >> 3) * YB + (bq & 7) * 32 + 8 * g, yp = (bq >> 3) * YB + (bq & 7) * 32 + 16 * (g >> 1) + (g & 1) * (2 * YB);
        v4u dm;
        {
            const u32 e = 7 - (bq & 7), hm = g == 2 + (bq >> 3) ? ((e & 1) ? 0xffff0000u : 0xffffu) : 0u;
#pragma unroll
            for (int i = 0; i < 4; ++i) dm[i] = (e >> 1) == i ? hm : 0u;
        }
        if (wave < 3) { aa += wave * CVS; ba += wave * CVS; }
        pin2(aa, ba); pin2(yo, yp); pin(dm);
        const u32 xb = W_XF0 + (rg * 4 + wave) * 16 * XS;
        u32 io = wave * 9216, ig = W_IMG + p * IMG + io, iq = umin(4 * wave, 11) * 1024 + L_QT - 8192 - io, xo = xb + (p & 14) * XS, uo = W_UT + ch0 * 4096 + wave * 1024, co = W_CSP + p * CSB + wave * (4 * 576);
        u32 fo = W_F + (wave ? rg * 16 + bq : (lane >> 2) * 16 + 4 * Pg + (lane & 3)) * 128;
        OPQS(io); OPQS(ig); OPQS(iq); OPQS(xo); OPQS(uo); OPQS(co); OPQV(fo);
        FENCE();
        if (wave < 2) {
#pragma unroll 1
            for (int sp = 0; sp < SPIN; ++sp) {
                asm volatile("" ::: "memory");
                const v2u f = bld2(vw, fo, 0, SC1);
                if (__builtin_amdgcn_ballot_w64(f.x != (u32)ep || f.y != (u32)(ep >> 32)) == 0) break;
                __builtin_amdgcn_s_sleep(1);
            }
        }
        barrier();
        STAMP(2);
        v2u cp[4];
        auto site = LAM(N_) {
            constexpr int n = N_.v, v = n - 57;
            if constexpr (n < 9) dmai<(n & 3) * 1024>(vw, l16, ig);
            else if constexpr (n < 13) dmai<(n - 9) * 1024>(vw, l16, ig);
            else if constexpr (n < 17) ld2<(n - 13) * 576>(cp[n - 13], cpo, vw, co);
            else if constexpr (n < 57) {
                if constexpr (n == 37) waitv<20>();
                xop(IC<n - 17>{}, xo);
                if constexpr ((n - 17) % 5 == 4 && n < 56) { xo += 2 * XS; xo = xo == xb + 16 * XS ? xb : xo; OPQS(xo); }
            } else dmai<(v & 3) * 1024>(vw, l16, ig);
            if constexpr (n == 3 || n == 7 || n == 60 || n == 64) { io += 4096; ig += 4096; }
            if constexpr (n == 8) { io += iq; ig += iq; }
            if constexpr (n == 12) { io += L_V - 8192 - iq; ig += L_V - 8192 - iq; }
            if constexpr (n == 3 || n == 7 || n == 8 || n == 12 || n == 60 || n == 64) { OPQS(io); OPQS(ig); setm0(io); }
        };
        auto site1 = LAM(N_) { site(N_); if constexpr (N_.v == 3 || N_.v == 7 || N_.v == 8 || N_.v == 60 || N_.v == 64) nop2(); };
        if (conv) {
#pragma unroll
            for (int i = 0; i < 3; ++i) dma_ws(vw, l16, uo + i * 4096, L_Q + i * CVS + 4 * HCS + 480 + wave * 1024);
            setm0(io); nop2();
            sfor<0, NPRE>(site1);
            asm volatile("s_waitcnt vmcnt(%0)\n\ts_barrier" ::"n"(NPRE) : "memory");
            STAMP(3);
            if (wave < 3) {
                f4 acc[8];
                v4u A[2][8], B[2][8];
                sfor<0, 24>(LAM(N_) { conv_read<8, N_.v, 0>(aa, ba, A[0], B[0]); });
                const u32 dd = wave == 0 ? D2[0] : wave == 1 ? D2[1] : D2[2];
                v4u ad = {dm.x & dd, dm.y & dd, dm.z & dd, dm.w & dd};
                pin(ad);
                FENCE();
                sfor<0, 5>(LAM(J_) { constexpr int j = J_.v ? J_.v + 3 : 0; mm16<true>(acc[j], ad, B[0][j]); });
                FENCE();
                sfor<0, 8 * 27>(LAM(Q_) {
                    constexpr int r = Q_.v / 27, n = Q_.v % 27, i = conv_ij(8, 0xf1, n) >> 3, j = conv_ij(8, 0xf1, n) & 7;
                    if constexpr (r && n == 0) waitl<9>();
                    if constexpr (r && n == 6) waitl<(r < 7 ? 6 : 0)>();
                    mm16<false>(acc[j], A[r & 1][i], B[r & 1][j - i]);
                    if constexpr (r < 7 && n < 24) conv_read<8, n, r + 1>(aa, ba, A[(r + 1) & 1], B[(r + 1) & 1]);
                    sfor<site_at(Q_.v), site_at(Q_.v + 1)>(site);
                    FENCE();
                });
                asm volatile("s_nop 7" : "+v"(acc[0]), "+v"(acc[4]), "+v"(acc[5]), "+v"(acc[6]), "+v"(acc[7]));
                const u32 so = W_Y + (ch0 + wave) * 256;
                const v2u y0 = y_val(acc[0]);
                const v4u y4 = y_pair(y_val(acc[4]), y_val(acc[5])), y6 = y_pair(y_val(acc[6]), y_val(acc[7]));
                bst2(y0, vw, yo, so, SC1);
                bst4(y4, vw, yp, so + 8 * YB, SC1);
                bst4(y6, vw, yp, so + 12 * YB, SC1);
            } else {
                f4 acc[2][4];
                v4u A[2][4], B[2][4], ad;
                v2u y1; v4u y2;
                sfor<0, 12>(LAM(N_) { conv_read<4, N_.v, 0>(aa, ba, A[0], B[0]); });
                FENCE();
                sfor<0, 3 * 8 * 9>(LAM(Q_) {
                    constexpr int cn = Q_.v / 72, r = Q_.v % 72 / 9, n = Q_.v % 9, i = conv_ij(4, 0xe, n) >> 3, j = conv_ij(4, 0xe, n) & 7;
                    constexpr int nc = r < 7 ? cn : cn + 1, nr = (r + 1) & 7;
                    if constexpr (r == 0 && n == 0) {
                        ad = (v4u){dm.x & D2[cn], dm.y & D2[cn], dm.z & D2[cn], dm.w & D2[cn]};
                        pin(ad);
                        waitl<0>();
                        nop2();
                        FENCE();
                        sfor<1, 4>(LAM(J_) { mm16<true>(acc[cn & 1][J_.v], ad, B[0][J_.v]); });
                    } else {
                        if constexpr (n == 0) waitl<3>();
                        if constexpr (n == 7) waitl<(nc < 3 ? 10 : 0)>();
                    }
                    mm16<false>(acc[cn & 1][j], A[r & 1][i], B[r & 1][j - i]);
                    if constexpr (nc < 3) {
                        if constexpr (n < 3) { conv_read<4, 2 * n, nr>(aa + nc * CVS, ba + nc * CVS, A[nr & 1], B[nr & 1]); conv_read<4, 2 * n + 1, nr>(aa + nc * CVS, ba + nc * CVS, A[nr & 1], B[nr & 1]); }
                        else conv_read<4, n + 3, nr>(aa + nc * CVS, ba + nc * CVS, A[nr & 1], B[nr & 1]);
                    }
                    if constexpr (cn > 0 && r == 1) {
                        const u32 so = W_Y + (ch0 + cn - 1) * 256;
                        if constexpr (n == 3) { pin(acc[(cn - 1) & 1][1]); y1 = y_val(acc[(cn - 1) & 1][1]); pin(y1); }
                        if constexpr (n == 4) bst2(y1, vw, yo, so + 2 * YB, SC1);
                        if constexpr (n == 5) { pin(acc[(cn - 1) & 1][2]); y1 = y_val(acc[(cn - 1) & 1][2]); pin(y1); }
                        if constexpr (n == 6) { pin(acc[(cn - 1) & 1][3]); y2 = y_pair(y1, y_val(acc[(cn - 1) & 1][3])); pin(y2); }
                        if constexpr (n == 7) bst4(y2, vw, yp, so + 4 * YB, SC1);
                    }
                    sfor<site_at(Q_.v), site_at(Q_.v + 1)>(site);
                    FENCE();
                });
                asm volatile("s_nop 7" : "+v"(acc[0][1]), "+v"(acc[0][2]), "+v"(acc[0][3]));
                const u32 so = W_Y + (ch0 + 2) * 256;
                y1 = y_val(acc[0][1]); y2 = y_pair(y_val(acc[0][2]), y_val(acc[0][3]));
                bst2(y1, vw, yo, so + 2 * YB, SC1);
                bst4(y2, vw, yp, so + 4 * YB, SC1);
            }
        } else {
            setm0(io); nop2();
            sfor<0, NSITE>(site1);
        }
        STAMP(14);
        asm volatile("" : "+v"(cp[0]), "+v"(cp[1]), "+v"(cp[2]), "+v"(cp[3]));
        *(LAS f2*)(L_EX + wave * 512 + l8) = (f2){(asf(cp[0].x) + asf(cp[1].x)) + (asf(cp[2].x) + asf(cp[3].x)), (asf(cp[0].y) + asf(cp[1].y)) + (asf(cp[2].y) + asf(cp[3].y))};
        barrier();
    }
    u32 yv[9];
    v4u wv[17];
    STAMP(15);

    sfor<0, 2>(LAM(L_) {
        constexpr int L = L_.v;
        int ln = lane;
        OPQV(ln); OPQV(l16); OPQV(l4);
        const int tl = ln & 31, h = ln >> 5, row = ln & 15, kg = ln >> 4, gq = row >> 2, rq = row & 3;
        const bool qa = (gq & 1) == (kg & 1);
        u32 aV_ = L_V + l16, aQ_ = L_Q + l16;
        u32 aKV = L_QT + vsel(qa, (rq >> 1) * 128 + (gq >> 1) * 64 + (rq & 1) * 32 + (kg >> 1) * 16, 400 - 16 * (kg >> 1));
        u32 aQq, aCS, aS;
        OPQV(aV_); OPQV(aKV); OPQV(aQ_);
        u32 nretry = 0;
        const u32 wb = W_WOT + Pg * WOG + wave * 36864, wlb = W_WOT + Pg * WOG + 4 * 36864 + (wave >> 1) * 9216;
        u32 wso = wb;
        auto wsite = LAM(N_) {
            constexpr int n = N_.v, k = (n < 36 ? n : n - 36) % 4;
            if constexpr (L == 0 && n < 45) {
                if constexpr (n == 36) wso = wlb; else if constexpr (n && k == 0) wso += 4096;
                if constexpr (k == 0) OPQS(wso);
                if constexpr (n < 28) ld4a<k * 1024, 144 + 4 * n>(l16, vw, wso);
                else ld4o<k * 1024>(wv[n - 28], l16, vw, wso);
            }
        };
        f16v aK; f4 qKV;
        float mean, al, rstd;
        v4u rV[4], cK[5];
        {
            const u32 xb = W_XF1 + (rg * 4 + wave) * 16 * XS;
            u32 xs[8];
            float ts;
            auto xsite = LAM(Q_) { if constexpr (L == 1) xop(IC<Q_.v / 4 * 5 + Q_.v % 4 + (Q_.v % 4 > 1)>{}, xs[Q_.v / 4]); };
            auto stat = [&]() __attribute__((always_inline)) {
                auto r = __builtin_amdgcn_permlane32_swap(asu(ts), asu(ts), false, false);
                mean = asf(r[0]) * (1.f / 576.f);
                const float var = __builtin_fmaxf(asf(r[1]) * (1.f / 576.f) - mean * mean, 0.f);
                rstd = __builtin_amdgcn_rsqf(var + 1e-5f);
                al = rstd * 1.4426950408889634f;
                pin2(mean, al); pin(rstd);
            };
            if constexpr (L == 1) {
#pragma unroll
                for (int j = 0; j < 8; ++j) { xs[j] = xb + (((p & 14) + 2 * j) & 15) * XS; OPQS(xs[j]); }
                FENCE();
                STAMP(37);
                __builtin_amdgcn_s_sleep(XSL);
                STAMP(38);
                sfor<0, 8>(LAM(J_) { xop(IC<5 * J_.v + 2>{}, xs[J_.v]); });
                STAMP(39);
                sfor<0, UPA_>(xsite);
                waitv<UPA_>();
                u32 m, m2;
                asm volatile("v_max3_u32 %0, v224, v226, v228\n\tv_max3_u32 %1, v230, v232, v234\n\tv_max3_u32 %0, %0, v236, v238\n\tv_max3_u32 %1, %1, v240, v242\n\t"
                             "v_max3_u32 %0, %0, v244, v246\n\tv_max3_u32 %1, %1, v248, v250\n\tv_max3_u32 %0, %0, v252, v254\n\tv_max_u32 %0, %0, %1" : "=&v"(m), "=&v"(m2));
                if (__builtin_expect(bad(m), 0)) {
#pragma unroll 1
                    for (int sp = 0; sp < SPIN; ++sp) {
                        u32 mk = 0;
                        sfor<0, 8>(LAM(J_) { mk |= (u32)bad(mx3vv<224 + 4 * J_.v>(0u)) << J_.v; });
                        if (!mk) break;
                        __builtin_amdgcn_s_sleep(RSX);
                        sfor<0, 8>(LAM(J_) { if (mk >> J_.v & 1) xop(IC<5 * J_.v + 2>{}, xs[J_.v]); });
                        waitv<0>();
                        ++nretry;
                    }
                }
                STAMP(25);
                STAMPV(63, nretry);
                nretry = 0;
                sfor<UPA_, xissued(0)>(xsite);
                sfor<0, 8>(LAM(J_) { swapw<224 + 4 * J_.v, 128 + 2 * J_.v>(); if constexpr (J_.v) { addv<225, 225 + 4 * J_.v>(); addv<227, 227 + 4 * J_.v>(); } });
                ts = swapsum<224>();
                stat();
            }
            v4u r0[3], r1[3], ones = {~0u, ~0u, ~0u, ~0u};
            f2 cq[4];
            u32 cm, cs_;
#pragma unroll
            for (int s = 0; s < 3; ++s) { r0[s] = lds4(l16 + s * 1024); r1[s] = lds4(aKV + s * QTS); }
            if constexpr (L == 0) pin(ones);
            FENCE();
            sfor<0, 36>(LAM(SI) {
                constexpr int n = SI.v, s = kstep(n), s3 = kstep(n + 3), i = s >> 1;
                if constexpr (L == 0 && s < 32 && !(s & 1)) {
                    constexpr int j = i >> 1, later = 39 - 5 * j - (i & 1 ? 4 : 1) + 9 + (n >> 1);
                    waitv<later>();
                    FENCE();
                }
                if constexpr (L == 1 && s < 32 && !(s & 3)) {
                    constexpr int j = i >> 1;
                    static_assert(xissued(n) >= 4 * j + 4, "");
                    waitv<xissued(n) - 4 * j - 4>();
                    auto late = LAM(J_) { constexpr int k = J_.v; return bad(umax(mx3(rda<16 * k>(), rda<16 * k + 4>(), rda<16 * k + 8>()), rda<16 * k + 12>())); };
                    if (__builtin_expect(late(IC<j>{}), 0)) {
#pragma unroll 1
                        for (int sp = 0; sp < SPIN; ++sp) {
                            sfor<4 * j, 4 * j + 4>(xsite);
                            waitv<0>();
                            ++nretry;
                            if (!late(IC<j>{})) break;
                        }
                    }
                    FENCE();
                }
                if constexpr (L == 0 && n == 32) asm volatile("s_waitcnt vmcnt(16)\n\ts_barrier" ::: "memory");
                waitl<(n < 34 ? 4 : 6)>();
                mm32b<n == 0, 4 * s>(aK, r0[n % 3]);
                if constexpr (n + 3 < 36) r0[n % 3] = lds4(l16 + s3 * 1024); else rV[n - 33] = lds4(aV_ + (n - 33) * 1024);
                sfor<xissued(n), xissued(n + 1)>(xsite);
                if constexpr (L == 0 && s < 32 && (s & 3) == 2) swapw<224 + 2 * (i - 1), 127 + i>();
                if constexpr (L == 0 && s > 3 && s < 31 && ((s & 3) == 3 || s == 30)) { addv<225, 225 + 4 * (i >> 1)>(); addv<227, 227 + 4 * (i >> 1)>(); }
                if constexpr (L == 0 && n == 34) ts = swapsum<224>();
                if constexpr (n == 1) { aQq = L_QT + vsel(qa && rq < 2, 256 + (gq >> 1) * 64 + (rq & 1) * 32 + (kg >> 1) * 16, 384); pin(aQq); }
                if constexpr (n == 3) { aCS = L_CS + h * 224; aS = L_STG + (wave * 32 + tl) * STGS + 72 * h; pin2(aCS, aS); }
                if constexpr (L == 0) {
                    constexpr int k = n >> 1;
                    if constexpr (n & 1) {
                        if constexpr (k == 0 || k == 3) { cs_ = W_O + ((k ? 1024 : 0) + (rg * 4 + wave) * 16 + p) * OS; OPQS(cs_); }
                        if constexpr (k == 6) { cs_ = W_XF1 + ((rg * 4 + tw) * 16 + pw) * XS; OPQS(cs_); }
                        if constexpr (k == 9) { cs_ = W_KV + (p * 16 + rg) * 2816 + wave * 704; OPQS(cs_); }
                        if constexpr (k == 10) { cs_ += 256 * 2816; OPQS(cs_); }
                        if constexpr (k == 11) { cs_ = W_ST + ((rg * 4 + tw) * 16 + pw) * 256; OPQS(cs_); }
                        if constexpr (k < 9 && k % 3 < 2) st4<(k % 3) * 1024>(ones, l16, vw, cs_);
                        else if constexpr (k < 9) st4<2048>(ones, cm, vw, cs_);
                        else if constexpr (k < 12) st4<0>(ones, cm, vw, cs_);
                        else wsite(IC<k - 12>{});
                    } else {
                        if constexpr (k == 2) { cm = vsel(ln < 16, l16, OOB); pin(cm); }
                        if constexpr (k == 8) { cm = vsel(ln < 32, l16, OOB); pin(cm); }
                        if constexpr (k == 9) { cm = vsel(ln < 44, l16, OOB); pin(cm); }
                        if constexpr (k == 11) { cm = vsel(ln < 32, row * 16 + (kg & 1) * (1024 * 256), OOB); pin(cm); }
                        if constexpr (k == 13) { cq[0].x = (cq[0].x + cq[1].x) + (cq[2].x + cq[3].x); cq[0].y = (cq[0].y + cq[1].y) + (cq[2].y + cq[3].y); pin(cq[0]); }
                    }
                }
                if constexpr (L == 0 && n == 35) stat();
                FENCE();
                mm16b<n == 0, 4 * s>(qKV, r1[n % 3]);
                if constexpr (n + 3 < 36) r1[n % 3] = lds4(aKV + s3 * QTS); else cK[n - 33] = lds4(aCS + (n - 33) * 16);
                if constexpr (n == 33) cK[3] = lds4(aCS + 48);
                if constexpr (n == 34) cK[4] = lds4(aCS + 192);
                if constexpr (n == 35) rV[3] = lds4(aV_ + 3 * 1024);
                FENCE();
                if constexpr (L == 0 && n >= 20 && n < 24) { cq[n - 20] = *(const LAS f2*)(l8 + (L_EX + (n - 20) * 512)); FENCE(); }
                if constexpr (L == 0 && n == 27) { *(LAS f2*)(csd) = cq[0]; FENCE(); }
            });
        }
        STAMP(4 + 12 * L);
        if constexpr (L == 1) STAMPV(56, nretry);
        v4u rQ[4], rq4[3];
        u32 pa, pb, kso;
        {
            f16v aV;
            float t[18], e1[18], e2[18], mx, mn, c1, c2, s1, s2;
            v4u cV[4];
            u32 qo = L_Q + wave * 9216, qg = W_IMG + p * IMG + qo;
            sfor<0, 36>(LAM(SI) {
                constexpr int s = SI.v;
                if constexpr (s >= 2 && s < 34 && !(s & 1)) waitl<2>();
                mm32b<s == 0, 4 * s>(aV, rV[s & 3]);
                if constexpr (s == 2) { pin(aK); pin(qKV); }
                if constexpr (s + 4 < 36) rV[s & 3] = lds4(aV_ + (s + 4) * 1024); else cV[s - 32] = lds4(aCS + 64 + (s - 32) * 16);
                if constexpr (s < 18) {
                    if constexpr (s == 0 || s == 8 || s == 16) { if constexpr (s) { qo += 4096; qg += 4096; } OPQS(qo); OPQS(qg); setm0(qo); }
                    if constexpr (s & 1) dmai<((s >> 1) & 3) * 1024>(vw, l16, qg);
                } else wsite(IC<s - 12>{});
                if constexpr (s == 1) {
                    const u32 r0 = L_STG + (4 * (4 * (kg & 1) + gq) + (kg >> 1)) * STGS + 8 * rq;
                    pa = r0 + 72 * (wave >> 1); pb = r0 + (144 + 40 * (wave >> 1) + 32 * (wave & 1));
                    kso = W_KV + ((L * 16 + p) * 16 + rg) * 2816 + (wave >> 1) * 1376;
                    pin2(pa, pb); OPQS(kso);
                }
                if constexpr (s >= 2 && s < 5) {
#pragma unroll
                    for (int i = 6 * (s - 2); i < 6 * (s - 2) + 6; ++i) { const u32 cv = cK[i >> 2][i & 3]; t[i] = __builtin_fmaf(-mean, asf(cv), i < 16 ? aK[i] : qKV[i - 16]); }
                }
                if constexpr (s == 5) { mx = __builtin_fmaxf(__builtin_fmaxf(t[0], t[1]), t[2]); mn = __builtin_fminf(__builtin_fminf(t[0], t[1]), t[2]);
#pragma unroll
                    for (int i = 3; i < 7; i += 2) { mx = __builtin_fmaxf(__builtin_fmaxf(mx, t[i]), t[i + 1]); mn = __builtin_fminf(__builtin_fminf(mn, t[i]), t[i + 1]); } }
                if constexpr (s == 6) {
#pragma unroll
                    for (int i = 7; i < 13; i += 2) { mx = __builtin_fmaxf(__builtin_fmaxf(mx, t[i]), t[i + 1]); mn = __builtin_fminf(__builtin_fminf(mn, t[i]), t[i + 1]); } }
                if constexpr (s == 7) {
#pragma unroll
                    for (int i = 13; i < 17; i += 2) { mx = __builtin_fmaxf(__builtin_fmaxf(mx, t[i]), t[i + 1]); mn = __builtin_fminf(__builtin_fminf(mn, t[i]), t[i + 1]); }
                    mx = __builtin_fmaxf(mx, t[17]); mn = __builtin_fminf(mn, t[17]); c1 = -al * mx; c2 = al * mn; }
                if constexpr (s >= 8 && s < 32) {
#pragma unroll
                    for (int q = (3 * (s - 8)) / 2; q < (3 * (s - 7)) / 2; ++q) {
                        const int i = q >> 1;
                        if (q & 1) { e2[i] = __builtin_amdgcn_exp2f(__builtin_fmaf(t[i], -al, c2)); s2 = i ? s2 + e2[i] : e2[i]; }
                        else { e1[i] = __builtin_amdgcn_exp2f(__builtin_fmaf(t[i], al, c1)); s1 = i ? s1 + e1[i] : e1[i]; }
                    }
                }
                if constexpr (s == 32) { s1 = __builtin_amdgcn_rcpf(s1); s2 = __builtin_amdgcn_rcpf(s2); }
                if constexpr (s >= 33) {
                    constexpr int i = s - 33;
                    *(LAS v2u*)(aS + 8 * i) = (v2u){pk(e1[4 * i] * s1, e1[4 * i + 1] * s1), pk(e1[4 * i + 2] * s1, e1[4 * i + 3] * s1)};
                }
                FENCE();
            });
            *(LAS v2u*)(aS + 24) = (v2u){pk(e1[12] * s1, e1[13] * s1), pk(e1[14] * s1, e1[15] * s1)};
            *(LAS v2u*)(aS + 32) = (v2u){pk(e1[16] * s1, e1[17] * s1), pk(e2[0] * s2, e2[1] * s2)};
#pragma unroll
            for (int i = 0; i < 4; ++i) *(LAS v2u*)(aS + 40 + 8 * i) = (v2u){pk(e2[4 * i + 2] * s2, e2[4 * i + 3] * s2), pk(e2[4 * i + 4] * s2, e2[4 * i + 5] * s2)};
            FENCE();
            pin(aV);
            const u32 aSv = aS + 144 - 32 * h;
            const float mr = -mean * rstd;
#pragma unroll
            for (int i = 0; i < 4; ++i) {
                const u32 q0 = cV[i].x, q1 = cV[i].y, q2 = cV[i].z, q3 = cV[i].w;
                *(LAS v2u*)(aSv + 8 * i) = (v2u){pk(__builtin_fmaf(mr, asf(q0), aV[4 * i] * rstd), __builtin_fmaf(mr, asf(q1), aV[4 * i + 1] * rstd)),
                                                 pk(__builtin_fmaf(mr, asf(q2), aV[4 * i + 2] * rstd), __builtin_fmaf(mr, asf(q3), aV[4 * i + 3] * rstd))};
            }
            const u32 q2 = cK[4].z, q3 = cK[4].w;
            *(LAS v2u*)(aSv + 32) = (v2u){pk(__builtin_fmaf(mr, asf(q2), qKV[2] * rstd), __builtin_fmaf(mr, asf(q3), qKV[3] * rstd)), 0x3f80u};
        }
        waitbar<(L ? 0 : 18)>();
        STAMP(5 + 12 * L);
        {
            f4 c[3];
            v4u B[4], A[4][3];
            auto rd = LAM(N_) {
                constexpr int ks = N_.v >> 2, f = N_.v & 3;
                if constexpr (f == 0) B[ks] = tr2(pb + ks * 32 * STGS, pb + (ks * 32 + 2) * STGS);
                else A[ks][f - 1] = tr2(pa + ks * 32 * STGS + 32 * (f - 1), pa + (ks * 32 + 2) * STGS + 32 * (f - 1));
            };
            sfor<0, 7>(LAM(N_) { rd(N_); FENCE(); });
            if constexpr (L == 0) { if (tid == 0) bst2((v2u){(u32)ep, (u32)(ep >> 32)}, vw, 0, W_F2 + (rg * 16 + p) * 128, SC1); }
            FENCE();
            sfor<0, 12>(LAM(I_) {
                constexpr int ks = I_.v / 3, ft = I_.v % 3;
                mm16<ks == 0>(c[ft], A[ks][ft], B[ks]);
                if constexpr (I_.v < 9) rd(IC<7 + I_.v>{});
                else { rQ[I_.v - 9] = lds4(aQ_ + (I_.v - 9) * 1024); rq4[I_.v - 9] = lds4(aQq + (I_.v - 9) * QTS); }
                FENCE();
            });
            rQ[3] = lds4(aQ_ + 3 * 1024);
            FENCE();
            pad3<5>(c[0], c[1], c[2]);
            bst4((v4u){pk(c[0][0], c[0][1]), pk(c[0][2], c[0][3]), pk(c[1][0], c[1][1]), pk(c[1][2], c[1][3])}, vw, kvA, kso, SC1);
            bst2((v2u){pk(c[2][0], c[2][1]), pk(c[2][2], c[2][3])}, vw, kvB, kso, SC1);
        }
        FENCE();
        STAMP(6 + 12 * L);
        v4u pq[5], kp[12], sel;
        u32 kvo, kv3, aF, aQ4, f2o, oso;
        u32 kb[4];
        v2u yfl;
        auto flags_ok = [&](u64 m) __attribute__((always_inline)) { return (__builtin_amdgcn_ballot_w64(yfl.x != (u32)ep || yfl.y != (u32)(ep >> 32)) & m) == 0; };
        auto flags_wait = [&](u64 m) __attribute__((always_inline)) {
            if (__builtin_expect(!flags_ok(m), 0)) {
#pragma unroll 1
                for (int sp = 0; sp < SPIN; ++sp) {
                    __builtin_amdgcn_s_sleep(1);
                    ld2<0>(yfl, f2o, vw, W_F2);
                    waitp<0>(yfl);
                    if (flags_ok(m)) break;
                }
            }
        };
        {
            f16v aQ; f4 qQ;
            v4u cQ[5];
            sfor<0, 36>(LAM(SI) {
                constexpr int s = SI.v;
                if constexpr (s >= 1 && s < 34) waitl<4>();
                mm32b<s == 0, 4 * s>(aQ, rQ[s & 3]);
                if constexpr (s + 4 < 36) rQ[s & 3] = lds4(aQ_ + (s + 4) * 1024); else cQ[s - 32] = lds4(aCS + 128 + (s - 32) * 16);
                if constexpr (s < 31 && s % 3 != 2) wsite(IC<24 + s - s / 3>{});
                if constexpr (s == 2) { kvo = kg * 2816 + wave * 256 + row * 16; kv3 = vsel(wave * 16 + row < 44, kvo + 2048, OOB); pin2(kvo, kv3); }
                if constexpr (s == 26) { for (int i = 0; i < 4; ++i) { kb[i] = W_KV + ((L * 16 + p) * 16 + 4 * i) * 2816; OPQS(kb[i]); } }
                if constexpr (s == 5 || s == 8) {
#pragma unroll
                    for (int i = (s - 5) / 3 * 2; i < (s - 5) / 3 * 2 + 2; ++i) sel[i] = row == 2 * i ? 0x3f80u : row == 2 * i + 1 ? 0x3f800000u : 0u;
                    if constexpr (s == 8) pin(sel);
                }
                if constexpr (s == 11) { const int hr = (tl >> 2) & 1, d = (tl & 3) + 4 * (tl >> 3); aF = vsel(h == hr, (hr * 19 + d) * 80 + L_KVA, L_Z); pin(aF); }
                if constexpr (s == 14) { aQ4 = vsel(kg == gq && rq < 3, ((gq >> 1) * 19 + 16 + rq) * 80 + L_KVA, L_Z); pin(aQ4); }
                if constexpr (s == 17) { oso = W_O + (L * 1024 + (rg * 4 + wave) * 16 + p) * OS; OPQS(oso); }
                if constexpr (L == 0 && s == 20) { f2o = vsel(kg == 1, rg * 16 + row, row * 16 + (kg ? pw : p)) * 128; pin(f2o); }
                if constexpr (L == 0 && s == 23) ld2<0>(yfl, f2o, vw, W_F2);
                FENCE();
                mm16b<s == 0, 4 * s>(qQ, rq4[s % 3]);
                if constexpr (s + 3 < 36) rq4[s % 3] = lds4(aQq + (s + 3) * QTS);
                if constexpr (s == 33) cQ[4] = lds4(aCS + 208);
                FENCE();
            });
            STAMP(49 + L);
            if constexpr (L == 0) { waitp<5>(yfl); flags_wait(0xffffull); }
#pragma unroll
            for (int i = 0; i < 4; ++i) {
                kp[i] = bld4(vw, kvo, kb[i], SC1);
                kp[4 + i] = bld4(vw, kvo + 1024, kb[i], SC1);
                kp[8 + i] = bld4(vw, kv3, kb[i], SC1);
            }
            FENCE();
            pin(aQ); pin(qQ);
            float t[18]; u32 ph[18];
#pragma unroll
            for (int i = 0; i < 18; ++i) { const u32 cv = cQ[i >> 2][i & 3]; t[i] = __builtin_fmaf(-mean, asf(cv), i < 16 ? aQ[i] : qQ[i - 16]); }
            hedgehog(t, al, ph);
#pragma unroll
            for (int i = 0; i < 5; ++i) { pq[i] = (v4u){ph[4 * i], ph[4 * i + 1], i < 4 ? ph[4 * i + 2] : 0u, i < 4 ? ph[4 * i + 3] : 0u}; pin(pq[i]); }
            FENCE();
            STAMP(7 + 12 * L);
            u32 m = umax(kp[0].x, kp[0].z);
#pragma unroll
            for (int i = 1; i < 12; ++i) m = mx3(m, kp[i].x, kp[i].z);
            nretry = 0;
            if (__builtin_expect(bad(m), 0)) {
#pragma unroll 1
                for (int sp = 0; sp < SPIN; ++sp) {
                    u32 mk = 0;
                    waitv<0>();
#pragma unroll
                    for (int i = 0; i < 4; ++i) {
                        pin2(kp[i], kp[4 + i]); pin(kp[8 + i]);
                        mk |= (u32)bad(mx3(mx3(kp[i].x, kp[i].z, kp[4 + i].x), mx3(kp[4 + i].z, kp[8 + i].x, kp[8 + i].z), 0u)) << i;
                    }
                    if (!mk) break;
                    __builtin_amdgcn_s_sleep(RSK);
#pragma unroll
                    for (int i = 0; i < 4; ++i)
                        if (mk >> i & 1) { ld4<0>(kp[i], kvo, vw, kb[i]); ld4<1024>(kp[4 + i], kvo, vw, kb[i]); ld4<0>(kp[8 + i], kv3, vw, kb[i]); }
                    ++nretry;
                }
            }
            STAMPV(57 + L, nretry);
            f4 a[3];
            sfor<0, 12>(LAM(I_) { constexpr int i = I_.v / 3, j = I_.v % 3; mm16<i == 0>(a[j], sel, kp[4 * j + i]); });
            pad3<7>(a[0], a[1], a[2]);
#pragma unroll
            for (int j = 0; j < 3; ++j) *(LAS v2u*)(kvw[j]) = (v2u){pk(a[j][0], a[j][1]), pk(a[j][2], a[j][3])};
            barrier();
            STAMP(8 + 12 * L);
        }
        {
            v4u A[5], Aq[5];
#pragma unroll
            for (int c = 0; c < 5; ++c) Aq[c] = lds4(aQ4 + 16 * c);
#pragma unroll
            for (int c = 0; c < 5; ++c) A[c] = lds4(aF + 16 * c);
            FENCE();
            f16v o; f4 oq;
            float rd; u32 ol;
            sfor<0, 5>(LAM(C_) { mm16<C_.v == 0>(oq, Aq[C_.v], pq[C_.v]); });
            sfor<0, 5>(LAM(C_) {
                mm32<C_.v == 0>(o, A[C_.v], pq[C_.v]);
                if constexpr (C_.v == 3) { pad<4>(oq); rd = __builtin_amdgcn_rcpf(oq[2] + 1e-6f); pin(rd); }
                if constexpr (C_.v == 4) { ol = pk(oq[0] * rd, oq[1] * rd); pin(ol); }
                FENCE();
            });
            pad<8>(o);
            bst4((v4u){pk(o[0] * rd, o[1] * rd), pk(o[2] * rd, o[3] * rd), pk(o[4] * rd, o[5] * rd), pk(o[6] * rd, o[7] * rd)}, vw, l16, oso, SC1);
            bst4((v4u){pk(o[8] * rd, o[9] * rd), pk(o[10] * rd, o[11] * rd), pk(o[12] * rd, o[13] * rd), pk(o[14] * rd, o[15] * rd)}, vw, l16 + 1024, oso, SC1);
            bst1(ol, vw, l4 + 2048, oso, SC1);
        }
        FENCE();
        STAMP(9 + 12 * L);
        u32 aB, ae, ao, yo, ys;
        f16v aW; float xl0, xl1;
        if constexpr (L == 0) {
            flags_wait(~0ull);
            ys = W_Y + rg * YB + 36 * pw * 256 + tw * 64; yo = (ln >> 2) * 256 + rq * 16;
            OPQS(ys); OPQV(yo);
        }
#pragma unroll
        for (int i = 0; i < 8; ++i) { aW[2 * i] = bf_lo(xr[i]); aW[2 * i + 1] = bf_hi(xr[i]); }
        xl0 = bf_lo(xr[8]); xl1 = bf_hi(xr[8]);
        aB = L_Q + ((wave >> 1) * 18 + (kg & 1)) * 1024 + ((kg >> 1) * 32 + 16 * (wave & 1) + row) * 16;
        ae = L_EX + (kg & 1) * 1024 + ((2 * (wave >> 1) + h) * 16 + row) * 16 + 8 * (wave & 1);
        ao = aQ_ + wave * 8192;
        pin(aW); pin2(xl0, xl1); pin2(aB, ae); pin(ao);
        {
            u32 so[4];
#pragma unroll
            for (int i = 0; i < 4; ++i) { so[i] = W_O + (L * 1024 + (rg * 4 + tw) * 16 + 4 * wave + i) * OS; OPQS(so[i]); }
            v4u og[8]; u32 lo[4];
            FENCE();
            STAMP(43 + 3 * L);
            waitv<0>();
            STAMP(44 + 3 * L);
            if constexpr (L == 0 && REM) {
                u64 pc;
                asm volatile("s_getpc_b64 %0" : "=s"(pc));
                const u32 o = umin(l4 + (wave * 256 + p * 1024 + (rg & 1) * 16384), REM - 512);
                asm volatile("s_mov_b32 m0, %2\n\ts_nop 0\n\tglobal_load_lds_dword %0, %1" ::"v"(o), "s"(pc), "s"(L_EX) : "m0", "memory");
            }
            __builtin_amdgcn_s_sleep(OSL);
            auto ldo = [&]() __attribute__((always_inline)) {
#pragma unroll
                for (int i = 0; i < 8; ++i) og[i] = bld4(vw, l16 + (i & 1) * 1024, so[i >> 1], SC1);
            };
            auto ldl = [&]() __attribute__((always_inline)) {
#pragma unroll
                for (int i = 0; i < 4; ++i) lo[i] = bld1(vw, l4 + 2048, so[i], SC1);
            };
            auto chk = [&]() __attribute__((always_inline)) {
                u32 m = mx3(umax(lo[0], lo[1]), lo[2], lo[3]);
#pragma unroll
                for (int i = 0; i < 8; i += 2) m = mx3(m, og[i].x, og[i + 1].x);
                return bad(m);
            };
            ldo(); ldl();
            FENCE();
            nretry = 0;
            if (__builtin_expect(chk(), 0)) {
#pragma unroll 1
                for (int sp = 0; sp < SPIN; ++sp) {
                    u32 mk = 0;
                    waitv<0>();
#pragma unroll
                    for (int i = 0; i < 4; ++i) {
                        pin2(og[2 * i], og[2 * i + 1]); pin(lo[i]);
                        mk |= (u32)bad(mx3(og[2 * i].x, og[2 * i + 1].x, lo[i])) << i;
                    }
                    if (!mk) break;
                    __builtin_amdgcn_s_sleep(RSO);
#pragma unroll
                    for (int i = 0; i < 4; ++i)
                        if (mk >> i & 1) { ld4<0>(og[2 * i], l16, vw, so[i]); ld4<1024>(og[2 * i + 1], l16, vw, so[i]); ld1<2048>(lo[i], l4, vw, so[i]); }
                    ++nretry;
                }
            }
            STAMPV(59 + L, nretry);
            STAMP(45 + 3 * L);
#pragma unroll
            for (int i = 0; i < 8; ++i) *(LAS v4u*)(ao + i * 1024) = og[i];
            *(LAS v4u*)(aQ_ + (32 + wave) * 1024) = (v4u){lo[0], lo[1], lo[2], lo[3]};
            barrier();
        }
        STAMP(10 + 12 * L);
        u32 sto = W_ST + (L * 1024 + (rg * 4 + tw) * 16) * 256;
        {
            f4 qW;
            v4u rb_[4], rl[9];
            v2u e0, e1;
            float s1, s2;
            u32 le;
#pragma unroll
            for (int i = 0; i < 4; ++i) { rl[i] = lds4(aB + i * 2048); rb_[i] = lds4(aQ_ + i * 1024); }
            FENCE();
            sfor<0, 36>(LAM(SI) {
                constexpr int s = SI.v;
                if constexpr (s < 28) mm32a<false, 144 + 4 * s>(aW, rb_[s & 3]); else mm32<false>(aW, wv[s - 28], rb_[s & 3]);
                if constexpr (s + 4 < 36) rb_[s & 3] = lds4(aQ_ + (s + 4) * 1024);
                if constexpr (s < 5) rl[4 + s] = lds4(aB + (4 + s) * 2048);
                if constexpr (s == 12) { pad<0>(qW); *(LAS f4*)(l16 + (L_EX + wave * 1024)) = qW; }
                if constexpr (s == 14) OPQS(sto);
                if constexpr (s == 24) { le = l4; pin(le); }
                if constexpr (L == 0 && s >= 16 && s < 22 && !(s & 1)) dma_ws(vw, yo, ys + (s - 16) * 2048, L_STG + wave * 3072 + (s - 16) * 512);
                FENCE();
                if constexpr (s < 9) { mm16<s == 0>(qW, wv[8 + s], rl[s]); FENCE(); }
                if constexpr (s == 30) { barrier(); e0 = lds2(ae); e1 = lds2(ae + 2048); FENCE(); }
                if constexpr (s == 33) { xl0 += asf(e0.x) + asf(e1.x); xl1 += asf(e0.y) + asf(e1.y); s1 = xl0 + xl1; s2 = __builtin_fmaf(xl0, xl0, xl1 * xl1); pin2(s1, s2); FENCE(); }
            });
            STAMP(11 + 12 * L);
            pad<11>(aW);
            float s3 = aW[0], s4 = aW[0] * aW[0];
#pragma unroll
            for (int i = 1; i < 16; ++i) { if (i & 1) { s1 += aW[i]; s2 = __builtin_fmaf(aW[i], aW[i], s2); } else { s3 += aW[i]; s4 = __builtin_fmaf(aW[i], aW[i], s4); } }
            bst1(asu(halves_sum(s1 + s3, s2 + s4)), vw, le, sto + pw * 256, SC1);
        }
        FENCE();
        {
            float t;
            u32 xso = L ? (rg * 128 + tw * 32) * 1152 + 72 * pw : W_XF1 + ((rg * 4 + tw) * 16 + pw) * XS, oo = tl * 1152 + 36 * h, one2 = 0x3f803f80u;
            OPQS(xso); OPQS(one2);
            if constexpr (L == 1) OPQV(oo);
            u32 yf[18], lS;
            if constexpr (L == 0) {
                const u32 ya = (18 * h + gq) * 64 + 32 * (kg & 1) + 8 * rq + (L_STG + wave * 3072);
                lS = tl * 16 + h * 8;
                pin(lS);
                waitv<1>();
#pragma unroll
                for (int i = 0; i < 5; ++i) {
                    const v2u v = __builtin_bit_cast(v2u, __builtin_amdgcn_ds_read_tr16_b64_v4i16((LAS s4*)(ya + 256 * i)));
                    yv[2 * i] = v.x; if (i < 4) yv[2 * i + 1] = v.y;
                }
            }
#pragma unroll
            for (int i = 0; i < 9; ++i) { yf[2 * i] = yv[i] << 16; yf[2 * i + 1] = yv[i] & 0xffff0000u; pin2(yf[2 * i], yf[2 * i + 1]); }
            FENCE();
            waitv<0>();
            if constexpr (SSL) __builtin_amdgcn_s_sleep(SSL);
            nretry = 0;
#pragma unroll 1
            for (int sp = 0; sp < SPIN; ++sp) {
                asm volatile("" : "+v"(l4)::"memory");
                u32 e[16];
#pragma unroll
                for (int i = 0; i < 16; ++i) e[i] = bld1(vw, l4 + i * 256, sto, SC1);
                FENCE();
                u32 m = umax(e[0], e[1]);
                t = asf(e[0]) + asf(e[1]);
#pragma unroll
                for (int i = 2; i < 16; i += 2) { m = mx3(m, e[i], e[i + 1]); t += asf(e[i]) + asf(e[i + 1]); pin2(m, t); }
                if (!bad(m)) break;
                ++nretry;
                __builtin_amdgcn_s_sleep(RSS);
            }
            STAMP(12 + 12 * L);
            STAMPV(61 + L, nretry);
            auto r = __builtin_amdgcn_permlane32_swap(asu(t), asu(t), false, false);
            const float m1 = asf(r[0]) * (1.f / 576.f);
            const float r1 = __builtin_amdgcn_rsqf(__builtin_fmaxf(asf(r[1]) * (1.f / 576.f) - m1 * m1, 0.f) + 1e-5f), mr = -m1 * r1;
#pragma unroll
            for (int i = 0; i < 9; ++i) {
                const float a = i < 8 ? aW[2 * i] : xl0, c = i < 8 ? aW[2 * i + 1] : xl1;
                xr[i] = pk(__builtin_fmaf(__builtin_fmaf(a, r1, mr), asf(yf[2 * i]), a), __builtin_fmaf(__builtin_fmaf(c, r1, mr), asf(yf[2 * i + 1]), c));
                if constexpr (L == 0) {
                    if (i == 3) { bst4((v4u){xr[0], xr[1], xr[2], xr[3]}, vw, l16, xso, SC1); FENCE(); }
                    if (i == 7) { bst4((v4u){xr[4], xr[5], xr[6], xr[7]}, vw, l16 + 1024, xso, SC1); FENCE(); }
                } else {
                    if (i == 3) { bst4((v4u){xr[0], xr[1], xr[2], xr[3]}, ro, oo, xso, 0); FENCE(); }
                    if (i == 7) { bst4((v4u){xr[4], xr[5], xr[6], xr[7]}, ro, oo + 16, xso, 0); FENCE(); }
                    if (i == 8) bst1(xr[8], ro, oo + 32, xso, 0);
                }
            }
            if constexpr (L == 0) {
                float s1 = 0.f, s2 = 0.f;
#pragma unroll
                for (int j = 0; j < 9; ++j) { s1 = dot2(xr[j], one2, s1); s2 = dot2(xr[j], xr[j], s2); }
                bst2((v2u){xr[8], asu(halves_sum(s1, s2))}, vw, lS + 2048, xso, SC1);
                STAMP(13);
            } else {
                if (b == 0 && tid == 0) bst2((v2u){(u32)ep, (u32)(ep >> 32)}, vw, 0, W_E, SC1);
            }
        }
    });
    STAMP_END();
}
#if defined(__HIP_DEVICE_COMPILE__)
#pragma clang attribute pop
#endif

extern "C" void kernel_launch(void* const* d_in, const int* in_sizes, int n_in, void* d_out, int out_size, void* d_ws, size_t ws_size, hipStream_t stream) {
    (void)in_sizes; (void)n_in; (void)out_size; (void)ws_size;
    hipFuncSetAttribute((const void*)k_main, hipFuncAttributeMaxDynamicSharedMemorySize, L_END);
    const u32 f = 0x00020000u, n = 2048 * 1152;
    Args a = {{d_in[5], n, f}, {d_in[6], n, f}, {{d_in[1], 576 * 3456, f}, {d_in[3], 576 * 1152, f}}, {{d_in[2], 32 * 648, f}, {d_in[2], 0, f}}, {d_in[0], n, f}, {d_ws, W_END, f}, {d_out, n, f}, d_in[7], d_in[8], d_in[9]};
    k_main<<<256, 256, L_END, stream>>>(a);
}
```

```cpp
#include <hip/hip_runtime.h>
#include <stdint.h>
#pragma clang diagnostic ignored "-Wint-to-pointer-cast"
#pragma clang diagnostic ignored "-Wunused-value"
#pragma clang diagnostic ignored "-Winline-asm"
#if defined(__HIP_DEVICE_COMPILE__)
#pragma clang attribute push(__attribute__((target("no-packed-fp32-ops"))), apply_to = function)
#endif
typedef unsigned short u16;
typedef unsigned u32;
typedef unsigned long long u64;
typedef u32 v4u __attribute__((ext_vector_type(4)));
typedef u32 v2u __attribute__((ext_vector_type(2)));
typedef float f4 __attribute__((ext_vector_type(4)));
typedef float f2 __attribute__((ext_vector_type(2)));
typedef float f16v __attribute__((ext_vector_type(16)));
typedef __bf16 b8 __attribute__((ext_vector_type(8)));
typedef __bf16 b2 __attribute__((ext_vector_type(2)));
typedef short s4 __attribute__((ext_vector_type(4)));
#define LAS __attribute__((address_space(3)))
#define DEV __device__ __forceinline__
#define SC1 16

constexpr u32 QTS = 416, STGS = 232, CVS = 21216, HCS = 4160;
constexpr u32 L_K = 0, L_V = 36864, L_QT = 73728, L_Q = 89088, L_STG = 125952, L_KVA = 155648, L_CS = 158784, L_EX = 159232, L_END = 163328;
constexpr u32 P_KS = 0, P_VS = 10240, P_WS = 20480, P_XS = 31232, P_PH = 42496, P_LS = 55296, L_Z = L_KVA + 38 * 80;
constexpr u32 IMG = L_STG, XS = 2560, OS = 2304, YB = 576 * 256, CSB = 16 * 576;
constexpr u32 WOG = 4 * 36864 + 18432;
constexpr u32 W_IMG = 0, W_WOT = W_IMG + 16 * IMG, W_CSP = W_WOT + 4 * WOG, W_UT = W_CSP + 16 * CSB,
              W_XF0 = W_UT + 576 * 4096, W_XF1 = W_XF0 + 1024 * XS, W_O = W_XF1 + 1024 * XS, W_Y = W_O + 2 * 1024 * OS, W_KV = W_Y + 576 * 4096,
              W_ST = W_KV + 2 * 256 * 2816, W_F = W_ST + 2 * 1024 * 256, W_F2 = W_F + 256 * 128, W_E = W_F2 + 256 * 128, W_END = W_E + 128, W_DBG = W_END;
constexpr u32 OOB = 0x7ffffff0u;
constexpr int SPIN = 1 << 14;
constexpr u32 CODE = 45000;
#define STOLD
#define OSL 8
#define RSX 1
#define RSK 1
#define RSO 1
#define RSS 1
#define XSL 13
#define MAP 0
#define WD0 3
#define WDN 6
#define SKIP 0
#define WARM 0
#define WEARLY 1
#define OWT 1
#define SSL 2

DEV float asf(u32 v) { return __builtin_bit_cast(float, v); }
DEV u32 asu(float v) { return __builtin_bit_cast(u32, v); }
DEV float bf_lo(u32 v) { return asf(v << 16); }
DEV float bf_hi(u32 v) { return asf(v & 0xffff0000u); }
DEV u32 pk(float lo, float hi) { b2 r = __builtin_convertvector((f2){lo, hi}, b2); return __builtin_bit_cast(u32, r); }
DEV float rb(float f) { return bf_lo(pk(f, f)); }
DEV float dot2(u32 a, u32 b, float c) { return __builtin_amdgcn_fdot2_f32_bf16(__builtin_bit_cast(b2, a), __builtin_bit_cast(b2, b), c, false); }
DEV u32 umin(u32 a, u32 b) { return a < b ? a : b; }
DEV u32 umax(u32 a, u32 b) { return a > b ? a : b; }
DEV u32 mx3(u32 a, u32 b, u32 c) { return umax(umax(a, b), c); }
template <bool Z> DEV void mm32(f16v& c, v4u a, v4u b) {
    if constexpr (Z) asm volatile("v_mfma_f32_32x32x16_bf16 %0, %1, %2, 0" : "=&v"(c) : "v"(a), "v"(b));
    else asm volatile("v_mfma_f32_32x32x16_bf16 %0, %1, %2, %0" : "+v"(c) : "v"(a), "v"(b));
}
template <bool Z> DEV void mm16(f4& c, v4u a, v4u b) {
    if constexpr (Z) asm volatile("v_mfma_f32_16x16x32_bf16 %0, %1, %2, 0" : "=&v"(c) : "v"(a), "v"(b));
    else asm volatile("v_mfma_f32_16x16x32_bf16 %0, %1, %2, %0" : "+v"(c) : "v"(a), "v"(b));
}
template <bool Z, int R> DEV void mm32b(f16v& c, v4u a) {
    if constexpr (Z) asm volatile("v_mfma_f32_32x32x16_bf16 %0, %1, a[%c2:%c3], 0" : "=&v"(c) : "v"(a), "n"(R), "n"(R + 3));
    else asm volatile("v_mfma_f32_32x32x16_bf16 %0, %1, a[%c2:%c3], %0" : "+v"(c) : "v"(a), "n"(R), "n"(R + 3));
}
template <bool Z, int R> DEV void mm16b(f4& c, v4u a) {
    if constexpr (Z) asm volatile("v_mfma_f32_16x16x32_bf16 %0, %1, a[%c2:%c3], 0" : "=&v"(c) : "v"(a), "n"(R), "n"(R + 3));
    else asm volatile("v_mfma_f32_16x16x32_bf16 %0, %1, a[%c2:%c3], %0" : "+v"(c) : "v"(a), "n"(R), "n"(R + 3));
}
template <bool Z, int R> DEV void mm32a(f16v& c, v4u b) {
    if constexpr (Z) asm volatile("v_mfma_f32_32x32x16_bf16 %0, a[%c2:%c3], %1, 0" : "=&v"(c) : "v"(b), "n"(R), "n"(R + 3));
    else asm volatile("v_mfma_f32_32x32x16_bf16 %0, a[%c2:%c3], %1, %0" : "+v"(c) : "v"(b), "n"(R), "n"(R + 3));
}
template <int N, class T> DEV void pad(T& c) { asm volatile("s_nop %1" : "+v"(c) : "n"(N)); }
template <class T> DEV void pin(T& c) { asm volatile("" : "+v"(c)); }
DEV void nop2() { asm volatile("s_nop 1"); }
template <int R> DEV u32 rda() { u32 v; asm volatile("v_accvgpr_read_b32 %0, a%c1" : "=v"(v) : "n"(R)); return v; }
template <int R> DEV void wra(u32 v) { asm volatile("v_accvgpr_write_b32 a%c1, %0" ::"v"(v), "n"(R)); }
__device__ void bst4(v4u, v4u, u32, u32, int) __asm("llvm.amdgcn.raw.buffer.store.v4i32");
__device__ void bst2(v2u, v4u, u32, u32, int) __asm("llvm.amdgcn.raw.buffer.store.v2i32");
__device__ void bst1(u32, v4u, u32, u32, int) __asm("llvm.amdgcn.raw.buffer.store.i32");
__device__ v4u bld4(v4u, u32, u32, int) __asm("llvm.amdgcn.raw.buffer.load.v4i32");
__device__ v2u bld2(v4u, u32, u32, int) __asm("llvm.amdgcn.raw.buffer.load.v2i32");
__device__ u32 bld1(v4u, u32, u32, int) __asm("llvm.amdgcn.raw.buffer.load.i32");
struct Buf { const void* p; u32 n, f; };
struct Args { Buf k, v, w[2], ph[2], x, ws, out; const void *sk, *sh, *sD; };
template <class T> DEV void sld(T& d, const void* ka, u32 off) {
    if constexpr (sizeof(T) == 16) asm volatile("s_load_dwordx4 %0, %1, %2" : "=s"(d) : "s"(ka), "s"(off));
    else asm volatile("s_load_dwordx2 %0, %1, %2" : "=s"(d) : "s"(ka), "s"(off));
}
template <int O, class T> DEV void sldi(T& d, const void* ka) {
    if constexpr (sizeof(T) == 16) asm volatile("s_load_dwordx4 %0, %1, %2" : "=s"(d) : "s"(ka), "n"(O));
    else asm volatile("s_load_dwordx2 %0, %1, %2" : "=s"(d) : "s"(ka), "n"(O));
}
DEV v4u lds4(u32 a) { return *(const LAS v4u*)a; }
DEV v2u lds2(u32 a) { return *(const LAS v2u*)a; }
DEV v2u lds2v(u32 a) { return *(const volatile LAS v2u*)a; }
DEV void barrier() { asm volatile("s_waitcnt lgkmcnt(0)\n\ts_barrier" ::: "memory"); }
template <int N> DEV void waitv() { asm volatile("s_waitcnt vmcnt(%0)" ::"n"(N) : "memory"); }
DEV void dma_in(const void* g, u32 voff, u32 lds) {
    asm volatile("s_mov_b32 m0, %2\n\ts_nop 0\n\tglobal_load_lds_dwordx4 %0, %1" ::"v"(voff), "s"(g), "s"(lds) : "m0", "memory");
}
DEV void dma_b(v4u rs, u32 voff, u32 lds) {
    asm volatile("s_mov_b32 m0, %2\n\ts_nop 0\n\tbuffer_load_dwordx4 %0, %1, 0 offen lds" ::"v"(voff), "s"(rs), "s"(lds) : "m0", "memory");
}
DEV void dma_s(v4u rs, u32 voff, u32 so, u32 lds) {
    asm volatile("s_mov_b32 m0, %3\n\ts_nop 0\n\tbuffer_load_dwordx4 %0, %1, %2 offen lds" ::"v"(voff), "s"(rs), "s"(so), "s"(lds) : "m0", "memory");
}
DEV void dma_ws(v4u rs, u32 voff, u32 so, u32 lds) {
    asm volatile("s_mov_b32 m0, %3\n\ts_nop 0\n\tbuffer_load_dwordx4 %0, %1, %2 offen sc1 lds" ::"v"(voff), "s"(rs), "s"(so), "s"(lds) : "m0", "memory");
}
template <int IM> DEV void gld4(v4u& d, u32 voff, const void* g) { asm volatile("global_load_dwordx4 %0, %1, %2 offset:%3" : "=v"(d) : "v"(voff), "s"(g), "n"(IM) : "memory"); }
DEV void gld2(v2u& d, u32 voff, const void* g) { asm volatile("global_load_dwordx2 %0, %1, %2" : "=v"(d) : "v"(voff), "s"(g) : "memory"); }
DEV void mm16k(f4& c, v2u a, v2u b) { asm volatile("v_mfma_f32_16x16x16_bf16 %0, %1, %2, 0" : "=&v"(c) : "v"(a), "v"(b)); }
DEV void mm16n(f4& c, v4u a, v4u b) { asm volatile("s_nop 1\n\tv_mfma_f32_16x16x32_bf16 %0, %1, %2, 0" : "=&v"(c) : "v"(a), "v"(b)); }
template <class A, class B> DEV void pin2(A& a, B& b) { asm volatile("" : "+v"(a), "+v"(b)); }
template <int IM> DEV void ld4(v4u& d, u32 voff, v4u rs, u32 so) { asm volatile("buffer_load_dwordx4 %0, %1, %2, %3 offen offset:%4 sc1" : "+v"(d) : "v"(voff), "s"(rs), "s"(so), "n"(IM) : "memory"); }
template <int IM> DEV void ld1(u32& d, u32 voff, v4u rs, u32 so) { asm volatile("buffer_load_dword %0, %1, %2, %3 offen offset:%4 sc1" : "+v"(d) : "v"(voff), "s"(rs), "s"(so), "n"(IM) : "memory"); }
template <int IM> DEV void ld4m(v4u& d, u32 voff, v4u rs, u32 so, u64 m) {
    asm volatile("s_mov_b64 exec, %5\n\tbuffer_load_dwordx4 %0, %1, %2, %3 offen offset:%4 sc1\n\ts_mov_b64 exec, -1" : "+v"(d) : "v"(voff), "s"(rs), "s"(so), "n"(IM), "s"(m) : "memory");
}
template <int IM, int R> DEV void ld4a(u32 voff, v4u rs, u32 so) { asm volatile("buffer_load_dwordx4 a[%c4:%c5], %0, %1, %2 offen offset:%3 sc1" ::"v"(voff), "s"(rs), "s"(so), "n"(IM), "n"(R), "n"(R + 3) : "memory"); }
template <int IM, int R> DEV void ld4v(u32 voff, v4u rs, u32 so) { asm volatile("buffer_load_dwordx4 v[%c4:%c5], %0, %1, %2 offen offset:%3 sc1" ::"v"(voff), "s"(rs), "s"(so), "n"(IM), "n"(R), "n"(R + 3) : "memory"); }
template <int R> DEV u32 mx3v(u32 a, u32 b) { u32 r; asm volatile("v_max3_u32 %0, %1, %2, v%c3" : "=v"(r) : "v"(a), "v"(b), "n"(R)); return r; }
template <int R> DEV u32 mx3vv(u32 a) { u32 r; asm volatile("v_max3_u32 %0, %1, v%c2, v%c3" : "=v"(r) : "v"(a), "n"(R), "n"(R + 2)); return r; }
template <int R, int A> DEV void swapw() { asm volatile("v_permlane32_swap_b32 v%c0, v%c1\n\tv_accvgpr_write_b32 a%c2, v%c0\n\tv_accvgpr_write_b32 a%c3, v%c1" ::"n"(R), "n"(R + 2), "n"(A), "n"(A + 1)); }
template <int R, int R2> DEV void addv() { asm volatile("v_add_f32 v%c0, v%c0, v%c1" ::"n"(R), "n"(R2)); }
template <int R> DEV float swapsum() { float r; asm volatile("s_nop 1\n\tv_permlane32_swap_b32 v%c1, v%c2\n\tv_add_f32 %0, v%c1, v%c2" : "=v"(r) : "n"(R + 1), "n"(R + 3)); return r; }
template <int IM, int R> DEV void ld1a(u32 voff, v4u rs, u32 so) { asm volatile("buffer_load_dword a%c4, %0, %1, %2 offen offset:%3 sc1" ::"v"(voff), "s"(rs), "s"(so), "n"(IM), "n"(R) : "memory"); }
template <int IM, int R> DEV void ld4am(u32 voff, v4u rs, u32 so, u64 m) {
    asm volatile("s_mov_b64 exec, %6\n\tbuffer_load_dwordx4 a[%c4:%c5], %0, %1, %2 offen offset:%3 sc1\n\ts_mov_b64 exec, -1" ::"v"(voff), "s"(rs), "s"(so), "n"(IM), "n"(R), "n"(R + 3), "s"(m) : "memory");
}
template <int IM> DEV void ld2(v2u& d, u32 voff, v4u rs, u32 so) { asm volatile("buffer_load_dwordx2 %0, %1, %2, %3 offen offset:%4 sc1" : "=v"(d) : "v"(voff), "s"(rs), "s"(so), "n"(IM) : "memory"); }
template <int IM> DEV void ld4o(v4u& d, u32 voff, v4u rs, u32 so) { asm volatile("buffer_load_dwordx4 %0, %1, %2, %3 offen offset:%4 sc1" : "=v"(d) : "v"(voff), "s"(rs), "s"(so), "n"(IM) : "memory"); }
template <int IM> DEV void ldh(u32& d, u32 voff, v4u rs, u32 so) { asm volatile("buffer_load_short_d16_hi %0, %1, %2, %3 offen offset:%4 sc1" : "+v"(d) : "v"(voff), "s"(rs), "s"(so), "n"(IM) : "memory"); }
template <int IM> DEV void st4(v4u d, u32 voff, v4u rs, u32 so) { asm volatile("buffer_store_dwordx4 %0, %1, %2, %3 offen offset:%4 sc1" ::"v"(d), "v"(voff), "s"(rs), "s"(so), "n"(IM) : "memory"); }
DEV void setm0(u32 v) { asm volatile("s_mov_b32 m0, %0" ::"s"(v) : "m0", "memory"); }
template <int IM> DEV void dmai(v4u rs, u32 voff, u32 so) { asm volatile("buffer_load_dwordx4 %0, %1, %2 offen offset:%3 sc1 lds" ::"v"(voff), "s"(rs), "s"(so), "n"(IM) : "memory"); }
DEV u32 pka(float a, float b) { u32 r; asm volatile("v_cvt_pk_bf16_f32 %0, %1, %2" : "=v"(r) : "v"(a), "v"(b)); return r; }
template <int N> DEV void waitl() { __builtin_amdgcn_s_waitcnt(0xc07f | (N << 8)); }
DEV u32 vsel(bool c, u32 a, u32 b) { asm volatile("" : "+v"(a)); asm volatile("" : "+v"(b)); return c ? a : b; }
DEV u32 ssel(bool c, u32 a, u32 b) { asm volatile("" : "+s"(a)); asm volatile("" : "+s"(b)); return c ? a : b; }
template <int N, class T> DEV void waitp(T& d) { asm volatile("s_waitcnt vmcnt(%1)" : "+v"(d) : "n"(N) : "memory"); }
template <int N> DEV void waitbar() { asm volatile("s_waitcnt vmcnt(%0) lgkmcnt(0)\n\ts_barrier" ::"n"(N) : "memory"); }
template <int N> DEV void pad3(f4& a, f4& b, f4& c) { asm volatile("s_nop %3" : "+v"(a), "+v"(b), "+v"(c) : "n"(N)); }
template <int I> struct IC { static constexpr int v = I; };
template <int B, int E, class F> DEV void sfor(F&& f) { if constexpr (B < E) { f(IC<B>{}); sfor<B + 1, E>(f); } }
#define LAM(I) [&](auto I) __attribute__((always_inline))
#define FENCE() __builtin_amdgcn_sched_barrier(0)
#define OPQV(x) asm volatile("" : "+v"(x))
#define OPQS(x) asm volatile("" : "+s"(x))
DEV float halves_sum(float a, float b) {
    auto r = __builtin_amdgcn_permlane32_swap(asu(a), asu(b), false, false);
    return asf(r[0]) + asf(r[1]);
}
DEV bool bad(u32 m) { return __builtin_amdgcn_ballot_w64(m == ~0u) != 0; }
#define STAMP_INIT()
template <int I> DEV void mark() { asm volatile("; MARK %0" ::"n"(I)); }
#define STAMP(i) mark<(i)>()
#define STAMP_END()
#define STAMPV(i, v)

DEV void hedgehog(const float (&t)[18], float a, u32 (&ph)[18]) {
    float mx = t[0], mn = t[0];
#pragma unroll
    for (int i = 1; i < 18; ++i) { mx = __builtin_fmaxf(mx, t[i]); mn = __builtin_fminf(mn, t[i]); }
    const float c1 = -a * mx, c2 = a * mn;
    float e1[18], e2[18], s1 = 0.f, s2 = 0.f;
#pragma unroll
    for (int i = 0; i < 18; ++i) {
        e1[i] = __builtin_amdgcn_exp2f(__builtin_fmaf(t[i], a, c1)); s1 = i ? s1 + e1[i] : e1[i];
        e2[i] = __builtin_amdgcn_exp2f(__builtin_fmaf(t[i], -a, c2)); s2 = i ? s2 + e2[i] : e2[i];
    }
    const float r1 = __builtin_amdgcn_rcpf(s1), r2 = __builtin_amdgcn_rcpf(s2);
#pragma unroll
    for (int i = 0; i < 9; ++i) { ph[i] = pk(e1[2 * i] * r1, e1[2 * i + 1] * r1); ph[9 + i] = pk(e2[2 * i] * r2, e2[2 * i + 1] * r2); }
}
DEV v4u tr2(u32 a0, u32 a1) {
    s4 lo = __builtin_amdgcn_ds_read_tr16_b64_v4i16((LAS s4*)a0), hi = __builtin_amdgcn_ds_read_tr16_b64_v4i16((LAS s4*)a1);
    v2u l = __builtin_bit_cast(v2u, lo), h = __builtin_bit_cast(v2u, hi);
    return (v4u){l.x, l.y, h.x, h.y};
}
DEV v2u y_val(f4 acc) { return (v2u){pk(acc[0], acc[1]), pk(acc[2], acc[3])}; }
DEV v4u y_pair(v2u a, v2u b) {
    auto r0 = __builtin_amdgcn_permlane16_swap(a.x, b.x, false, false), r1 = __builtin_amdgcn_permlane16_swap(a.y, b.y, false, false);
    return (v4u){r0[0], r1[0], r0[1], r1[1]};
}
template <int N, int n, int r>
DEV void conv_read(u32 aa, u32 ba, v4u (&A)[N], v4u (&B)[N]) {
    constexpr int i = n / 3, k = n % 3;
    if constexpr (k == 0) B[i] = lds4(ba + 64 * (8 * i - r + 7));
    else { const v2u v = lds2(aa + 2 + 64 * (r + 8 * i) + 8 * (k - 1)); if constexpr (k == 1) { A[i].x = v.x; A[i].y = v.y; } else { A[i].z = v.x; A[i].w = v.y; } }
}
constexpr int conv_ij(int N, int MASK, int n) {
    for (int d = 0; d < N; ++d) for (int i = 0; i < N; ++i) for (int j = i; j < N; ++j)
        if (((MASK >> j) & 1) && (i > j - i ? i : j - i) == d && n-- == 0) return i * 8 + j;
    return -1;
}
#define NPRE_ 10
#define SEND_ 212
#define UPA_ 8
#define UPO_ 12
constexpr int NPRE = NPRE_, NSITE = 13 + 4 + 40 + 9, SITE_END = SEND_;
constexpr int site_at(int q) { return q <= SITE_END ? NPRE + (q * (NSITE - NPRE) + SITE_END - 1) / SITE_END : NSITE; }
constexpr int xissued(int n) { return UPO_ + (5 * n + 3) / 4 < 32 ? UPO_ + (5 * n + 3) / 4 : 32; }
constexpr int kstep(int n) { return n % 9 < 8 ? 8 * (n / 9) + n % 9 : 32 + n / 9; }

__global__ void __launch_bounds__(256, 1) k_main(Args) {
    const void* ka = (const void*)(uintptr_t)__builtin_amdgcn_kernarg_segment_ptr();
    v4u rk, rv, rm, rq, rp, rx, vw, ro;
    const char *sk, *sh, *sD;
    int tid = __builtin_amdgcn_workitem_id_x(), b = __builtin_amdgcn_workgroup_id_x();
    sldi<0>(rk, ka); sldi<16>(rv, ka); sldi<144>(sk, ka); sldi<112>(vw, ka); sldi<96>(rx, ka); sldi<128>(ro, ka); sldi<152>(sh, ka); sldi<160>(sD, ka);
    OPQS(b); OPQV(tid);
    const int bs = b >> 3;
    if constexpr (MAP == 1) b = (b & 6) + (b >> 7) + 64 * (b & 1) + (b & 56) + 2 * (b & 64);
    if constexpr (MAP == 2) b = (b & 4) + ((b >> 6) & 3) + 32 * (b & 3) + (b & 24) + 4 * (b & 32);
    int wave = __builtin_amdgcn_readfirstlane(tid >> 6);
    sld(rm, ka, (wave >> 1) * 16); sld(rq, ka, (b & 24) == 24 ? 48 : 32); sld(rp, ka, 64 + (b & 8) * 2);
    OPQS(b); OPQS(wave); OPQV(tid);
    {
        u64 pc;
        asm volatile("s_getpc_b64 %0" : "=s"(pc));
        const u32 o = umin(tid * 8 + bs * 2048, CODE - 512);
        asm volatile("s_mov_b32 m0, %2\n\ts_nop 0\n\tglobal_load_lds_dword %0, %1" ::"v"(o), "s"(pc), "s"(L_EX) : "m0", "memory");
    }
    STAMP_INIT();
    asm volatile("" ::: "a255", "v255");
    u64 ep;
    v2u sw0, sw1;
    {
        const int lane = tid & 63, c = lane & 15, cg = ((b >> 4) & 6) + ((b >> 3) & 1), tg = 4 * (b & 7) + 2 * (b >> 7) + ((b >> 4) & 1);
        u32 s0 = c * 8 + (wave * 128 + cg * 576), s1 = (c & 7) * 8 + cg * 576, vo[5], lo[5];
        const u32 l0 = ((lane * 7282u) >> 16) * 1008 + lane * 16, t0 = (64 * tg + 7 * wave) * 1152 + cg * 144;
#pragma unroll
        for (int i = 0; i < 5; ++i) {
            const int isv = i < 2 ? 0 : i > 2 ? 1 : wave >> 1;
            vo[i] = l0 + (t0 + (28 * i - 3 - 67 * isv) * 1152); lo[i] = P_KS + wave * 1008 + i * 4032 + isv * (P_VS - 10080);
            OPQV(vo[i]); OPQS(lo[i]);
        }
        pin2(s0, s1);
        FENCE();
        asm volatile("s_waitcnt lgkmcnt(0)" : "+s"(rk), "+s"(rv), "+s"(rm), "+s"(rq), "+s"(rp), "+s"(rx), "+s"(vw), "+s"(ro), "+s"(sk), "+s"(sh), "+s"(sD));
        STAMP(40);
#pragma unroll
        for (int i = 0; i < 5; ++i) dma_b(i < 2 ? rk : i > 2 ? rv : rm, vo[i], lo[i]);
        asm volatile("global_load_dwordx2 %0, %1, %2" : "=v"(sw0) : "v"(s0), "s"(sk) : "memory");
        asm volatile("global_load_dwordx2 %0, %1, %2 offset:512" : "=v"(sw1) : "v"(s1), "s"(sk) : "memory");
        sld(ep, (const void*)(((u64)vw.y << 32) | vw.x), W_E);
    }
    OPQS(b); OPQS(wave); OPQV(tid);
    const int lane = tid & 63, rg = 2 * (b & 7) + (b >> 7), p = (b >> 3) & 15;
    const int Pg = p >> 2, tw = p & 3, pw = 4 * Pg + wave, ci = (rg + p) & 15;
    const bool conv = ci < 12;
    u32 l16 = lane * 16, l4 = lane * 4;
    OPQV(l16); OPQV(l4);
    const int ch0 = 36 * p + 3 * ci;
    u32 xr[9], kvw[3], kvA, kvB, l8, lP, l16B, cpo, csd;
    {
        const int sec = p & 3, cg = 2 * Pg + (p & 1), tg = 2 * rg + ((p >> 1) & 1);
        const int c = lane & 15;
        FENCE();
        STAMP(41);
        const v4u z4 = {0, 0, 0, 0};
        u32 mk0, mk1, ua0, ua1, uo0, uo1, aw, al_, ap, d0_, d1_, d2_, cso, axs, aid, azc;
        v2u Bi; v4u Sel;
        const bool s3 = sec == 3;
        const u32 wf_ = rg >> 2, jl = rg & 3;
        u32 lw, ll, lx, lf;
        const u32 wst = s3 ? 1152 : 3456;
        {
            const u32 cb = (sec < 2 ? (sec + 1) * 1152 : 0) + Pg * 288;
            lw = ((lane * 3641u) >> 16) * (wst - 288) + l16 + ((36 * rg + 3 * wave) * wst + cb);
            lf = vsel(lane < 54, l16 - ((lane * 21846u) >> 16) * 12 + (Pg * 5184 + wave * 1296), OOB);
            lx = ((lane * 3450u) >> 16) * 848 + l16 + ((128 * rg + 32 * tw + 3 * wave) * 1152 + Pg * 288);
        }
        auto wdma = LAM(K_) {
            constexpr int k = K_.v, i = k / 3;
            if constexpr (k == 7) {
                const u32 cb = (sec < 2 ? (sec + 1) * 1152 : 0) + Pg * 288, r4 = (lane * 43u) >> 8, sg = ((s3 ? 0 : (4 * Pg + wf_) & 14) + 4 * jl + wave) & 15;
                ll = vsel(lane < 24, r4 * (wst - 96) + (r4 >> 1) * (16 * wst) + l16 + ((36 * sg + 16) * wst + cb + ((72 * wf_) & ~15u)), OOB);
                dma_b(rq, ll, P_LS + wave * 1024);
            } else if constexpr (k % 3 == 0) dma_s(rq, lw, 12 * i * wst, P_WS + wave * 864 + i * 3456);
            else if constexpr (k % 3 == 1) dma_s(rp, lf, i * 648, P_PH + wave * 3072 + i * 1536);
            else dma_s(rx, lx, 12 * i * 1152, P_XS + wave * 912 + i * 3648);
            FENCE();
        };
        u32 ib, bA, bB, lp, ds, dr;
        auto wadr = LAM(K_) {
            constexpr int k = K_.v;
            const u32 g = lane >> 4, m = lane & 31, kh = lane >> 5;
            if constexpr (k == 0) {
                const u32 hD = (m >> 2) & 1;
                aw = (18 * hD + (m & 3) + 4 * (m >> 3)) * 288 + 16 * kh + (P_WS + ((72 * wave) & ~15u));
                pin(aw);
            }
            if constexpr (k == 1) {
                const u32 hD = (m >> 2) & 1;
                al_ = ((((m >> 1) & 1) + 2 * ((m >> 3) & 1)) * 1024 + (2 * hD + (m & 1)) * 96) * (wave == wf_) + 16 * kh + (wave == wf_ ? P_LS : L_Z);
                pin(al_);
            }
            if constexpr (k == 2) {
                ap = (8 * kh + (c >> 2)) * 48 + 32 * (g & 1) + 8 * (c & 3) + (P_PH + (64 * wave - 4 * (wave & 1)) * 48);
                pin(ap);
            }
            if constexpr (k == 3) {
                ib = W_IMG + pw * IMG; const u32 ob = W_WOT + Pg * WOG;
                bA = ssel(s3, ob + wave * 36864, ib + sec * L_V + (sec >> 1) * (L_Q - 2 * L_V));
                bB = ssel(s3, ob + 4 * 36864 + 128 * (wave >> 1) + 32 * (wave & 1), ib + L_QT + 128 * sec);
                OPQS(ib); OPQS(bA); OPQS(bB);
            }
            if constexpr (k == 4) {
                lp = vsel(m < 16, ((m & 3) + 8 * (m >> 2) + 32 * kh) * 16 + bA, vsel(m < 18, (m & 1) * (s3 ? 16 : 32) + kh * (s3 ? 512 : 16) + bB, OOB));
                pin(lp);
            }
            if constexpr (k == 5) {
                ds = vsel(m < 16, 1024, s3 ? 256 : QTS); dr = vsel(m < 16, 2048, s3 ? 1024 : 2 * QTS);
                d0_ = lp + ((rg - (s3 ? 0 : pw & 14)) & 15) * dr; d1_ = d0_ + ds;
                pin2(ds, dr); pin2(d0_, d1_);
            }
            if constexpr (k == 6) {
                d2_ = umax(lp + (16 + (jl >> 1)) * dr + (jl & 1) * ds, wave == wf_ ? 0u : OOB);
                pin(d2_);
            }
            if constexpr (k == 7) {
                cso = vsel(m < (s3 ? 0 : 18), (18 * kh + m) * 4 + (W_CSP + pw * CSB + rg * 576 + sec * 192), OOB);
                pin(cso);
            }
            if constexpr (k == 8) {
                axs = m * 304 + 36 * kh + (P_XS + 72 * wave);
                pin(axs);
            }
            if constexpr (k == 9) {
                aid = vsel(lane < 36 * (sec & 1), lane * 50 + (lane >= 18 ? 1536 - 900 : 0) + (P_PH + wave * 3072), L_END - 16);
                pin(aid);
            }
            if constexpr (k == 10) {
                azc = vsel(lane < (sec ? 0 : 6), (lane & 1) * 16 + ((lane >> 1) + (lane < 4 ? 2 * rg : 30 + jl)) * QTS + (ib + L_QT + 384), OOB);
                pin(azc);
            }
        };
        sfor<0, WD0>(wdma);
        STAMP(37);
        {
            const u32 g = lane >> 4, m = lane & 31, kh = lane >> 5;
            const u32 hm = g == (c >> 2) ? ((c & 1) ? 0xffff0000u : 0xffffu) : 0u;
            mk0 = (c & 2) ? 0u : hm; mk1 = (c & 2) ? hm : 0u;
            Bi = (v2u){mk0 & 0x3f803f80u, mk1 & 0x3f803f80u}; Sel = (v4u){Bi.x, Bi.y, Bi.x, Bi.y};
            const u32 r0 = (8 * (c >> 2) + (c & 3)) * 144;
            ua0 = r0 + 8 * g + (P_KS + 32 * wave);
            ua1 = r0 + 32 * 144 * kh + 8 * (g & 1) + (P_KS + 128 + 576 * (wave & 1));
            uo0 = c * 4096 + 16 * g;
            uo1 = (c & 7) * 4096 + 64 * (c >> 3) + 16 * g + (wave < 2 ? 64 * 4096 + 8 - 8 * wave : OOB - (1u << 20));
            pin2(mk0, mk1); pin2(Bi, Sel); pin2(ua0, ua1); pin2(uo0, uo1);
            *(LAS v4u*)(l16 + kh * 1024 + (P_PH + 1024 + wave * 3072)) = z4;
            *(LAS v4u*)(vsel(lane < (wave ? 30 : 6), l16 + ssel(wave, L_Q + 4 * HCS - CVS + wave * CVS, L_Z), L_END - 16)) = z4;
        }
        FENCE();
        FENCE();
        sfor<0, 11>(LAM(K_) {
            constexpr int k = K_.v, d0 = WD0 + k * (9 - WD0) / WDN, d1 = WD0 + (k + 1) * (9 - WD0) / WDN;
            if constexpr (k < WDN) sfor<d0, d1>(wdma);
            wadr(K_);
            FENCE();
        });
        STAMP(42);
        FENCE();
        STAMP(26);
        asm volatile("s_waitcnt vmcnt(9)" : "+v"(sw0), "+v"(sw1)::"memory");
        v2u Bd[2][4];
#pragma unroll
        for (int s = 0; s < 2; ++s)
#pragma unroll
            for (int j = 0; j < 4; ++j) {
                const v2u sw = s ? sw1 : sw0;
                const u32 wd = __builtin_amdgcn_perm(0u, j < 2 ? sw.y : sw.x, (j & 1) ? 0x01000100u : 0x03020302u);
                Bd[s][j] = (v2u){wd & mk0, wd & mk1};
                pin(Bd[s][j]);
            }
        u32 so_u = W_UT + 128 * tg + cg * 72 * 4096, so_u2 = so_u + wave * 65536;
        OPQS(so_u); OPQS(so_u2);
        FENCE();
        barrier();
        STAMP(27);
        {
            u32 uo[5][2];
            v2u ka[5][5]; f4 pr[5][5], sm[5];
            v4u bb[2];
#pragma unroll
            for (int n = 0; n < 25; ++n) {
                const int T = n < 10 ? n >> 1 : n % 5, j = n < 10 ? n & 1 : n / 5;
                ka[T][j] = lds2v((T < 4 ? ua0 + (32 * (T >> 1) + 4 * (T & 1)) * 144 : ua1) + (j < 4 ? j * 144 : P_VS));
            }
            FENCE();
            auto P = [&](auto T_, auto J_) __attribute__((always_inline)) { constexpr int T = T_.v, j = J_.v; mm16k(pr[T][j], ka[T][j], j < 4 ? Bd[T == 4][j] : Bi); };
            sfor<0, 5>(LAM(T_) { P(T_, IC<0>{}); P(T_, IC<1>{}); });
            STAMP(52);
            sfor<0, 16>(LAM(I_) {
                constexpr int I = I_.v, T = I % 5, ph = I / 5 + 1, U = (I + 4) % 5;
                if constexpr (I < 15) {
                    f4& a = ph == 1 ? pr[T][0] : sm[T];
                    const u32 w0 = pka(a[0], a[1]), w1 = pka(a[2], a[3]), w2 = pka(pr[T][ph][0], pr[T][ph][1]), w3 = pka(pr[T][ph][2], pr[T][ph][3]);
                    bb[I & 1] = (v4u){w0, w1, w2, w3};
                    P(IC<T>{}, IC<ph + 1>{});
                } else {
                    uo[0][0] = pka(sm[0][0], sm[0][1]); uo[0][1] = pka(sm[0][2], sm[0][3]);
                }
                if constexpr (I > 0) mm16<true>(sm[U], Sel, bb[(I - 1) & 1]);
            });
            STAMP(53);
            sfor<0, 5>(LAM(T_) {
                constexpr int T = T_.v;
                u32 w0, w1;
                if constexpr (T) { w0 = pka(sm[T][0], sm[T][1]); w1 = pka(sm[T][2], sm[T][3]); } else { w0 = uo[0][0]; w1 = uo[0][1]; }
                pin(pr[T][4]);
                FENCE();
                uo[T][0] = pk(bf_hi(w1) * pr[T][4][3], bf_lo(w1) * pr[T][4][2]);
                uo[T][1] = pk(bf_hi(w0) * pr[T][4][1], bf_lo(w0) * pr[T][4][0]);
                if constexpr (T == 1 || T == 3) bst4((v4u){uo[T][0], uo[T][1], uo[T - 1][0], uo[T - 1][1]}, vw, uo0 + 32 * (T - 1), so_u2, SC1);
                else if constexpr (T == 4) bst2((v2u){uo[4][0], uo[4][1]}, vw, uo1, so_u, SC1);
                else pin2(uo[T][0], uo[T][1]);
                FENCE();
            });
        }
        STAMP(28);
        asm volatile("s_waitcnt vmcnt(3)\n\ts_barrier" ::: "memory");
        STAMP(54);
        {
            u32 one2 = 0x3f803f80u;
            OPQS(one2);
            *(LAS u16*)(aid) = 0x3f80;
            v4u bw[2][2], wa[2][3];
            bw[0][0] = tr2(ap, ap + 192); wa[0][0] = lds4(aw); wa[1][0] = lds4(al_);
            bw[0][1] = tr2(ap + 768, ap + 960); wa[0][1] = lds4(aw + 32); wa[1][1] = lds4(al_ + 32);
            bw[1][0] = tr2(ap + 1440, ap + 1440 + 192);
            bw[1][1] = tr2(ap + 1440 + 768, ap + 1440 + 960); wa[0][2] = lds4(aw + 64); wa[1][2] = lds4(al_ + 64);
            FENCE();
#pragma unroll
            for (int j = 0; j < 9; ++j) xr[j] = *(const LAS u32*)(axs + 4 * j);
            FENCE();
            f16v d[2][2];
#pragma unroll
            for (int i = 0; i < 4; ++i) { mm32<true>(d[i >> 1][i & 1], wa[i & 1][i >> 1], bw[i >> 1][0]); mm32<false>(d[i >> 1][i & 1], wa[i & 1][(i >> 1) + 1], bw[i >> 1][1]); }
            FENCE();
            STAMP(55);
            {
                float s1 = 0.f, s2 = 0.f;
#pragma unroll
                for (int j = 0; j < 9; ++j) { s1 = dot2(xr[j], one2, s1); s2 = dot2(xr[j], xr[j], s2); }
                const u32 so = W_XF0 + ((rg * 4 + tw) * 16 + pw) * XS;
                bst4((v4u){xr[0], xr[1], xr[2], xr[3]}, vw, l16, so, SC1);
                bst4((v4u){xr[4], xr[5], xr[6], xr[7]}, vw, l16 + 1024, so, SC1);
                l8 = 2 * l4;
                bst2((v2u){xr[8], asu(halves_sum(s1, s2))}, vw, (lane & 31) * 16 + (lane >> 5) * 8 + 2048, so, SC1);
                if (!SKIP || sec == 0) bst4(z4, vw, azc, 0, SC1);
            }
            FENCE();
            float cs[2];
#pragma unroll
            for (int hh = 0; hh < 2; ++hh) {
                asm volatile("" : "+v"(d[hh][0]), "+v"(d[hh][1]));
                u32 w[12];
#pragma unroll
                for (int j = 0; j < 12; ++j) w[j] = pk(d[hh][j >> 3][2 * (j & 7)], d[hh][j >> 3][2 * (j & 7) + 1]);
                bst4((v4u){w[0], w[1], w[2], w[3]}, vw, d0_ + 64 * hh, 0, SC1);
                bst4((v4u){w[4], w[5], w[6], w[7]}, vw, d1_ + 64 * hh, 0, SC1);
                if (!SKIP || wave == wf_) bst4((v4u){w[8], w[9], w[10], w[11]}, vw, d2_ + 64 * hh, 0, SC1);
                cs[hh] = 0.f;
#pragma unroll
                for (int j = 0; j < 12; ++j) cs[hh] = dot2(w[j], one2, cs[hh]);
                FENCE();
            }
            bst1(asu(halves_sum(cs[0], cs[1])), vw, cso, 0, SC1);
        }
        v4u hd[3]; v2u he[3], hz;
        {
            const char* gp = sh + umin(ch0, 573) * 4096;
            const u32 o1 = tid * 16, o2 = (o1 + 16) & 4095;
            gld2(hz, umin(lane >> 3, 2) * 4096, gp);
#pragma unroll
            for (int i = 0; i < 3; ++i) { gld4<0>(hd[i], o1, gp + i * 4096); gld2(he[i], o2, gp + i * 4096); }
        }
        STAMP(29);
        asm volatile("s_waitcnt vmcnt(7)\n\ts_barrier" ::: "memory");
        OPQS(ep);
        ep += 1;
        const v2u epv = {(u32)ep, (u32)(ep >> 32)};
        if (tid == 0) bst2(epv, vw, 0, W_F + (rg * 16 + p) * 128, SC1);
        STAMP(1);
        {
            const u32 e = (lane >> 4) & 1, kg = lane >> 4;
#pragma unroll
            for (int j = 0; j < 3; ++j) {
                const u32 pi = 16 * (wave + 4 * j) + c, hh = pi >= 86, ip = pi - 86 * hh, q = ip - 72, s = 2 * (ip - 84) + e;
                u32 v = vsel(ip < 64, (ip & 15) * 80 + (ip >> 4) * 8 + 32 * e, (2 * ip + e) * 80 + 64 - 128 * 80);
                v = vsel(ip < 72, v, (16 + (q >> 2)) * 80 + (q & 3) * 8 + 32 * e);
                v = vsel(ip < 84, v, s * 80 + 16 * 80 + 64);
                kvw[j] = vsel(lane >= 32 || pi >= 172 || s == 3, L_EX + l8, v + hh * 1520 + L_KVA);
                OPQV(kvw[j]);
            }
            *(LAS v2u*)(vsel(tid < 38, tid * 80 + (L_KVA + 72), L_END - 16)) = (v2u){0, 0};
            const bool dt = wave & 1;
            kvA = dt ? vsel(c < 3, 1152 + (c * 4 + kg) * 16, OOB) : l16;
            kvB = vsel(lane < (dt ? 4 : 16), l8 + (dt ? 1344 : 1024), OOB);
            l16B = l16 + XS; lP = (lane & 31) * 16 + (lane >> 5) * XS;
            const u32 sc = (lane * 57u) >> 10, r = lane - 18 * sc, hh = r >= 9, pr = r - 9 * hh;
            cpo = vsel(lane < 54, sc * (192 - 144) + l8, OOB);
            csd = vsel(lane < 54, hh * 224 + vsel(pr < 8, sc * 64 + pr * 8, 192 + sc * 8) + L_CS, L_EX + 2048 + l8);
            pin2(kvA, kvB); pin2(l16B, lP); pin2(cpo, csd); pin(l8);
        }
        FENCE();
        if (wave) waitv<0>(); else waitv<1>();
        STAMP(51);
        if (conv) {
            u32 o = L_Q + 32 + tid * 16;
            OPQV(o);
#pragma unroll
            for (int i = 0; i < 3; ++i) {
                asm volatile("" : "+v"(hd[i]), "+v"(he[i]));
                const u32 d0 = hd[i].x, d1 = hd[i].y, d2 = hd[i].z, d3 = hd[i].w, d4 = he[i].x, d5 = he[i].y;
                const u32 a0 = __builtin_amdgcn_alignbit(d1, d0, 16), a1 = __builtin_amdgcn_alignbit(d2, d1, 16), a2 = __builtin_amdgcn_alignbit(d3, d2, 16),
                          a3 = __builtin_amdgcn_alignbit(d4, d3, 16), a4 = __builtin_amdgcn_alignbit(d5, d4, 16);
                *(LAS v4u*)(o + i * CVS) = hd[i];
                *(LAS v4u*)(o + i * CVS + HCS) = (v4u){a0, a1, a2, a3};
                *(LAS v4u*)(o + i * CVS + 2 * HCS) = (v4u){d1, d2, d3, d4};
                *(LAS v4u*)(o + i * CVS + 3 * HCS) = (v4u){a1, a2, a3, a4};
            }
            asm volatile("" : "+v"(hz));
            const u32 k = (lane >> 1) & 3;
            const u64 t = (lane & 1) && k ? (((u64)hz.y << 32) | hz.x) << (64 - 16 * k) : 0;
            if (tid < 24) *(LAS v4u*)(L_Q + (lane >> 3) * CVS + k * HCS + (lane & 1) * 16) = (v4u){0, 0, (u32)t, (u32)(t >> 32)};
        }
    }
    auto xop = [&](auto Q_, u32 so) __attribute__((always_inline)) {
        constexpr int j = Q_.v / 5, k = Q_.v % 5;
        if constexpr (k == 0) ld4a<0, 16 * j>(l16, vw, so);
        if constexpr (k == 1) ld4a<1024, 16 * j + 4>(l16, vw, so);
        if constexpr (k == 2) ld4v<2048, 224 + 4 * j>(lP, vw, so);
        if constexpr (k == 3) ld4a<0, 16 * j + 8>(l16B, vw, so);
        if constexpr (k == 4) ld4a<1024, 16 * j + 12>(l16B, vw, so);
    };
    {
        u32 D2[3];
#pragma unroll
        for (int i = 0; i < 3; ++i) { const u32 cc = umin(ch0, 573) + i, t = ((const __attribute__((address_space(4))) u32*)(uintptr_t)sD)[cc >> 1]; D2[i] = (cc & 1 ? t >> 16 : t & 0xffffu) * 0x10001u; OPQS(D2[i]); }
        const int bq = lane & 15, g = lane >> 4, cc = (bq + 1) & 3;
        u32 aa = L_Q + cc * HCS + 2 * (bq - 8 * g + 24 - cc), ba = L_Q + 4 * HCS + 480 + 2 * (16 * bq + 8 * g - 16) - 7 * 64;
        u32 yo = (bq >> 3) * YB + (bq & 7) * 32 + 8 * g, yp = (bq >> 3) * YB + (bq & 7) * 32 + 16 * (g >> 1) + (g & 1) * (2 * YB);
        v4u dm;
        {
            const u32 e = 7 - (bq & 7), hm = g == 2 + (bq >> 3) ? ((e & 1) ? 0xffff0000u : 0xffffu) : 0u;
#pragma unroll
            for (int i = 0; i < 4; ++i) dm[i] = (e >> 1) == i ? hm : 0u;
        }
        if (wave < 3) { aa += wave * CVS; ba += wave * CVS; }
        pin2(aa, ba); pin2(yo, yp); pin(dm);
        const u32 xb = W_XF0 + (rg * 4 + wave) * 16 * XS;
        u32 io = wave * 9216, ig = W_IMG + p * IMG + io, iq = umin(4 * wave, 11) * 1024 + L_QT - 8192 - io, xo = xb + (p & 14) * XS, uo = W_UT + ch0 * 4096 + wave * 1024, co = W_CSP + p * CSB + wave * (4 * 576);
        u32 fo = W_F + (wave ? rg * 16 + bq : (lane >> 2) * 16 + 4 * Pg + (lane & 3)) * 128;
        OPQS(io); OPQS(ig); OPQS(iq); OPQS(xo); OPQS(uo); OPQS(co); OPQV(fo);
        FENCE();
        if (wave < 2) {
#pragma unroll 1
            for (int sp = 0; sp < SPIN; ++sp) {
                asm volatile("" ::: "memory");
                const v2u f = bld2(vw, fo, 0, SC1);
                if (__builtin_amdgcn_ballot_w64(f.x != (u32)ep || f.y != (u32)(ep >> 32)) == 0) break;
                __builtin_amdgcn_s_sleep(1);
            }
        }
        barrier();
        STAMP(2);
        v2u cp[4];
        auto site = LAM(N_) {
            constexpr int n = N_.v, v = n - 57;
            if constexpr (n < 9) dmai<(n & 3) * 1024>(vw, l16, ig);
            else if constexpr (n < 13) dmai<(n - 9) * 1024>(vw, l16, ig);
            else if constexpr (n < 17) ld2<(n - 13) * 576>(cp[n - 13], cpo, vw, co);
            else if constexpr (n < 57) {
                if constexpr (n == 37) waitv<20>();
                xop(IC<n - 17>{}, xo);
                if constexpr ((n - 17) % 5 == 4 && n < 56) { xo += 2 * XS; xo = xo == xb + 16 * XS ? xb : xo; OPQS(xo); }
            } else dmai<(v & 3) * 1024>(vw, l16, ig);
            if constexpr (n == 3 || n == 7 || n == 60 || n == 64) { io += 4096; ig += 4096; }
            if constexpr (n == 8) { io += iq; ig += iq; }
            if constexpr (n == 12) { io += L_V - 8192 - iq; ig += L_V - 8192 - iq; }
            if constexpr (n == 3 || n == 7 || n == 8 || n == 12 || n == 60 || n == 64) { OPQS(io); OPQS(ig); setm0(io); }
        };
        auto site1 = LAM(N_) { site(N_); if constexpr (N_.v == 3 || N_.v == 7 || N_.v == 8 || N_.v == 60 || N_.v == 64) nop2(); };
        if (conv) {
#pragma unroll
            for (int i = 0; i < 3; ++i) dma_ws(vw, l16, uo + i * 4096, L_Q + i * CVS + 4 * HCS + 480 + wave * 1024);
            setm0(io); nop2();
            sfor<0, NPRE>(site1);
            asm volatile("s_waitcnt vmcnt(%0)\n\ts_barrier" ::"n"(NPRE) : "memory");
            STAMP(3);
            if (wave < 3) {
                f4 acc[8];
                v4u A[2][8], B[2][8];
                sfor<0, 24>(LAM(N_) { conv_read<8, N_.v, 0>(aa, ba, A[0], B[0]); });
                const u32 dd = wave == 0 ? D2[0] : wave == 1 ? D2[1] : D2[2];
                v4u ad = {dm.x & dd, dm.y & dd, dm.z & dd, dm.w & dd};
                pin(ad);
                FENCE();
                sfor<0, 5>(LAM(J_) { constexpr int j = J_.v ? J_.v + 3 : 0; mm16<true>(acc[j], ad, B[0][j]); });
                FENCE();
                sfor<0, 8 * 27>(LAM(Q_) {
                    constexpr int r = Q_.v / 27, n = Q_.v % 27, i = conv_ij(8, 0xf1, n) >> 3, j = conv_ij(8, 0xf1, n) & 7;
                    if constexpr (r && n == 0) waitl<9>();
                    if constexpr (r && n == 6) waitl<(r < 7 ? 6 : 0)>();
                    mm16<false>(acc[j], A[r & 1][i], B[r & 1][j - i]);
                    if constexpr (r < 7 && n < 24) conv_read<8, n, r + 1>(aa, ba, A[(r + 1) & 1], B[(r + 1) & 1]);
                    sfor<site_at(Q_.v), site_at(Q_.v + 1)>(site);
                    FENCE();
                });
                asm volatile("s_nop 7" : "+v"(acc[0]), "+v"(acc[4]), "+v"(acc[5]), "+v"(acc[6]), "+v"(acc[7]));
                const u32 so = W_Y + (ch0 + wave) * 256;
                const v2u y0 = y_val(acc[0]);
                const v4u y4 = y_pair(y_val(acc[4]), y_val(acc[5])), y6 = y_pair(y_val(acc[6]), y_val(acc[7]));
                bst2(y0, vw, yo, so, SC1);
                bst4(y4, vw, yp, so + 8 * YB, SC1);
                bst4(y6, vw, yp, so + 12 * YB, SC1);
            } else {
                f4 acc[2][4];
                v4u A[2][4], B[2][4], ad;
                v2u y1; v4u y2;
                sfor<0, 12>(LAM(N_) { conv_read<4, N_.v, 0>(aa, ba, A[0], B[0]); });
                FENCE();
                sfor<0, 3 * 8 * 9>(LAM(Q_) {
                    constexpr int cn = Q_.v / 72, r = Q_.v % 72 / 9, n = Q_.v % 9, i = conv_ij(4, 0xe, n) >> 3, j = conv_ij(4, 0xe, n) & 7;
                    constexpr int nc = r < 7 ? cn : cn + 1, nr = (r + 1) & 7;
                    if constexpr (r == 0 && n == 0) {
                        ad = (v4u){dm.x & D2[cn], dm.y & D2[cn], dm.z & D2[cn], dm.w & D2[cn]};
                        pin(ad);
                        waitl<0>();
                        nop2();
                        FENCE();
                        sfor<1, 4>(LAM(J_) { mm16<true>(acc[cn & 1][J_.v], ad, B[0][J_.v]); });
                    } else {
                        if constexpr (n == 0) waitl<3>();
                        if constexpr (n == 7) waitl<(nc < 3 ? 10 : 0)>();
                    }
                    mm16<false>(acc[cn & 1][j], A[r & 1][i], B[r & 1][j - i]);
                    if constexpr (nc < 3) {
                        if constexpr (n < 3) { conv_read<4, 2 * n, nr>(aa + nc * CVS, ba + nc * CVS, A[nr & 1], B[nr & 1]); conv_read<4, 2 * n + 1, nr>(aa + nc * CVS, ba + nc * CVS, A[nr & 1], B[nr & 1]); }
                        else conv_read<4, n + 3, nr>(aa + nc * CVS, ba + nc * CVS, A[nr & 1], B[nr & 1]);
                    }
                    if constexpr (cn > 0 && r == 1) {
                        const u32 so = W_Y + (ch0 + cn - 1) * 256;
                        if constexpr (n == 3) { pin(acc[(cn - 1) & 1][1]); y1 = y_val(acc[(cn - 1) & 1][1]); pin(y1); }
                        if constexpr (n == 4) bst2(y1, vw, yo, so + 2 * YB, SC1);
                        if constexpr (n == 5) { pin(acc[(cn - 1) & 1][2]); y1 = y_val(acc[(cn - 1) & 1][2]); pin(y1); }
                        if constexpr (n == 6) { pin(acc[(cn - 1) & 1][3]); y2 = y_pair(y1, y_val(acc[(cn - 1) & 1][3])); pin(y2); }
                        if constexpr (n == 7) bst4(y2, vw, yp, so + 4 * YB, SC1);
                    }
                    sfor<site_at(Q_.v), site_at(Q_.v + 1)>(site);
                    FENCE();
                });
                asm volatile("s_nop 7" : "+v"(acc[0][1]), "+v"(acc[0][2]), "+v"(acc[0][3]));
                const u32 so = W_Y + (ch0 + 2) * 256;
                y1 = y_val(acc[0][1]); y2 = y_pair(y_val(acc[0][2]), y_val(acc[0][3]));
                bst2(y1, vw, yo, so + 2 * YB, SC1);
                bst4(y2, vw, yp, so + 4 * YB, SC1);
            }
        } else {
            setm0(io); nop2();
            sfor<0, NSITE>(site1);
        }
        STAMP(14);
        asm volatile("" : "+v"(cp[0]), "+v"(cp[1]), "+v"(cp[2]), "+v"(cp[3]));
        *(LAS f2*)(L_EX + wave * 512 + l8) = (f2){(asf(cp[0].x) + asf(cp[1].x)) + (asf(cp[2].x) + asf(cp[3].x)), (asf(cp[0].y) + asf(cp[1].y)) + (asf(cp[2].y) + asf(cp[3].y))};
        barrier();
    }
    u32 yv[9];
    v4u wv[17];
    STAMP(15);

    sfor<0, 2>(LAM(L_) {
        constexpr int L = L_.v;
        int ln = lane;
        OPQV(ln); OPQV(l16); OPQV(l4);
        const int tl = ln & 31, h = ln >> 5, row = ln & 15, kg = ln >> 4, gq = row >> 2, rq = row & 3;
        const bool qa = (gq & 1) == (kg & 1);
        u32 aV_ = L_V + l16, aQ_ = L_Q + l16;
        u32 aKV = L_QT + vsel(qa, (rq >> 1) * 128 + (gq >> 1) * 64 + (rq & 1) * 32 + (kg >> 1) * 16, 400 - 16 * (kg >> 1));
        u32 aQq, aCS, aS;
        OPQV(aV_); OPQV(aKV); OPQV(aQ_);
        u32 nretry = 0;
        const u32 wb = W_WOT + Pg * WOG + wave * 36864, wlb = W_WOT + Pg * WOG + 4 * 36864 + (wave >> 1) * 9216;
        u32 wso = wb;
        auto wsite = LAM(N_) {
            constexpr int n = N_.v, k = (n < 36 ? n : n - 36) % 4;
            if constexpr (L == 0 && n < 45) {
                if constexpr (n == 36) wso = wlb; else if constexpr (n && k == 0) wso += 4096;
                if constexpr (k == 0) OPQS(wso);
                if constexpr (n < 28) ld4a<k * 1024, 144 + 4 * n>(l16, vw, wso);
                else ld4o<k * 1024>(wv[n - 28], l16, vw, wso);
            }
        };
        f16v aK; f4 qKV;
        float mean, al, rstd;
        v4u rV[4], cK[5];
        {
            const u32 xb = W_XF1 + (rg * 4 + wave) * 16 * XS;
            u32 xs[8];
            float ts;
            auto xsite = LAM(Q_) { if constexpr (L == 1) xop(IC<Q_.v / 4 * 5 + Q_.v % 4 + (Q_.v % 4 > 1)>{}, xs[Q_.v / 4]); };
            auto stat = [&]() __attribute__((always_inline)) {
                auto r = __builtin_amdgcn_permlane32_swap(asu(ts), asu(ts), false, false);
                mean = asf(r[0]) * (1.f / 576.f);
                const float var = __builtin_fmaxf(asf(r[1]) * (1.f / 576.f) - mean * mean, 0.f);
                rstd = __builtin_amdgcn_rsqf(var + 1e-5f);
                al = rstd * 1.4426950408889634f;
                pin2(mean, al); pin(rstd);
            };
            if constexpr (L == 1) {
#pragma unroll
                for (int j = 0; j < 8; ++j) { xs[j] = xb + (((p & 14) + 2 * j) & 15) * XS; OPQS(xs[j]); }
                FENCE();
                if constexpr (XSL) __builtin_amdgcn_s_sleep(XSL);
                sfor<0, 8>(LAM(J_) { xop(IC<5 * J_.v + 2>{}, xs[J_.v]); });
                sfor<0, UPA_>(xsite);
                waitv<UPA_>();
                u32 m, m2;
                asm volatile("v_max3_u32 %0, v224, v226, v228\n\tv_max3_u32 %1, v230, v232, v234\n\tv_max3_u32 %0, %0, v236, v238\n\tv_max3_u32 %1, %1, v240, v242\n\t"
                             "v_max3_u32 %0, %0, v244, v246\n\tv_max3_u32 %1, %1, v248, v250\n\tv_max3_u32 %0, %0, v252, v254\n\tv_max_u32 %0, %0, %1" : "=&v"(m), "=&v"(m2));
                if (__builtin_expect(bad(m), 0)) {
#pragma unroll 1
                    for (int sp = 0; sp < SPIN; ++sp) {
                        u32 mk = 0;
                        sfor<0, 8>(LAM(J_) { mk |= (u32)bad(mx3vv<224 + 4 * J_.v>(0u)) << J_.v; });
                        if (!mk) break;
                        __builtin_amdgcn_s_sleep(RSX);
                        sfor<0, 8>(LAM(J_) { if (mk >> J_.v & 1) xop(IC<5 * J_.v + 2>{}, xs[J_.v]); });
                        waitv<0>();
                        ++nretry;
                    }
                }
                STAMP(25);
                STAMPV(63, nretry);
                nretry = 0;
                sfor<UPA_, xissued(0)>(xsite);
                sfor<0, 8>(LAM(J_) { swapw<224 + 4 * J_.v, 128 + 2 * J_.v>(); if constexpr (J_.v) { addv<225, 225 + 4 * J_.v>(); addv<227, 227 + 4 * J_.v>(); } });
                ts = swapsum<224>();
                stat();
            }
            v4u r0[3], r1[3], ones = {~0u, ~0u, ~0u, ~0u};
            f2 cq[4];
            u32 cm, cs_;
#pragma unroll
            for (int s = 0; s < 3; ++s) { r0[s] = lds4(l16 + s * 1024); r1[s] = lds4(aKV + s * QTS); }
            if constexpr (L == 0) pin(ones);
            FENCE();
            sfor<0, 36>(LAM(SI) {
                constexpr int n = SI.v, s = kstep(n), s3 = kstep(n + 3), i = s >> 1;
                if constexpr (L == 0 && s < 32 && !(s & 1)) {
                    constexpr int j = i >> 1, later = 39 - 5 * j - (i & 1 ? 4 : 1) + 9 + (n >> 1);
                    waitv<later>();
                    FENCE();
                }
                if constexpr (L == 1 && s < 32 && !(s & 3)) {
                    constexpr int j = i >> 1;
                    static_assert(xissued(n) >= 4 * j + 4, "");
                    waitv<xissued(n) - 4 * j - 4>();
                    auto late = LAM(J_) { constexpr int k = J_.v; return bad(umax(mx3(rda<16 * k>(), rda<16 * k + 4>(), rda<16 * k + 8>()), rda<16 * k + 12>())); };
                    if (__builtin_expect(late(IC<j>{}), 0)) {
#pragma unroll 1
                        for (int sp = 0; sp < SPIN; ++sp) {
                            sfor<4 * j, 4 * j + 4>(xsite);
                            waitv<0>();
                            ++nretry;
                            if (!late(IC<j>{})) break;
                        }
                    }
                    FENCE();
                }
                if constexpr (L == 0 && n == 32) asm volatile("s_waitcnt vmcnt(16)\n\ts_barrier" ::: "memory");
                waitl<(n < 34 ? 4 : 6)>();
                mm32b<n == 0, 4 * s>(aK, r0[n % 3]);
                if constexpr (n + 3 < 36) r0[n % 3] = lds4(l16 + s3 * 1024); else rV[n - 33] = lds4(aV_ + (n - 33) * 1024);
                sfor<xissued(n), xissued(n + 1)>(xsite);
                if constexpr (L == 0 && s < 32 && (s & 3) == 2) swapw<224 + 2 * (i - 1), 127 + i>();
                if constexpr (L == 0 && s > 3 && s < 31 && ((s & 3) == 3 || s == 30)) { addv<225, 225 + 4 * (i >> 1)>(); addv<227, 227 + 4 * (i >> 1)>(); }
                if constexpr (L == 0 && n == 34) ts = swapsum<224>();
                if constexpr (n == 1) { aQq = L_QT + vsel(qa && rq < 2, 256 + (gq >> 1) * 64 + (rq & 1) * 32 + (kg >> 1) * 16, 384); pin(aQq); }
                if constexpr (n == 3) { aCS = L_CS + h * 224; aS = L_STG + (wave * 32 + tl) * STGS + 72 * h; pin2(aCS, aS); }
                if constexpr (L == 0) {
                    constexpr int k = n >> 1;
                    if constexpr (n & 1) {
                        if constexpr (k == 0 || k == 3) { cs_ = W_O + ((k ? 1024 : 0) + (rg * 4 + wave) * 16 + p) * OS; OPQS(cs_); }
                        if constexpr (k == 6) { cs_ = W_XF1 + ((rg * 4 + tw) * 16 + pw) * XS; OPQS(cs_); }
                        if constexpr (k == 9) { cs_ = W_KV + (p * 16 + rg) * 2816 + wave * 704; OPQS(cs_); }
                        if constexpr (k == 10) { cs_ += 256 * 2816; OPQS(cs_); }
                        if constexpr (k == 11) { cs_ = W_ST + ((rg * 4 + tw) * 16 + pw) * 256; OPQS(cs_); }
                        if constexpr (k < 9 && k % 3 < 2) st4<(k % 3) * 1024>(ones, l16, vw, cs_);
                        else if constexpr (k < 9) st4<2048>(ones, cm, vw, cs_);
                        else if constexpr (k < 12) st4<0>(ones, cm, vw, cs_);
                        else wsite(IC<k - 12>{});
                    } else {
                        if constexpr (k == 2) { cm = vsel(ln < 16, l16, OOB); pin(cm); }
                        if constexpr (k == 8) { cm = vsel(ln < 32, l16, OOB); pin(cm); }
                        if constexpr (k == 9) { cm = vsel(ln < 44, l16, OOB); pin(cm); }
                        if constexpr (k == 11) { cm = vsel(ln < 32, row * 16 + (kg & 1) * (1024 * 256), OOB); pin(cm); }
                        if constexpr (k == 13) { cq[0].x = (cq[0].x + cq[1].x) + (cq[2].x + cq[3].x); cq[0].y = (cq[0].y + cq[1].y) + (cq[2].y + cq[3].y); pin(cq[0]); }
                    }
                }
                if constexpr (L == 0 && n == 35) stat();
                FENCE();
                mm16b<n == 0, 4 * s>(qKV, r1[n % 3]);
                if constexpr (n + 3 < 36) r1[n % 3] = lds4(aKV + s3 * QTS); else cK[n - 33] = lds4(aCS + (n - 33) * 16);
                if constexpr (n == 33) cK[3] = lds4(aCS + 48);
                if constexpr (n == 34) cK[4] = lds4(aCS + 192);
                if constexpr (n == 35) rV[3] = lds4(aV_ + 3 * 1024);
                FENCE();
                if constexpr (L == 0 && n >= 20 && n < 24) { cq[n - 20] = *(const LAS f2*)(l8 + (L_EX + (n - 20) * 512)); FENCE(); }
                if constexpr (L == 0 && n == 27) { *(LAS f2*)(csd) = cq[0]; FENCE(); }
            });
        }
        STAMP(4 + 12 * L);
        if constexpr (L == 1) STAMPV(56, nretry);
        v4u rQ[4], rq4[3];
        u32 pa, pb, kso;
        {
            f16v aV;
            float t[18], e1[18], e2[18], mx, mn, c1, c2, s1, s2;
            v4u cV[4];
            u32 qo = L_Q + wave * 9216, qg = W_IMG + p * IMG + qo;
            sfor<0, 36>(LAM(SI) {
                constexpr int s = SI.v;
                if constexpr (s >= 2 && s < 34 && !(s & 1)) waitl<2>();
                mm32b<s == 0, 4 * s>(aV, rV[s & 3]);
                if constexpr (s == 2) { pin(aK); pin(qKV); }
                if constexpr (s + 4 < 36) rV[s & 3] = lds4(aV_ + (s + 4) * 1024); else cV[s - 32] = lds4(aCS + 64 + (s - 32) * 16);
                if constexpr (s < 18) {
                    if constexpr (s == 0 || s == 8 || s == 16) { if constexpr (s) { qo += 4096; qg += 4096; } OPQS(qo); OPQS(qg); setm0(qo); }
                    if constexpr (s & 1) dmai<((s >> 1) & 3) * 1024>(vw, l16, qg);
                } else wsite(IC<s - 12>{});
                if constexpr (s == 1) {
                    const u32 r0 = L_STG + (4 * (4 * (kg & 1) + gq) + (kg >> 1)) * STGS + 8 * rq;
                    pa = r0 + 72 * (wave >> 1); pb = r0 + (144 + 40 * (wave >> 1) + 32 * (wave & 1));
                    kso = W_KV + ((L * 16 + p) * 16 + rg) * 2816 + (wave >> 1) * 1376;
                    pin2(pa, pb); OPQS(kso);
                }
                if constexpr (s >= 2 && s < 5) {
#pragma unroll
                    for (int i = 6 * (s - 2); i < 6 * (s - 2) + 6; ++i) { const u32 cv = cK[i >> 2][i & 3]; t[i] = __builtin_fmaf(-mean, asf(cv), i < 16 ? aK[i] : qKV[i - 16]); }
                }
                if constexpr (s == 5) { mx = __builtin_fmaxf(__builtin_fmaxf(t[0], t[1]), t[2]); mn = __builtin_fminf(__builtin_fminf(t[0], t[1]), t[2]);
#pragma unroll
                    for (int i = 3; i < 7; i += 2) { mx = __builtin_fmaxf(__builtin_fmaxf(mx, t[i]), t[i + 1]); mn = __builtin_fminf(__builtin_fminf(mn, t[i]), t[i + 1]); } }
                if constexpr (s == 6) {
#pragma unroll
                    for (int i = 7; i < 13; i += 2) { mx = __builtin_fmaxf(__builtin_fmaxf(mx, t[i]), t[i + 1]); mn = __builtin_fminf(__builtin_fminf(mn, t[i]), t[i + 1]); } }
                if constexpr (s == 7) {
#pragma unroll
                    for (int i = 13; i < 17; i += 2) { mx = __builtin_fmaxf(__builtin_fmaxf(mx, t[i]), t[i + 1]); mn = __builtin_fminf(__builtin_fminf(mn, t[i]), t[i + 1]); }
                    mx = __builtin_fmaxf(mx, t[17]); mn = __builtin_fminf(mn, t[17]); c1 = -al * mx; c2 = al * mn; }
                if constexpr (s >= 8 && s < 32) {
#pragma unroll
                    for (int q = (3 * (s - 8)) / 2; q < (3 * (s - 7)) / 2; ++q) {
                        const int i = q >> 1;
                        if (q & 1) { e2[i] = __builtin_amdgcn_exp2f(__builtin_fmaf(t[i], -al, c2)); s2 = i ? s2 + e2[i] : e2[i]; }
                        else { e1[i] = __builtin_amdgcn_exp2f(__builtin_fmaf(t[i], al, c1)); s1 = i ? s1 + e1[i] : e1[i]; }
                    }
                }
                if constexpr (s == 32) { s1 = __builtin_amdgcn_rcpf(s1); s2 = __builtin_amdgcn_rcpf(s2); }
                if constexpr (s >= 33) {
                    constexpr int i = s - 33;
                    *(LAS v2u*)(aS + 8 * i) = (v2u){pk(e1[4 * i] * s1, e1[4 * i + 1] * s1), pk(e1[4 * i + 2] * s1, e1[4 * i + 3] * s1)};
                }
                FENCE();
            });
            *(LAS v2u*)(aS + 24) = (v2u){pk(e1[12] * s1, e1[13] * s1), pk(e1[14] * s1, e1[15] * s1)};
            *(LAS v2u*)(aS + 32) = (v2u){pk(e1[16] * s1, e1[17] * s1), pk(e2[0] * s2, e2[1] * s2)};
#pragma unroll
            for (int i = 0; i < 4; ++i) *(LAS v2u*)(aS + 40 + 8 * i) = (v2u){pk(e2[4 * i + 2] * s2, e2[4 * i + 3] * s2), pk(e2[4 * i + 4] * s2, e2[4 * i + 5] * s2)};
            FENCE();
            pin(aV);
            const u32 aSv = aS + 144 - 32 * h;
            const float mr = -mean * rstd;
#pragma unroll
            for (int i = 0; i < 4; ++i) {
                const u32 q0 = cV[i].x, q1 = cV[i].y, q2 = cV[i].z, q3 = cV[i].w;
                *(LAS v2u*)(aSv + 8 * i) = (v2u){pk(__builtin_fmaf(mr, asf(q0), aV[4 * i] * rstd), __builtin_fmaf(mr, asf(q1), aV[4 * i + 1] * rstd)),
                                                 pk(__builtin_fmaf(mr, asf(q2), aV[4 * i + 2] * rstd), __builtin_fmaf(mr, asf(q3), aV[4 * i + 3] * rstd))};
            }
            const u32 q2 = cK[4].z, q3 = cK[4].w;
            *(LAS v2u*)(aSv + 32) = (v2u){pk(__builtin_fmaf(mr, asf(q2), qKV[2] * rstd), __builtin_fmaf(mr, asf(q3), qKV[3] * rstd)), 0x3f80u};
        }
        waitbar<(L ? 0 : 18)>();
        STAMP(5 + 12 * L);
        {
            f4 c[3];
            v4u B[4], A[4][3];
            auto rd = LAM(N_) {
                constexpr int ks = N_.v >> 2, f = N_.v & 3;
                if constexpr (f == 0) B[ks] = tr2(pb + ks * 32 * STGS, pb + (ks * 32 + 2) * STGS);
                else A[ks][f - 1] = tr2(pa + ks * 32 * STGS + 32 * (f - 1), pa + (ks * 32 + 2) * STGS + 32 * (f - 1));
            };
            sfor<0, 7>(LAM(N_) { rd(N_); FENCE(); });
            if constexpr (L == 0) { if (tid == 0) bst2((v2u){(u32)ep, (u32)(ep >> 32)}, vw, 0, W_F2 + (rg * 16 + p) * 128, SC1); }
            FENCE();
            sfor<0, 12>(LAM(I_) {
                constexpr int ks = I_.v / 3, ft = I_.v % 3;
                mm16<ks == 0>(c[ft], A[ks][ft], B[ks]);
                if constexpr (I_.v < 9) rd(IC<7 + I_.v>{});
                else { rQ[I_.v - 9] = lds4(aQ_ + (I_.v - 9) * 1024); rq4[I_.v - 9] = lds4(aQq + (I_.v - 9) * QTS); }
                FENCE();
            });
            rQ[3] = lds4(aQ_ + 3 * 1024);
            FENCE();
            pad3<5>(c[0], c[1], c[2]);
            bst4((v4u){pk(c[0][0], c[0][1]), pk(c[0][2], c[0][3]), pk(c[1][0], c[1][1]), pk(c[1][2], c[1][3])}, vw, kvA, kso, SC1);
            bst2((v2u){pk(c[2][0], c[2][1]), pk(c[2][2], c[2][3])}, vw, kvB, kso, SC1);
        }
        FENCE();
        STAMP(6 + 12 * L);
        v4u pq[5], kp[12], sel;
        u32 kvo, kv3, aF, aQ4, f2o, oso;
        u32 kb[4];
        v2u yfl;
        auto flags_ok = [&](u64 m) __attribute__((always_inline)) { return (__builtin_amdgcn_ballot_w64(yfl.x != (u32)ep || yfl.y != (u32)(ep >> 32)) & m) == 0; };
        auto flags_wait = [&](u64 m) __attribute__((always_inline)) {
            if (__builtin_expect(!flags_ok(m), 0)) {
#pragma unroll 1
                for (int sp = 0; sp < SPIN; ++sp) {
                    __builtin_amdgcn_s_sleep(1);
                    ld2<0>(yfl, f2o, vw, W_F2);
                    waitp<0>(yfl);
                    if (flags_ok(m)) break;
                }
            }
        };
        {
            f16v aQ; f4 qQ;
            v4u cQ[5];
            sfor<0, 36>(LAM(SI) {
                constexpr int s = SI.v;
                if constexpr (s >= 1 && s < 34) waitl<4>();
                mm32b<s == 0, 4 * s>(aQ, rQ[s & 3]);
                if constexpr (s + 4 < 36) rQ[s & 3] = lds4(aQ_ + (s + 4) * 1024); else cQ[s - 32] = lds4(aCS + 128 + (s - 32) * 16);
                if constexpr (s < 31 && s % 3 != 2) wsite(IC<24 + s - s / 3>{});
                if constexpr (s == 2) { kvo = kg * 2816 + wave * 256 + row * 16; kv3 = vsel(wave * 16 + row < 44, kvo + 2048, OOB); pin2(kvo, kv3); }
                if constexpr (s == 26) { for (int i = 0; i < 4; ++i) { kb[i] = W_KV + ((L * 16 + p) * 16 + 4 * i) * 2816; OPQS(kb[i]); } }
                if constexpr (s == 5 || s == 8) {
#pragma unroll
                    for (int i = (s - 5) / 3 * 2; i < (s - 5) / 3 * 2 + 2; ++i) sel[i] = row == 2 * i ? 0x3f80u : row == 2 * i + 1 ? 0x3f800000u : 0u;
                    if constexpr (s == 8) pin(sel);
                }
                if constexpr (s == 11) { const int hr = (tl >> 2) & 1, d = (tl & 3) + 4 * (tl >> 3); aF = vsel(h == hr, (hr * 19 + d) * 80 + L_KVA, L_Z); pin(aF); }
                if constexpr (s == 14) { aQ4 = vsel(kg == gq && rq < 3, ((gq >> 1) * 19 + 16 + rq) * 80 + L_KVA, L_Z); pin(aQ4); }
                if constexpr (WARM && s == WARM) {
                    const u32 sO = W_O + (L * 1024 + (rg * 4 + wave) * 16 + p) * OS, sX = W_XF1 + ((rg * 4 + tw) * 16 + pw) * XS - 18 * 128, sS = W_ST + (L * 1024 + (rg * 4 + tw) * 16 + pw) * 256 - 38 * 128;
                    const u32 wo = vsel(ln < 18, sO, vsel(ln < 38, L ? OOB - 8192 : sX, vsel(ln < 40, sS, OOB - 8192))) + 8 * l16;
                    asm volatile("s_mov_b32 m0, %2\n\ts_nop 0\n\tbuffer_load_dword %0, %1, 0 offen sc1 lds" ::"v"(wo), "s"(vw), "s"(L_EX) : "m0", "memory");
                }
                if constexpr (s == 17) { oso = W_O + (L * 1024 + (rg * 4 + wave) * 16 + p) * OS; OPQS(oso); }
                if constexpr (L == 0 && s == 20) { f2o = vsel(kg == 1, rg * 16 + row, row * 16 + (kg ? pw : p)) * 128; pin(f2o); }
                if constexpr (L == 0 && s == 23) ld2<0>(yfl, f2o, vw, W_F2);
                FENCE();
                mm16b<s == 0, 4 * s>(qQ, rq4[s % 3]);
                if constexpr (s + 3 < 36) rq4[s % 3] = lds4(aQq + (s + 3) * QTS);
                if constexpr (s == 33) cQ[4] = lds4(aCS + 208);
                FENCE();
            });
            STAMP(49 + L);
            if constexpr (L == 0) { waitp<5>(yfl); flags_wait(0xffffull); }
#pragma unroll
            for (int i = 0; i < 4; ++i) {
                kp[i] = bld4(vw, kvo, kb[i], SC1);
                kp[4 + i] = bld4(vw, kvo + 1024, kb[i], SC1);
                kp[8 + i] = bld4(vw, kv3, kb[i], SC1);
            }
            FENCE();
            pin(aQ); pin(qQ);
            float t[18]; u32 ph[18];
#pragma unroll
            for (int i = 0; i < 18; ++i) { const u32 cv = cQ[i >> 2][i & 3]; t[i] = __builtin_fmaf(-mean, asf(cv), i < 16 ? aQ[i] : qQ[i - 16]); }
            hedgehog(t, al, ph);
#pragma unroll
            for (int i = 0; i < 5; ++i) { pq[i] = (v4u){ph[4 * i], ph[4 * i + 1], i < 4 ? ph[4 * i + 2] : 0u, i < 4 ? ph[4 * i + 3] : 0u}; pin(pq[i]); }
            FENCE();
            STAMP(7 + 12 * L);
            u32 m = umax(kp[0].x, kp[0].z);
#pragma unroll
            for (int i = 1; i < 12; ++i) m = mx3(m, kp[i].x, kp[i].z);
            nretry = 0;
            if (__builtin_expect(bad(m), 0)) {
#pragma unroll 1
                for (int sp = 0; sp < SPIN; ++sp) {
                    u32 mk = 0;
                    waitv<0>();
#pragma unroll
                    for (int i = 0; i < 4; ++i) {
                        pin2(kp[i], kp[4 + i]); pin(kp[8 + i]);
                        mk |= (u32)bad(mx3(mx3(kp[i].x, kp[i].z, kp[4 + i].x), mx3(kp[4 + i].z, kp[8 + i].x, kp[8 + i].z), 0u)) << i;
                    }
                    if (!mk) break;
                    __builtin_amdgcn_s_sleep(RSK);
#pragma unroll
                    for (int i = 0; i < 4; ++i)
                        if (mk >> i & 1) { ld4<0>(kp[i], kvo, vw, kb[i]); ld4<1024>(kp[4 + i], kvo, vw, kb[i]); ld4<0>(kp[8 + i], kv3, vw, kb[i]); }
                    ++nretry;
                }
            }
            STAMPV(57 + L, nretry);
            f4 a[3];
            sfor<0, 12>(LAM(I_) { constexpr int i = I_.v / 3, j = I_.v % 3; mm16<i == 0>(a[j], sel, kp[4 * j + i]); });
            pad3<7>(a[0], a[1], a[2]);
#pragma unroll
            for (int j = 0; j < 3; ++j) *(LAS v2u*)(kvw[j]) = (v2u){pk(a[j][0], a[j][1]), pk(a[j][2], a[j][3])};
            barrier();
            STAMP(8 + 12 * L);
        }
        {
            v4u A[5], Aq[5];
#pragma unroll
            for (int c = 0; c < 5; ++c) Aq[c] = lds4(aQ4 + 16 * c);
#pragma unroll
            for (int c = 0; c < 5; ++c) A[c] = lds4(aF + 16 * c);
            FENCE();
            f16v o; f4 oq;
            float rd; u32 ol;
            sfor<0, 5>(LAM(C_) { mm16<C_.v == 0>(oq, Aq[C_.v], pq[C_.v]); });
            sfor<0, 5>(LAM(C_) {
                mm32<C_.v == 0>(o, A[C_.v], pq[C_.v]);
                if constexpr (C_.v == 3) { pad<4>(oq); rd = __builtin_amdgcn_rcpf(oq[2] + 1e-6f); pin(rd); }
                if constexpr (C_.v == 4) { ol = pk(oq[0] * rd, oq[1] * rd); pin(ol); }
                FENCE();
            });
            pad<8>(o);
            bst4((v4u){pk(o[0] * rd, o[1] * rd), pk(o[2] * rd, o[3] * rd), pk(o[4] * rd, o[5] * rd), pk(o[6] * rd, o[7] * rd)}, vw, l16, oso, SC1);
            bst4((v4u){pk(o[8] * rd, o[9] * rd), pk(o[10] * rd, o[11] * rd), pk(o[12] * rd, o[13] * rd), pk(o[14] * rd, o[15] * rd)}, vw, l16 + 1024, oso, SC1);
            bst1(ol, vw, l4 + 2048, oso, SC1);
        }
        FENCE();
        STAMP(9 + 12 * L);
        u32 aB, ae, ao, yo, ys;
        f16v aW; float xl0, xl1;
        if constexpr (L == 0) {
            flags_wait(~0ull);
            ys = W_Y + rg * YB + 36 * pw * 256 + tw * 64; yo = (ln >> 2) * 256 + rq * 16;
            OPQS(ys); OPQV(yo);
        }
#pragma unroll
        for (int i = 0; i < 8; ++i) { aW[2 * i] = bf_lo(xr[i]); aW[2 * i + 1] = bf_hi(xr[i]); }
        xl0 = bf_lo(xr[8]); xl1 = bf_hi(xr[8]);
        aB = L_Q + ((wave >> 1) * 18 + (kg & 1)) * 1024 + ((kg >> 1) * 32 + 16 * (wave & 1) + row) * 16;
        ae = L_EX + (kg & 1) * 1024 + ((2 * (wave >> 1) + h) * 16 + row) * 16 + 8 * (wave & 1);
        ao = aQ_ + wave * 8192;
        pin(aW); pin2(xl0, xl1); pin2(aB, ae); pin(ao);
        {
            u32 so[4];
#pragma unroll
            for (int i = 0; i < 4; ++i) { so[i] = W_O + (L * 1024 + (rg * 4 + tw) * 16 + 4 * wave + i) * OS; OPQS(so[i]); }
            v4u og[8]; u32 lo[4];
            FENCE();
            STAMP(43 + 3 * L);
            if constexpr (OWT) waitv<0>();
            STAMP(44 + 3 * L);
            if constexpr (OSL) __builtin_amdgcn_s_sleep(OSL);
            auto ldo = [&]() __attribute__((always_inline)) {
#pragma unroll
                for (int i = 0; i < 8; ++i) og[i] = bld4(vw, l16 + (i & 1) * 1024, so[i >> 1], SC1);
            };
            auto ldl = [&]() __attribute__((always_inline)) {
#pragma unroll
                for (int i = 0; i < 4; ++i) lo[i] = bld1(vw, l4 + 2048, so[i], SC1);
            };
            auto chk = [&]() __attribute__((always_inline)) {
                u32 m = mx3(umax(lo[0], lo[1]), lo[2], lo[3]);
#pragma unroll
                for (int i = 0; i < 8; i += 2) m = mx3(m, og[i].x, og[i + 1].x);
                return bad(m);
            };
            ldo(); ldl();
            FENCE();
            nretry = 0;
            if (__builtin_expect(chk(), 0)) {
#pragma unroll 1
                for (int sp = 0; sp < SPIN; ++sp) {
                    u32 mk = 0;
                    waitv<0>();
#pragma unroll
                    for (int i = 0; i < 4; ++i) {
                        pin2(og[2 * i], og[2 * i + 1]); pin(lo[i]);
                        mk |= (u32)bad(mx3(og[2 * i].x, og[2 * i + 1].x, lo[i])) << i;
                    }
                    if (!mk) break;
                    __builtin_amdgcn_s_sleep(RSO);
#pragma unroll
                    for (int i = 0; i < 4; ++i)
                        if (mk >> i & 1) { ld4<0>(og[2 * i], l16, vw, so[i]); ld4<1024>(og[2 * i + 1], l16, vw, so[i]); ld1<2048>(lo[i], l4, vw, so[i]); }
                    ++nretry;
                }
            }
            STAMPV(59 + L, nretry);
            STAMP(45 + 3 * L);
#pragma unroll
            for (int i = 0; i < 8; ++i) *(LAS v4u*)(ao + i * 1024) = og[i];
            *(LAS v4u*)(aQ_ + (32 + wave) * 1024) = (v4u){lo[0], lo[1], lo[2], lo[3]};
            barrier();
        }
        STAMP(10 + 12 * L);
        u32 sto = W_ST + (L * 1024 + (rg * 4 + tw) * 16) * 256;
        {
            f4 qW;
            v4u rb_[4], rl[9];
            v2u e0, e1;
            float s1, s2;
            u32 le;
#pragma unroll
            for (int i = 0; i < 4; ++i) { rl[i] = lds4(aB + i * 2048); rb_[i] = lds4(aQ_ + i * 1024); }
            FENCE();
            sfor<0, 36>(LAM(SI) {
                constexpr int s = SI.v;
                if constexpr (s < 28) mm32a<false, 144 + 4 * s>(aW, rb_[s & 3]); else mm32<false>(aW, wv[s - 28], rb_[s & 3]);
                if constexpr (s + 4 < 36) rb_[s & 3] = lds4(aQ_ + (s + 4) * 1024);
                if constexpr (s < 5) rl[4 + s] = lds4(aB + (4 + s) * 2048);
                if constexpr (s == 12) { pad<0>(qW); *(LAS f4*)(l16 + (L_EX + wave * 1024)) = qW; }
                if constexpr (s == 14) OPQS(sto);
                if constexpr (s == 24) { le = l4; pin(le); }
                if constexpr (L == 0 && s >= 16 && s < 22 && !(s & 1)) dma_ws(vw, yo, ys + (s - 16) * 2048, L_STG + wave * 3072 + (s - 16) * 512);
                FENCE();
                if constexpr (s < 9) { mm16<s == 0>(qW, wv[8 + s], rl[s]); FENCE(); }
                if constexpr (s == 30) { barrier(); e0 = lds2(ae); e1 = lds2(ae + 2048); FENCE(); }
                if constexpr (s == 33) { xl0 += asf(e0.x) + asf(e1.x); xl1 += asf(e0.y) + asf(e1.y); s1 = xl0 + xl1; s2 = __builtin_fmaf(xl0, xl0, xl1 * xl1); pin2(s1, s2); FENCE(); }
            });
            STAMP(11 + 12 * L);
            pad<11>(aW);
            float s3 = aW[0], s4 = aW[0] * aW[0];
#pragma unroll
            for (int i = 1; i < 16; ++i) { if (i & 1) { s1 += aW[i]; s2 = __builtin_fmaf(aW[i], aW[i], s2); } else { s3 += aW[i]; s4 = __builtin_fmaf(aW[i], aW[i], s4); } }
            bst1(asu(halves_sum(s1 + s3, s2 + s4)), vw, le, sto + pw * 256, SC1);
        }
        FENCE();
        {
            float t;
            u32 xso = L ? (rg * 128 + tw * 32) * 1152 + 72 * pw : W_XF1 + ((rg * 4 + tw) * 16 + pw) * XS, oo = tl * 1152 + 36 * h, one2 = 0x3f803f80u;
            OPQS(xso); OPQS(one2);
            if constexpr (L == 1) OPQV(oo);
            u32 yf[18], lS;
            if constexpr (L == 0) {
                const u32 ya = (18 * h + gq) * 64 + 32 * (kg & 1) + 8 * rq + (L_STG + wave * 3072);
                lS = tl * 16 + h * 8;
                pin(lS);
                waitv<1>();
#pragma unroll
                for (int i = 0; i < 5; ++i) {
                    const v2u v = __builtin_bit_cast(v2u, __builtin_amdgcn_ds_read_tr16_b64_v4i16((LAS s4*)(ya + 256 * i)));
                    yv[2 * i] = v.x; if (i < 4) yv[2 * i + 1] = v.y;
                }
            }
#pragma unroll
            for (int i = 0; i < 9; ++i) { yf[2 * i] = yv[i] << 16; yf[2 * i + 1] = yv[i] & 0xffff0000u; pin2(yf[2 * i], yf[2 * i + 1]); }
            FENCE();
            waitv<0>();
            if constexpr (SSL) __builtin_amdgcn_s_sleep(SSL);
            nretry = 0;
#pragma unroll 1
            for (int sp = 0; sp < SPIN; ++sp) {
                asm volatile("" : "+v"(l4)::"memory");
                u32 e[16];
#pragma unroll
                for (int i = 0; i < 16; ++i) e[i] = bld1(vw, l4 + i * 256, sto, SC1);
                FENCE();
                u32 m = umax(e[0], e[1]);
                t = asf(e[0]) + asf(e[1]);
#pragma unroll
                for (int i = 2; i < 16; i += 2) { m = mx3(m, e[i], e[i + 1]); t += asf(e[i]) + asf(e[i + 1]); pin2(m, t); }
                if (!bad(m)) break;
                ++nretry;
                __builtin_amdgcn_s_sleep(RSS);
            }
            STAMP(12 + 12 * L);
            STAMPV(61 + L, nretry);
            auto r = __builtin_amdgcn_permlane32_swap(asu(t), asu(t), false, false);
            const float m1 = asf(r[0]) * (1.f / 576.f);
            const float r1 = __builtin_amdgcn_rsqf(__builtin_fmaxf(asf(r[1]) * (1.f / 576.f) - m1 * m1, 0.f) + 1e-5f), mr = -m1 * r1;
#pragma unroll
            for (int i = 0; i < 9; ++i) {
                const float a = i < 8 ? aW[2 * i] : xl0, c = i < 8 ? aW[2 * i + 1] : xl1;
                xr[i] = pk(__builtin_fmaf(__builtin_fmaf(a, r1, mr), asf(yf[2 * i]), a), __builtin_fmaf(__builtin_fmaf(c, r1, mr), asf(yf[2 * i + 1]), c));
                if constexpr (L == 0) {
                    if (i == 3) { bst4((v4u){xr[0], xr[1], xr[2], xr[3]}, vw, l16, xso, SC1); FENCE(); }
                    if (i == 7) { bst4((v4u){xr[4], xr[5], xr[6], xr[7]}, vw, l16 + 1024, xso, SC1); FENCE(); }
                } else {
                    if (i == 3) { bst4((v4u){xr[0], xr[1], xr[2], xr[3]}, ro, oo, xso, 0); FENCE(); }
                    if (i == 7) { bst4((v4u){xr[4], xr[5], xr[6], xr[7]}, ro, oo + 16, xso, 0); FENCE(); }
                    if (i == 8) bst1(xr[8], ro, oo + 32, xso, 0);
                }
            }
            if constexpr (L == 0) {
                float s1 = 0.f, s2 = 0.f;
#pragma unroll
                for (int j = 0; j < 9; ++j) { s1 = dot2(xr[j], one2, s1); s2 = dot2(xr[j], xr[j], s2); }
                bst2((v2u){xr[8], asu(halves_sum(s1, s2))}, vw, lS + 2048, xso, SC1);
                STAMP(13);
            } else {
                if (b == 0 && tid == 0) bst2((v2u){(u32)ep, (u32)(ep >> 32)}, vw, 0, W_E, SC1);
            }
        }
    });
    STAMP_END();
}
#if defined(__HIP_DEVICE_COMPILE__)
#pragma clang attribute pop
#endif

extern "C" void kernel_launch(void* const* d_in, const int* in_sizes, int n_in, void* d_out, int out_size, void* d_ws, size_t ws_size, hipStream_t stream) {
    (void)in_sizes; (void)n_in; (void)out_size; (void)ws_size;
    hipFuncSetAttribute((const void*)k_main, hipFuncAttributeMaxDynamicSharedMemorySize, L_END);
    const u32 f = 0x00020000u, n = 2048 * 1152;
    Args a = {{d_in[5], n, f}, {d_in[6], n, f}, {{d_in[1], 576 * 3456, f}, {d_in[3], 576 * 1152, f}}, {{d_in[2], 32 * 648, f}, {d_in[2], 0, f}}, {d_in[0], n, f}, {d_ws, W_END, f}, {d_out, n, f}, d_in[7], d_in[8], d_in[9]};
    k_main<<<256, 256, L_END, stream>>>(a);
}
```

```cpp
#include <hip/hip_runtime.h>
#include <stdint.h>
#pragma clang diagnostic ignored "-Wint-to-pointer-cast"
#pragma clang diagnostic ignored "-Wunused-value"
#pragma clang diagnostic ignored "-Winline-asm"
#if defined(__HIP_DEVICE_COMPILE__)
#pragma clang attribute push(__attribute__((target("no-packed-fp32-ops"))), apply_to = function)
#endif
typedef unsigned short u16;
typedef unsigned u32;
typedef unsigned long long u64;
typedef u32 v4u __attribute__((ext_vector_type(4)));
typedef u32 v2u __attribute__((ext_vector_type(2)));
typedef float f4 __attribute__((ext_vector_type(4)));
typedef float f2 __attribute__((ext_vector_type(2)));
typedef float f16v __attribute__((ext_vector_type(16)));
typedef __bf16 b8 __attribute__((ext_vector_type(8)));
typedef __bf16 b2 __attribute__((ext_vector_type(2)));
typedef short s4 __attribute__((ext_vector_type(4)));
#define LAS __attribute__((address_space(3)))
#define DEV __device__ __forceinline__
#define SC1 16

constexpr u32 QTS = 416, STGS = 232, CVS = 21216, HCS = 4160;
constexpr u32 L_K = 0, L_V = 36864, L_QT = 73728, L_Q = 89088, L_STG = 125952, L_KVA = 155648, L_CS = 158784, L_EX = 159232, L_END = 163328;
constexpr u32 P_KS = 0, P_VS = 10240, P_WS = 20480, P_XS = 31232, P_PH = 42496, P_LS = 55296, L_Z = L_KVA + 38 * 80;
constexpr u32 IMG = L_STG, XS = 2560, OS = 2304, YB = 576 * 256, CSB = 16 * 576;
constexpr u32 WOG = 4 * 36864 + 18432;
constexpr u32 W_IMG = 0, W_WOT = W_IMG + 16 * IMG, W_CSP = W_WOT + 4 * WOG, W_UT = W_CSP + 16 * CSB,
              W_XF0 = W_UT + 576 * 4096, W_XF1 = W_XF0 + 1024 * XS, W_O = W_XF1 + 1024 * XS, W_Y = W_O + 2 * 1024 * OS, W_KV = W_Y + 576 * 4096,
              W_ST = W_KV + 2 * 256 * 2816, W_F = W_ST + 2 * 1024 * 256, W_F2 = W_F + 256 * 128, W_F3 = W_F2 + 256 * 128, W_E = W_F3 + 256 * 128, W_END = W_E + 128, W_DBG = W_END;
constexpr u32 OOB = 0x7ffffff0u;
constexpr int SPIN = 1 << 14;
constexpr u32 CODE = 45900;
#define STOLD
#define OSL 8
#define RSX 1
#define RSK 1
#define RSO 1
#define RSS 1
#define XSL 13
#define MAP 0
#define WD0 3
#define WDN 6
#define WV_ 0
#define ASL 1
#define XB_ 20
#define PB_ 14
#define WARM 0
#define WEARLY 1
#define OWT 1
#define SSL 2

DEV float asf(u32 v) { return __builtin_bit_cast(float, v); }
DEV u32 asu(float v) { return __builtin_bit_cast(u32, v); }
DEV float bf_lo(u32 v) { return asf(v << 16); }
DEV float bf_hi(u32 v) { return asf(v & 0xffff0000u); }
DEV u32 pk(float lo, float hi) { b2 r = __builtin_convertvector((f2){lo, hi}, b2); return __builtin_bit_cast(u32, r); }
DEV float rb(float f) { return bf_lo(pk(f, f)); }
DEV float dot2(u32 a, u32 b, float c) { return __builtin_amdgcn_fdot2_f32_bf16(__builtin_bit_cast(b2, a), __builtin_bit_cast(b2, b), c, false); }
DEV u32 umin(u32 a, u32 b) { return a < b ? a : b; }
DEV u32 umax(u32 a, u32 b) { return a > b ? a : b; }
DEV u32 mx3(u32 a, u32 b, u32 c) { return umax(umax(a, b), c); }
template <bool Z> DEV void mm32(f16v& c, v4u a, v4u b) {
    if constexpr (Z) asm volatile("v_mfma_f32_32x32x16_bf16 %0, %1, %2, 0" : "=&v"(c) : "v"(a), "v"(b));
    else asm volatile("v_mfma_f32_32x32x16_bf16 %0, %1, %2, %0" : "+v"(c) : "v"(a), "v"(b));
}
template <bool Z> DEV void mm16(f4& c, v4u a, v4u b) {
    if constexpr (Z) asm volatile("v_mfma_f32_16x16x32_bf16 %0, %1, %2, 0" : "=&v"(c) : "v"(a), "v"(b));
    else asm volatile("v_mfma_f32_16x16x32_bf16 %0, %1, %2, %0" : "+v"(c) : "v"(a), "v"(b));
}
template <bool Z, int R> DEV void mm32b(f16v& c, v4u a) {
    if constexpr (Z) asm volatile("v_mfma_f32_32x32x16_bf16 %0, %1, a[%c2:%c3], 0" : "=&v"(c) : "v"(a), "n"(R), "n"(R + 3));
    else asm volatile("v_mfma_f32_32x32x16_bf16 %0, %1, a[%c2:%c3], %0" : "+v"(c) : "v"(a), "n"(R), "n"(R + 3));
}
template <bool Z, int R> DEV void mm16b(f4& c, v4u a) {
    if constexpr (Z) asm volatile("v_mfma_f32_16x16x32_bf16 %0, %1, a[%c2:%c3], 0" : "=&v"(c) : "v"(a), "n"(R), "n"(R + 3));
    else asm volatile("v_mfma_f32_16x16x32_bf16 %0, %1, a[%c2:%c3], %0" : "+v"(c) : "v"(a), "n"(R), "n"(R + 3));
}
template <bool Z, int R> DEV void mm32a(f16v& c, v4u b) {
    if constexpr (Z) asm volatile("v_mfma_f32_32x32x16_bf16 %0, a[%c2:%c3], %1, 0" : "=&v"(c) : "v"(b), "n"(R), "n"(R + 3));
    else asm volatile("v_mfma_f32_32x32x16_bf16 %0, a[%c2:%c3], %1, %0" : "+v"(c) : "v"(b), "n"(R), "n"(R + 3));
}
template <int N, class T> DEV void pad(T& c) { asm volatile("s_nop %1" : "+v"(c) : "n"(N)); }
template <class T> DEV void pin(T& c) { asm volatile("" : "+v"(c)); }
DEV void nop2() { asm volatile("s_nop 1"); }
template <int R> DEV u32 rda() { u32 v; asm volatile("v_accvgpr_read_b32 %0, a%c1" : "=v"(v) : "n"(R)); return v; }
template <int R> DEV void wra(u32 v) { asm volatile("v_accvgpr_write_b32 a%c1, %0" ::"v"(v), "n"(R)); }
__device__ void bst4(v4u, v4u, u32, u32, int) __asm("llvm.amdgcn.raw.buffer.store.v4i32");
__device__ void bst2(v2u, v4u, u32, u32, int) __asm("llvm.amdgcn.raw.buffer.store.v2i32");
__device__ void bst1(u32, v4u, u32, u32, int) __asm("llvm.amdgcn.raw.buffer.store.i32");
__device__ v4u bld4(v4u, u32, u32, int) __asm("llvm.amdgcn.raw.buffer.load.v4i32");
__device__ v2u bld2(v4u, u32, u32, int) __asm("llvm.amdgcn.raw.buffer.load.v2i32");
__device__ u32 bld1(v4u, u32, u32, int) __asm("llvm.amdgcn.raw.buffer.load.i32");
struct Buf { const void* p; u32 n, f; };
struct Args { Buf k, v, w[2], ph[2], x, ws, out; const void *sk, *sh, *sD; };
template <class T> DEV void sld(T& d, const void* ka, u32 off) {
    if constexpr (sizeof(T) == 16) asm volatile("s_load_dwordx4 %0, %1, %2" : "=s"(d) : "s"(ka), "s"(off));
    else asm volatile("s_load_dwordx2 %0, %1, %2" : "=s"(d) : "s"(ka), "s"(off));
}
template <int O, class T> DEV void sldi(T& d, const void* ka) {
    if constexpr (sizeof(T) == 16) asm volatile("s_load_dwordx4 %0, %1, %2" : "=s"(d) : "s"(ka), "n"(O));
    else asm volatile("s_load_dwordx2 %0, %1, %2" : "=s"(d) : "s"(ka), "n"(O));
}
DEV v4u lds4(u32 a) { return *(const LAS v4u*)a; }
DEV v2u lds2(u32 a) { return *(const LAS v2u*)a; }
DEV v2u lds2v(u32 a) { return *(const volatile LAS v2u*)a; }
DEV void barrier() { asm volatile("s_waitcnt lgkmcnt(0)\n\ts_barrier" ::: "memory"); }
template <int N> DEV void waitv() { asm volatile("s_waitcnt vmcnt(%0)" ::"n"(N) : "memory"); }
DEV void dma_in(const void* g, u32 voff, u32 lds) {
    asm volatile("s_mov_b32 m0, %2\n\ts_nop 0\n\tglobal_load_lds_dwordx4 %0, %1" ::"v"(voff), "s"(g), "s"(lds) : "m0", "memory");
}
DEV void dma_b(v4u rs, u32 voff, u32 lds) {
    asm volatile("s_mov_b32 m0, %2\n\ts_nop 0\n\tbuffer_load_dwordx4 %0, %1, 0 offen lds" ::"v"(voff), "s"(rs), "s"(lds) : "m0", "memory");
}
DEV void dma_s(v4u rs, u32 voff, u32 so, u32 lds) {
    asm volatile("s_mov_b32 m0, %3\n\ts_nop 0\n\tbuffer_load_dwordx4 %0, %1, %2 offen lds" ::"v"(voff), "s"(rs), "s"(so), "s"(lds) : "m0", "memory");
}
DEV void dma_ws(v4u rs, u32 voff, u32 so, u32 lds) {
    asm volatile("s_mov_b32 m0, %3\n\ts_nop 0\n\tbuffer_load_dwordx4 %0, %1, %2 offen sc1 lds" ::"v"(voff), "s"(rs), "s"(so), "s"(lds) : "m0", "memory");
}
template <int IM> DEV void gld4(v4u& d, u32 voff, const void* g) { asm volatile("global_load_dwordx4 %0, %1, %2 offset:%3" : "=v"(d) : "v"(voff), "s"(g), "n"(IM) : "memory"); }
DEV void gld2(v2u& d, u32 voff, const void* g) { asm volatile("global_load_dwordx2 %0, %1, %2" : "=v"(d) : "v"(voff), "s"(g) : "memory"); }
DEV void mm16k(f4& c, v2u a, v2u b) { asm volatile("v_mfma_f32_16x16x16_bf16 %0, %1, %2, 0" : "=&v"(c) : "v"(a), "v"(b)); }
DEV void mm16n(f4& c, v4u a, v4u b) { asm volatile("s_nop 1\n\tv_mfma_f32_16x16x32_bf16 %0, %1, %2, 0" : "=&v"(c) : "v"(a), "v"(b)); }
template <class A, class B> DEV void pin2(A& a, B& b) { asm volatile("" : "+v"(a), "+v"(b)); }
template <int IM> DEV void ld4(v4u& d, u32 voff, v4u rs, u32 so) { asm volatile("buffer_load_dwordx4 %0, %1, %2, %3 offen offset:%4 sc1" : "+v"(d) : "v"(voff), "s"(rs), "s"(so), "n"(IM) : "memory"); }
template <int IM> DEV void ld1(u32& d, u32 voff, v4u rs, u32 so) { asm volatile("buffer_load_dword %0, %1, %2, %3 offen offset:%4 sc1" : "+v"(d) : "v"(voff), "s"(rs), "s"(so), "n"(IM) : "memory"); }
template <int IM> DEV void ld4m(v4u& d, u32 voff, v4u rs, u32 so, u64 m) {
    asm volatile("s_mov_b64 exec, %5\n\tbuffer_load_dwordx4 %0, %1, %2, %3 offen offset:%4 sc1\n\ts_mov_b64 exec, -1" : "+v"(d) : "v"(voff), "s"(rs), "s"(so), "n"(IM), "s"(m) : "memory");
}
template <int IM, int R> DEV void ld4a(u32 voff, v4u rs, u32 so) { asm volatile("buffer_load_dwordx4 a[%c4:%c5], %0, %1, %2 offen offset:%3 sc1" ::"v"(voff), "s"(rs), "s"(so), "n"(IM), "n"(R), "n"(R + 3) : "memory"); }
template <int IM, int R> DEV void ld4v(u32 voff, v4u rs, u32 so) { asm volatile("buffer_load_dwordx4 v[%c4:%c5], %0, %1, %2 offen offset:%3 sc1" ::"v"(voff), "s"(rs), "s"(so), "n"(IM), "n"(R), "n"(R + 3) : "memory"); }
template <int R> DEV u32 mx3v(u32 a, u32 b) { u32 r; asm volatile("v_max3_u32 %0, %1, %2, v%c3" : "=v"(r) : "v"(a), "v"(b), "n"(R)); return r; }
template <int R> DEV u32 mx3vv(u32 a) { u32 r; asm volatile("v_max3_u32 %0, %1, v%c2, v%c3" : "=v"(r) : "v"(a), "n"(R), "n"(R + 2)); return r; }
template <int R, int A> DEV void swapw() { asm volatile("v_permlane32_swap_b32 v%c0, v%c1\n\tv_accvgpr_write_b32 a%c2, v%c0\n\tv_accvgpr_write_b32 a%c3, v%c1" ::"n"(R), "n"(R + 2), "n"(A), "n"(A + 1)); }
template <int R, int R2> DEV void addv() { asm volatile("v_add_f32 v%c0, v%c0, v%c1" ::"n"(R), "n"(R2)); }
template <int R> DEV float swapsum() { float r; asm volatile("s_nop 1\n\tv_permlane32_swap_b32 v%c1, v%c2\n\tv_add_f32 %0, v%c1, v%c2" : "=v"(r) : "n"(R + 1), "n"(R + 3)); return r; }
template <int IM, int R> DEV void ld1a(u32 voff, v4u rs, u32 so) { asm volatile("buffer_load_dword a%c4, %0, %1, %2 offen offset:%3 sc1" ::"v"(voff), "s"(rs), "s"(so), "n"(IM), "n"(R) : "memory"); }
template <int IM, int R> DEV void ld4am(u32 voff, v4u rs, u32 so, u64 m) {
    asm volatile("s_mov_b64 exec, %6\n\tbuffer_load_dwordx4 a[%c4:%c5], %0, %1, %2 offen offset:%3 sc1\n\ts_mov_b64 exec, -1" ::"v"(voff), "s"(rs), "s"(so), "n"(IM), "n"(R), "n"(R + 3), "s"(m) : "memory");
}
template <int IM> DEV void ld2(v2u& d, u32 voff, v4u rs, u32 so) { asm volatile("buffer_load_dwordx2 %0, %1, %2, %3 offen offset:%4 sc1" : "=v"(d) : "v"(voff), "s"(rs), "s"(so), "n"(IM) : "memory"); }
template <int IM> DEV void ld4o(v4u& d, u32 voff, v4u rs, u32 so) { asm volatile("buffer_load_dwordx4 %0, %1, %2, %3 offen offset:%4 sc1" : "=v"(d) : "v"(voff), "s"(rs), "s"(so), "n"(IM) : "memory"); }
template <int IM> DEV void ldh(u32& d, u32 voff, v4u rs, u32 so) { asm volatile("buffer_load_short_d16_hi %0, %1, %2, %3 offen offset:%4 sc1" : "+v"(d) : "v"(voff), "s"(rs), "s"(so), "n"(IM) : "memory"); }
template <int IM> DEV void st4(v4u d, u32 voff, v4u rs, u32 so) { asm volatile("buffer_store_dwordx4 %0, %1, %2, %3 offen offset:%4 sc1" ::"v"(d), "v"(voff), "s"(rs), "s"(so), "n"(IM) : "memory"); }
DEV void setm0(u32 v) { asm volatile("s_mov_b32 m0, %0" ::"s"(v) : "m0", "memory"); }
template <int IM> DEV void dmai(v4u rs, u32 voff, u32 so) { asm volatile("buffer_load_dwordx4 %0, %1, %2 offen offset:%3 sc1 lds" ::"v"(voff), "s"(rs), "s"(so), "n"(IM) : "memory"); }
DEV u32 pka(float a, float b) { u32 r; asm volatile("v_cvt_pk_bf16_f32 %0, %1, %2" : "=v"(r) : "v"(a), "v"(b)); return r; }
template <int N> DEV void waitl() { __builtin_amdgcn_s_waitcnt(0xc07f | (N << 8)); }
DEV u32 vsel(bool c, u32 a, u32 b) { asm volatile("" : "+v"(a)); asm volatile("" : "+v"(b)); return c ? a : b; }
DEV u32 ssel(bool c, u32 a, u32 b) { asm volatile("" : "+s"(a)); asm volatile("" : "+s"(b)); return c ? a : b; }
template <int N, class T> DEV void waitp(T& d) { asm volatile("s_waitcnt vmcnt(%1)" : "+v"(d) : "n"(N) : "memory"); }
template <int N> DEV void waitbar() { asm volatile("s_waitcnt vmcnt(%0) lgkmcnt(0)\n\ts_barrier" ::"n"(N) : "memory"); }
template <int N> DEV void pad3(f4& a, f4& b, f4& c) { asm volatile("s_nop %3" : "+v"(a), "+v"(b), "+v"(c) : "n"(N)); }
template <int I> struct IC { static constexpr int v = I; };
template <int B, int E, class F> DEV void sfor(F&& f) { if constexpr (B < E) { f(IC<B>{}); sfor<B + 1, E>(f); } }
#define LAM(I) [&](auto I) __attribute__((always_inline))
#define FENCE() __builtin_amdgcn_sched_barrier(0)
#define OPQV(x) asm volatile("" : "+v"(x))
#define OPQS(x) asm volatile("" : "+s"(x))
DEV float halves_sum(float a, float b) {
    auto r = __builtin_amdgcn_permlane32_swap(asu(a), asu(b), false, false);
    return asf(r[0]) + asf(r[1]);
}
DEV bool bad(u32 m) { return __builtin_amdgcn_ballot_w64(m == ~0u) != 0; }
#define STAMP_INIT()
template <int I> DEV void mark() { asm volatile("; MARK %0" ::"n"(I)); }
#define STAMP(i) mark<(i)>()
#define STAMP_END()
#define STAMPV(i, v)

DEV void hedgehog(const float (&t)[18], float a, u32 (&ph)[18]) {
    float mx = t[0], mn = t[0];
#pragma unroll
    for (int i = 1; i < 18; ++i) { mx = __builtin_fmaxf(mx, t[i]); mn = __builtin_fminf(mn, t[i]); }
    const float c1 = -a * mx, c2 = a * mn;
    float e1[18], e2[18], s1 = 0.f, s2 = 0.f;
#pragma unroll
    for (int i = 0; i < 18; ++i) {
        e1[i] = __builtin_amdgcn_exp2f(__builtin_fmaf(t[i], a, c1)); s1 = i ? s1 + e1[i] : e1[i];
        e2[i] = __builtin_amdgcn_exp2f(__builtin_fmaf(t[i], -a, c2)); s2 = i ? s2 + e2[i] : e2[i];
    }
    const float r1 = __builtin_amdgcn_rcpf(s1), r2 = __builtin_amdgcn_rcpf(s2);
#pragma unroll
    for (int i = 0; i < 9; ++i) { ph[i] = pk(e1[2 * i] * r1, e1[2 * i + 1] * r1); ph[9 + i] = pk(e2[2 * i] * r2, e2[2 * i + 1] * r2); }
}
DEV v4u tr2(u32 a0, u32 a1) {
    s4 lo = __builtin_amdgcn_ds_read_tr16_b64_v4i16((LAS s4*)a0), hi = __builtin_amdgcn_ds_read_tr16_b64_v4i16((LAS s4*)a1);
    v2u l = __builtin_bit_cast(v2u, lo), h = __builtin_bit_cast(v2u, hi);
    return (v4u){l.x, l.y, h.x, h.y};
}
DEV v2u y_val(f4 acc) { return (v2u){pk(acc[0], acc[1]), pk(acc[2], acc[3])}; }
DEV v4u y_pair(v2u a, v2u b) {
    auto r0 = __builtin_amdgcn_permlane16_swap(a.x, b.x, false, false), r1 = __builtin_amdgcn_permlane16_swap(a.y, b.y, false, false);
    return (v4u){r0[0], r1[0], r0[1], r1[1]};
}
template <int N, int n, int r>
DEV void conv_read(u32 aa, u32 ba, v4u (&A)[N], v4u (&B)[N]) {
    constexpr int i = n / 3, k = n % 3;
    if constexpr (k == 0) B[i] = lds4(ba + 64 * (8 * i - r + 7));
    else { const v2u v = lds2(aa + 2 + 64 * (r + 8 * i) + 8 * (k - 1)); if constexpr (k == 1) { A[i].x = v.x; A[i].y = v.y; } else { A[i].z = v.x; A[i].w = v.y; } }
}
constexpr int conv_ij(int N, int MASK, int n) {
    for (int d = 0; d < N; ++d) for (int i = 0; i < N; ++i) for (int j = i; j < N; ++j)
        if (((MASK >> j) & 1) && (i > j - i ? i : j - i) == d && n-- == 0) return i * 8 + j;
    return -1;
}
#define NPRE_ 10
#define SEND_ 212
#define UPA_ 8
#define UPO_ 12
constexpr int NPRE = NPRE_, NSITE = 13 + 4 + 40 + 9 + 1, SITE_END = SEND_;
constexpr int site_at(int q) { return q <= SITE_END ? NPRE + (q * (NSITE - NPRE) + SITE_END - 1) / SITE_END : NSITE; }
constexpr int xissued(int n) { return UPO_ + (5 * n + 3) / 4 < 32 ? UPO_ + (5 * n + 3) / 4 : 32; }
constexpr int kstep(int n) { return n % 9 < 8 ? 8 * (n / 9) + n % 9 : 32 + n / 9; }

__global__ void __launch_bounds__(256, 1) k_main(Args) {
    const void* ka = (const void*)(uintptr_t)__builtin_amdgcn_kernarg_segment_ptr();
    v4u rk, rv, rm, rq, rp, rx, vw, ro;
    const char *sk, *sh, *sD;
    int tid = __builtin_amdgcn_workitem_id_x(), b = __builtin_amdgcn_workgroup_id_x();
    sldi<0>(rk, ka); sldi<16>(rv, ka); sldi<144>(sk, ka); sldi<112>(vw, ka); sldi<96>(rx, ka); sldi<128>(ro, ka); sldi<152>(sh, ka); sldi<160>(sD, ka);
    OPQS(b); OPQV(tid);
    const int bs = b >> 3;
    if constexpr (MAP == 1) b = (b & 6) + (b >> 7) + 64 * (b & 1) + (b & 56) + 2 * (b & 64);
    if constexpr (MAP == 2) b = (b & 4) + ((b >> 6) & 3) + 32 * (b & 3) + (b & 24) + 4 * (b & 32);
    int wave = __builtin_amdgcn_readfirstlane(tid >> 6);
    sld(rm, ka, (wave >> 1) * 16); sld(rq, ka, (b & 24) == 24 ? 48 : 32); sld(rp, ka, 64 + (b & 8) * 2);
    OPQS(b); OPQS(wave); OPQV(tid);
    {
        u64 pc;
        asm volatile("s_getpc_b64 %0" : "=s"(pc));
        const u32 o = umin(tid * 8 + bs * 2048, CODE - 512);
        asm volatile("s_mov_b32 m0, %2\n\ts_nop 0\n\tglobal_load_lds_dword %0, %1" ::"v"(o), "s"(pc), "s"(L_EX) : "m0", "memory");
    }
    STAMP_INIT();
    asm volatile("" ::: "a255", "v255");
    u64 ep;
    v2u sw0, sw1;
    {
        const int lane = tid & 63, c = lane & 15, cg = ((b >> 4) & 6) + ((b >> 3) & 1), tg = 4 * (b & 7) + 2 * (b >> 7) + ((b >> 4) & 1);
        u32 s0 = c * 8 + (wave * 128 + cg * 576), s1 = (c & 7) * 8 + cg * 576, vo[5], lo[5];
        const u32 l0 = ((lane * 7282u) >> 16) * 1008 + lane * 16, t0 = (64 * tg + 7 * wave) * 1152 + cg * 144;
#pragma unroll
        for (int i = 0; i < 5; ++i) {
            const int isv = i < 2 ? 0 : i > 2 ? 1 : wave >> 1;
            vo[i] = l0 + (t0 + (28 * i - 3 - 67 * isv) * 1152); lo[i] = P_KS + wave * 1008 + i * 4032 + isv * (P_VS - 10080);
            OPQV(vo[i]); OPQS(lo[i]);
        }
        pin2(s0, s1);
        FENCE();
        asm volatile("s_waitcnt lgkmcnt(0)" : "+s"(rk), "+s"(rv), "+s"(rm), "+s"(rq), "+s"(rp), "+s"(rx), "+s"(vw), "+s"(ro), "+s"(sk), "+s"(sh), "+s"(sD));
        STAMP(40);
#pragma unroll
        for (int i = 0; i < 5; ++i) dma_b(i < 2 ? rk : i > 2 ? rv : rm, vo[i], lo[i]);
        asm volatile("global_load_dwordx2 %0, %1, %2" : "=v"(sw0) : "v"(s0), "s"(sk) : "memory");
        asm volatile("global_load_dwordx2 %0, %1, %2 offset:512" : "=v"(sw1) : "v"(s1), "s"(sk) : "memory");
        sld(ep, (const void*)(((u64)vw.y << 32) | vw.x), W_E);
    }
    OPQS(b); OPQS(wave); OPQV(tid);
    const int lane = tid & 63, rg = 2 * (b & 7) + (b >> 7), p = (b >> 3) & 15;
    const int Pg = p >> 2, tw = p & 3, pw = 4 * Pg + wave, ci = (rg + p) & 15;
    const bool conv = ci < 12;
    u32 l16 = lane * 16, l4 = lane * 4;
    OPQV(l16); OPQV(l4);
    const int ch0 = 36 * p + 3 * ci;
    v4u hd[3]; v2u he[3], hz;
    u32 xr[9], kvw[3], kvA, kvB, l8, lP, l16B, cpo, csd;
    {
        const int sec = p & 3, cg = 2 * Pg + (p & 1), tg = 2 * rg + ((p >> 1) & 1);
        const int c = lane & 15;
        FENCE();
        STAMP(41);
        const v4u z4 = {0, 0, 0, 0};
        u32 mk0, mk1, ua0, ua1, uo0, uo1, aw, al_, ap, d0_, d1_, d2_, cso, axs, aid, azc;
        v2u Bi; v4u Sel;
        const bool s3 = sec == 3;
        const u32 wf_ = rg >> 2, jl = rg & 3;
        u32 lw, ll, lx, lf;
        const u32 wst = s3 ? 1152 : 3456;
        {
            const u32 cb = (sec < 2 ? (sec + 1) * 1152 : 0) + Pg * 288;
            lw = ((lane * 3641u) >> 16) * (wst - 288) + l16 + ((36 * rg + 3 * wave) * wst + cb);
            lf = vsel(lane < 54, l16 - ((lane * 21846u) >> 16) * 12 + (Pg * 5184 + wave * 1296), OOB);
            lx = ((lane * 3450u) >> 16) * 848 + l16 + ((128 * rg + 32 * tw + 3 * wave) * 1152 + Pg * 288);
        }
        auto wdma = LAM(K_) {
            constexpr int k = K_.v, i = k / 3;
            if constexpr (k == 7) {
                const u32 cb = (sec < 2 ? (sec + 1) * 1152 : 0) + Pg * 288, r4 = (lane * 43u) >> 8, sg = ((s3 ? 0 : (4 * Pg + wf_) & 14) + 4 * jl + wave) & 15;
                ll = vsel(lane < 24, r4 * (wst - 96) + (r4 >> 1) * (16 * wst) + l16 + ((36 * sg + 16) * wst + cb + ((72 * wf_) & ~15u)), OOB);
                dma_b(rq, ll, P_LS + wave * 1024);
            } else if constexpr (k % 3 == 0) dma_s(rq, lw, 12 * i * wst, P_WS + wave * 864 + i * 3456);
            else if constexpr (k % 3 == 1) dma_s(rp, lf, i * 648, P_PH + wave * 3072 + i * 1536);
            else dma_s(rx, lx, 12 * i * 1152, P_XS + wave * 912 + i * 3648);
            FENCE();
        };
        u32 ib, bA, bB, lp, ds, dr;
        auto wadr = LAM(K_) {
            constexpr int k = K_.v;
            const u32 g = lane >> 4, m = lane & 31, kh = lane >> 5;
            if constexpr (k == 0) {
                const u32 hD = (m >> 2) & 1;
                aw = (18 * hD + (m & 3) + 4 * (m >> 3)) * 288 + 16 * kh + (P_WS + ((72 * wave) & ~15u));
                pin(aw);
            }
            if constexpr (k == 1) {
                const u32 hD = (m >> 2) & 1;
                al_ = ((((m >> 1) & 1) + 2 * ((m >> 3) & 1)) * 1024 + (2 * hD + (m & 1)) * 96) * (wave == wf_) + 16 * kh + (wave == wf_ ? P_LS : L_Z);
                pin(al_);
            }
            if constexpr (k == 2) {
                ap = (8 * kh + (c >> 2)) * 48 + 32 * (g & 1) + 8 * (c & 3) + (P_PH + (64 * wave - 4 * (wave & 1)) * 48);
                pin(ap);
            }
            if constexpr (k == 3) {
                ib = W_IMG + pw * IMG; const u32 ob = W_WOT + Pg * WOG;
                bA = ssel(s3, ob + wave * 36864, ib + sec * L_V + (sec >> 1) * (L_Q - 2 * L_V));
                bB = ssel(s3, ob + 4 * 36864 + 128 * (wave >> 1) + 32 * (wave & 1), ib + L_QT + 128 * sec);
                OPQS(ib); OPQS(bA); OPQS(bB);
            }
            if constexpr (k == 4) {
                lp = vsel(m < 16, ((m & 3) + 8 * (m >> 2) + 32 * kh) * 16 + bA, vsel(m < 18, (m & 1) * (s3 ? 16 : 32) + kh * (s3 ? 512 : 16) + bB, OOB));
                pin(lp);
            }
            if constexpr (k == 5) {
                ds = vsel(m < 16, 1024, s3 ? 256 : QTS); dr = vsel(m < 16, 2048, s3 ? 1024 : 2 * QTS);
                d0_ = lp + ((rg - (s3 ? 0 : pw & 14)) & 15) * dr; d1_ = d0_ + ds;
                pin2(ds, dr); pin2(d0_, d1_);
            }
            if constexpr (k == 6) {
                d2_ = umax(lp + (16 + (jl >> 1)) * dr + (jl & 1) * ds, wave == wf_ ? 0u : OOB);
                pin(d2_);
            }
            if constexpr (k == 7) {
                cso = vsel(m < (s3 ? 0 : 18), (18 * kh + m) * 4 + (W_CSP + pw * CSB + rg * 576 + sec * 192), OOB);
                pin(cso);
            }
            if constexpr (k == 8) {
                axs = m * 304 + 36 * kh + (P_XS + 72 * wave);
                pin(axs);
            }
            if constexpr (k == 9) {
                aid = vsel(lane < 36 * (sec & 1), lane * 50 + (lane >= 18 ? 1536 - 900 : 0) + (P_PH + wave * 3072), L_END - 16);
                pin(aid);
            }
            if constexpr (k == 10) {
                azc = vsel(lane < (sec ? 0 : 6), (lane & 1) * 16 + ((lane >> 1) + (lane < 4 ? 2 * rg : 30 + jl)) * QTS + (ib + L_QT + 384), OOB);
                pin(azc);
            }
        };
        sfor<0, WD0>(wdma);
        STAMP(37);
        {
            const u32 g = lane >> 4, m = lane & 31, kh = lane >> 5;
            const u32 hm = g == (c >> 2) ? ((c & 1) ? 0xffff0000u : 0xffffu) : 0u;
            mk0 = (c & 2) ? 0u : hm; mk1 = (c & 2) ? hm : 0u;
            Bi = (v2u){mk0 & 0x3f803f80u, mk1 & 0x3f803f80u}; Sel = (v4u){Bi.x, Bi.y, Bi.x, Bi.y};
            const u32 r0 = (8 * (c >> 2) + (c & 3)) * 144;
            ua0 = r0 + 8 * g + (P_KS + 32 * wave);
            ua1 = r0 + 32 * 144 * kh + 8 * (g & 1) + (P_KS + 128 + 576 * (wave & 1));
            uo0 = c * 4096 + 16 * g;
            uo1 = (c & 7) * 4096 + 64 * (c >> 3) + 16 * g + (wave < 2 ? 64 * 4096 + 8 - 8 * wave : OOB - (1u << 20));
            pin2(mk0, mk1); pin2(Bi, Sel); pin2(ua0, ua1); pin2(uo0, uo1);
            *(LAS v4u*)(l16 + kh * 1024 + (P_PH + 1024 + wave * 3072)) = z4;
            *(LAS v4u*)(vsel(lane < (wave ? 30 : 6), l16 + ssel(wave, L_Q + 4 * HCS - CVS + wave * CVS, L_Z), L_END - 16)) = z4;
        }
        FENCE();
        FENCE();
        sfor<0, 11>(LAM(K_) {
            constexpr int k = K_.v, d0 = WD0 + k * (9 - WD0) / WDN, d1 = WD0 + (k + 1) * (9 - WD0) / WDN;
            if constexpr (k < WDN) sfor<d0, d1>(wdma);
            wadr(K_);
            FENCE();
        });
        auto hld = [&]() __attribute__((always_inline)) {
            const char* gp = sh + umin(ch0, 573) * 4096;
            const u32 o1 = tid * 16, o2 = (o1 + 16) & 4095;
            gld2(hz, umin(lane >> 3, 2) * 4096, gp);
#pragma unroll
            for (int i = 0; i < 3; ++i) { gld4<0>(hd[i], o1, gp + i * 4096); gld2(he[i], o2, gp + i * 4096); }
        };
        STAMP(42);
        FENCE();
        STAMP(26);
        asm volatile("s_waitcnt vmcnt(9)" : "+v"(sw0), "+v"(sw1)::"memory");
        v2u Bd[2][4];
#pragma unroll
        for (int s = 0; s < 2; ++s)
#pragma unroll
            for (int j = 0; j < 4; ++j) {
                const v2u sw = s ? sw1 : sw0;
                const u32 wd = __builtin_amdgcn_perm(0u, j < 2 ? sw.y : sw.x, (j & 1) ? 0x01000100u : 0x03020302u);
                Bd[s][j] = (v2u){wd & mk0, wd & mk1};
                pin(Bd[s][j]);
            }
        u32 so_u = W_UT + 128 * tg + cg * 72 * 4096, so_u2 = so_u + wave * 65536;
        OPQS(so_u); OPQS(so_u2);
        FENCE();
        barrier();
        STAMP(27);
        {
            u32 uo[5][2];
            v2u ka[5][5]; f4 pr[5][5], sm[5];
            v4u bb[2];
#pragma unroll
            for (int n = 0; n < 25; ++n) {
                const int T = n < 10 ? n >> 1 : n % 5, j = n < 10 ? n & 1 : n / 5;
                ka[T][j] = lds2v((T < 4 ? ua0 + (32 * (T >> 1) + 4 * (T & 1)) * 144 : ua1) + (j < 4 ? j * 144 : P_VS));
            }
            FENCE();
            auto P = [&](auto T_, auto J_) __attribute__((always_inline)) { constexpr int T = T_.v, j = J_.v; mm16k(pr[T][j], ka[T][j], j < 4 ? Bd[T == 4][j] : Bi); };
            sfor<0, 5>(LAM(T_) { P(T_, IC<0>{}); P(T_, IC<1>{}); });
            STAMP(52);
            sfor<0, 16>(LAM(I_) {
                constexpr int I = I_.v, T = I % 5, ph = I / 5 + 1, U = (I + 4) % 5;
                if constexpr (I < 15) {
                    f4& a = ph == 1 ? pr[T][0] : sm[T];
                    const u32 w0 = pka(a[0], a[1]), w1 = pka(a[2], a[3]), w2 = pka(pr[T][ph][0], pr[T][ph][1]), w3 = pka(pr[T][ph][2], pr[T][ph][3]);
                    bb[I & 1] = (v4u){w0, w1, w2, w3};
                    P(IC<T>{}, IC<ph + 1>{});
                } else {
                    uo[0][0] = pka(sm[0][0], sm[0][1]); uo[0][1] = pka(sm[0][2], sm[0][3]);
                }
                if constexpr (I > 0) mm16<true>(sm[U], Sel, bb[(I - 1) & 1]);
            });
            STAMP(53);
            sfor<0, 5>(LAM(T_) {
                constexpr int T = T_.v;
                u32 w0, w1;
                if constexpr (T) { w0 = pka(sm[T][0], sm[T][1]); w1 = pka(sm[T][2], sm[T][3]); } else { w0 = uo[0][0]; w1 = uo[0][1]; }
                pin(pr[T][4]);
                FENCE();
                uo[T][0] = pk(bf_hi(w1) * pr[T][4][3], bf_lo(w1) * pr[T][4][2]);
                uo[T][1] = pk(bf_hi(w0) * pr[T][4][1], bf_lo(w0) * pr[T][4][0]);
                if constexpr (T == 1 || T == 3) bst4((v4u){uo[T][0], uo[T][1], uo[T - 1][0], uo[T - 1][1]}, vw, uo0 + 32 * (T - 1), so_u2, SC1);
                else if constexpr (T == 4) bst2((v2u){uo[4][0], uo[4][1]}, vw, uo1, so_u, SC1);
                else pin2(uo[T][0], uo[T][1]);
                FENCE();
            });
        }
        STAMP(28);
        asm volatile("s_waitcnt vmcnt(3)\n\ts_barrier" ::: "memory");
        STAMP(54);
        {
            u32 one2 = 0x3f803f80u;
            OPQS(one2);
            *(LAS u16*)(aid) = 0x3f80;
            v4u bw[2][2], wa[2][3];
#pragma unroll
            for (int j = 0; j < 9; ++j) xr[j] = *(const LAS u32*)(axs + 4 * j);
            FENCE();
            bw[0][0] = tr2(ap, ap + 192); wa[0][0] = lds4(aw); wa[1][0] = lds4(al_);
            bw[0][1] = tr2(ap + 768, ap + 960); wa[0][1] = lds4(aw + 32); wa[1][1] = lds4(al_ + 32);
            bw[1][0] = tr2(ap + 1440, ap + 1440 + 192);
            bw[1][1] = tr2(ap + 1440 + 768, ap + 1440 + 960); wa[0][2] = lds4(aw + 64); wa[1][2] = lds4(al_ + 64);
            FENCE();
            const u32 so = W_XF0 + ((rg * 4 + tw) * 16 + pw) * XS;
            bst4((v4u){xr[0], xr[1], xr[2], xr[3]}, vw, l16, so, SC1);
            bst4((v4u){xr[4], xr[5], xr[6], xr[7]}, vw, l16 + 1024, so, SC1);
            FENCE();
            f16v d[2][2];
#pragma unroll
            for (int i = 0; i < 4; ++i) { mm32<true>(d[i >> 1][i & 1], wa[i & 1][i >> 1], bw[i >> 1][0]); mm32<false>(d[i >> 1][i & 1], wa[i & 1][(i >> 1) + 1], bw[i >> 1][1]); }
            FENCE();
            STAMP(55);
            {
                float s1 = 0.f, s2 = 0.f;
#pragma unroll
                for (int j = 0; j < 9; ++j) { s1 = dot2(xr[j], one2, s1); s2 = dot2(xr[j], xr[j], s2); }
                l8 = 2 * l4;
                bst2((v2u){xr[8], asu(halves_sum(s1, s2))}, vw, (lane & 31) * 16 + (lane >> 5) * 8 + 2048, so, SC1);
                FENCE();
                bst4(z4, vw, azc, 0, SC1);
            }
            FENCE();
            float cs[2];
#pragma unroll
            for (int hh = 0; hh < 2; ++hh) {
                asm volatile("" : "+v"(d[hh][0]), "+v"(d[hh][1]));
                u32 w[12];
#pragma unroll
                for (int j = 0; j < 12; ++j) w[j] = pk(d[hh][j >> 3][2 * (j & 7)], d[hh][j >> 3][2 * (j & 7) + 1]);
                bst4((v4u){w[0], w[1], w[2], w[3]}, vw, d0_ + 64 * hh, 0, SC1);
                bst4((v4u){w[4], w[5], w[6], w[7]}, vw, d1_ + 64 * hh, 0, SC1);
                bst4((v4u){w[8], w[9], w[10], w[11]}, vw, d2_ + 64 * hh, 0, SC1);
                cs[hh] = 0.f;
#pragma unroll
                for (int j = 0; j < 12; ++j) cs[hh] = dot2(w[j], one2, cs[hh]);
                FENCE();
            }
            bst1(asu(halves_sum(cs[0], cs[1])), vw, cso, 0, SC1);
        }
        STAMP(29);
        asm volatile("s_waitcnt vmcnt(8)\n\ts_barrier" ::: "memory");
        OPQS(ep);
        ep += 1;
        const v2u epv = {(u32)ep, (u32)(ep >> 32)};
        if (tid == 128) bst2(epv, vw, 0, W_F + (rg * 16 + p) * 128, SC1);
        FENCE();
        hld();
        STAMP(37);
        {
            const u32 e = (lane >> 4) & 1, kg = lane >> 4;
#pragma unroll
            for (int j = 0; j < 3; ++j) {
                const u32 pi = 16 * (wave + 4 * j) + c, hh = pi >= 86, ip = pi - 86 * hh, q = ip - 72, s = 2 * (ip - 84) + e;
                u32 v = vsel(ip < 64, (ip & 15) * 80 + (ip >> 4) * 8 + 32 * e, (2 * ip + e) * 80 + 64 - 128 * 80);
                v = vsel(ip < 72, v, (16 + (q >> 2)) * 80 + (q & 3) * 8 + 32 * e);
                v = vsel(ip < 84, v, s * 80 + 16 * 80 + 64);
                kvw[j] = vsel(lane >= 32 || pi >= 172 || s == 3, L_EX + l8, v + hh * 1520 + L_KVA);
                OPQV(kvw[j]);
            }
            *(LAS v2u*)(vsel(tid < 38, tid * 80 + (L_KVA + 72), L_END - 16)) = (v2u){0, 0};
            const bool dt = wave & 1;
            kvA = dt ? vsel(c < 3, 1152 + (c * 4 + kg) * 16, OOB) : l16;
            kvB = vsel(lane < (dt ? 4 : 16), l8 + (dt ? 1344 : 1024), OOB);
            l16B = l16 + XS; lP = (lane & 31) * 16 + (lane >> 5) * XS;
            const u32 sc = (lane * 57u) >> 10, r = lane - 18 * sc, hh = r >= 9, pr = r - 9 * hh;
            cpo = vsel(lane < 54, sc * (192 - 144) + l8, OOB);
            csd = vsel(lane < 54, hh * 224 + vsel(pr < 8, sc * 64 + pr * 8, 192 + sc * 8) + L_CS, L_EX + 2048 + l8);
            pin2(kvA, kvB); pin2(l16B, lP); pin2(cpo, csd); pin(l8);
        }
        FENCE();
    }
    auto xop = [&](auto Q_, u32 so) __attribute__((always_inline)) {
        constexpr int j = Q_.v / 5, k = Q_.v % 5;
        if constexpr (k == 0) ld4a<0, 16 * j>(l16, vw, so);
        if constexpr (k == 1) ld4a<1024, 16 * j + 4>(l16, vw, so);
        if constexpr (k == 2) ld4v<2048, 224 + 4 * j>(lP, vw, so);
        if constexpr (k == 3) ld4a<0, 16 * j + 8>(l16B, vw, so);
        if constexpr (k == 4) ld4a<1024, 16 * j + 12>(l16B, vw, so);
    };
    {
        u32 D2[3];
#pragma unroll
        for (int i = 0; i < 3; ++i) { const u32 cc = umin(ch0, 573) + i, t = ((const __attribute__((address_space(4))) u32*)(uintptr_t)sD)[cc >> 1]; D2[i] = (cc & 1 ? t >> 16 : t & 0xffffu) * 0x10001u; OPQS(D2[i]); }
        const int bq = lane & 15, g = lane >> 4, cc = (bq + 1) & 3;
        u32 aa = L_Q + cc * HCS + 2 * (bq - 8 * g + 24 - cc), ba = L_Q + 4 * HCS + 480 + 2 * (16 * bq + 8 * g - 16) - 7 * 64;
        u32 yp = (bq >> 3) * YB + (bq & 7) * 32 + 16 * (g >> 1) + (g & 1) * (2 * YB);
        v4u dm;
        {
            const u32 e = 7 - (bq & 7), hm = g == 2 + (bq >> 3) ? ((e & 1) ? 0xffff0000u : 0xffffu) : 0u;
#pragma unroll
            for (int i = 0; i < 4; ++i) dm[i] = (e >> 1) == i ? hm : 0u;
        }
        if (wave < 3) { aa += wave * CVS; ba += wave * CVS; }
        pin2(aa, ba); pin2(yp, dm);
        const u32 xb = W_XF0 + (rg * 4 + wave) * 16 * XS;
        u32 io = wave * 9216, ig = W_IMG + p * IMG + io, iq = umin(4 * wave, 11) * 1024 + L_QT - 8192 - io, xo = xb + (p & 14) * XS, uo = W_UT + ch0 * 4096 + wave * 1024, co = W_CSP + p * CSB + wave * (4 * 576);
        u32 fo = vsel(wave < 2, W_F + (wave ? rg * 16 + bq : (lane >> 2) * 16 + 4 * Pg + (lane & 3)) * 128, OOB);
        u32 fb = W_F3 + ((4 * wave + (bq >> 2)) * 16 + 4 * Pg + (lane & 3)) * 128;
        OPQS(io); OPQS(ig); OPQS(iq); OPQS(xo); OPQS(uo); OPQS(co); OPQV(fo); OPQV(fb);
        FENCE();
        v2u fl;
        auto late = [&]() __attribute__((always_inline)) { return __builtin_amdgcn_ballot_w64(fl.x != (u32)ep || fl.y != (u32)(ep >> 32)) != 0; };
        auto spin = [&](u32 o) __attribute__((always_inline)) {
#pragma unroll 1
            for (int sp = 0; sp < SPIN; ++sp) {
                __builtin_amdgcn_s_sleep(ASL);
                ld2<0>(fl, o, vw, 0);
                waitp<0>(fl);
                if (!late()) break;
            }
        };
        waitv<7>();
        barrier();
        STAMP(1);
        if (tid == 192) bst2((v2u){(u32)ep, (u32)(ep >> 32)}, vw, 0, W_F3 + (rg * 16 + p) * 128, SC1);
        ld2<0>(fl, fo, vw, 0);
        waitv<2>();
        if (wave < 3) waitv<1>();
        STAMP(51);
        if (conv) {
            u32 o = L_Q + 32 + tid * 16;
            OPQV(o);
#pragma unroll
            for (int i = 0; i < 3; ++i) {
                asm volatile("" : "+v"(hd[i]), "+v"(he[i]));
                const u32 d0 = hd[i].x, d1 = hd[i].y, d2 = hd[i].z, d3 = hd[i].w, d4 = he[i].x, d5 = he[i].y;
                const u32 a0 = __builtin_amdgcn_alignbit(d1, d0, 16), a1 = __builtin_amdgcn_alignbit(d2, d1, 16), a2 = __builtin_amdgcn_alignbit(d3, d2, 16),
                          a3 = __builtin_amdgcn_alignbit(d4, d3, 16), a4 = __builtin_amdgcn_alignbit(d5, d4, 16);
                *(LAS v4u*)(o + i * CVS) = hd[i];
                *(LAS v4u*)(o + i * CVS + HCS) = (v4u){a0, a1, a2, a3};
                *(LAS v4u*)(o + i * CVS + 2 * HCS) = (v4u){d1, d2, d3, d4};
                *(LAS v4u*)(o + i * CVS + 3 * HCS) = (v4u){a1, a2, a3, a4};
            }
            asm volatile("" : "+v"(hz));
            const u32 k = (lane >> 1) & 3;
            const u64 t = (lane & 1) && k ? (((u64)hz.y << 32) | hz.x) << (64 - 16 * k) : 0;
            if (tid < 24) *(LAS v4u*)(L_Q + (lane >> 3) * CVS + k * HCS + (lane & 1) * 16) = (v4u){0, 0, (u32)t, (u32)(t >> 32)};
        }
        waitp<0>(fl);
        if (wave < 2 && late()) spin(fo);
        barrier();
        STAMP(2);
        v2u cp[4];
        auto site = LAM(N_) {
            constexpr int m = N_.v, n = m <= XB_ ? -1 : m <= XB_ + 17 ? m - XB_ - 1 : m - 1, x = m < PB_ ? m : m <= XB_ ? m - 1 : m - 18, v = n - 57;
            if constexpr (m == XB_ + 1) {
                waitp<XB_ - PB_>(fl);
                if (__builtin_expect(late(), 0)) spin(fb);
                asm volatile("s_barrier" ::: "memory");
                STAMP(38);
            }
            if constexpr (m == PB_) ld2<0>(fl, fb, vw, 0);
            else if constexpr (n >= 0 && n < 9) dmai<(n & 3) * 1024>(vw, l16, ig);
            else if constexpr (n >= 9 && n < 13) dmai<(n - 9) * 1024>(vw, l16, ig);
            else if constexpr (n >= 13 && n < 17) ld2<(n - 13) * 576>(cp[n - 13], cpo, vw, co);
            else if constexpr (n < 57) {
                xop(IC<x>{}, xo);
                if constexpr (x % 5 == 4 && x < 39) { xo += 2 * XS; xo = xo == xb + 16 * XS ? xb : xo; OPQS(xo); }
            } else {
                if constexpr (v == WV_) waitv<40 - XB_ + WV_>();
                dmai<(v & 3) * 1024>(vw, l16, ig);
            }
            if constexpr (n == 3 || n == 7 || n == 60 || n == 64) { io += 4096; ig += 4096; }
            if constexpr (n == 8) { io += iq; ig += iq; }
            if constexpr (n == 12) { io += L_V - 8192 - iq; ig += L_V - 8192 - iq; }
            if constexpr (n == 3 || n == 7 || n == 8 || n == 12 || n == 60 || n == 64) { OPQS(io); OPQS(ig); setm0(io); }
        };
        static_assert(NPRE <= PB_ && PB_ <= XB_ && XB_ <= 20, "");
        auto site1 = LAM(N_) { site(N_); if constexpr (N_.v == XB_ + 4 || N_.v == XB_ + 8 || N_.v == XB_ + 9 || N_.v == 61 || N_.v == 65) nop2(); };
        if (conv) {
#pragma unroll
            for (int i = 0; i < 3; ++i) dma_ws(vw, l16, uo + i * 4096, L_Q + i * CVS + 4 * HCS + 480 + wave * 1024);
            setm0(io); nop2();
            sfor<0, NPRE>(site1);
            asm volatile("s_waitcnt vmcnt(%0)\n\ts_barrier" ::"n"(NPRE) : "memory");
            STAMP(3);
            if (wave < 3) {
                f4 acc[8];
                v4u A[2][8], B[2][8];
                sfor<0, 24>(LAM(N_) { conv_read<8, N_.v, 0>(aa, ba, A[0], B[0]); });
                const u32 dd = wave == 0 ? D2[0] : wave == 1 ? D2[1] : D2[2];
                v4u ad = {dm.x & dd, dm.y & dd, dm.z & dd, dm.w & dd};
                pin(ad);
                FENCE();
                sfor<0, 6>(LAM(J_) { constexpr int j = J_.v < 2 ? J_.v : J_.v + 2; mm16<true>(acc[j], ad, B[0][j]); });
                FENCE();
                sfor<0, 8 * 29>(LAM(Q_) {
                    constexpr int r = Q_.v / 29, n = Q_.v % 29, i = conv_ij(8, 0xf3, n) >> 3, j = conv_ij(8, 0xf3, n) & 7;
                    if constexpr (r && n == 0) waitl<9>();
                    if constexpr (r && n == 8) waitl<(r < 7 ? 8 : 0)>();
                    mm16<false>(acc[j], A[r & 1][i], B[r & 1][j - i]);
                    if constexpr (r < 7 && n < 24) conv_read<8, n, r + 1>(aa, ba, A[(r + 1) & 1], B[(r + 1) & 1]);
                    sfor<site_at(Q_.v * 216 / 232), site_at((Q_.v + 1) * 216 / 232)>(site);
                    FENCE();
                });
                asm volatile("s_nop 7" : "+v"(acc[0]), "+v"(acc[1]), "+v"(acc[4]), "+v"(acc[5]), "+v"(acc[6]), "+v"(acc[7]));
                const u32 so = W_Y + (ch0 + wave) * 256;
                const v4u y0 = y_pair(y_val(acc[0]), y_val(acc[1]));
                const v4u y4 = y_pair(y_val(acc[4]), y_val(acc[5])), y6 = y_pair(y_val(acc[6]), y_val(acc[7]));
                bst4(y0, vw, yp, so, SC1);
                bst4(y4, vw, yp, so + 8 * YB, SC1);
                bst4(y6, vw, yp, so + 12 * YB, SC1);
            } else {
                f4 acc[2][4];
                v4u A[2][4], B[2][4], ad;
                v2u y1; v4u y2;
                sfor<0, 12>(LAM(N_) { conv_read<4, N_.v, 0>(aa, ba, A[0], B[0]); });
                FENCE();
                sfor<0, 3 * 8 * 7>(LAM(Q_) {
                    constexpr int cn = Q_.v / 56, r = Q_.v % 56 / 7, n = Q_.v % 7, i = conv_ij(4, 0xc, n) >> 3, j = conv_ij(4, 0xc, n) & 7;
                    constexpr int nc = r < 7 ? cn : cn + 1, nr = (r + 1) & 7;
                    if constexpr (r == 0 && n == 0) {
                        ad = (v4u){dm.x & D2[cn], dm.y & D2[cn], dm.z & D2[cn], dm.w & D2[cn]};
                        pin(ad);
                        waitl<0>();
                        nop2();
                        FENCE();
                        sfor<2, 4>(LAM(J_) { mm16<true>(acc[cn & 1][J_.v], ad, B[0][J_.v]); });
                    } else {
                        if constexpr (n == 0) waitl<3>();
                        if constexpr (n == 5) waitl<(nc < 3 ? 10 : 0)>();
                    }
                    mm16<false>(acc[cn & 1][j], A[r & 1][i], B[r & 1][j - i]);
                    if constexpr (nc < 3) {
                        if constexpr (n < 5) { conv_read<4, 2 * n, nr>(aa + nc * CVS, ba + nc * CVS, A[nr & 1], B[nr & 1]); conv_read<4, 2 * n + 1, nr>(aa + nc * CVS, ba + nc * CVS, A[nr & 1], B[nr & 1]); }
                        else conv_read<4, n + 5, nr>(aa + nc * CVS, ba + nc * CVS, A[nr & 1], B[nr & 1]);
                    }
                    if constexpr (cn > 0 && r == 1) {
                        const u32 so = W_Y + (ch0 + cn - 1) * 256;
                        if constexpr (n == 2) { pin(acc[(cn - 1) & 1][2]); y1 = y_val(acc[(cn - 1) & 1][2]); pin(y1); }
                        if constexpr (n == 3) { pin(acc[(cn - 1) & 1][3]); y2 = y_pair(y1, y_val(acc[(cn - 1) & 1][3])); pin(y2); }
                        if constexpr (n == 4) bst4(y2, vw, yp, so + 4 * YB, SC1);
                    }
                    sfor<site_at(Q_.v * 216 / 168), site_at((Q_.v + 1) * 216 / 168)>(site);
                    FENCE();
                });
                asm volatile("s_nop 7" : "+v"(acc[0][2]), "+v"(acc[0][3]));
                const u32 so = W_Y + (ch0 + 2) * 256;
                y2 = y_pair(y_val(acc[0][2]), y_val(acc[0][3]));
                bst4(y2, vw, yp, so + 4 * YB, SC1);
            }
        } else {
            setm0(io); nop2();
            sfor<0, NSITE>(site1);
        }
        STAMP(14);
        asm volatile("" : "+v"(cp[0]), "+v"(cp[1]), "+v"(cp[2]), "+v"(cp[3]));
        *(LAS f2*)(L_EX + wave * 512 + l8) = (f2){(asf(cp[0].x) + asf(cp[1].x)) + (asf(cp[2].x) + asf(cp[3].x)), (asf(cp[0].y) + asf(cp[1].y)) + (asf(cp[2].y) + asf(cp[3].y))};
        barrier();
    }
    u32 yv[9];
    v4u wv[17];
    STAMP(15);

    sfor<0, 2>(LAM(L_) {
        constexpr int L = L_.v;
        int ln = lane;
        OPQV(ln); OPQV(l16); OPQV(l4);
        const int tl = ln & 31, h = ln >> 5, row = ln & 15, kg = ln >> 4, gq = row >> 2, rq = row & 3;
        const bool qa = (gq & 1) == (kg & 1);
        u32 aV_ = L_V + l16, aQ_ = L_Q + l16;
        u32 aKV = L_QT + vsel(qa, (rq >> 1) * 128 + (gq >> 1) * 64 + (rq & 1) * 32 + (kg >> 1) * 16, 400 - 16 * (kg >> 1));
        u32 aQq, aCS, aS;
        OPQV(aV_); OPQV(aKV); OPQV(aQ_);
        u32 nretry = 0;
        const u32 wb = W_WOT + Pg * WOG + wave * 36864, wlb = W_WOT + Pg * WOG + 4 * 36864 + (wave >> 1) * 9216;
        u32 wso = wb;
        auto wsite = LAM(N_) {
            constexpr int n = N_.v, k = (n < 36 ? n : n - 36) % 4;
            if constexpr (L == 0 && n < 45) {
                if constexpr (n == 36) wso = wlb; else if constexpr (n && k == 0) wso += 4096;
                if constexpr (k == 0) OPQS(wso);
                if constexpr (n < 28) ld4a<k * 1024, 144 + 4 * n>(l16, vw, wso);
                else ld4o<k * 1024>(wv[n - 28], l16, vw, wso);
            }
        };
        f16v aK; f4 qKV;
        float mean, al, rstd;
        v4u rV[4], cK[5];
        {
            const u32 xb = W_XF1 + (rg * 4 + wave) * 16 * XS;
            u32 xs[8];
            float ts;
            auto xsite = LAM(Q_) { if constexpr (L == 1) xop(IC<Q_.v / 4 * 5 + Q_.v % 4 + (Q_.v % 4 > 1)>{}, xs[Q_.v / 4]); };
            auto stat = [&]() __attribute__((always_inline)) {
                auto r = __builtin_amdgcn_permlane32_swap(asu(ts), asu(ts), false, false);
                mean = asf(r[0]) * (1.f / 576.f);
                const float var = __builtin_fmaxf(asf(r[1]) * (1.f / 576.f) - mean * mean, 0.f);
                rstd = __builtin_amdgcn_rsqf(var + 1e-5f);
                al = rstd * 1.4426950408889634f;
                pin2(mean, al); pin(rstd);
            };
            if constexpr (L == 1) {
#pragma unroll
                for (int j = 0; j < 8; ++j) { xs[j] = xb + (((p & 14) + 2 * j) & 15) * XS; OPQS(xs[j]); }
                FENCE();
                if constexpr (XSL) __builtin_amdgcn_s_sleep(XSL);
                sfor<0, 8>(LAM(J_) { xop(IC<5 * J_.v + 2>{}, xs[J_.v]); });
                sfor<0, UPA_>(xsite);
                waitv<UPA_>();
                u32 m, m2;
                asm volatile("v_max3_u32 %0, v224, v226, v228\n\tv_max3_u32 %1, v230, v232, v234\n\tv_max3_u32 %0, %0, v236, v238\n\tv_max3_u32 %1, %1, v240, v242\n\t"
                             "v_max3_u32 %0, %0, v244, v246\n\tv_max3_u32 %1, %1, v248, v250\n\tv_max3_u32 %0, %0, v252, v254\n\tv_max_u32 %0, %0, %1" : "=&v"(m), "=&v"(m2));
                if (__builtin_expect(bad(m), 0)) {
#pragma unroll 1
                    for (int sp = 0; sp < SPIN; ++sp) {
                        u32 mk = 0;
                        sfor<0, 8>(LAM(J_) { mk |= (u32)bad(mx3vv<224 + 4 * J_.v>(0u)) << J_.v; });
                        if (!mk) break;
                        __builtin_amdgcn_s_sleep(RSX);
                        sfor<0, 8>(LAM(J_) { if (mk >> J_.v & 1) xop(IC<5 * J_.v + 2>{}, xs[J_.v]); });
                        waitv<0>();
                        ++nretry;
                    }
                }
                STAMP(25);
                STAMPV(63, nretry);
                nretry = 0;
                sfor<UPA_, xissued(0)>(xsite);
                sfor<0, 8>(LAM(J_) { swapw<224 + 4 * J_.v, 128 + 2 * J_.v>(); if constexpr (J_.v) { addv<225, 225 + 4 * J_.v>(); addv<227, 227 + 4 * J_.v>(); } });
                ts = swapsum<224>();
                stat();
            }
            v4u r0[3], r1[3], ones = {~0u, ~0u, ~0u, ~0u};
            f2 cq[4];
            u32 cm, cs_;
#pragma unroll
            for (int s = 0; s < 3; ++s) { r0[s] = lds4(l16 + s * 1024); r1[s] = lds4(aKV + s * QTS); }
            if constexpr (L == 0) pin(ones);
            FENCE();
            sfor<0, 36>(LAM(SI) {
                constexpr int n = SI.v, s = kstep(n), s3 = kstep(n + 3), i = s >> 1;
                if constexpr (L == 0 && s < 32 && !(s & 1)) {
                    constexpr int j = i >> 1, later = 39 - 5 * j - (i & 1 ? 4 : 1) + 9 + (n >> 1);
                    waitv<later>();
                    FENCE();
                }
                if constexpr (L == 1 && s < 32 && !(s & 3)) {
                    constexpr int j = i >> 1;
                    static_assert(xissued(n) >= 4 * j + 4, "");
                    waitv<xissued(n) - 4 * j - 4>();
                    auto late = LAM(J_) { constexpr int k = J_.v; return bad(umax(mx3(rda<16 * k>(), rda<16 * k + 4>(), rda<16 * k + 8>()), rda<16 * k + 12>())); };
                    if (__builtin_expect(late(IC<j>{}), 0)) {
#pragma unroll 1
                        for (int sp = 0; sp < SPIN; ++sp) {
                            sfor<4 * j, 4 * j + 4>(xsite);
                            waitv<0>();
                            ++nretry;
                            if (!late(IC<j>{})) break;
                        }
                    }
                    FENCE();
                }
                if constexpr (L == 0 && n == 32) asm volatile("s_waitcnt vmcnt(16)\n\ts_barrier" ::: "memory");
                waitl<(n < 34 ? 4 : 6)>();
                mm32b<n == 0, 4 * s>(aK, r0[n % 3]);
                if constexpr (n + 3 < 36) r0[n % 3] = lds4(l16 + s3 * 1024); else rV[n - 33] = lds4(aV_ + (n - 33) * 1024);
                sfor<xissued(n), xissued(n + 1)>(xsite);
                if constexpr (L == 0 && s < 32 && (s & 3) == 2) swapw<224 + 2 * (i - 1), 127 + i>();
                if constexpr (L == 0 && s > 3 && s < 31 && ((s & 3) == 3 || s == 30)) { addv<225, 225 + 4 * (i >> 1)>(); addv<227, 227 + 4 * (i >> 1)>(); }
                if constexpr (L == 0 && n == 34) ts = swapsum<224>();
                if constexpr (n == 1) { aQq = L_QT + vsel(qa && rq < 2, 256 + (gq >> 1) * 64 + (rq & 1) * 32 + (kg >> 1) * 16, 384); pin(aQq); }
                if constexpr (n == 3) { aCS = L_CS + h * 224; aS = L_STG + (wave * 32 + tl) * STGS + 72 * h; pin2(aCS, aS); }
                if constexpr (L == 0) {
                    constexpr int k = n >> 1;
                    if constexpr (n & 1) {
                        if constexpr (k == 0 || k == 3) { cs_ = W_O + ((k ? 1024 : 0) + (rg * 4 + wave) * 16 + p) * OS; OPQS(cs_); }
                        if constexpr (k == 6) { cs_ = W_XF1 + ((rg * 4 + tw) * 16 + pw) * XS; OPQS(cs_); }
                        if constexpr (k == 9) { cs_ = W_KV + (p * 16 + rg) * 2816 + wave * 704; OPQS(cs_); }
                        if constexpr (k == 10) { cs_ += 256 * 2816; OPQS(cs_); }
                        if constexpr (k == 11) { cs_ = W_ST + ((rg * 4 + tw) * 16 + pw) * 256; OPQS(cs_); }
                        if constexpr (k < 9 && k % 3 < 2) st4<(k % 3) * 1024>(ones, l16, vw, cs_);
                        else if constexpr (k < 9) st4<2048>(ones, cm, vw, cs_);
                        else if constexpr (k < 12) st4<0>(ones, cm, vw, cs_);
                        else wsite(IC<k - 12>{});
                    } else {
                        if constexpr (k == 2) { cm = vsel(ln < 16, l16, OOB); pin(cm); }
                        if constexpr (k == 8) { cm = vsel(ln < 32, l16, OOB); pin(cm); }
                        if constexpr (k == 9) { cm = vsel(ln < 44, l16, OOB); pin(cm); }
                        if constexpr (k == 11) { cm = vsel(ln < 32, row * 16 + (kg & 1) * (1024 * 256), OOB); pin(cm); }
                        if constexpr (k == 13) { cq[0].x = (cq[0].x + cq[1].x) + (cq[2].x + cq[3].x); cq[0].y = (cq[0].y + cq[1].y) + (cq[2].y + cq[3].y); pin(cq[0]); }
                    }
                }
                if constexpr (L == 0 && n == 35) stat();
                FENCE();
                mm16b<n == 0, 4 * s>(qKV, r1[n % 3]);
                if constexpr (n + 3 < 36) r1[n % 3] = lds4(aKV + s3 * QTS); else cK[n - 33] = lds4(aCS + (n - 33) * 16);
                if constexpr (n == 33) cK[3] = lds4(aCS + 48);
                if constexpr (n == 34) cK[4] = lds4(aCS + 192);
                if constexpr (n == 35) rV[3] = lds4(aV_ + 3 * 1024);
                FENCE();
                if constexpr (L == 0 && n >= 20 && n < 24) { cq[n - 20] = *(const LAS f2*)(l8 + (L_EX + (n - 20) * 512)); FENCE(); }
                if constexpr (L == 0 && n == 27) { *(LAS f2*)(csd) = cq[0]; FENCE(); }
            });
        }
        STAMP(4 + 12 * L);
        if constexpr (L == 1) STAMPV(56, nretry);
        v4u rQ[4], rq4[3];
        u32 pa, pb, kso;
        {
            f16v aV;
            float t[18], e1[18], e2[18], mx, mn, c1, c2, s1, s2;
            v4u cV[4];
            u32 qo = L_Q + wave * 9216, qg = W_IMG + p * IMG + qo;
            sfor<0, 36>(LAM(SI) {
                constexpr int s = SI.v;
                if constexpr (s >= 2 && s < 34 && !(s & 1)) waitl<2>();
                mm32b<s == 0, 4 * s>(aV, rV[s & 3]);
                if constexpr (s == 2) { pin(aK); pin(qKV); }
                if constexpr (s + 4 < 36) rV[s & 3] = lds4(aV_ + (s + 4) * 1024); else cV[s - 32] = lds4(aCS + 64 + (s - 32) * 16);
                if constexpr (s < 18) {
                    if constexpr (s == 0 || s == 8 || s == 16) { if constexpr (s) { qo += 4096; qg += 4096; } OPQS(qo); OPQS(qg); setm0(qo); }
                    if constexpr (s & 1) dmai<((s >> 1) & 3) * 1024>(vw, l16, qg);
                } else wsite(IC<s - 12>{});
                if constexpr (s == 1) {
                    const u32 r0 = L_STG + (4 * (4 * (kg & 1) + gq) + (kg >> 1)) * STGS + 8 * rq;
                    pa = r0 + 72 * (wave >> 1); pb = r0 + (144 + 40 * (wave >> 1) + 32 * (wave & 1));
                    kso = W_KV + ((L * 16 + p) * 16 + rg) * 2816 + (wave >> 1) * 1376;
                    pin2(pa, pb); OPQS(kso);
                }
                if constexpr (s >= 2 && s < 5) {
#pragma unroll
                    for (int i = 6 * (s - 2); i < 6 * (s - 2) + 6; ++i) { const u32 cv = cK[i >> 2][i & 3]; t[i] = __builtin_fmaf(-mean, asf(cv), i < 16 ? aK[i] : qKV[i - 16]); }
                }
                if constexpr (s == 5) { mx = __builtin_fmaxf(__builtin_fmaxf(t[0], t[1]), t[2]); mn = __builtin_fminf(__builtin_fminf(t[0], t[1]), t[2]);
#pragma unroll
                    for (int i = 3; i < 7; i += 2) { mx = __builtin_fmaxf(__builtin_fmaxf(mx, t[i]), t[i + 1]); mn = __builtin_fminf(__builtin_fminf(mn, t[i]), t[i + 1]); } }
                if constexpr (s == 6) {
#pragma unroll
                    for (int i = 7; i < 13; i += 2) { mx = __builtin_fmaxf(__builtin_fmaxf(mx, t[i]), t[i + 1]); mn = __builtin_fminf(__builtin_fminf(mn, t[i]), t[i + 1]); } }
                if constexpr (s == 7) {
#pragma unroll
                    for (int i = 13; i < 17; i += 2) { mx = __builtin_fmaxf(__builtin_fmaxf(mx, t[i]), t[i + 1]); mn = __builtin_fminf(__builtin_fminf(mn, t[i]), t[i + 1]); }
                    mx = __builtin_fmaxf(mx, t[17]); mn = __builtin_fminf(mn, t[17]); c1 = -al * mx; c2 = al * mn; }
                if constexpr (s >= 8 && s < 32) {
#pragma unroll
                    for (int q = (3 * (s - 8)) / 2; q < (3 * (s - 7)) / 2; ++q) {
                        const int i = q >> 1;
                        if (q & 1) { e2[i] = __builtin_amdgcn_exp2f(__builtin_fmaf(t[i], -al, c2)); s2 = i ? s2 + e2[i] : e2[i]; }
                        else { e1[i] = __builtin_amdgcn_exp2f(__builtin_fmaf(t[i], al, c1)); s1 = i ? s1 + e1[i] : e1[i]; }
                    }
                }
                if constexpr (s == 32) { s1 = __builtin_amdgcn_rcpf(s1); s2 = __builtin_amdgcn_rcpf(s2); }
                if constexpr (s >= 33) {
                    constexpr int i = s - 33;
                    *(LAS v2u*)(aS + 8 * i) = (v2u){pk(e1[4 * i] * s1, e1[4 * i + 1] * s1), pk(e1[4 * i + 2] * s1, e1[4 * i + 3] * s1)};
                }
                FENCE();
            });
            *(LAS v2u*)(aS + 24) = (v2u){pk(e1[12] * s1, e1[13] * s1), pk(e1[14] * s1, e1[15] * s1)};
            *(LAS v2u*)(aS + 32) = (v2u){pk(e1[16] * s1, e1[17] * s1), pk(e2[0] * s2, e2[1] * s2)};
#pragma unroll
            for (int i = 0; i < 4; ++i) *(LAS v2u*)(aS + 40 + 8 * i) = (v2u){pk(e2[4 * i + 2] * s2, e2[4 * i + 3] * s2), pk(e2[4 * i + 4] * s2, e2[4 * i + 5] * s2)};
            FENCE();
            pin(aV);
            const u32 aSv = aS + 144 - 32 * h;
            const float mr = -mean * rstd;
#pragma unroll
            for (int i = 0; i < 4; ++i) {
                const u32 q0 = cV[i].x, q1 = cV[i].y, q2 = cV[i].z, q3 = cV[i].w;
                *(LAS v2u*)(aSv + 8 * i) = (v2u){pk(__builtin_fmaf(mr, asf(q0), aV[4 * i] * rstd), __builtin_fmaf(mr, asf(q1), aV[4 * i + 1] * rstd)),
                                                 pk(__builtin_fmaf(mr, asf(q2), aV[4 * i + 2] * rstd), __builtin_fmaf(mr, asf(q3), aV[4 * i + 3] * rstd))};
            }
            const u32 q2 = cK[4].z, q3 = cK[4].w;
            *(LAS v2u*)(aSv + 32) = (v2u){pk(__builtin_fmaf(mr, asf(q2), qKV[2] * rstd), __builtin_fmaf(mr, asf(q3), qKV[3] * rstd)), 0x3f80u};
        }
        waitbar<(L ? 0 : 18)>();
        STAMP(5 + 12 * L);
        {
            f4 c[3];
            v4u B[4], A[4][3];
            auto rd = LAM(N_) {
                constexpr int ks = N_.v >> 2, f = N_.v & 3;
                if constexpr (f == 0) B[ks] = tr2(pb + ks * 32 * STGS, pb + (ks * 32 + 2) * STGS);
                else A[ks][f - 1] = tr2(pa + ks * 32 * STGS + 32 * (f - 1), pa + (ks * 32 + 2) * STGS + 32 * (f - 1));
            };
            sfor<0, 7>(LAM(N_) { rd(N_); FENCE(); });
            if constexpr (L == 0) { if (tid == 0) bst2((v2u){(u32)ep, (u32)(ep >> 32)}, vw, 0, W_F2 + (rg * 16 + p) * 128, SC1); }
            FENCE();
            sfor<0, 12>(LAM(I_) {
                constexpr int ks = I_.v / 3, ft = I_.v % 3;
                mm16<ks == 0>(c[ft], A[ks][ft], B[ks]);
                if constexpr (I_.v < 9) rd(IC<7 + I_.v>{});
                else { rQ[I_.v - 9] = lds4(aQ_ + (I_.v - 9) * 1024); rq4[I_.v - 9] = lds4(aQq + (I_.v - 9) * QTS); }
                FENCE();
            });
            rQ[3] = lds4(aQ_ + 3 * 1024);
            FENCE();
            pad3<5>(c[0], c[1], c[2]);
            bst4((v4u){pk(c[0][0], c[0][1]), pk(c[0][2], c[0][3]), pk(c[1][0], c[1][1]), pk(c[1][2], c[1][3])}, vw, kvA, kso, SC1);
            bst2((v2u){pk(c[2][0], c[2][1]), pk(c[2][2], c[2][3])}, vw, kvB, kso, SC1);
        }
        FENCE();
        STAMP(6 + 12 * L);
        v4u pq[5], kp[12], sel;
        u32 kvo, kv3, aF, aQ4, f2o, oso;
        u32 kb[4];
        v2u yfl;
        auto flags_ok = [&](u64 m) __attribute__((always_inline)) { return (__builtin_amdgcn_ballot_w64(yfl.x != (u32)ep || yfl.y != (u32)(ep >> 32)) & m) == 0; };
        auto flags_wait = [&](u64 m) __attribute__((always_inline)) {
            if (__builtin_expect(!flags_ok(m), 0)) {
#pragma unroll 1
                for (int sp = 0; sp < SPIN; ++sp) {
                    __builtin_amdgcn_s_sleep(1);
                    ld2<0>(yfl, f2o, vw, W_F2);
                    waitp<0>(yfl);
                    if (flags_ok(m)) break;
                }
            }
        };
        {
            f16v aQ; f4 qQ;
            v4u cQ[5];
            sfor<0, 36>(LAM(SI) {
                constexpr int s = SI.v;
                if constexpr (s >= 1 && s < 34) waitl<4>();
                mm32b<s == 0, 4 * s>(aQ, rQ[s & 3]);
                if constexpr (s + 4 < 36) rQ[s & 3] = lds4(aQ_ + (s + 4) * 1024); else cQ[s - 32] = lds4(aCS + 128 + (s - 32) * 16);
                if constexpr (s < 31 && s % 3 != 2) wsite(IC<24 + s - s / 3>{});
                if constexpr (s == 2) { kvo = kg * 2816 + wave * 256 + row * 16; kv3 = vsel(wave * 16 + row < 44, kvo + 2048, OOB); pin2(kvo, kv3); }
                if constexpr (s == 26) { for (int i = 0; i < 4; ++i) { kb[i] = W_KV + ((L * 16 + p) * 16 + 4 * i) * 2816; OPQS(kb[i]); } }
                if constexpr (s == 5 || s == 8) {
#pragma unroll
                    for (int i = (s - 5) / 3 * 2; i < (s - 5) / 3 * 2 + 2; ++i) sel[i] = row == 2 * i ? 0x3f80u : row == 2 * i + 1 ? 0x3f800000u : 0u;
                    if constexpr (s == 8) pin(sel);
                }
                if constexpr (s == 11) { const int hr = (tl >> 2) & 1, d = (tl & 3) + 4 * (tl >> 3); aF = vsel(h == hr, (hr * 19 + d) * 80 + L_KVA, L_Z); pin(aF); }
                if constexpr (s == 14) { aQ4 = vsel(kg == gq && rq < 3, ((gq >> 1) * 19 + 16 + rq) * 80 + L_KVA, L_Z); pin(aQ4); }
                if constexpr (WARM && s == WARM) {
                    const u32 sO = W_O + (L * 1024 + (rg * 4 + wave) * 16 + p) * OS, sX = W_XF1 + ((rg * 4 + tw) * 16 + pw) * XS - 18 * 128, sS = W_ST + (L * 1024 + (rg * 4 + tw) * 16 + pw) * 256 - 38 * 128;
                    const u32 wo = vsel(ln < 18, sO, vsel(ln < 38, L ? OOB - 8192 : sX, vsel(ln < 40, sS, OOB - 8192))) + 8 * l16;
                    asm volatile("s_mov_b32 m0, %2\n\ts_nop 0\n\tbuffer_load_dword %0, %1, 0 offen sc1 lds" ::"v"(wo), "s"(vw), "s"(L_EX) : "m0", "memory");
                }
                if constexpr (s == 17) { oso = W_O + (L * 1024 + (rg * 4 + wave) * 16 + p) * OS; OPQS(oso); }
                if constexpr (L == 0 && s == 20) { f2o = vsel(kg == 1, rg * 16 + row, row * 16 + (kg ? pw : p)) * 128; pin(f2o); }
                if constexpr (L == 0 && s == 23) ld2<0>(yfl, f2o, vw, W_F2);
                FENCE();
                mm16b<s == 0, 4 * s>(qQ, rq4[s % 3]);
                if constexpr (s + 3 < 36) rq4[s % 3] = lds4(aQq + (s + 3) * QTS);
                if constexpr (s == 33) cQ[4] = lds4(aCS + 208);
                FENCE();
            });
            STAMP(49 + L);
            if constexpr (L == 0) { waitp<5>(yfl); flags_wait(0xffffull); }
#pragma unroll
            for (int i = 0; i < 4; ++i) {
                kp[i] = bld4(vw, kvo, kb[i], SC1);
                kp[4 + i] = bld4(vw, kvo + 1024, kb[i], SC1);
                kp[8 + i] = bld4(vw, kv3, kb[i], SC1);
            }
            FENCE();
            pin(aQ); pin(qQ);
            float t[18]; u32 ph[18];
#pragma unroll
            for (int i = 0; i < 18; ++i) { const u32 cv = cQ[i >> 2][i & 3]; t[i] = __builtin_fmaf(-mean, asf(cv), i < 16 ? aQ[i] : qQ[i - 16]); }
            hedgehog(t, al, ph);
#pragma unroll
            for (int i = 0; i < 5; ++i) { pq[i] = (v4u){ph[4 * i], ph[4 * i + 1], i < 4 ? ph[4 * i + 2] : 0u, i < 4 ? ph[4 * i + 3] : 0u}; pin(pq[i]); }
            FENCE();
            STAMP(7 + 12 * L);
            u32 m = umax(kp[0].x, kp[0].z);
#pragma unroll
            for (int i = 1; i < 12; ++i) m = mx3(m, kp[i].x, kp[i].z);
            nretry = 0;
            if (__builtin_expect(bad(m), 0)) {
#pragma unroll 1
                for (int sp = 0; sp < SPIN; ++sp) {
                    u32 mk = 0;
                    waitv<0>();
#pragma unroll
                    for (int i = 0; i < 4; ++i) {
                        pin2(kp[i], kp[4 + i]); pin(kp[8 + i]);
                        mk |= (u32)bad(mx3(mx3(kp[i].x, kp[i].z, kp[4 + i].x), mx3(kp[4 + i].z, kp[8 + i].x, kp[8 + i].z), 0u)) << i;
                    }
                    if (!mk) break;
                    __builtin_amdgcn_s_sleep(RSK);
#pragma unroll
                    for (int i = 0; i < 4; ++i)
                        if (mk >> i & 1) { ld4<0>(kp[i], kvo, vw, kb[i]); ld4<1024>(kp[4 + i], kvo, vw, kb[i]); ld4<0>(kp[8 + i], kv3, vw, kb[i]); }
                    ++nretry;
                }
            }
            STAMPV(57 + L, nretry);
            f4 a[3];
            sfor<0, 12>(LAM(I_) { constexpr int i = I_.v / 3, j = I_.v % 3; mm16<i == 0>(a[j], sel, kp[4 * j + i]); });
            pad3<7>(a[0], a[1], a[2]);
#pragma unroll
            for (int j = 0; j < 3; ++j) *(LAS v2u*)(kvw[j]) = (v2u){pk(a[j][0], a[j][1]), pk(a[j][2], a[j][3])};
            barrier();
            STAMP(8 + 12 * L);
        }
        {
            v4u A[5], Aq[5];
#pragma unroll
            for (int c = 0; c < 5; ++c) Aq[c] = lds4(aQ4 + 16 * c);
#pragma unroll
            for (int c = 0; c < 5; ++c) A[c] = lds4(aF + 16 * c);
            FENCE();
            f16v o; f4 oq;
            float rd; u32 ol;
            sfor<0, 5>(LAM(C_) { mm16<C_.v == 0>(oq, Aq[C_.v], pq[C_.v]); });
            sfor<0, 5>(LAM(C_) {
                mm32<C_.v == 0>(o, A[C_.v], pq[C_.v]);
                if constexpr (C_.v == 3) { pad<4>(oq); rd = __builtin_amdgcn_rcpf(oq[2] + 1e-6f); pin(rd); }
                if constexpr (C_.v == 4) { ol = pk(oq[0] * rd, oq[1] * rd); pin(ol); }
                FENCE();
            });
            pad<8>(o);
            bst4((v4u){pk(o[0] * rd, o[1] * rd), pk(o[2] * rd, o[3] * rd), pk(o[4] * rd, o[5] * rd), pk(o[6] * rd, o[7] * rd)}, vw, l16, oso, SC1);
            bst4((v4u){pk(o[8] * rd, o[9] * rd), pk(o[10] * rd, o[11] * rd), pk(o[12] * rd, o[13] * rd), pk(o[14] * rd, o[15] * rd)}, vw, l16 + 1024, oso, SC1);
            bst1(ol, vw, l4 + 2048, oso, SC1);
        }
        FENCE();
        STAMP(9 + 12 * L);
        u32 aB, ae, ao, yo, ys;
        f16v aW; float xl0, xl1;
        if constexpr (L == 0) {
            flags_wait(~0ull);
            ys = W_Y + rg * YB + 36 * pw * 256 + tw * 64; yo = (ln >> 2) * 256 + rq * 16;
            OPQS(ys); OPQV(yo);
        }
#pragma unroll
        for (int i = 0; i < 8; ++i) { aW[2 * i] = bf_lo(xr[i]); aW[2 * i + 1] = bf_hi(xr[i]); }
        xl0 = bf_lo(xr[8]); xl1 = bf_hi(xr[8]);
        aB = L_Q + ((wave >> 1) * 18 + (kg & 1)) * 1024 + ((kg >> 1) * 32 + 16 * (wave & 1) + row) * 16;
        ae = L_EX + (kg & 1) * 1024 + ((2 * (wave >> 1) + h) * 16 + row) * 16 + 8 * (wave & 1);
        ao = aQ_ + wave * 8192;
        pin(aW); pin2(xl0, xl1); pin2(aB, ae); pin(ao);
        {
            u32 so[4];
#pragma unroll
            for (int i = 0; i < 4; ++i) { so[i] = W_O + (L * 1024 + (rg * 4 + tw) * 16 + 4 * wave + i) * OS; OPQS(so[i]); }
            v4u og[8]; u32 lo[4];
            FENCE();
            STAMP(43 + 3 * L);
            if constexpr (OWT) waitv<0>();
            STAMP(44 + 3 * L);
            if constexpr (OSL) __builtin_amdgcn_s_sleep(OSL);
            auto ldo = [&]() __attribute__((always_inline)) {
#pragma unroll
                for (int i = 0; i < 8; ++i) og[i] = bld4(vw, l16 + (i & 1) * 1024, so[i >> 1], SC1);
            };
            auto ldl = [&]() __attribute__((always_inline)) {
#pragma unroll
                for (int i = 0; i < 4; ++i) lo[i] = bld1(vw, l4 + 2048, so[i], SC1);
            };
            auto chk = [&]() __attribute__((always_inline)) {
                u32 m = mx3(umax(lo[0], lo[1]), lo[2], lo[3]);
#pragma unroll
                for (int i = 0; i < 8; i += 2) m = mx3(m, og[i].x, og[i + 1].x);
                return bad(m);
            };
            ldo(); ldl();
            FENCE();
            nretry = 0;
            if (__builtin_expect(chk(), 0)) {
#pragma unroll 1
                for (int sp = 0; sp < SPIN; ++sp) {
                    u32 mk = 0;
                    waitv<0>();
#pragma unroll
                    for (int i = 0; i < 4; ++i) {
                        pin2(og[2 * i], og[2 * i + 1]); pin(lo[i]);
                        mk |= (u32)bad(mx3(og[2 * i].x, og[2 * i + 1].x, lo[i])) << i;
                    }
                    if (!mk) break;
                    __builtin_amdgcn_s_sleep(RSO);
#pragma unroll
                    for (int i = 0; i < 4; ++i)
                        if (mk >> i & 1) { ld4<0>(og[2 * i], l16, vw, so[i]); ld4<1024>(og[2 * i + 1], l16, vw, so[i]); ld1<2048>(lo[i], l4, vw, so[i]); }
                    ++nretry;
                }
            }
            STAMPV(59 + L, nretry);
            STAMP(45 + 3 * L);
#pragma unroll
            for (int i = 0; i < 8; ++i) *(LAS v4u*)(ao + i * 1024) = og[i];
            *(LAS v4u*)(aQ_ + (32 + wave) * 1024) = (v4u){lo[0], lo[1], lo[2], lo[3]};
            barrier();
        }
        STAMP(10 + 12 * L);
        u32 sto = W_ST + (L * 1024 + (rg * 4 + tw) * 16) * 256;
        {
            f4 qW;
            v4u rb_[4], rl[9];
            v2u e0, e1;
            float s1, s2;
            u32 le;
#pragma unroll
            for (int i = 0; i < 4; ++i) { rl[i] = lds4(aB + i * 2048); rb_[i] = lds4(aQ_ + i * 1024); }
            FENCE();
            sfor<0, 36>(LAM(SI) {
                constexpr int s = SI.v;
                if constexpr (s < 28) mm32a<false, 144 + 4 * s>(aW, rb_[s & 3]); else mm32<false>(aW, wv[s - 28], rb_[s & 3]);
                if constexpr (s + 4 < 36) rb_[s & 3] = lds4(aQ_ + (s + 4) * 1024);
                if constexpr (s < 5) rl[4 + s] = lds4(aB + (4 + s) * 2048);
                if constexpr (s == 12) { pad<0>(qW); *(LAS f4*)(l16 + (L_EX + wave * 1024)) = qW; }
                if constexpr (s == 14) OPQS(sto);
                if constexpr (s == 24) { le = l4; pin(le); }
                if constexpr (L == 0 && s >= 16 && s < 22 && !(s & 1)) dma_ws(vw, yo, ys + (s - 16) * 2048, L_STG + wave * 3072 + (s - 16) * 512);
                FENCE();
                if constexpr (s < 9) { mm16<s == 0>(qW, wv[8 + s], rl[s]); FENCE(); }
                if constexpr (s == 30) { barrier(); e0 = lds2(ae); e1 = lds2(ae + 2048); FENCE(); }
                if constexpr (s == 33) { xl0 += asf(e0.x) + asf(e1.x); xl1 += asf(e0.y) + asf(e1.y); s1 = xl0 + xl1; s2 = __builtin_fmaf(xl0, xl0, xl1 * xl1); pin2(s1, s2); FENCE(); }
            });
            STAMP(11 + 12 * L);
            pad<11>(aW);
            float s3 = aW[0], s4 = aW[0] * aW[0];
#pragma unroll
            for (int i = 1; i < 16; ++i) { if (i & 1) { s1 += aW[i]; s2 = __builtin_fmaf(aW[i], aW[i], s2); } else { s3 += aW[i]; s4 = __builtin_fmaf(aW[i], aW[i], s4); } }
            bst1(asu(halves_sum(s1 + s3, s2 + s4)), vw, le, sto + pw * 256, SC1);
        }
        FENCE();
        {
            float t;
            u32 xso = L ? (rg * 128 + tw * 32) * 1152 + 72 * pw : W_XF1 + ((rg * 4 + tw) * 16 + pw) * XS, oo = tl * 1152 + 36 * h, one2 = 0x3f803f80u;
            OPQS(xso); OPQS(one2);
            if constexpr (L == 1) OPQV(oo);
            u32 yf[18], lS;
            if constexpr (L == 0) {
                const u32 ya = (18 * h + gq) * 64 + 32 * (kg & 1) + 8 * rq + (L_STG + wave * 3072);
                lS = tl * 16 + h * 8;
                pin(lS);
                waitv<1>();
#pragma unroll
                for (int i = 0; i < 5; ++i) {
                    const v2u v = __builtin_bit_cast(v2u, __builtin_amdgcn_ds_read_tr16_b64_v4i16((LAS s4*)(ya + 256 * i)));
                    yv[2 * i] = v.x; if (i < 4) yv[2 * i + 1] = v.y;
                }
            }
#pragma unroll
            for (int i = 0; i < 9; ++i) { yf[2 * i] = yv[i] << 16; yf[2 * i + 1] = yv[i] & 0xffff0000u; pin2(yf[2 * i], yf[2 * i + 1]); }
            FENCE();
            waitv<0>();
            if constexpr (SSL) __builtin_amdgcn_s_sleep(SSL);
            nretry = 0;
#pragma unroll 1
            for (int sp = 0; sp < SPIN; ++sp) {
                asm volatile("" : "+v"(l4)::"memory");
                u32 e[16];
#pragma unroll
                for (int i = 0; i < 16; ++i) e[i] = bld1(vw, l4 + i * 256, sto, SC1);
                FENCE();
                u32 m = umax(e[0], e[1]);
                t = asf(e[0]) + asf(e[1]);
#pragma unroll
                for (int i = 2; i < 16; i += 2) { m = mx3(m, e[i], e[i + 1]); t += asf(e[i]) + asf(e[i + 1]); pin2(m, t); }
                if (!bad(m)) break;
                ++nretry;
                __builtin_amdgcn_s_sleep(RSS);
            }
            STAMP(12 + 12 * L);
            STAMPV(61 + L, nretry);
            auto r = __builtin_amdgcn_permlane32_swap(asu(t), asu(t), false, false);
            const float m1 = asf(r[0]) * (1.f / 576.f);
            const float r1 = __builtin_amdgcn_rsqf(__builtin_fmaxf(asf(r[1]) * (1.f / 576.f) - m1 * m1, 0.f) + 1e-5f), mr = -m1 * r1;
#pragma unroll
            for (int i = 0; i < 9; ++i) {
                const float a = i < 8 ? aW[2 * i] : xl0, c = i < 8 ? aW[2 * i + 1] : xl1;
                xr[i] = pk(__builtin_fmaf(__builtin_fmaf(a, r1, mr), asf(yf[2 * i]), a), __builtin_fmaf(__builtin_fmaf(c, r1, mr), asf(yf[2 * i + 1]), c));
                if constexpr (L == 0) {
                    if (i == 3) { bst4((v4u){xr[0], xr[1], xr[2], xr[3]}, vw, l16, xso, SC1); FENCE(); }
                    if (i == 7) { bst4((v4u){xr[4], xr[5], xr[6], xr[7]}, vw, l16 + 1024, xso, SC1); FENCE(); }
                } else {
                    if (i == 3) { bst4((v4u){xr[0], xr[1], xr[2], xr[3]}, ro, oo, xso, 0); FENCE(); }
                    if (i == 7) { bst4((v4u){xr[4], xr[5], xr[6], xr[7]}, ro, oo + 16, xso, 0); FENCE(); }
                    if (i == 8) bst1(xr[8], ro, oo + 32, xso, 0);
                }
            }
            if constexpr (L == 0) {
                float s1 = 0.f, s2 = 0.f;
#pragma unroll
                for (int j = 0; j < 9; ++j) { s1 = dot2(xr[j], one2, s1); s2 = dot2(xr[j], xr[j], s2); }
                bst2((v2u){xr[8], asu(halves_sum(s1, s2))}, vw, lS + 2048, xso, SC1);
                STAMP(13);
            } else {
                if (b == 0 && tid == 0) bst2((v2u){(u32)ep, (u32)(ep >> 32)}, vw, 0, W_E, SC1);
            }
        }
    });
    STAMP_END();
}
#if defined(__HIP_DEVICE_COMPILE__)
#pragma clang attribute pop
#endif

extern "C" void kernel_launch(void* const* d_in, const int* in_sizes, int n_in, void* d_out, int out_size, void* d_ws, size_t ws_size, hipStream_t stream) {
    (void)in_sizes; (void)n_in; (void)out_size; (void)ws_size;
    hipFuncSetAttribute((const void*)k_main, hipFuncAttributeMaxDynamicSharedMemorySize, L_END);
    const u32 f = 0x00020000u, n = 2048 * 1152;
    Args a = {{d_in[5], n, f}, {d_in[6], n, f}, {{d_in[1], 576 * 3456, f}, {d_in[3], 576 * 1152, f}}, {{d_in[2], 32 * 648, f}, {d_in[2], 0, f}}, {d_in[0], n, f}, {d_ws, W_END, f}, {d_out, n, f}, d_in[7], d_in[8], d_in[9]};
    k_main<<<256, 256, L_END, stream>>>(a);
}
```

```cpp
#include <hip/hip_runtime.h>
#include <stdint.h>
#pragma clang diagnostic ignored "-Wint-to-pointer-cast"
#pragma clang diagnostic ignored "-Wunused-value"
#pragma clang diagnostic ignored "-Winline-asm"
#if defined(__HIP_DEVICE_COMPILE__)
#pragma clang attribute push(__attribute__((target("no-packed-fp32-ops"))), apply_to = function)
#endif
typedef unsigned short u16;
typedef unsigned u32;
typedef unsigned long long u64;
typedef u32 v4u __attribute__((ext_vector_type(4)));
typedef u32 v2u __attribute__((ext_vector_type(2)));
typedef float f4 __attribute__((ext_vector_type(4)));
typedef float f2 __attribute__((ext_vector_type(2)));
typedef float f16v __attribute__((ext_vector_type(16)));
typedef __bf16 b8 __attribute__((ext_vector_type(8)));
typedef __bf16 b2 __attribute__((ext_vector_type(2)));
typedef short s4 __attribute__((ext_vector_type(4)));
#define LAS __attribute__((address_space(3)))
#define DEV __device__ __forceinline__
#define SC1 16

constexpr u32 QTS = 416, STGS = 232, CVS = 21216, HCS = 4160;
constexpr u32 L_K = 0, L_V = 36864, L_QT = 73728, L_Q = 89088, L_STG = 125952, L_KVA = 155648, L_CS = 158784, L_EX = 159232, L_END = 163328;
constexpr u32 P_KS = 0, P_VS = 10240, P_WS = 20480, P_XS = 31232, P_PH = 42496, P_LS = 55296, L_Z = L_KVA + 38 * 80;
constexpr u32 IMG = L_STG, XS = 2560, OS = 2304, YB = 576 * 256, CSB = 16 * 576;
constexpr u32 WOG = 4 * 36864 + 18432;
constexpr u32 W_IMG = 0, W_WOT = W_IMG + 16 * IMG, W_CSP = W_WOT + 4 * WOG, W_UT = W_CSP + 16 * CSB,
              W_XF0 = W_UT + 576 * 4096, W_XF1 = W_XF0 + 1024 * XS, W_O = W_XF1 + 1024 * XS, W_Y = W_O + 2 * 1024 * OS, W_KV = W_Y + 576 * 4096,
              W_ST = W_KV + 2 * 256 * 2816, W_F = W_ST + 2 * 1024 * 256, W_F2 = W_F + 256 * 128, W_F3 = W_F2 + 256 * 128, W_E = W_F3 + 256 * 128, W_END = W_E + 128, W_DBG = W_END;
constexpr u32 OOB = 0x7ffffff0u;
constexpr int SPIN = 1 << 14;
constexpr u32 CODE = 46000;
#define STOLD
#define OSL 8
#define RSX 1
#define RSK 1
#define RSO 1
#define RSS 1
#define XSL 13
#define MAP 0
#define WD0 3
#define WDN 6
#define WV_ 0
#define ASL 1
#define XB_ 20
#define PB_ 14
#define WARM 0
#define WEARLY 1
#define OWT 1
#define SSL 2

DEV float asf(u32 v) { return __builtin_bit_cast(float, v); }
DEV u32 asu(float v) { return __builtin_bit_cast(u32, v); }
DEV float bf_lo(u32 v) { return asf(v << 16); }
DEV float bf_hi(u32 v) { return asf(v & 0xffff0000u); }
DEV u32 pk(float lo, float hi) { b2 r = __builtin_convertvector((f2){lo, hi}, b2); return __builtin_bit_cast(u32, r); }
DEV float rb(float f) { return bf_lo(pk(f, f)); }
DEV float dot2(u32 a, u32 b, float c) { return __builtin_amdgcn_fdot2_f32_bf16(__builtin_bit_cast(b2, a), __builtin_bit_cast(b2, b), c, false); }
DEV u32 umin(u32 a, u32 b) { return a < b ? a : b; }
DEV u32 umax(u32 a, u32 b) { return a > b ? a : b; }
DEV u32 mx3(u32 a, u32 b, u32 c) { return umax(umax(a, b), c); }
template <bool Z> DEV void mm32(f16v& c, v4u a, v4u b) {
    if constexpr (Z) asm volatile("v_mfma_f32_32x32x16_bf16 %0, %1, %2, 0" : "=&v"(c) : "v"(a), "v"(b));
    else asm volatile("v_mfma_f32_32x32x16_bf16 %0, %1, %2, %0" : "+v"(c) : "v"(a), "v"(b));
}
template <bool Z> DEV void mm16(f4& c, v4u a, v4u b) {
    if constexpr (Z) asm volatile("v_mfma_f32_16x16x32_bf16 %0, %1, %2, 0" : "=&v"(c) : "v"(a), "v"(b));
    else asm volatile("v_mfma_f32_16x16x32_bf16 %0, %1, %2, %0" : "+v"(c) : "v"(a), "v"(b));
}
template <bool Z, int R> DEV void mm32b(f16v& c, v4u a) {
    if constexpr (Z) asm volatile("v_mfma_f32_32x32x16_bf16 %0, %1, a[%c2:%c3], 0" : "=&v"(c) : "v"(a), "n"(R), "n"(R + 3));
    else asm volatile("v_mfma_f32_32x32x16_bf16 %0, %1, a[%c2:%c3], %0" : "+v"(c) : "v"(a), "n"(R), "n"(R + 3));
}
template <bool Z, int R> DEV void mm16b(f4& c, v4u a) {
    if constexpr (Z) asm volatile("v_mfma_f32_16x16x32_bf16 %0, %1, a[%c2:%c3], 0" : "=&v"(c) : "v"(a), "n"(R), "n"(R + 3));
    else asm volatile("v_mfma_f32_16x16x32_bf16 %0, %1, a[%c2:%c3], %0" : "+v"(c) : "v"(a), "n"(R), "n"(R + 3));
}
template <bool Z, int R> DEV void mm32a(f16v& c, v4u b) {
    if constexpr (Z) asm volatile("v_mfma_f32_32x32x16_bf16 %0, a[%c2:%c3], %1, 0" : "=&v"(c) : "v"(b), "n"(R), "n"(R + 3));
    else asm volatile("v_mfma_f32_32x32x16_bf16 %0, a[%c2:%c3], %1, %0" : "+v"(c) : "v"(b), "n"(R), "n"(R + 3));
}
template <int N, class T> DEV void pad(T& c) { asm volatile("s_nop %1" : "+v"(c) : "n"(N)); }
template <class T> DEV void pin(T& c) { asm volatile("" : "+v"(c)); }
DEV void nop2() { asm volatile("s_nop 1"); }
template <int R> DEV u32 rda() { u32 v; asm volatile("v_accvgpr_read_b32 %0, a%c1" : "=v"(v) : "n"(R)); return v; }
template <int R> DEV void wra(u32 v) { asm volatile("v_accvgpr_write_b32 a%c1, %0" ::"v"(v), "n"(R)); }
__device__ void bst4(v4u, v4u, u32, u32, int) __asm("llvm.amdgcn.raw.buffer.store.v4i32");
__device__ void bst2(v2u, v4u, u32, u32, int) __asm("llvm.amdgcn.raw.buffer.store.v2i32");
__device__ void bst1(u32, v4u, u32, u32, int) __asm("llvm.amdgcn.raw.buffer.store.i32");
__device__ v4u bld4(v4u, u32, u32, int) __asm("llvm.amdgcn.raw.buffer.load.v4i32");
__device__ v2u bld2(v4u, u32, u32, int) __asm("llvm.amdgcn.raw.buffer.load.v2i32");
__device__ u32 bld1(v4u, u32, u32, int) __asm("llvm.amdgcn.raw.buffer.load.i32");
struct Buf { const void* p; u32 n, f; };
struct Args { Buf k, v, w[2], ph[2], x, ws, out; const void *sk, *sh, *sD; };
template <class T> DEV void sld(T& d, const void* ka, u32 off) {
    if constexpr (sizeof(T) == 16) asm volatile("s_load_dwordx4 %0, %1, %2" : "=s"(d) : "s"(ka), "s"(off));
    else asm volatile("s_load_dwordx2 %0, %1, %2" : "=s"(d) : "s"(ka), "s"(off));
}
template <int O, class T> DEV void sldi(T& d, const void* ka) {
    if constexpr (sizeof(T) == 16) asm volatile("s_load_dwordx4 %0, %1, %2" : "=s"(d) : "s"(ka), "n"(O));
    else asm volatile("s_load_dwordx2 %0, %1, %2" : "=s"(d) : "s"(ka), "n"(O));
}
DEV v4u lds4(u32 a) { return *(const LAS v4u*)a; }
DEV v2u lds2(u32 a) { return *(const LAS v2u*)a; }
DEV v2u lds2v(u32 a) { return *(const volatile LAS v2u*)a; }
DEV void barrier() { asm volatile("s_waitcnt lgkmcnt(0)\n\ts_barrier" ::: "memory"); }
template <int N> DEV void waitv() { asm volatile("s_waitcnt vmcnt(%0)" ::"n"(N) : "memory"); }
DEV void dma_in(const void* g, u32 voff, u32 lds) {
    asm volatile("s_mov_b32 m0, %2\n\ts_nop 0\n\tglobal_load_lds_dwordx4 %0, %1" ::"v"(voff), "s"(g), "s"(lds) : "m0", "memory");
}
DEV void dma_b(v4u rs, u32 voff, u32 lds) {
    asm volatile("s_mov_b32 m0, %2\n\ts_nop 0\n\tbuffer_load_dwordx4 %0, %1, 0 offen lds" ::"v"(voff), "s"(rs), "s"(lds) : "m0", "memory");
}
DEV void dma_s(v4u rs, u32 voff, u32 so, u32 lds) {
    asm volatile("s_mov_b32 m0, %3\n\ts_nop 0\n\tbuffer_load_dwordx4 %0, %1, %2 offen lds" ::"v"(voff), "s"(rs), "s"(so), "s"(lds) : "m0", "memory");
}
DEV void dma_ws(v4u rs, u32 voff, u32 so, u32 lds) {
    asm volatile("s_mov_b32 m0, %3\n\ts_nop 0\n\tbuffer_load_dwordx4 %0, %1, %2 offen sc1 lds" ::"v"(voff), "s"(rs), "s"(so), "s"(lds) : "m0", "memory");
}
template <int IM> DEV void gld4(v4u& d, u32 voff, const void* g) { asm volatile("global_load_dwordx4 %0, %1, %2 offset:%3" : "=v"(d) : "v"(voff), "s"(g), "n"(IM) : "memory"); }
DEV void gld2(v2u& d, u32 voff, const void* g) { asm volatile("global_load_dwordx2 %0, %1, %2" : "=v"(d) : "v"(voff), "s"(g) : "memory"); }
DEV void mm16k(f4& c, v2u a, v2u b) { asm volatile("v_mfma_f32_16x16x16_bf16 %0, %1, %2, 0" : "=&v"(c) : "v"(a), "v"(b)); }
DEV void mm16n(f4& c, v4u a, v4u b) { asm volatile("s_nop 1\n\tv_mfma_f32_16x16x32_bf16 %0, %1, %2, 0" : "=&v"(c) : "v"(a), "v"(b)); }
template <class A, class B> DEV void pin2(A& a, B& b) { asm volatile("" : "+v"(a), "+v"(b)); }
template <int IM> DEV void ld4(v4u& d, u32 voff, v4u rs, u32 so) { asm volatile("buffer_load_dwordx4 %0, %1, %2, %3 offen offset:%4 sc1" : "+v"(d) : "v"(voff), "s"(rs), "s"(so), "n"(IM) : "memory"); }
template <int IM> DEV void ld1(u32& d, u32 voff, v4u rs, u32 so) { asm volatile("buffer_load_dword %0, %1, %2, %3 offen offset:%4 sc1" : "+v"(d) : "v"(voff), "s"(rs), "s"(so), "n"(IM) : "memory"); }
template <int IM> DEV void ld4m(v4u& d, u32 voff, v4u rs, u32 so, u64 m) {
    asm volatile("s_mov_b64 exec, %5\n\tbuffer_load_dwordx4 %0, %1, %2, %3 offen offset:%4 sc1\n\ts_mov_b64 exec, -1" : "+v"(d) : "v"(voff), "s"(rs), "s"(so), "n"(IM), "s"(m) : "memory");
}
template <int IM, int R> DEV void ld4a(u32 voff, v4u rs, u32 so) { asm volatile("buffer_load_dwordx4 a[%c4:%c5], %0, %1, %2 offen offset:%3 sc1" ::"v"(voff), "s"(rs), "s"(so), "n"(IM), "n"(R), "n"(R + 3) : "memory"); }
template <int IM, int R> DEV void ld4v(u32 voff, v4u rs, u32 so) { asm volatile("buffer_load_dwordx4 v[%c4:%c5], %0, %1, %2 offen offset:%3 sc1" ::"v"(voff), "s"(rs), "s"(so), "n"(IM), "n"(R), "n"(R + 3) : "memory"); }
template <int R> DEV u32 mx3v(u32 a, u32 b) { u32 r; asm volatile("v_max3_u32 %0, %1, %2, v%c3" : "=v"(r) : "v"(a), "v"(b), "n"(R)); return r; }
template <int R> DEV u32 mx3vv(u32 a) { u32 r; asm volatile("v_max3_u32 %0, %1, v%c2, v%c3" : "=v"(r) : "v"(a), "n"(R), "n"(R + 2)); return r; }
template <int R, int A> DEV void swapw() { asm volatile("v_permlane32_swap_b32 v%c0, v%c1\n\tv_accvgpr_write_b32 a%c2, v%c0\n\tv_accvgpr_write_b32 a%c3, v%c1" ::"n"(R), "n"(R + 2), "n"(A), "n"(A + 1)); }
template <int R, int R2> DEV void addv() { asm volatile("v_add_f32 v%c0, v%c0, v%c1" ::"n"(R), "n"(R2)); }
template <int R> DEV float swapsum() { float r; asm volatile("s_nop 1\n\tv_permlane32_swap_b32 v%c1, v%c2\n\tv_add_f32 %0, v%c1, v%c2" : "=v"(r) : "n"(R + 1), "n"(R + 3)); return r; }
template <int IM, int R> DEV void ld1a(u32 voff, v4u rs, u32 so) { asm volatile("buffer_load_dword a%c4, %0, %1, %2 offen offset:%3 sc1" ::"v"(voff), "s"(rs), "s"(so), "n"(IM), "n"(R) : "memory"); }
template <int IM, int R> DEV void ld4am(u32 voff, v4u rs, u32 so, u64 m) {
    asm volatile("s_mov_b64 exec, %6\n\tbuffer_load_dwordx4 a[%c4:%c5], %0, %1, %2 offen offset:%3 sc1\n\ts_mov_b64 exec, -1" ::"v"(voff), "s"(rs), "s"(so), "n"(IM), "n"(R), "n"(R + 3), "s"(m) : "memory");
}
template <int IM> DEV void ld2(v2u& d, u32 voff, v4u rs, u32 so) { asm volatile("buffer_load_dwordx2 %0, %1, %2, %3 offen offset:%4 sc1" : "=v"(d) : "v"(voff), "s"(rs), "s"(so), "n"(IM) : "memory"); }
template <int IM> DEV void ld4o(v4u& d, u32 voff, v4u rs, u32 so) { asm volatile("buffer_load_dwordx4 %0, %1, %2, %3 offen offset:%4 sc1" : "=v"(d) : "v"(voff), "s"(rs), "s"(so), "n"(IM) : "memory"); }
template <int IM> DEV void ldh(u32& d, u32 voff, v4u rs, u32 so) { asm volatile("buffer_load_short_d16_hi %0, %1, %2, %3 offen offset:%4 sc1" : "+v"(d) : "v"(voff), "s"(rs), "s"(so), "n"(IM) : "memory"); }
template <int IM> DEV void st4(v4u d, u32 voff, v4u rs, u32 so) { asm volatile("buffer_store_dwordx4 %0, %1, %2, %3 offen offset:%4 sc1" ::"v"(d), "v"(voff), "s"(rs), "s"(so), "n"(IM) : "memory"); }
DEV void setm0(u32 v) { asm volatile("s_mov_b32 m0, %0" ::"s"(v) : "m0", "memory"); }
template <int IM> DEV void dmai(v4u rs, u32 voff, u32 so) { asm volatile("buffer_load_dwordx4 %0, %1, %2 offen offset:%3 sc1 lds" ::"v"(voff), "s"(rs), "s"(so), "n"(IM) : "memory"); }
DEV u32 pka(float a, float b) { u32 r; asm volatile("v_cvt_pk_bf16_f32 %0, %1, %2" : "=v"(r) : "v"(a), "v"(b)); return r; }
template <int N> DEV void waitl() { __builtin_amdgcn_s_waitcnt(0xc07f | (N << 8)); }
DEV u32 vsel(bool c, u32 a, u32 b) { asm volatile("" : "+v"(a)); asm volatile("" : "+v"(b)); return c ? a : b; }
DEV u32 ssel(bool c, u32 a, u32 b) { asm volatile("" : "+s"(a)); asm volatile("" : "+s"(b)); return c ? a : b; }
template <int N, class T> DEV void waitp(T& d) { asm volatile("s_waitcnt vmcnt(%1)" : "+v"(d) : "n"(N) : "memory"); }
template <int N> DEV void waitbar() { asm volatile("s_waitcnt vmcnt(%0) lgkmcnt(0)\n\ts_barrier" ::"n"(N) : "memory"); }
template <int N> DEV void pad3(f4& a, f4& b, f4& c) { asm volatile("s_nop %3" : "+v"(a), "+v"(b), "+v"(c) : "n"(N)); }
template <int I> struct IC { static constexpr int v = I; };
template <int B, int E, class F> DEV void sfor(F&& f) { if constexpr (B < E) { f(IC<B>{}); sfor<B + 1, E>(f); } }
#define LAM(I) [&](auto I) __attribute__((always_inline))
#define FENCE() __builtin_amdgcn_sched_barrier(0)
#define OPQV(x) asm volatile("" : "+v"(x))
#define OPQS(x) asm volatile("" : "+s"(x))
DEV float halves_sum(float a, float b) {
    auto r = __builtin_amdgcn_permlane32_swap(asu(a), asu(b), false, false);
    return asf(r[0]) + asf(r[1]);
}
DEV bool bad(u32 m) { return __builtin_amdgcn_ballot_w64(m == ~0u) != 0; }
#define STAMP_INIT()
template <int I> DEV void mark() { asm volatile("; MARK %0" ::"n"(I)); }
#define STAMP(i) mark<(i)>()
#define STAMP_END()
#define STAMPV(i, v)

DEV void hedgehog(const float (&t)[18], float a, u32 (&ph)[18]) {
    float mx = t[0], mn = t[0];
#pragma unroll
    for (int i = 1; i < 18; ++i) { mx = __builtin_fmaxf(mx, t[i]); mn = __builtin_fminf(mn, t[i]); }
    const float c1 = -a * mx, c2 = a * mn;
    float e1[18], e2[18], s1 = 0.f, s2 = 0.f;
#pragma unroll
    for (int i = 0; i < 18; ++i) {
        e1[i] = __builtin_amdgcn_exp2f(__builtin_fmaf(t[i], a, c1)); s1 = i ? s1 + e1[i] : e1[i];
        e2[i] = __builtin_amdgcn_exp2f(__builtin_fmaf(t[i], -a, c2)); s2 = i ? s2 + e2[i] : e2[i];
    }
    const float r1 = __builtin_amdgcn_rcpf(s1), r2 = __builtin_amdgcn_rcpf(s2);
#pragma unroll
    for (int i = 0; i < 9; ++i) { ph[i] = pk(e1[2 * i] * r1, e1[2 * i + 1] * r1); ph[9 + i] = pk(e2[2 * i] * r2, e2[2 * i + 1] * r2); }
}
DEV v4u tr2(u32 a0, u32 a1) {
    s4 lo = __builtin_amdgcn_ds_read_tr16_b64_v4i16((LAS s4*)a0), hi = __builtin_amdgcn_ds_read_tr16_b64_v4i16((LAS s4*)a1);
    v2u l = __builtin_bit_cast(v2u, lo), h = __builtin_bit_cast(v2u, hi);
    return (v4u){l.x, l.y, h.x, h.y};
}
DEV v2u y_val(f4 acc) { return (v2u){pk(acc[0], acc[1]), pk(acc[2], acc[3])}; }
DEV v4u y_pair(v2u a, v2u b) {
    auto r0 = __builtin_amdgcn_permlane16_swap(a.x, b.x, false, false), r1 = __builtin_amdgcn_permlane16_swap(a.y, b.y, false, false);
    return (v4u){r0[0], r1[0], r0[1], r1[1]};
}
template <int N, int n, int r>
DEV void conv_read(u32 aa, u32 ba, v4u (&A)[N], v4u (&B)[N]) {
    constexpr int i = n / 3, k = n % 3;
    if constexpr (k == 0) B[i] = lds4(ba + 64 * (8 * i - r + 7));
    else { const v2u v = lds2(aa + 2 + 64 * (r + 8 * i) + 8 * (k - 1)); if constexpr (k == 1) { A[i].x = v.x; A[i].y = v.y; } else { A[i].z = v.x; A[i].w = v.y; } }
}
constexpr int conv_ij(int N, int MASK, int n) {
    for (int d = 0; d < N; ++d) for (int i = 0; i < N; ++i) for (int j = i; j < N; ++j)
        if (((MASK >> j) & 1) && (i > j - i ? i : j - i) == d && n-- == 0) return i * 8 + j;
    return -1;
}
#define NPRE_ 10
#define SEND_ 212
#define UPA_ 8
#define UPO_ 12
constexpr int NPRE = NPRE_, NSITE = 13 + 4 + 40 + 9 + 1, SITE_END = SEND_;
constexpr int site_at(int q) { return q <= SITE_END ? NPRE + (q * (NSITE - NPRE) + SITE_END - 1) / SITE_END : NSITE; }
constexpr int xissued(int n) { return UPO_ + (5 * n + 3) / 4 < 32 ? UPO_ + (5 * n + 3) / 4 : 32; }
constexpr int kstep(int n) { return n % 9 < 8 ? 8 * (n / 9) + n % 9 : 32 + n / 9; }

__global__ void __launch_bounds__(256, 1) k_main(Args) {
    const void* ka = (const void*)(uintptr_t)__builtin_amdgcn_kernarg_segment_ptr();
    v4u rk, rv, rm, rq, rp, rx, vw, ro;
    const char *sk, *sh, *sD;
    int tid = __builtin_amdgcn_workitem_id_x(), b = __builtin_amdgcn_workgroup_id_x();
    sldi<0>(rk, ka); sldi<16>(rv, ka); sldi<144>(sk, ka); sldi<112>(vw, ka); sldi<96>(rx, ka); sldi<128>(ro, ka); sldi<152>(sh, ka); sldi<160>(sD, ka);
    OPQS(b); OPQV(tid);
    const int bs = b >> 3;
    if constexpr (MAP == 1) b = (b & 6) + (b >> 7) + 64 * (b & 1) + (b & 56) + 2 * (b & 64);
    if constexpr (MAP == 2) b = (b & 4) + ((b >> 6) & 3) + 32 * (b & 3) + (b & 24) + 4 * (b & 32);
    int wave = __builtin_amdgcn_readfirstlane(tid >> 6);
    sld(rm, ka, (wave >> 1) * 16); sld(rq, ka, (b & 24) == 24 ? 48 : 32); sld(rp, ka, 64 + (b & 8) * 2);
    OPQS(b); OPQS(wave); OPQV(tid);
    {
        u64 pc;
        asm volatile("s_getpc_b64 %0" : "=s"(pc));
        const u32 o = umin(tid * 8 + bs * 2048, CODE - 512);
        asm volatile("s_mov_b32 m0, %2\n\ts_nop 0\n\tglobal_load_lds_dword %0, %1" ::"v"(o), "s"(pc), "s"(L_EX) : "m0", "memory");
    }
    STAMP_INIT();
    asm volatile("" ::: "a255", "v255");
    u64 ep;
    v2u sw0, sw1;
    {
        const int lane = tid & 63, c = lane & 15, cg = ((b >> 4) & 6) + ((b >> 3) & 1), tg = 4 * (b & 7) + 2 * (b >> 7) + ((b >> 4) & 1);
        u32 s0 = c * 8 + (wave * 128 + cg * 576), s1 = (c & 7) * 8 + cg * 576, vo[5], lo[5];
        const u32 l0 = ((lane * 7282u) >> 16) * 1008 + lane * 16, t0 = (64 * tg + 7 * wave) * 1152 + cg * 144;
#pragma unroll
        for (int i = 0; i < 5; ++i) {
            const int isv = i < 2 ? 0 : i > 2 ? 1 : wave >> 1;
            vo[i] = l0 + (t0 + (28 * i - 3 - 67 * isv) * 1152); lo[i] = P_KS + wave * 1008 + i * 4032 + isv * (P_VS - 10080);
            OPQV(vo[i]); OPQS(lo[i]);
        }
        pin2(s0, s1);
        FENCE();
        asm volatile("s_waitcnt lgkmcnt(0)" : "+s"(rk), "+s"(rv), "+s"(rm), "+s"(rq), "+s"(rp), "+s"(rx), "+s"(vw), "+s"(ro), "+s"(sk), "+s"(sh), "+s"(sD));
        STAMP(40);
#pragma unroll
        for (int i = 0; i < 5; ++i) dma_b(i < 2 ? rk : i > 2 ? rv : rm, vo[i], lo[i]);
        asm volatile("global_load_dwordx2 %0, %1, %2" : "=v"(sw0) : "v"(s0), "s"(sk) : "memory");
        asm volatile("global_load_dwordx2 %0, %1, %2 offset:512" : "=v"(sw1) : "v"(s1), "s"(sk) : "memory");
        sld(ep, (const void*)(((u64)vw.y << 32) | vw.x), W_E);
    }
    OPQS(b); OPQS(wave); OPQV(tid);
    const int lane = tid & 63, rg = 2 * (b & 7) + (b >> 7), p = (b >> 3) & 15;
    const int Pg = p >> 2, tw = p & 3, pw = 4 * Pg + wave, ci = (rg + p) & 15;
    const bool conv = ci < 12;
    u32 l16 = lane * 16, l4 = lane * 4;
    OPQV(l16); OPQV(l4);
    const int ch0 = 36 * p + 3 * ci;
    v4u hd[3]; v2u he[3], hz;
    u32 xr[9], kvw[3], kvA, kvB, l8, lP, l16B, cpo, csd;
    {
        const int sec = p & 3, cg = 2 * Pg + (p & 1), tg = 2 * rg + ((p >> 1) & 1);
        const int c = lane & 15;
        FENCE();
        STAMP(41);
        const v4u z4 = {0, 0, 0, 0};
        u32 mk0, mk1, ua0, ua1, uo0, uo1, aw, al_, ap, d0_, d1_, d2_, cso, axs, aid, azc;
        v2u Bi; v4u Sel;
        const bool s3 = sec == 3;
        const u32 wf_ = rg >> 2, jl = rg & 3;
        u32 lw, ll, lx, lf;
        const u32 wst = s3 ? 1152 : 3456;
        {
            const u32 cb = (sec < 2 ? (sec + 1) * 1152 : 0) + Pg * 288;
            lw = ((lane * 3641u) >> 16) * (wst - 288) + l16 + ((36 * rg + 3 * wave) * wst + cb);
            lf = vsel(lane < 54, l16 - ((lane * 21846u) >> 16) * 12 + (Pg * 5184 + wave * 1296), OOB);
            lx = ((lane * 3450u) >> 16) * 848 + l16 + ((128 * rg + 32 * tw + 3 * wave) * 1152 + Pg * 288);
        }
        auto wdma = LAM(K_) {
            constexpr int k = K_.v, i = k / 3;
            if constexpr (k == 7) {
                const u32 cb = (sec < 2 ? (sec + 1) * 1152 : 0) + Pg * 288, r4 = (lane * 43u) >> 8, sg = ((s3 ? 0 : (4 * Pg + wf_) & 14) + 4 * jl + wave) & 15;
                ll = vsel(lane < 24, r4 * (wst - 96) + (r4 >> 1) * (16 * wst) + l16 + ((36 * sg + 16) * wst + cb + ((72 * wf_) & ~15u)), OOB);
                dma_b(rq, ll, P_LS + wave * 1024);
            } else if constexpr (k % 3 == 0) dma_s(rq, lw, 12 * i * wst, P_WS + wave * 864 + i * 3456);
            else if constexpr (k % 3 == 1) dma_s(rp, lf, i * 648, P_PH + wave * 3072 + i * 1536);
            else dma_s(rx, lx, 12 * i * 1152, P_XS + wave * 912 + i * 3648);
            FENCE();
        };
        u32 ib, bA, bB, lp, ds, dr;
        auto wadr = LAM(K_) {
            constexpr int k = K_.v;
            const u32 g = lane >> 4, m = lane & 31, kh = lane >> 5;
            if constexpr (k == 0) {
                const u32 hD = (m >> 2) & 1;
                aw = (18 * hD + (m & 3) + 4 * (m >> 3)) * 288 + 16 * kh + (P_WS + ((72 * wave) & ~15u));
                pin(aw);
            }
            if constexpr (k == 1) {
                const u32 hD = (m >> 2) & 1;
                al_ = ((((m >> 1) & 1) + 2 * ((m >> 3) & 1)) * 1024 + (2 * hD + (m & 1)) * 96) * (wave == wf_) + 16 * kh + (wave == wf_ ? P_LS : L_Z);
                pin(al_);
            }
            if constexpr (k == 2) {
                ap = (8 * kh + (c >> 2)) * 48 + 32 * (g & 1) + 8 * (c & 3) + (P_PH + (64 * wave - 4 * (wave & 1)) * 48);
                pin(ap);
            }
            if constexpr (k == 3) {
                ib = W_IMG + pw * IMG; const u32 ob = W_WOT + Pg * WOG;
                bA = ssel(s3, ob + wave * 36864, ib + sec * L_V + (sec >> 1) * (L_Q - 2 * L_V));
                bB = ssel(s3, ob + 4 * 36864 + 128 * (wave >> 1) + 32 * (wave & 1), ib + L_QT + 128 * sec);
                OPQS(ib); OPQS(bA); OPQS(bB);
            }
            if constexpr (k == 4) {
                lp = vsel(m < 16, ((m & 3) + 8 * (m >> 2) + 32 * kh) * 16 + bA, vsel(m < 18, (m & 1) * (s3 ? 16 : 32) + kh * (s3 ? 512 : 16) + bB, OOB));
                pin(lp);
            }
            if constexpr (k == 5) {
                ds = vsel(m < 16, 1024, s3 ? 256 : QTS); dr = vsel(m < 16, 2048, s3 ? 1024 : 2 * QTS);
                d0_ = lp + ((rg - (s3 ? 0 : pw & 14)) & 15) * dr; d1_ = d0_ + ds;
                pin2(ds, dr); pin2(d0_, d1_);
            }
            if constexpr (k == 6) {
                d2_ = umax(lp + (16 + (jl >> 1)) * dr + (jl & 1) * ds, wave == wf_ ? 0u : OOB);
                pin(d2_);
            }
            if constexpr (k == 7) {
                cso = vsel(m < (s3 ? 0 : 18), (18 * kh + m) * 4 + (W_CSP + pw * CSB + rg * 576 + sec * 192), OOB);
                pin(cso);
            }
            if constexpr (k == 8) {
                axs = m * 304 + 36 * kh + (P_XS + 72 * wave);
                pin(axs);
            }
            if constexpr (k == 9) {
                aid = vsel(lane < 36 * (sec & 1), lane * 50 + (lane >= 18 ? 1536 - 900 : 0) + (P_PH + wave * 3072), L_END - 16);
                pin(aid);
            }
            if constexpr (k == 10) {
                azc = vsel(lane < (sec ? 0 : 6), (lane & 1) * 16 + ((lane >> 1) + (lane < 4 ? 2 * rg : 30 + jl)) * QTS + (ib + L_QT + 384), OOB);
                pin(azc);
            }
        };
        sfor<0, WD0>(wdma);
        STAMP(37);
        {
            const u32 g = lane >> 4, m = lane & 31, kh = lane >> 5;
            const u32 hm = g == (c >> 2) ? ((c & 1) ? 0xffff0000u : 0xffffu) : 0u;
            mk0 = (c & 2) ? 0u : hm; mk1 = (c & 2) ? hm : 0u;
            Bi = (v2u){mk0 & 0x3f803f80u, mk1 & 0x3f803f80u}; Sel = (v4u){Bi.x, Bi.y, Bi.x, Bi.y};
            const u32 r0 = (8 * (c >> 2) + (c & 3)) * 144;
            ua0 = r0 + 8 * g + (P_KS + 32 * wave);
            ua1 = r0 + 32 * 144 * kh + 8 * (g & 1) + (P_KS + 128 + 576 * (wave & 1));
            uo0 = c * 4096 + 16 * g;
            uo1 = (c & 7) * 4096 + 64 * (c >> 3) + 16 * g + (wave < 2 ? 64 * 4096 + 8 - 8 * wave : OOB - (1u << 20));
            pin2(mk0, mk1); pin2(Bi, Sel); pin2(ua0, ua1); pin2(uo0, uo1);
            *(LAS v4u*)(l16 + kh * 1024 + (P_PH + 1024 + wave * 3072)) = z4;
            *(LAS v4u*)(vsel(lane < (wave ? 30 : 6), l16 + ssel(wave, L_Q + 4 * HCS - CVS + wave * CVS, L_Z), L_END - 16)) = z4;
        }
        FENCE();
        FENCE();
        sfor<0, 11>(LAM(K_) {
            constexpr int k = K_.v, d0 = WD0 + k * (9 - WD0) / WDN, d1 = WD0 + (k + 1) * (9 - WD0) / WDN;
            if constexpr (k < WDN) sfor<d0, d1>(wdma);
            wadr(K_);
            FENCE();
        });
        auto hld = [&]() __attribute__((always_inline)) {
            const char* gp = sh + umin(ch0, 573) * 4096;
            const u32 o1 = tid * 16, o2 = (o1 + 16) & 4095;
            gld2(hz, umin(lane >> 3, 2) * 4096, gp);
#pragma unroll
            for (int i = 0; i < 3; ++i) { gld4<0>(hd[i], o1, gp + i * 4096); gld2(he[i], o2, gp + i * 4096); }
        };
        STAMP(42);
        FENCE();
        STAMP(26);
        asm volatile("s_waitcnt vmcnt(9)" : "+v"(sw0), "+v"(sw1)::"memory");
        v2u Bd[2][4];
#pragma unroll
        for (int s = 0; s < 2; ++s)
#pragma unroll
            for (int j = 0; j < 4; ++j) {
                const v2u sw = s ? sw1 : sw0;
                const u32 wd = __builtin_amdgcn_perm(0u, j < 2 ? sw.y : sw.x, (j & 1) ? 0x01000100u : 0x03020302u);
                Bd[s][j] = (v2u){wd & mk0, wd & mk1};
                pin(Bd[s][j]);
            }
        u32 so_u = W_UT + 128 * tg + cg * 72 * 4096, so_u2 = so_u + wave * 65536;
        OPQS(so_u); OPQS(so_u2);
        FENCE();
        barrier();
        STAMP(27);
        {
            u32 uo[5][2];
            v2u ka[5][5]; f4 pr[5][5], sm[5];
            v4u bb[2];
#pragma unroll
            for (int n = 0; n < 25; ++n) {
                const int T = n < 10 ? n >> 1 : n % 5, j = n < 10 ? n & 1 : n / 5;
                ka[T][j] = lds2v((T < 4 ? ua0 + (32 * (T >> 1) + 4 * (T & 1)) * 144 : ua1) + (j < 4 ? j * 144 : P_VS));
            }
            FENCE();
            auto P = [&](auto T_, auto J_) __attribute__((always_inline)) { constexpr int T = T_.v, j = J_.v; mm16k(pr[T][j], ka[T][j], j < 4 ? Bd[T == 4][j] : Bi); };
            sfor<0, 5>(LAM(T_) { P(T_, IC<0>{}); P(T_, IC<1>{}); });
            STAMP(52);
            sfor<0, 16>(LAM(I_) {
                constexpr int I = I_.v, T = I % 5, ph = I / 5 + 1, U = (I + 4) % 5;
                if constexpr (I < 15) {
                    f4& a = ph == 1 ? pr[T][0] : sm[T];
                    const u32 w0 = pka(a[0], a[1]), w1 = pka(a[2], a[3]), w2 = pka(pr[T][ph][0], pr[T][ph][1]), w3 = pka(pr[T][ph][2], pr[T][ph][3]);
                    bb[I & 1] = (v4u){w0, w1, w2, w3};
                    P(IC<T>{}, IC<ph + 1>{});
                } else {
                    uo[0][0] = pka(sm[0][0], sm[0][1]); uo[0][1] = pka(sm[0][2], sm[0][3]);
                }
                if constexpr (I > 0) mm16<true>(sm[U], Sel, bb[(I - 1) & 1]);
            });
            STAMP(53);
            sfor<0, 5>(LAM(T_) {
                constexpr int T = T_.v;
                u32 w0, w1;
                if constexpr (T) { w0 = pka(sm[T][0], sm[T][1]); w1 = pka(sm[T][2], sm[T][3]); } else { w0 = uo[0][0]; w1 = uo[0][1]; }
                pin(pr[T][4]);
                FENCE();
                uo[T][0] = pk(bf_hi(w1) * pr[T][4][3], bf_lo(w1) * pr[T][4][2]);
                uo[T][1] = pk(bf_hi(w0) * pr[T][4][1], bf_lo(w0) * pr[T][4][0]);
                if constexpr (T == 1 || T == 3) bst4((v4u){uo[T][0], uo[T][1], uo[T - 1][0], uo[T - 1][1]}, vw, uo0 + 32 * (T - 1), so_u2, SC1);
                else if constexpr (T == 4) bst2((v2u){uo[4][0], uo[4][1]}, vw, uo1, so_u, SC1);
                else pin2(uo[T][0], uo[T][1]);
                FENCE();
            });
        }
        STAMP(28);
        asm volatile("s_waitcnt vmcnt(3)\n\ts_barrier" ::: "memory");
        STAMP(54);
        {
            u32 one2 = 0x3f803f80u;
            OPQS(one2);
            *(LAS u16*)(aid) = 0x3f80;
            v4u bw[2][2], wa[2][3];
#pragma unroll
            for (int j = 0; j < 9; ++j) xr[j] = *(const LAS u32*)(axs + 4 * j);
            FENCE();
            bw[0][0] = tr2(ap, ap + 192); wa[0][0] = lds4(aw); wa[1][0] = lds4(al_);
            bw[0][1] = tr2(ap + 768, ap + 960); wa[0][1] = lds4(aw + 32); wa[1][1] = lds4(al_ + 32);
            bw[1][0] = tr2(ap + 1440, ap + 1440 + 192);
            bw[1][1] = tr2(ap + 1440 + 768, ap + 1440 + 960); wa[0][2] = lds4(aw + 64); wa[1][2] = lds4(al_ + 64);
            FENCE();
            const u32 so = W_XF0 + ((rg * 4 + tw) * 16 + pw) * XS;
            bst4((v4u){xr[0], xr[1], xr[2], xr[3]}, vw, l16, so, SC1);
            bst4((v4u){xr[4], xr[5], xr[6], xr[7]}, vw, l16 + 1024, so, SC1);
            FENCE();
            f16v d[2][2];
#pragma unroll
            for (int i = 0; i < 4; ++i) { mm32<true>(d[i >> 1][i & 1], wa[i & 1][i >> 1], bw[i >> 1][0]); mm32<false>(d[i >> 1][i & 1], wa[i & 1][(i >> 1) + 1], bw[i >> 1][1]); }
            FENCE();
            STAMP(55);
            {
                float s1 = 0.f, s2 = 0.f;
#pragma unroll
                for (int j = 0; j < 9; ++j) { s1 = dot2(xr[j], one2, s1); s2 = dot2(xr[j], xr[j], s2); }
                l8 = 2 * l4;
                bst2((v2u){xr[8], asu(halves_sum(s1, s2))}, vw, (lane & 31) * 16 + (lane >> 5) * 8 + 2048, so, SC1);
                FENCE();
                bst4(z4, vw, azc, 0, SC1);
            }
            FENCE();
            float cs[2];
#pragma unroll
            for (int hh = 0; hh < 2; ++hh) {
                asm volatile("" : "+v"(d[hh][0]), "+v"(d[hh][1]));
                u32 w[12];
#pragma unroll
                for (int j = 0; j < 12; ++j) w[j] = pk(d[hh][j >> 3][2 * (j & 7)], d[hh][j >> 3][2 * (j & 7) + 1]);
                bst4((v4u){w[0], w[1], w[2], w[3]}, vw, d0_ + 64 * hh, 0, SC1);
                bst4((v4u){w[4], w[5], w[6], w[7]}, vw, d1_ + 64 * hh, 0, SC1);
                bst4((v4u){w[8], w[9], w[10], w[11]}, vw, d2_ + 64 * hh, 0, SC1);
                cs[hh] = 0.f;
#pragma unroll
                for (int j = 0; j < 12; ++j) cs[hh] = dot2(w[j], one2, cs[hh]);
                FENCE();
            }
            bst1(asu(halves_sum(cs[0], cs[1])), vw, cso, 0, SC1);
        }
        STAMP(29);
        asm volatile("s_waitcnt vmcnt(8)\n\ts_barrier" ::: "memory");
        OPQS(ep);
        ep += 1;
        const v2u epv = {(u32)ep, (u32)(ep >> 32)};
        if (tid == 128) bst2(epv, vw, 0, W_F + (rg * 16 + p) * 128, SC1);
        FENCE();
        hld();
        STAMP(37);
        {
            const u32 e = (lane >> 4) & 1, kg = lane >> 4;
#pragma unroll
            for (int j = 0; j < 3; ++j) {
                const u32 pi = 16 * (wave + 4 * j) + c, hh = pi >= 86, ip = pi - 86 * hh, q = ip - 72, s = 2 * (ip - 84) + e;
                u32 v = vsel(ip < 64, (ip & 15) * 80 + (ip >> 4) * 8 + 32 * e, (2 * ip + e) * 80 + 64 - 128 * 80);
                v = vsel(ip < 72, v, (16 + (q >> 2)) * 80 + (q & 3) * 8 + 32 * e);
                v = vsel(ip < 84, v, s * 80 + 16 * 80 + 64);
                kvw[j] = vsel(lane >= 32 || pi >= 172 || s == 3, L_EX + l8, v + hh * 1520 + L_KVA);
                OPQV(kvw[j]);
            }
            *(LAS v2u*)(vsel(tid < 38, tid * 80 + (L_KVA + 72), L_END - 16)) = (v2u){0, 0};
            const bool dt = wave & 1;
            kvA = dt ? vsel(c < 3, 1152 + (c * 4 + kg) * 16, OOB) : l16;
            kvB = vsel(lane < (dt ? 4 : 16), l8 + (dt ? 1344 : 1024), OOB);
            l16B = l16 + XS; lP = (lane & 31) * 16 + (lane >> 5) * XS;
            const u32 sc = (lane * 57u) >> 10, r = lane - 18 * sc, hh = r >= 9, pr = r - 9 * hh;
            cpo = vsel(lane < 54, sc * (192 - 144) + l8, OOB);
            csd = vsel(lane < 54, hh * 224 + vsel(pr < 8, sc * 64 + pr * 8, 192 + sc * 8) + L_CS, L_EX + 2048 + l8);
            pin2(kvA, kvB); pin2(l16B, lP); pin2(cpo, csd); pin(l8);
        }
        FENCE();
    }
    auto xop = [&](auto Q_, u32 so) __attribute__((always_inline)) {
        constexpr int j = Q_.v / 5, k = Q_.v % 5;
        if constexpr (k == 0) ld4a<0, 16 * j>(l16, vw, so);
        if constexpr (k == 1) ld4a<1024, 16 * j + 4>(l16, vw, so);
        if constexpr (k == 2) ld4v<2048, 224 + 4 * j>(lP, vw, so);
        if constexpr (k == 3) ld4a<0, 16 * j + 8>(l16B, vw, so);
        if constexpr (k == 4) ld4a<1024, 16 * j + 12>(l16B, vw, so);
    };
    {
        u32 D2[3];
#pragma unroll
        for (int i = 0; i < 3; ++i) { const u32 cc = umin(ch0, 573) + i, t = ((const __attribute__((address_space(4))) u32*)(uintptr_t)sD)[cc >> 1]; D2[i] = (cc & 1 ? t >> 16 : t & 0xffffu) * 0x10001u; OPQS(D2[i]); }
        const int bq = lane & 15, g = lane >> 4, cc = (bq + 1) & 3;
        u32 aa = L_Q + cc * HCS + 2 * (bq - 8 * g + 24 - cc), ba = L_Q + 4 * HCS + 480 + 2 * (16 * bq + 8 * g - 16) - 7 * 64;
        u32 yo = (bq >> 3) * YB + (bq & 7) * 32 + 8 * g, yp = (bq >> 3) * YB + (bq & 7) * 32 + 16 * (g >> 1) + (g & 1) * (2 * YB);
        v4u dm;
        {
            const u32 e = 7 - (bq & 7), hm = g == 2 + (bq >> 3) ? ((e & 1) ? 0xffff0000u : 0xffffu) : 0u;
#pragma unroll
            for (int i = 0; i < 4; ++i) dm[i] = (e >> 1) == i ? hm : 0u;
        }
        if (wave < 3) { aa += wave * CVS; ba += wave * CVS; }
        pin2(aa, ba); pin2(yo, yp); pin(dm);
        const u32 xb = W_XF0 + (rg * 4 + wave) * 16 * XS;
        u32 io = wave * 9216, ig = W_IMG + p * IMG + io, iq = umin(4 * wave, 11) * 1024 + L_QT - 8192 - io, xo = xb + (p & 14) * XS, uo = W_UT + ch0 * 4096 + wave * 1024, co = W_CSP + p * CSB + wave * (4 * 576);
        u32 fo = vsel(wave < 2, W_F + (wave ? rg * 16 + bq : (lane >> 2) * 16 + 4 * Pg + (lane & 3)) * 128, OOB);
        u32 fb = W_F3 + ((4 * wave + (bq >> 2)) * 16 + 4 * Pg + (lane & 3)) * 128;
        OPQS(io); OPQS(ig); OPQS(iq); OPQS(xo); OPQS(uo); OPQS(co); OPQV(fo); OPQV(fb);
        FENCE();
        v2u fl;
        auto late = [&]() __attribute__((always_inline)) { return __builtin_amdgcn_ballot_w64(fl.x != (u32)ep || fl.y != (u32)(ep >> 32)) != 0; };
        auto spin = [&](u32 o) __attribute__((always_inline)) {
#pragma unroll 1
            for (int sp = 0; sp < SPIN; ++sp) {
                __builtin_amdgcn_s_sleep(ASL);
                ld2<0>(fl, o, vw, 0);
                waitp<0>(fl);
                if (!late()) break;
            }
        };
        waitv<7>();
        barrier();
        STAMP(1);
        if (tid == 192) bst2((v2u){(u32)ep, (u32)(ep >> 32)}, vw, 0, W_F3 + (rg * 16 + p) * 128, SC1);
        ld2<0>(fl, fo, vw, 0);
        waitv<2>();
        if (wave < 3) waitv<1>();
        STAMP(51);
        if (conv) {
            u32 o = L_Q + 32 + tid * 16;
            OPQV(o);
#pragma unroll
            for (int i = 0; i < 3; ++i) {
                asm volatile("" : "+v"(hd[i]), "+v"(he[i]));
                const u32 d0 = hd[i].x, d1 = hd[i].y, d2 = hd[i].z, d3 = hd[i].w, d4 = he[i].x, d5 = he[i].y;
                const u32 a0 = __builtin_amdgcn_alignbit(d1, d0, 16), a1 = __builtin_amdgcn_alignbit(d2, d1, 16), a2 = __builtin_amdgcn_alignbit(d3, d2, 16),
                          a3 = __builtin_amdgcn_alignbit(d4, d3, 16), a4 = __builtin_amdgcn_alignbit(d5, d4, 16);
                *(LAS v4u*)(o + i * CVS) = hd[i];
                *(LAS v4u*)(o + i * CVS + HCS) = (v4u){a0, a1, a2, a3};
                *(LAS v4u*)(o + i * CVS + 2 * HCS) = (v4u){d1, d2, d3, d4};
                *(LAS v4u*)(o + i * CVS + 3 * HCS) = (v4u){a1, a2, a3, a4};
            }
            asm volatile("" : "+v"(hz));
            const u32 k = (lane >> 1) & 3;
            const u64 t = (lane & 1) && k ? (((u64)hz.y << 32) | hz.x) << (64 - 16 * k) : 0;
            if (tid < 24) *(LAS v4u*)(L_Q + (lane >> 3) * CVS + k * HCS + (lane & 1) * 16) = (v4u){0, 0, (u32)t, (u32)(t >> 32)};
        }
        waitp<0>(fl);
        if (wave < 2 && late()) spin(fo);
        barrier();
        STAMP(2);
        v2u cp[4];
        auto site = LAM(N_) {
            constexpr int m = N_.v, n = m <= XB_ ? -1 : m <= XB_ + 17 ? m - XB_ - 1 : m - 1, x = m < PB_ ? m : m <= XB_ ? m - 1 : m - 18, v = n - 57;
            if constexpr (m == XB_ + 1) {
                waitp<XB_ - PB_>(fl);
                if (__builtin_expect(late(), 0)) spin(fb);
                asm volatile("s_barrier" ::: "memory");
                STAMP(38);
            }
            if constexpr (m == PB_) ld2<0>(fl, fb, vw, 0);
            else if constexpr (n >= 0 && n < 9) dmai<(n & 3) * 1024>(vw, l16, ig);
            else if constexpr (n >= 9 && n < 13) dmai<(n - 9) * 1024>(vw, l16, ig);
            else if constexpr (n >= 13 && n < 17) ld2<(n - 13) * 576>(cp[n - 13], cpo, vw, co);
            else if constexpr (n < 57) {
                xop(IC<x>{}, xo);
                if constexpr (x % 5 == 4 && x < 39) { xo += 2 * XS; xo = xo == xb + 16 * XS ? xb : xo; OPQS(xo); }
            } else {
                if constexpr (v == WV_) waitv<40 - XB_ + WV_>();
                dmai<(v & 3) * 1024>(vw, l16, ig);
            }
            if constexpr (n == 3 || n == 7 || n == 60 || n == 64) { io += 4096; ig += 4096; }
            if constexpr (n == 8) { io += iq; ig += iq; }
            if constexpr (n == 12) { io += L_V - 8192 - iq; ig += L_V - 8192 - iq; }
            if constexpr (n == 3 || n == 7 || n == 8 || n == 12 || n == 60 || n == 64) { OPQS(io); OPQS(ig); setm0(io); }
        };
        static_assert(NPRE <= PB_ && PB_ <= XB_ && XB_ <= 20, "");
        auto site1 = LAM(N_) { site(N_); if constexpr (N_.v == XB_ + 4 || N_.v == XB_ + 8 || N_.v == XB_ + 9 || N_.v == 61 || N_.v == 65) nop2(); };
        if (conv) {
#pragma unroll
            for (int i = 0; i < 3; ++i) dma_ws(vw, l16, uo + i * 4096, L_Q + i * CVS + 4 * HCS + 480 + wave * 1024);
            setm0(io); nop2();
            sfor<0, NPRE>(site1);
            asm volatile("s_waitcnt vmcnt(%0)\n\ts_barrier" ::"n"(NPRE) : "memory");
            STAMP(3);
            if (wave < 3) {
                f4 acc[8];
                v4u A[2][8], B[2][8];
                sfor<0, 24>(LAM(N_) { conv_read<8, N_.v, 0>(aa, ba, A[0], B[0]); });
                const u32 dd = wave == 0 ? D2[0] : wave == 1 ? D2[1] : D2[2];
                v4u ad = {dm.x & dd, dm.y & dd, dm.z & dd, dm.w & dd};
                pin(ad);
                FENCE();
                sfor<0, 5>(LAM(J_) { constexpr int j = J_.v ? J_.v + 3 : 0; mm16<true>(acc[j], ad, B[0][j]); });
                FENCE();
                sfor<0, 8 * 27>(LAM(Q_) {
                    constexpr int r = Q_.v / 27, n = Q_.v % 27, i = conv_ij(8, 0xf1, n) >> 3, j = conv_ij(8, 0xf1, n) & 7;
                    if constexpr (r && n == 0) waitl<9>();
                    if constexpr (r && n == 6) waitl<(r < 7 ? 6 : 0)>();
                    mm16<false>(acc[j], A[r & 1][i], B[r & 1][j - i]);
                    if constexpr (r < 7 && n < 24) conv_read<8, n, r + 1>(aa, ba, A[(r + 1) & 1], B[(r + 1) & 1]);
                    sfor<site_at(Q_.v), site_at(Q_.v + 1)>(site);
                    FENCE();
                });
                asm volatile("s_nop 7" : "+v"(acc[0]), "+v"(acc[4]), "+v"(acc[5]), "+v"(acc[6]), "+v"(acc[7]));
                const u32 so = W_Y + (ch0 + wave) * 256;
                const v2u y0 = y_val(acc[0]);
                const v4u y4 = y_pair(y_val(acc[4]), y_val(acc[5])), y6 = y_pair(y_val(acc[6]), y_val(acc[7]));
                bst2(y0, vw, yo, so, SC1);
                bst4(y4, vw, yp, so + 8 * YB, SC1);
                bst4(y6, vw, yp, so + 12 * YB, SC1);
            } else {
                f4 acc[2][4];
                v4u A[2][4], B[2][4], ad;
                v2u y1; v4u y2;
                sfor<0, 12>(LAM(N_) { conv_read<4, N_.v, 0>(aa, ba, A[0], B[0]); });
                FENCE();
                sfor<0, 3 * 8 * 9>(LAM(Q_) {
                    constexpr int cn = Q_.v / 72, r = Q_.v % 72 / 9, n = Q_.v % 9, i = conv_ij(4, 0xe, n) >> 3, j = conv_ij(4, 0xe, n) & 7;
                    constexpr int nc = r < 7 ? cn : cn + 1, nr = (r + 1) & 7;
                    if constexpr (r == 0 && n == 0) {
                        ad = (v4u){dm.x & D2[cn], dm.y & D2[cn], dm.z & D2[cn], dm.w & D2[cn]};
                        pin(ad);
                        waitl<0>();
                        nop2();
                        FENCE();
                        sfor<1, 4>(LAM(J_) { mm16<true>(acc[cn & 1][J_.v], ad, B[0][J_.v]); });
                    } else {
                        if constexpr (n == 0) waitl<3>();
                        if constexpr (n == 7) waitl<(nc < 3 ? 10 : 0)>();
                    }
                    mm16<false>(acc[cn & 1][j], A[r & 1][i], B[r & 1][j - i]);
                    if constexpr (nc < 3) {
                        if constexpr (n < 3) { conv_read<4, 2 * n, nr>(aa + nc * CVS, ba + nc * CVS, A[nr & 1], B[nr & 1]); conv_read<4, 2 * n + 1, nr>(aa + nc * CVS, ba + nc * CVS, A[nr & 1], B[nr & 1]); }
                        else conv_read<4, n + 3, nr>(aa + nc * CVS, ba + nc * CVS, A[nr & 1], B[nr & 1]);
                    }
                    if constexpr (cn > 0 && r == 1) {
                        const u32 so = W_Y + (ch0 + cn - 1) * 256;
                        if constexpr (n == 3) { pin(acc[(cn - 1) & 1][1]); y1 = y_val(acc[(cn - 1) & 1][1]); pin(y1); }
                        if constexpr (n == 4) bst2(y1, vw, yo, so + 2 * YB, SC1);
                        if constexpr (n == 5) { pin(acc[(cn - 1) & 1][2]); y1 = y_val(acc[(cn - 1) & 1][2]); pin(y1); }
                        if constexpr (n == 6) { pin(acc[(cn - 1) & 1][3]); y2 = y_pair(y1, y_val(acc[(cn - 1) & 1][3])); pin(y2); }
                        if constexpr (n == 7) bst4(y2, vw, yp, so + 4 * YB, SC1);
                    }
                    sfor<site_at(Q_.v), site_at(Q_.v + 1)>(site);
                    FENCE();
                });
                asm volatile("s_nop 7" : "+v"(acc[0][1]), "+v"(acc[0][2]), "+v"(acc[0][3]));
                const u32 so = W_Y + (ch0 + 2) * 256;
                y1 = y_val(acc[0][1]); y2 = y_pair(y_val(acc[0][2]), y_val(acc[0][3]));
                bst2(y1, vw, yo, so + 2 * YB, SC1);
                bst4(y2, vw, yp, so + 4 * YB, SC1);
            }
        } else {
            setm0(io); nop2();
            sfor<0, NSITE>(site1);
        }
        STAMP(14);
        asm volatile("" : "+v"(cp[0]), "+v"(cp[1]), "+v"(cp[2]), "+v"(cp[3]));
        *(LAS f2*)(L_EX + wave * 512 + l8) = (f2){(asf(cp[0].x) + asf(cp[1].x)) + (asf(cp[2].x) + asf(cp[3].x)), (asf(cp[0].y) + asf(cp[1].y)) + (asf(cp[2].y) + asf(cp[3].y))};
        barrier();
    }
    u32 yv[9];
    v4u wv[17];
    STAMP(15);

    sfor<0, 2>(LAM(L_) {
        constexpr int L = L_.v;
        int ln = lane;
        OPQV(ln); OPQV(l16); OPQV(l4);
        const int tl = ln & 31, h = ln >> 5, row = ln & 15, kg = ln >> 4, gq = row >> 2, rq = row & 3;
        const bool qa = (gq & 1) == (kg & 1);
        u32 aV_ = L_V + l16, aQ_ = L_Q + l16;
        u32 aKV = L_QT + vsel(qa, (rq >> 1) * 128 + (gq >> 1) * 64 + (rq & 1) * 32 + (kg >> 1) * 16, 400 - 16 * (kg >> 1));
        u32 aQq, aCS, aS;
        OPQV(aV_); OPQV(aKV); OPQV(aQ_);
        u32 nretry = 0;
        const u32 wb = W_WOT + Pg * WOG + wave * 36864, wlb = W_WOT + Pg * WOG + 4 * 36864 + (wave >> 1) * 9216;
        u32 wso = wb;
        auto wsite = LAM(N_) {
            constexpr int n = N_.v, k = (n < 36 ? n : n - 36) % 4;
            if constexpr (L == 0 && n < 45) {
                if constexpr (n == 36) wso = wlb; else if constexpr (n && k == 0) wso += 4096;
                if constexpr (k == 0) OPQS(wso);
                if constexpr (n < 28) ld4a<k * 1024, 144 + 4 * n>(l16, vw, wso);
                else ld4o<k * 1024>(wv[n - 28], l16, vw, wso);
            }
        };
        f16v aK; f4 qKV;
        float mean, al, rstd;
        v4u rV[4], cK[5];
        {
            const u32 xb = W_XF1 + (rg * 4 + wave) * 16 * XS;
            u32 xs[8];
            float ts;
            auto xsite = LAM(Q_) { if constexpr (L == 1) xop(IC<Q_.v / 4 * 5 + Q_.v % 4 + (Q_.v % 4 > 1)>{}, xs[Q_.v / 4]); };
            auto stat = [&]() __attribute__((always_inline)) {
                auto r = __builtin_amdgcn_permlane32_swap(asu(ts), asu(ts), false, false);
                mean = asf(r[0]) * (1.f / 576.f);
                const float var = __builtin_fmaxf(asf(r[1]) * (1.f / 576.f) - mean * mean, 0.f);
                rstd = __builtin_amdgcn_rsqf(var + 1e-5f);
                al = rstd * 1.4426950408889634f;
                pin2(mean, al); pin(rstd);
            };
            if constexpr (L == 1) {
#pragma unroll
                for (int j = 0; j < 8; ++j) { xs[j] = xb + (((p & 14) + 2 * j) & 15) * XS; OPQS(xs[j]); }
                FENCE();
                if constexpr (XSL) __builtin_amdgcn_s_sleep(XSL);
                sfor<0, 8>(LAM(J_) { xop(IC<5 * J_.v + 2>{}, xs[J_.v]); });
                sfor<0, UPA_>(xsite);
                waitv<UPA_>();
                u32 m, m2;
                asm volatile("v_max3_u32 %0, v224, v226, v228\n\tv_max3_u32 %1, v230, v232, v234\n\tv_max3_u32 %0, %0, v236, v238\n\tv_max3_u32 %1, %1, v240, v242\n\t"
                             "v_max3_u32 %0, %0, v244, v246\n\tv_max3_u32 %1, %1, v248, v250\n\tv_max3_u32 %0, %0, v252, v254\n\tv_max_u32 %0, %0, %1" : "=&v"(m), "=&v"(m2));
                if (__builtin_expect(bad(m), 0)) {
#pragma unroll 1
                    for (int sp = 0; sp < SPIN; ++sp) {
                        u32 mk = 0;
                        sfor<0, 8>(LAM(J_) { mk |= (u32)bad(mx3vv<224 + 4 * J_.v>(0u)) << J_.v; });
                        if (!mk) break;
                        __builtin_amdgcn_s_sleep(RSX);
                        sfor<0, 8>(LAM(J_) { if (mk >> J_.v & 1) xop(IC<5 * J_.v + 2>{}, xs[J_.v]); });
                        waitv<0>();
                        ++nretry;
                    }
                }
                STAMP(25);
                STAMPV(63, nretry);
                nretry = 0;
                sfor<UPA_, xissued(0)>(xsite);
                sfor<0, 8>(LAM(J_) { swapw<224 + 4 * J_.v, 128 + 2 * J_.v>(); if constexpr (J_.v) { addv<225, 225 + 4 * J_.v>(); addv<227, 227 + 4 * J_.v>(); } });
                ts = swapsum<224>();
                stat();
            }
            v4u r0[3], r1[3], ones = {~0u, ~0u, ~0u, ~0u};
            f2 cq[4];
            u32 cm, cs_;
#pragma unroll
            for (int s = 0; s < 3; ++s) { r0[s] = lds4(l16 + s * 1024); r1[s] = lds4(aKV + s * QTS); }
            if constexpr (L == 0) pin(ones);
            FENCE();
            sfor<0, 36>(LAM(SI) {
                constexpr int n = SI.v, s = kstep(n), s3 = kstep(n + 3), i = s >> 1;
                if constexpr (L == 0 && s < 32 && !(s & 1)) {
                    constexpr int j = i >> 1, later = 39 - 5 * j - (i & 1 ? 4 : 1) + 9 + (n >> 1);
                    waitv<later>();
                    FENCE();
                }
                if constexpr (L == 1 && s < 32 && !(s & 3)) {
                    constexpr int j = i >> 1;
                    static_assert(xissued(n) >= 4 * j + 4, "");
                    waitv<xissued(n) - 4 * j - 4>();
                    auto late = LAM(J_) { constexpr int k = J_.v; return bad(umax(mx3(rda<16 * k>(), rda<16 * k + 4>(), rda<16 * k + 8>()), rda<16 * k + 12>())); };
                    if (__builtin_expect(late(IC<j>{}), 0)) {
#pragma unroll 1
                        for (int sp = 0; sp < SPIN; ++sp) {
                            sfor<4 * j, 4 * j + 4>(xsite);
                            waitv<0>();
                            ++nretry;
                            if (!late(IC<j>{})) break;
                        }
                    }
                    FENCE();
                }
                if constexpr (L == 0 && n == 32) asm volatile("s_waitcnt vmcnt(16)\n\ts_barrier" ::: "memory");
                waitl<(n < 34 ? 4 : 6)>();
                mm32b<n == 0, 4 * s>(aK, r0[n % 3]);
                if constexpr (n + 3 < 36) r0[n % 3] = lds4(l16 + s3 * 1024); else rV[n - 33] = lds4(aV_ + (n - 33) * 1024);
                sfor<xissued(n), xissued(n + 1)>(xsite);
                if constexpr (L == 0 && s < 32 && (s & 3) == 2) swapw<224 + 2 * (i - 1), 127 + i>();
                if constexpr (L == 0 && s > 3 && s < 31 && ((s & 3) == 3 || s == 30)) { addv<225, 225 + 4 * (i >> 1)>(); addv<227, 227 + 4 * (i >> 1)>(); }
                if constexpr (L == 0 && n == 34) ts = swapsum<224>();
                if constexpr (n == 1) { aQq = L_QT + vsel(qa && rq < 2, 256 + (gq >> 1) * 64 + (rq & 1) * 32 + (kg >> 1) * 16, 384); pin(aQq); }
                if constexpr (n == 3) { aCS = L_CS + h * 224; aS = L_STG + (wave * 32 + tl) * STGS + 72 * h; pin2(aCS, aS); }
                if constexpr (L == 0) {
                    constexpr int k = n >> 1;
                    if constexpr (n & 1) {
                        if constexpr (k == 0 || k == 3) { cs_ = W_O + ((k ? 1024 : 0) + (rg * 4 + wave) * 16 + p) * OS; OPQS(cs_); }
                        if constexpr (k == 6) { cs_ = W_XF1 + ((rg * 4 + tw) * 16 + pw) * XS; OPQS(cs_); }
                        if constexpr (k == 9) { cs_ = W_KV + (p * 16 + rg) * 2816 + wave * 704; OPQS(cs_); }
                        if constexpr (k == 10) { cs_ += 256 * 2816; OPQS(cs_); }
                        if constexpr (k == 11) { cs_ = W_ST + ((rg * 4 + tw) * 16 + pw) * 256; OPQS(cs_); }
                        if constexpr (k < 9 && k % 3 < 2) st4<(k % 3) * 1024>(ones, l16, vw, cs_);
                        else if constexpr (k < 9) st4<2048>(ones, cm, vw, cs_);
                        else if constexpr (k < 12) st4<0>(ones, cm, vw, cs_);
                        else wsite(IC<k - 12>{});
                    } else {
                        if constexpr (k == 2) { cm = vsel(ln < 16, l16, OOB); pin(cm); }
                        if constexpr (k == 8) { cm = vsel(ln < 32, l16, OOB); pin(cm); }
                        if constexpr (k == 9) { cm = vsel(ln < 44, l16, OOB); pin(cm); }
                        if constexpr (k == 11) { cm = vsel(ln < 32, row * 16 + (kg & 1) * (1024 * 256), OOB); pin(cm); }
                        if constexpr (k == 13) { cq[0].x = (cq[0].x + cq[1].x) + (cq[2].x + cq[3].x); cq[0].y = (cq[0].y + cq[1].y) + (cq[2].y + cq[3].y); pin(cq[0]); }
                    }
                }
                if constexpr (L == 0 && n == 35) stat();
                FENCE();
                mm16b<n == 0, 4 * s>(qKV, r1[n % 3]);
                if constexpr (n + 3 < 36) r1[n % 3] = lds4(aKV + s3 * QTS); else cK[n - 33] = lds4(aCS + (n - 33) * 16);
                if constexpr (n == 33) cK[3] = lds4(aCS + 48);
                if constexpr (n == 34) cK[4] = lds4(aCS + 192);
                if constexpr (n == 35) rV[3] = lds4(aV_ + 3 * 1024);
                FENCE();
                if constexpr (L == 0 && n >= 20 && n < 24) { cq[n - 20] = *(const LAS f2*)(l8 + (L_EX + (n - 20) * 512)); FENCE(); }
                if constexpr (L == 0 && n == 27) { *(LAS f2*)(csd) = cq[0]; FENCE(); }
            });
        }
        STAMP(4 + 12 * L);
        if constexpr (L == 1) STAMPV(56, nretry);
        v4u rQ[4], rq4[3];
        u32 pa, pb, kso;
        {
            f16v aV;
            float t[18], e1[18], e2[18], mx, mn, c1, c2, s1, s2;
            v4u cV[4];
            u32 qo = L_Q + wave * 9216, qg = W_IMG + p * IMG + qo;
            sfor<0, 36>(LAM(SI) {
                constexpr int s = SI.v;
                if constexpr (s >= 2 && s < 34 && !(s & 1)) waitl<2>();
                mm32b<s == 0, 4 * s>(aV, rV[s & 3]);
                if constexpr (s == 2) { pin(aK); pin(qKV); }
                if constexpr (s + 4 < 36) rV[s & 3] = lds4(aV_ + (s + 4) * 1024); else cV[s - 32] = lds4(aCS + 64 + (s - 32) * 16);
                if constexpr (s < 18) {
                    if constexpr (s == 0 || s == 8 || s == 16) { if constexpr (s) { qo += 4096; qg += 4096; } OPQS(qo); OPQS(qg); setm0(qo); }
                    if constexpr (s & 1) dmai<((s >> 1) & 3) * 1024>(vw, l16, qg);
                } else wsite(IC<s - 12>{});
                if constexpr (s == 1) {
                    const u32 r0 = L_STG + (4 * (4 * (kg & 1) + gq) + (kg >> 1)) * STGS + 8 * rq;
                    pa = r0 + 72 * (wave >> 1); pb = r0 + (144 + 40 * (wave >> 1) + 32 * (wave & 1));
                    kso = W_KV + ((L * 16 + p) * 16 + rg) * 2816 + (wave >> 1) * 1376;
                    pin2(pa, pb); OPQS(kso);
                }
                if constexpr (s >= 2 && s < 5) {
#pragma unroll
                    for (int i = 6 * (s - 2); i < 6 * (s - 2) + 6; ++i) { const u32 cv = cK[i >> 2][i & 3]; t[i] = __builtin_fmaf(-mean, asf(cv), i < 16 ? aK[i] : qKV[i - 16]); }
                }
                if constexpr (s == 5) { mx = __builtin_fmaxf(__builtin_fmaxf(t[0], t[1]), t[2]); mn = __builtin_fminf(__builtin_fminf(t[0], t[1]), t[2]);
#pragma unroll
                    for (int i = 3; i < 7; i += 2) { mx = __builtin_fmaxf(__builtin_fmaxf(mx, t[i]), t[i + 1]); mn = __builtin_fminf(__builtin_fminf(mn, t[i]), t[i + 1]); } }
                if constexpr (s == 6) {
#pragma unroll
                    for (int i = 7; i < 13; i += 2) { mx = __builtin_fmaxf(__builtin_fmaxf(mx, t[i]), t[i + 1]); mn = __builtin_fminf(__builtin_fminf(mn, t[i]), t[i + 1]); } }
                if constexpr (s == 7) {
#pragma unroll
                    for (int i = 13; i < 17; i += 2) { mx = __builtin_fmaxf(__builtin_fmaxf(mx, t[i]), t[i + 1]); mn = __builtin_fminf(__builtin_fminf(mn, t[i]), t[i + 1]); }
                    mx = __builtin_fmaxf(mx, t[17]); mn = __builtin_fminf(mn, t[17]); c1 = -al * mx; c2 = al * mn; }
                if constexpr (s >= 8 && s < 32) {
#pragma unroll
                    for (int q = (3 * (s - 8)) / 2; q < (3 * (s - 7)) / 2; ++q) {
                        const int i = q >> 1;
                        if (q & 1) { e2[i] = __builtin_amdgcn_exp2f(__builtin_fmaf(t[i], -al, c2)); s2 = i ? s2 + e2[i] : e2[i]; }
                        else { e1[i] = __builtin_amdgcn_exp2f(__builtin_fmaf(t[i], al, c1)); s1 = i ? s1 + e1[i] : e1[i]; }
                    }
                }
                if constexpr (s == 32) { s1 = __builtin_amdgcn_rcpf(s1); s2 = __builtin_amdgcn_rcpf(s2); }
                if constexpr (s >= 33) {
                    constexpr int i = s - 33;
                    *(LAS v2u*)(aS + 8 * i) = (v2u){pk(e1[4 * i] * s1, e1[4 * i + 1] * s1), pk(e1[4 * i + 2] * s1, e1[4 * i + 3] * s1)};
                }
                FENCE();
            });
            *(LAS v2u*)(aS + 24) = (v2u){pk(e1[12] * s1, e1[13] * s1), pk(e1[14] * s1, e1[15] * s1)};
            *(LAS v2u*)(aS + 32) = (v2u){pk(e1[16] * s1, e1[17] * s1), pk(e2[0] * s2, e2[1] * s2)};
#pragma unroll
            for (int i = 0; i < 4; ++i) *(LAS v2u*)(aS + 40 + 8 * i) = (v2u){pk(e2[4 * i + 2] * s2, e2[4 * i + 3] * s2), pk(e2[4 * i + 4] * s2, e2[4 * i + 5] * s2)};
            FENCE();
            pin(aV);
            const u32 aSv = aS + 144 - 32 * h;
            const float mr = -mean * rstd;
#pragma unroll
            for (int i = 0; i < 4; ++i) {
                const u32 q0 = cV[i].x, q1 = cV[i].y, q2 = cV[i].z, q3 = cV[i].w;
                *(LAS v2u*)(aSv + 8 * i) = (v2u){pk(__builtin_fmaf(mr, asf(q0), aV[4 * i] * rstd), __builtin_fmaf(mr, asf(q1), aV[4 * i + 1] * rstd)),
                                                 pk(__builtin_fmaf(mr, asf(q2), aV[4 * i + 2] * rstd), __builtin_fmaf(mr, asf(q3), aV[4 * i + 3] * rstd))};
            }
            const u32 q2 = cK[4].z, q3 = cK[4].w;
            *(LAS v2u*)(aSv + 32) = (v2u){pk(__builtin_fmaf(mr, asf(q2), qKV[2] * rstd), __builtin_fmaf(mr, asf(q3), qKV[3] * rstd)), 0x3f80u};
        }
        waitbar<(L ? 0 : 18)>();
        STAMP(5 + 12 * L);
        {
            f4 c[3];
            v4u B[4], A[4][3];
            auto rd = LAM(N_) {
                constexpr int ks = N_.v >> 2, f = N_.v & 3;
                if constexpr (f == 0) B[ks] = tr2(pb + ks * 32 * STGS, pb + (ks * 32 + 2) * STGS);
                else A[ks][f - 1] = tr2(pa + ks * 32 * STGS + 32 * (f - 1), pa + (ks * 32 + 2) * STGS + 32 * (f - 1));
            };
            sfor<0, 7>(LAM(N_) { rd(N_); FENCE(); });
            if constexpr (L == 0) { if (tid == 0) bst2((v2u){(u32)ep, (u32)(ep >> 32)}, vw, 0, W_F2 + (rg * 16 + p) * 128, SC1); }
            FENCE();
            sfor<0, 12>(LAM(I_) {
                constexpr int ks = I_.v / 3, ft = I_.v % 3;
                mm16<ks == 0>(c[ft], A[ks][ft], B[ks]);
                if constexpr (I_.v < 9) rd(IC<7 + I_.v>{});
                else { rQ[I_.v - 9] = lds4(aQ_ + (I_.v - 9) * 1024); rq4[I_.v - 9] = lds4(aQq + (I_.v - 9) * QTS); }
                FENCE();
            });
            rQ[3] = lds4(aQ_ + 3 * 1024);
            FENCE();
            pad3<5>(c[0], c[1], c[2]);
            bst4((v4u){pk(c[0][0], c[0][1]), pk(c[0][2], c[0][3]), pk(c[1][0], c[1][1]), pk(c[1][2], c[1][3])}, vw, kvA, kso, SC1);
            bst2((v2u){pk(c[2][0], c[2][1]), pk(c[2][2], c[2][3])}, vw, kvB, kso, SC1);
        }
        FENCE();
        STAMP(6 + 12 * L);
        v4u pq[5], kp[12], sel;
        u32 kvo, kv3, aF, aQ4, f2o, oso;
        u32 kb[4];
        v2u yfl;
        auto flags_ok = [&](u64 m) __attribute__((always_inline)) { return (__builtin_amdgcn_ballot_w64(yfl.x != (u32)ep || yfl.y != (u32)(ep >> 32)) & m) == 0; };
        auto flags_wait = [&](u64 m) __attribute__((always_inline)) {
            if (__builtin_expect(!flags_ok(m), 0)) {
#pragma unroll 1
                for (int sp = 0; sp < SPIN; ++sp) {
                    __builtin_amdgcn_s_sleep(1);
                    ld2<0>(yfl, f2o, vw, W_F2);
                    waitp<0>(yfl);
                    if (flags_ok(m)) break;
                }
            }
        };
        {
            f16v aQ; f4 qQ;
            v4u cQ[5];
            sfor<0, 36>(LAM(SI) {
                constexpr int s = SI.v;
                if constexpr (s >= 1 && s < 34) waitl<4>();
                mm32b<s == 0, 4 * s>(aQ, rQ[s & 3]);
                if constexpr (s + 4 < 36) rQ[s & 3] = lds4(aQ_ + (s + 4) * 1024); else cQ[s - 32] = lds4(aCS + 128 + (s - 32) * 16);
                if constexpr (s < 31 && s % 3 != 2) wsite(IC<24 + s - s / 3>{});
                if constexpr (s == 2) { kvo = kg * 2816 + wave * 256 + row * 16; kv3 = vsel(wave * 16 + row < 44, kvo + 2048, OOB); pin2(kvo, kv3); }
                if constexpr (s == 26) { for (int i = 0; i < 4; ++i) { kb[i] = W_KV + ((L * 16 + p) * 16 + 4 * i) * 2816; OPQS(kb[i]); } }
                if constexpr (s == 5 || s == 8) {
#pragma unroll
                    for (int i = (s - 5) / 3 * 2; i < (s - 5) / 3 * 2 + 2; ++i) sel[i] = row == 2 * i ? 0x3f80u : row == 2 * i + 1 ? 0x3f800000u : 0u;
                    if constexpr (s == 8) pin(sel);
                }
                if constexpr (s == 11) { const int hr = (tl >> 2) & 1, d = (tl & 3) + 4 * (tl >> 3); aF = vsel(h == hr, (hr * 19 + d) * 80 + L_KVA, L_Z); pin(aF); }
                if constexpr (s == 14) { aQ4 = vsel(kg == gq && rq < 3, ((gq >> 1) * 19 + 16 + rq) * 80 + L_KVA, L_Z); pin(aQ4); }
                if constexpr (WARM && s == WARM) {
                    const u32 sO = W_O + (L * 1024 + (rg * 4 + wave) * 16 + p) * OS, sX = W_XF1 + ((rg * 4 + tw) * 16 + pw) * XS - 18 * 128, sS = W_ST + (L * 1024 + (rg * 4 + tw) * 16 + pw) * 256 - 38 * 128;
                    const u32 wo = vsel(ln < 18, sO, vsel(ln < 38, L ? OOB - 8192 : sX, vsel(ln < 40, sS, OOB - 8192))) + 8 * l16;
                    asm volatile("s_mov_b32 m0, %2\n\ts_nop 0\n\tbuffer_load_dword %0, %1, 0 offen sc1 lds" ::"v"(wo), "s"(vw), "s"(L_EX) : "m0", "memory");
                }
                if constexpr (s == 17) { oso = W_O + (L * 1024 + (rg * 4 + wave) * 16 + p) * OS; OPQS(oso); }
                if constexpr (L == 0 && s == 20) { f2o = vsel(kg == 1, rg * 16 + row, row * 16 + (kg ? pw : p)) * 128; pin(f2o); }
                if constexpr (L == 0 && s == 23) ld2<0>(yfl, f2o, vw, W_F2);
                FENCE();
                mm16b<s == 0, 4 * s>(qQ, rq4[s % 3]);
                if constexpr (s + 3 < 36) rq4[s % 3] = lds4(aQq + (s + 3) * QTS);
                if constexpr (s == 33) cQ[4] = lds4(aCS + 208);
                FENCE();
            });
            STAMP(49 + L);
            if constexpr (L == 0) { waitp<5>(yfl); flags_wait(0xffffull); }
#pragma unroll
            for (int i = 0; i < 4; ++i) {
                kp[i] = bld4(vw, kvo, kb[i], SC1);
                kp[4 + i] = bld4(vw, kvo + 1024, kb[i], SC1);
                kp[8 + i] = bld4(vw, kv3, kb[i], SC1);
            }
            FENCE();
            pin(aQ); pin(qQ);
            float t[18]; u32 ph[18];
#pragma unroll
            for (int i = 0; i < 18; ++i) { const u32 cv = cQ[i >> 2][i & 3]; t[i] = __builtin_fmaf(-mean, asf(cv), i < 16 ? aQ[i] : qQ[i - 16]); }
            hedgehog(t, al, ph);
#pragma unroll
            for (int i = 0; i < 5; ++i) { pq[i] = (v4u){ph[4 * i], ph[4 * i + 1], i < 4 ? ph[4 * i + 2] : 0u, i < 4 ? ph[4 * i + 3] : 0u}; pin(pq[i]); }
            FENCE();
            STAMP(7 + 12 * L);
            u32 m = umax(kp[0].x, kp[0].z);
#pragma unroll
            for (int i = 1; i < 12; ++i) m = mx3(m, kp[i].x, kp[i].z);
            nretry = 0;
            if (__builtin_expect(bad(m), 0)) {
#pragma unroll 1
                for (int sp = 0; sp < SPIN; ++sp) {
                    u32 mk = 0;
                    waitv<0>();
#pragma unroll
                    for (int i = 0; i < 4; ++i) {
                        pin2(kp[i], kp[4 + i]); pin(kp[8 + i]);
                        mk |= (u32)bad(mx3(mx3(kp[i].x, kp[i].z, kp[4 + i].x), mx3(kp[4 + i].z, kp[8 + i].x, kp[8 + i].z), 0u)) << i;
                    }
                    if (!mk) break;
                    __builtin_amdgcn_s_sleep(RSK);
#pragma unroll
                    for (int i = 0; i < 4; ++i)
                        if (mk >> i & 1) { ld4<0>(kp[i], kvo, vw, kb[i]); ld4<1024>(kp[4 + i], kvo, vw, kb[i]); ld4<0>(kp[8 + i], kv3, vw, kb[i]); }
                    ++nretry;
                }
            }
            STAMPV(57 + L, nretry);
            f4 a[3];
            sfor<0, 12>(LAM(I_) { constexpr int i = I_.v / 3, j = I_.v % 3; mm16<i == 0>(a[j], sel, kp[4 * j + i]); });
            pad3<7>(a[0], a[1], a[2]);
#pragma unroll
            for (int j = 0; j < 3; ++j) *(LAS v2u*)(kvw[j]) = (v2u){pk(a[j][0], a[j][1]), pk(a[j][2], a[j][3])};
            barrier();
            STAMP(8 + 12 * L);
        }
        {
            v4u A[5], Aq[5];
#pragma unroll
            for (int c = 0; c < 5; ++c) Aq[c] = lds4(aQ4 + 16 * c);
#pragma unroll
            for (int c = 0; c < 5; ++c) A[c] = lds4(aF + 16 * c);
            FENCE();
            f16v o; f4 oq;
            float rd; u32 ol;
            sfor<0, 5>(LAM(C_) { mm16<C_.v == 0>(oq, Aq[C_.v], pq[C_.v]); });
            sfor<0, 5>(LAM(C_) {
                mm32<C_.v == 0>(o, A[C_.v], pq[C_.v]);
                if constexpr (C_.v == 3) { pad<4>(oq); rd = __builtin_amdgcn_rcpf(oq[2] + 1e-6f); pin(rd); }
                if constexpr (C_.v == 4) { ol = pk(oq[0] * rd, oq[1] * rd); pin(ol); }
                FENCE();
            });
            pad<8>(o);
            bst4((v4u){pk(o[0] * rd, o[1] * rd), pk(o[2] * rd, o[3] * rd), pk(o[4] * rd, o[5] * rd), pk(o[6] * rd, o[7] * rd)}, vw, l16, oso, SC1);
            bst4((v4u){pk(o[8] * rd, o[9] * rd), pk(o[10] * rd, o[11] * rd), pk(o[12] * rd, o[13] * rd), pk(o[14] * rd, o[15] * rd)}, vw, l16 + 1024, oso, SC1);
            bst1(ol, vw, l4 + 2048, oso, SC1);
        }
        FENCE();
        STAMP(9 + 12 * L);
        u32 aB, ae, ao, yo, ys;
        f16v aW; float xl0, xl1;
        if constexpr (L == 0) {
            flags_wait(~0ull);
            ys = W_Y + rg * YB + 36 * pw * 256 + tw * 64; yo = (ln >> 2) * 256 + rq * 16;
            OPQS(ys); OPQV(yo);
        }
#pragma unroll
        for (int i = 0; i < 8; ++i) { aW[2 * i] = bf_lo(xr[i]); aW[2 * i + 1] = bf_hi(xr[i]); }
        xl0 = bf_lo(xr[8]); xl1 = bf_hi(xr[8]);
        aB = L_Q + ((wave >> 1) * 18 + (kg & 1)) * 1024 + ((kg >> 1) * 32 + 16 * (wave & 1) + row) * 16;
        ae = L_EX + (kg & 1) * 1024 + ((2 * (wave >> 1) + h) * 16 + row) * 16 + 8 * (wave & 1);
        ao = aQ_ + wave * 8192;
        pin(aW); pin2(xl0, xl1); pin2(aB, ae); pin(ao);
        {
            u32 so[4];
#pragma unroll
            for (int i = 0; i < 4; ++i) { so[i] = W_O + (L * 1024 + (rg * 4 + tw) * 16 + 4 * wave + i) * OS; OPQS(so[i]); }
            v4u og[8]; u32 lo[4];
            FENCE();
            STAMP(43 + 3 * L);
            if constexpr (OWT) waitv<0>();
            STAMP(44 + 3 * L);
            if constexpr (OSL) __builtin_amdgcn_s_sleep(OSL);
            auto ldo = [&]() __attribute__((always_inline)) {
#pragma unroll
                for (int i = 0; i < 8; ++i) og[i] = bld4(vw, l16 + (i & 1) * 1024, so[i >> 1], SC1);
            };
            auto ldl = [&]() __attribute__((always_inline)) {
#pragma unroll
                for (int i = 0; i < 4; ++i) lo[i] = bld1(vw, l4 + 2048, so[i], SC1);
            };
            auto chk = [&]() __attribute__((always_inline)) {
                u32 m = mx3(umax(lo[0], lo[1]), lo[2], lo[3]);
#pragma unroll
                for (int i = 0; i < 8; i += 2) m = mx3(m, og[i].x, og[i + 1].x);
                return bad(m);
            };
            ldo(); ldl();
            FENCE();
            nretry = 0;
            if (__builtin_expect(chk(), 0)) {
#pragma unroll 1
                for (int sp = 0; sp < SPIN; ++sp) {
                    u32 mk = 0;
                    waitv<0>();
#pragma unroll
                    for (int i = 0; i < 4; ++i) {
                        pin2(og[2 * i], og[2 * i + 1]); pin(lo[i]);
                        mk |= (u32)bad(mx3(og[2 * i].x, og[2 * i + 1].x, lo[i])) << i;
                    }
                    if (!mk) break;
                    __builtin_amdgcn_s_sleep(RSO);
#pragma unroll
                    for (int i = 0; i < 4; ++i)
                        if (mk >> i & 1) { ld4<0>(og[2 * i], l16, vw, so[i]); ld4<1024>(og[2 * i + 1], l16, vw, so[i]); ld1<2048>(lo[i], l4, vw, so[i]); }
                    ++nretry;
                }
            }
            STAMPV(59 + L, nretry);
            STAMP(45 + 3 * L);
#pragma unroll
            for (int i = 0; i < 8; ++i) *(LAS v4u*)(ao + i * 1024) = og[i];
            *(LAS v4u*)(aQ_ + (32 + wave) * 1024) = (v4u){lo[0], lo[1], lo[2], lo[3]};
            barrier();
        }
        STAMP(10 + 12 * L);
        u32 sto = W_ST + (L * 1024 + (rg * 4 + tw) * 16) * 256;
        {
            f4 qW;
            v4u rb_[4], rl[9];
            v2u e0, e1;
            float s1, s2;
            u32 le;
#pragma unroll
            for (int i = 0; i < 4; ++i) { rl[i] = lds4(aB + i * 2048); rb_[i] = lds4(aQ_ + i * 1024); }
            FENCE();
            sfor<0, 36>(LAM(SI) {
                constexpr int s = SI.v;
                if constexpr (s < 28) mm32a<false, 144 + 4 * s>(aW, rb_[s & 3]); else mm32<false>(aW, wv[s - 28], rb_[s & 3]);
                if constexpr (s + 4 < 36) rb_[s & 3] = lds4(aQ_ + (s + 4) * 1024);
                if constexpr (s < 5) rl[4 + s] = lds4(aB + (4 + s) * 2048);
                if constexpr (s == 12) { pad<0>(qW); *(LAS f4*)(l16 + (L_EX + wave * 1024)) = qW; }
                if constexpr (s == 14) OPQS(sto);
                if constexpr (s == 24) { le = l4; pin(le); }
                if constexpr (L == 0 && s >= 16 && s < 22 && !(s & 1)) dma_ws(vw, yo, ys + (s - 16) * 2048, L_STG + wave * 3072 + (s - 16) * 512);
                FENCE();
                if constexpr (s < 9) { mm16<s == 0>(qW, wv[8 + s], rl[s]); FENCE(); }
                if constexpr (s == 30) { barrier(); e0 = lds2(ae); e1 = lds2(ae + 2048); FENCE(); }
                if constexpr (s == 33) { xl0 += asf(e0.x) + asf(e1.x); xl1 += asf(e0.y) + asf(e1.y); s1 = xl0 + xl1; s2 = __builtin_fmaf(xl0, xl0, xl1 * xl1); pin2(s1, s2); FENCE(); }
            });
            STAMP(11 + 12 * L);
            pad<11>(aW);
            float s3 = aW[0], s4 = aW[0] * aW[0];
#pragma unroll
            for (int i = 1; i < 16; ++i) { if (i & 1) { s1 += aW[i]; s2 = __builtin_fmaf(aW[i], aW[i], s2); } else { s3 += aW[i]; s4 = __builtin_fmaf(aW[i], aW[i], s4); } }
            bst1(asu(halves_sum(s1 + s3, s2 + s4)), vw, le, sto + pw * 256, SC1);
        }
        FENCE();
        {
            float t;
            u32 xso = L ? (rg * 128 + tw * 32) * 1152 + 72 * pw : W_XF1 + ((rg * 4 + tw) * 16 + pw) * XS, oo = tl * 1152 + 36 * h, one2 = 0x3f803f80u;
            OPQS(xso); OPQS(one2);
            if constexpr (L == 1) OPQV(oo);
            u32 yf[18], lS;
            if constexpr (L == 0) {
                const u32 ya = (18 * h + gq) * 64 + 32 * (kg & 1) + 8 * rq + (L_STG + wave * 3072);
                lS = tl * 16 + h * 8;
                pin(lS);
                waitv<1>();
#pragma unroll
                for (int i = 0; i < 5; ++i) {
                    const v2u v = __builtin_bit_cast(v2u, __builtin_amdgcn_ds_read_tr16_b64_v4i16((LAS s4*)(ya + 256 * i)));
                    yv[2 * i] = v.x; if (i < 4) yv[2 * i + 1] = v.y;
                }
            }
#pragma unroll
            for (int i = 0; i < 9; ++i) { yf[2 * i] = yv[i] << 16; yf[2 * i + 1] = yv[i] & 0xffff0000u; pin2(yf[2 * i], yf[2 * i + 1]); }
            FENCE();
            waitv<0>();
            if constexpr (SSL) __builtin_amdgcn_s_sleep(SSL);
            nretry = 0;
#pragma unroll 1
            for (int sp = 0; sp < SPIN; ++sp) {
                asm volatile("" : "+v"(l4)::"memory");
                u32 e[16];
#pragma unroll
                for (int i = 0; i < 16; ++i) e[i] = bld1(vw, l4 + i * 256, sto, SC1);
                FENCE();
                u32 m = umax(e[0], e[1]);
                t = asf(e[0]) + asf(e[1]);
#pragma unroll
                for (int i = 2; i < 16; i += 2) { m = mx3(m, e[i], e[i + 1]); t += asf(e[i]) + asf(e[i + 1]); pin2(m, t); }
                if (!bad(m)) break;
                ++nretry;
                __builtin_amdgcn_s_sleep(RSS);
            }
            STAMP(12 + 12 * L);
            STAMPV(61 + L, nretry);
            auto r = __builtin_amdgcn_permlane32_swap(asu(t), asu(t), false, false);
            const float m1 = asf(r[0]) * (1.f / 576.f);
            const float r1 = __builtin_amdgcn_rsqf(__builtin_fmaxf(asf(r[1]) * (1.f / 576.f) - m1 * m1, 0.f) + 1e-5f), mr = -m1 * r1;
#pragma unroll
            for (int i = 0; i < 9; ++i) {
                const float a = i < 8 ? aW[2 * i] : xl0, c = i < 8 ? aW[2 * i + 1] : xl1;
                xr[i] = pk(__builtin_fmaf(__builtin_fmaf(a, r1, mr), asf(yf[2 * i]), a), __builtin_fmaf(__builtin_fmaf(c, r1, mr), asf(yf[2 * i + 1]), c));
                if constexpr (L == 0) {
                    if (i == 3) { bst4((v4u){xr[0], xr[1], xr[2], xr[3]}, vw, l16, xso, SC1); FENCE(); }
                    if (i == 7) { bst4((v4u){xr[4], xr[5], xr[6], xr[7]}, vw, l16 + 1024, xso, SC1); FENCE(); }
                } else {
                    if (i == 3) { bst4((v4u){xr[0], xr[1], xr[2], xr[3]}, ro, oo, xso, 0); FENCE(); }
                    if (i == 7) { bst4((v4u){xr[4], xr[5], xr[6], xr[7]}, ro, oo + 16, xso, 0); FENCE(); }
                    if (i == 8) bst1(xr[8], ro, oo + 32, xso, 0);
                }
            }
            if constexpr (L == 0) {
                float s1 = 0.f, s2 = 0.f;
#pragma unroll
                for (int j = 0; j < 9; ++j) { s1 = dot2(xr[j], one2, s1); s2 = dot2(xr[j], xr[j], s2); }
                bst2((v2u){xr[8], asu(halves_sum(s1, s2))}, vw, lS + 2048, xso, SC1);
                STAMP(13);
            } else {
                if (b == 0 && tid == 0) bst2((v2u){(u32)ep, (u32)(ep >> 32)}, vw, 0, W_E, SC1);
            }
        }
    });
    STAMP_END();
}
#if defined(__HIP_DEVICE_COMPILE__)
#pragma clang attribute pop
#endif

extern "C" void kernel_launch(void* const* d_in, const int* in_sizes, int n_in, void* d_out, int out_size, void* d_ws, size_t ws_size, hipStream_t stream) {
    (void)in_sizes; (void)n_in; (void)out_size; (void)ws_size;
    hipFuncSetAttribute((const void*)k_main, hipFuncAttributeMaxDynamicSharedMemorySize, L_END);
    const u32 f = 0x00020000u, n = 2048 * 1152;
    Args a = {{d_in[5], n, f}, {d_in[6], n, f}, {{d_in[1], 576 * 3456, f}, {d_in[3], 576 * 1152, f}}, {{d_in[2], 32 * 648, f}, {d_in[2], 0, f}}, {d_in[0], n, f}, {d_ws, W_END, f}, {d_out, n, f}, d_in[7], d_in[8], d_in[9]};
    k_main<<<256, 256, L_END, stream>>>(a);
}
```
